```python
import torch

def _softmax(x, axis=-1):
    return torch.softmax(x, dim=axis)

def _gelu(x):
    return torch.nn.functional.gelu(x, approximate='tanh')

def _silu(x):
    return torch.nn.functional.silu(x)

def _sigmoid(x):
    return torch.sigmoid(x)

def _layernorm(x, axis=-1, eps=1e-05):
    m = x.mean(dim=axis, keepdim=True)
    v = x.var(dim=axis, keepdim=True, unbiased=False)
    return (x - m) / torch.sqrt(v + eps)

def _scatter_add(tgt, idx, src, axis=0):
    return tgt.clone().index_add_(axis, idx, src)

def _segment_sum(src, seg, n):
    out = torch.zeros((n,) + src.shape[1:], dtype=src.dtype)
    return out.index_add_(0, seg, src)

def _pad(x, pw):
    flat = tuple((v for pair in reversed(pw) for v in pair))
    return torch.nn.functional.pad(x, flat)
kernel_name = 'adaLN_cond__compressed_kv_cla__glu_gated_geglu__decode'

def setup_inputs():

    _w = lambda a: (lambda t: t.to(torch.bfloat16) if t.is_floating_point() else t)(torch.as_tensor(a))

    def _A_setup():
        B, H, Sq, Sk, D, M = (1, 32, 1, 32768, 128, 512)
        Dc = 4 * D
        return {'x': _w((lambda *s: torch.randn(*s, dtype=torch.bfloat16) * 0.1)(B, Sq, M)), 'c_kv': _w((lambda *s: torch.randn(*s, dtype=torch.bfloat16) * 3.2)(B, Sk, Dc)), 'w_q': _w((lambda *s: torch.randn(*s, dtype=torch.bfloat16) * 0.8)(M, H, D) * M ** (-0.5)), 'w_uk': _w((lambda *s: torch.randn(*s, dtype=torch.bfloat16) * 0.8)(Dc, H, D) * Dc ** (-0.5)), 'w_uv': _w((lambda *s: torch.randn(*s, dtype=torch.bfloat16) * 3.2)(Dc, H, D) * Dc ** (-0.5)), 'w_o': _w((lambda *s: torch.randn(*s, dtype=torch.bfloat16) * 3.2)(H, D, M) * (H * D) ** (-0.5))}
    _A_in = _A_setup()

    def _B_setup():
        B, S, Dm, Df = (1, 1, 512, 1792)
        return {'x': _w((lambda *s: torch.randn(*s, dtype=torch.bfloat16) * 0.1)(B, S, Dm)), 'w_gate': _w((lambda *s: torch.randn(*s, dtype=torch.bfloat16) * 1.6)(Dm, Df) * Dm ** (-0.5)), 'w_up': _w((lambda *s: torch.randn(*s, dtype=torch.bfloat16) * 1.6)(Dm, Df) * Dm ** (-0.5)), 'w_down': _w((lambda *s: torch.randn(*s, dtype=torch.bfloat16) * 1.6)(Df, Dm) * Df ** (-0.5))}
    _B_in = _B_setup()
    return {'A_x': _A_in['x'], 'A_c_kv': _A_in['c_kv'], 'A_w_q': _A_in['w_q'], 'A_w_uk': _A_in['w_uk'], 'A_w_uv': _A_in['w_uv'], 'A_w_o': _A_in['w_o'], 'B_x': _B_in['x'], 'B_w_gate': _B_in['w_gate'], 'B_w_up': _B_in['w_up'], 'B_w_down': _B_in['w_down'], 'c': _w((lambda *s: torch.randn(*s, dtype=torch.bfloat16) * 0.1)(1, 512) * 8.0), 'w_mod': _w((lambda *s: torch.randn(*s, dtype=torch.bfloat16) * 0.1)(512, 6 * 512) * 512 ** (-0.5) * 8.0)}

def reference(A_x, A_c_kv, A_w_q, A_w_uk, A_w_uv, A_w_o, B_x, B_w_gate, B_w_up, B_w_down, c, w_mod):

    def _A_ref(x, c_kv, w_q, w_uk, w_uv, w_o):
        B, Sq, M = (x.shape[0], x.shape[1], x.shape[2])
        Sk = c_kv.shape[1]
        H, D = (w_q.shape[1], w_q.shape[2])
        q = torch.einsum('bqm,mhd->bhqd', x, w_q)
        k = torch.einsum('bkc,chd->bhkd', c_kv, w_uk)
        v = torch.einsum('bkc,chd->bhkd', c_kv, w_uv)
        scores = torch.einsum('bhqd,bhkd->bhqk', q, k) * D ** (-0.5)
        off = Sk - Sq
        qi = torch.arange(Sq)[:, None]
        kj = torch.arange(Sk)[None, :]
        cmask = kj <= qi + off
        scores = torch.where(cmask[None, None], scores, -1000000000.0)
        p = _softmax(scores, axis=-1)
        ctx = torch.einsum('bhqk,bhkd->bhqd', p, v)
        return torch.einsum('bhqd,hdm->bqm', ctx, w_o)

    def _B_ref(x, w_gate, w_up, w_down):
        g = torch.einsum('bsd,df->bsf', x, w_gate)
        u = torch.einsum('bsd,df->bsf', x, w_up)
        h = _gelu(g) * u
        return torch.einsum('bsf,fd->bsd', h, w_down)
    x = A_x
    mod = (_silu(c) @ w_mod).reshape(c.shape[0], 6, 1, -1)
    sa, ca, ga, sb, cb, gb = (mod[:, 0].clone(), mod[:, 1].clone(), mod[:, 2].clone(), mod[:, 3].clone(), mod[:, 4].clone(), mod[:, 5].clone())
    h = _layernorm(x, axis=-1) * (1 + ca) + sa
    x = x + ga * _A_ref(h, A_c_kv, A_w_q, A_w_uk, A_w_uv, A_w_o)
    h = _layernorm(x, axis=-1) * (1 + cb) + sb
    x = x + gb * _B_ref(h, B_w_gate, B_w_up, B_w_down)
    return x
```

```cpp
#include <hip/hip_runtime.h>

typedef unsigned u32;
typedef unsigned long long u64;
typedef unsigned short u16;
typedef u32 v4u __attribute__((ext_vector_type(4)));
typedef u32 v2u __attribute__((ext_vector_type(2)));
typedef float f32x2 __attribute__((ext_vector_type(2)));
typedef float f32x4 __attribute__((ext_vector_type(4)));
typedef float f32x16 __attribute__((ext_vector_type(16)));
typedef short bf16x8 __attribute__((ext_vector_type(8)));
typedef short s16x4 __attribute__((ext_vector_type(4)));
typedef __bf16 bf16x2_t __attribute__((ext_vector_type(2)));
#define LAS __attribute__((address_space(3)))
#define DI __device__ __forceinline__

#define CFG(name, val) constexpr int name = val
CFG(BUDGET, 1 << 20);
CFG(POLL_SLEEP, 4);
CFG(HINT_SLEEP, 4);
#define PACE 180, 270, 65, 580, 10, 620, 100, 140, 20, 195, 580, 0, -70, 0, 90, 55
constexpr int pace_[] = {PACE};
CFG(ST_LOOK, pace_[0]);
CFG(A_FIRST, pace_[1]);
CFG(A2_DELAY, pace_[2]);
CFG(Q_FIRST, pace_[3]);
CFG(T_START1, pace_[4]);
CFG(T_START, pace_[5]);
CFG(C_DELAY, pace_[6]);
CFG(C_DELAY2, pace_[7]);
CFG(G_DELAY, pace_[8]);
CFG(F_DELAY, pace_[9]);
CFG(T_MID, pace_[10]);
CFG(S_DELAY, pace_[11]);
CFG(WQ_DELAY, pace_[13]);
CFG(T_OPEN, pace_[14]);
CFG(SKEW_MAX, pace_[15]);
CFG(P_SKEW, pace_[12]);
#define DEPTH_ 3
#define RAMP_ 0
#define D0_ 0
CFG(RAMP, RAMP_); CFG(D0, D0_);
constexpr int upto(int t) { return t + DEPTH_; }
static_assert(DEPTH_ == 3, "");
CFG(DEPTH, DEPTH_);
CFG(HC_K, 5);
#define QD 2
#define PRIO_Y 3
#define PRIO_P0 2
#define PRIO_P1 1
#define EARLY_W 0
#define HOLD C_Q
#define QSLEEP 4
#define INQK 6
#define KH 13
#define LAZY 48.f
CFG(CODE_LAST, 20480);
#define WS_BASE(sz) (((sz) - (32u << 20)) & ~(size_t)((2u << 20) - 1))

constexpr u32 WS_EPOCH = 0;
DI u32 piece(u32 base, u32 size, u32 i) { return base + i * size; }
constexpr u32 WS_ST = 16384;
constexpr u32 WS_HP = 32768;
constexpr u32 WS_QABS = 65536;
constexpr u32 WS_QCHK = 98304;
constexpr u32 WS_QP = 131072;
constexpr u32 WS_ML = 262144;
constexpr u32 WS_SC = 360448;
constexpr u32 WS_X1 = 327680;
constexpr u32 HSTR = 1152, WS_HC = 368640;
constexpr u32 WS_OUTP = 524288;
constexpr u32 WS_YP = 1u << 20;
constexpr u32 WS_PAY = 2u << 20;
constexpr u32 PBLK = 256, PHD = 4 * 2 * 128 * PBLK + 4096;
constexpr u32 WS_TL = 16u << 20;

constexpr u32 L_CKV = 0;
constexpr u32 L_IMG = 131072;
constexpr u32 L_P2 = L_IMG;
constexpr u32 L_P = L_IMG + 8192;
constexpr u32 L_REL = L_IMG + 16384;
constexpr u32 L_LP = L_IMG + 19456;
constexpr u32 L_RF = 128000;
constexpr u32 L_CNT = 130048;
constexpr u32 C_Z = L_CNT, C_E = L_CNT + 4, C_M = L_CNT + 8, C_L = L_CNT + 12, C_Q = L_CNT + 16, C_E0 = L_CNT + 20, C_TB = L_CNT + 256, C_T = L_CNT + 32;
constexpr u32 NOREF = 0xffc00000u;
constexpr u32 L_W = 0;
constexpr u32 L_H2 = 32768;
constexpr u32 L_GU = 34816;
constexpr u32 F_QP = 131072, F_TB = F_QP + 4096;
constexpr u32 F_WO = 0;
constexpr u32 F_UV = 131072;
constexpr u32 F_A = 0;
constexpr u32 F_ST = 8192;
constexpr u32 F_GO = 8384;
constexpr u32 F_CX = 8448;
constexpr u32 F_ML = 12288;
constexpr u32 F_RD = 20480, F_RV = 22528;
constexpr u32 LDS_BYTES = 163840;

struct Params {
    const u16 *wmod, *c, *x;
    char* ws;
    const u16 *wq, *ckv, *wuk, *wo, *wuv, *wg, *wu, *wd;
    u16* out;
};

DI float asf(u32 v) { return __builtin_bit_cast(float, v); }
DI u32 asu(float v) { return __builtin_bit_cast(u32, v); }
DI float bflo(u32 v) { return asf(v << 16); }
DI float bfhi(u32 v) { return asf(v & 0xffff0000u); }
DI u32 pack2(float a, float b) { f32x2 v = {a, b}; return __builtin_bit_cast(u32, __builtin_convertvector(v, bf16x2_t)); }
DI float dot2(u32 w, u32 x, float acc) { return __builtin_amdgcn_fdot2_f32_bf16(__builtin_bit_cast(bf16x2_t, w), __builtin_bit_cast(bf16x2_t, x), acc, false); }
DI void pair_rows(v4u& x, v4u& y) {
#pragma unroll
    for (int k = 0; k < 4; ++k) { const u32 a = x[k], b = y[k]; x[k] = __builtin_amdgcn_perm(b, a, 0x05040100u); y[k] = __builtin_amdgcn_perm(b, a, 0x07060302u); }
}
template <int CTRL> DI float dpp(float v) { return asf((u32)__builtin_amdgcn_update_dpp(0, (int)asu(v), CTRL, 0xf, 0xf, true)); }
DI float red32(float a, float b) { auto r = __builtin_amdgcn_permlane32_swap(asu(a), asu(b), false, false); return asf((u32)r[0]) + asf((u32)r[1]); }
DI float red16(float a, float b) { auto r = __builtin_amdgcn_permlane16_swap(asu(a), asu(b), false, false); return asf((u32)r[0]) + asf((u32)r[1]); }
DI float max3(float a, float b, float c) { return __builtin_fmaxf(__builtin_fmaxf(a, b), c); }
DI float max32(float a) { auto r = __builtin_amdgcn_permlane32_swap(asu(a), asu(a), false, false); return max3(asf((u32)r[0]), asf((u32)r[1]), a); }
DI float max16(float a) { auto r = __builtin_amdgcn_permlane16_swap(asu(a), asu(a), false, false); return max3(asf((u32)r[0]), asf((u32)r[1]), a); }
DI float max8(const f32x4& a, const f32x4& b) { return max3(max3(max3(a.x, a.y, a.z), a.w, b.x), max3(b.y, b.z, b.w), a.x); }
DI u32 perm_lo(u32 a, u32 b) { return __builtin_amdgcn_perm(b, a, 0x05040100u); }
DI u32 perm_hi(u32 a, u32 b) { return __builtin_amdgcn_perm(b, a, 0x07060302u); }
DI u32 rfl(u32 v) { return (u32)__builtin_amdgcn_readfirstlane((int)v); }
DI void both16(u32 a, u32& e, u32& o) { auto r = __builtin_amdgcn_permlane16_swap(a, a, false, false); e = (u32)r[0]; o = (u32)r[1]; }
DI void both32(u32 a, u32& l, u32& h) { auto r = __builtin_amdgcn_permlane32_swap(a, a, false, false); l = (u32)r[0]; h = (u32)r[1]; }
DI float rowsum(float v) { v += dpp<0x128>(v); v += dpp<0x124>(v); v += dpp<0x122>(v); v += dpp<0x121>(v); return v; }
DI float ex2(float v) { return __builtin_amdgcn_exp2f(v); }

DI v4u ld_nt(const void* base, u32 off) { v4u r; asm volatile("global_load_dwordx4 %0, %1, %2 nt" : "=v"(r) : "v"(off), "s"(base) : "memory"); return r; }
DI v4u ld_pl(const void* base, u32 off) { v4u r; asm volatile("global_load_dwordx4 %0, %1, %2" : "=v"(r) : "v"(off), "s"(base) : "memory"); return r; }
DI v2u ld_sc2(const void* base, u32 off) { v2u r; asm volatile("global_load_dwordx2 %0, %1, %2 sc1" : "=v"(r) : "v"(off), "s"(base) : "memory"); return r; }
DI v2u ld_u64(const void* base, u32 off) { v2u r; asm volatile("global_load_dwordx2 %0, %1, %2" : "=v"(r) : "v"(off), "s"(base) : "memory"); return r; }
DI u32 ld_u32(const void* base, u32 off) { u32 r; asm volatile("global_load_dword %0, %1, %2" : "=v"(r) : "v"(off), "s"(base) : "memory"); return r; }
DI u32 ld_u32_nt(const void* base, u32 off) { u32 r; asm volatile("global_load_dword %0, %1, %2 nt" : "=v"(r) : "v"(off), "s"(base) : "memory"); return r; }
DI u32 ld_u16(const void* base, u32 off) { u32 r; asm volatile("global_load_ushort %0, %1, %2" : "=v"(r) : "v"(off), "s"(base) : "memory"); return r; }
DI void st_sc(void* base, u32 off, v4u v) { asm volatile("global_store_dwordx4 %0, %1, %2 sc1\n\ts_nop 1" :: "v"(off), "v"(v), "s"(base) : "memory"); }
DI void st_sc2(void* base, u32 off, v2u v) { asm volatile("global_store_dwordx2 %0, %1, %2 sc1\n\ts_nop 1" :: "v"(off), "v"(v), "s"(base) : "memory"); }
DI void dma_nt(const void* base, u32 off, u32 lds_dst) {
    u32 keep;
    asm volatile("s_mov_b32 %0, m0\n\ts_mov_b32 m0, %2\n\ts_nop 0\n\tglobal_load_lds_dwordx4 %1, %3 nt\n\ts_mov_b32 m0, %0" : "=&s"(keep) : "v"(off), "s"(lds_dst), "s"(base) : "memory");
}
DI void dma_sc(const void* base, u32 off, u32 lds_dst) {
    u32 keep;
    asm volatile("s_mov_b32 %0, m0\n\ts_mov_b32 m0, %2\n\ts_nop 0\n\tglobal_load_lds_dwordx4 %1, %3 sc1\n\ts_mov_b32 m0, %0" : "=&s"(keep) : "v"(off), "s"(lds_dst), "s"(base) : "memory");
}
DI void ld_nt_into(v4u& r, const void* base, u32 off) { asm volatile("global_load_dwordx4 %0, %1, %2 nt" : "+v"(r) : "v"(off), "s"(base) : "memory"); }
DI void ld_sc_into(v4u& r, const void* base, u32 off) { asm volatile("global_load_dwordx4 %0, %1, %2 sc1" : "+v"(r) : "v"(off), "s"(base) : "memory"); }
#define VMWAIT_(n) asm volatile("s_waitcnt vmcnt(" #n ")" ::: "memory")
#define VMWAIT(n) VMWAIT_(n)
#define LGKM0() asm volatile("s_waitcnt lgkmcnt(0)" ::: "memory")
#define PIN(x) asm volatile("" : "+v"(x))
DI void barrier() { LGKM0(); __builtin_amdgcn_s_barrier(); asm volatile("" ::: "memory"); }

template <typename T> DI LAS T* lp(u32 off) { return (LAS T*)(size_t)off; }

#define STAMP(i) do {} while (0)
#define STAMP_OUT() do {} while (0)
#define CYC0() do {} while (0)
#define STAMPC(i) do {} while (0)

#define GL_(k, d, base, o) "s_bitcmp1_b32 %[need], " #k "\n\ts_cbranch_scc0 .Lg%=_" #k "\n\tglobal_load_dwordx4 %[" #d "], %[off], %[" #base "] offset:%[" #o "] sc1\n.Lg%=_" #k ":\n\t"
template <u32 S> DI void pass(const char* ws, u32 off, u32 need, v4u (&v)[1]) {
    asm volatile("global_load_dwordx4 %0, %1, %2 sc1\n\ts_waitcnt vmcnt(0)" : "=v"(v[0]) : "v"(off), "s"(ws) : "memory");
}
template <u32 S> DI void pass(const char* ws, u32 off, u32 need, v4u (&v)[2]) {
    asm volatile("global_load_dwordx4 %0, %2, %3 sc1\n\tglobal_load_dwordx4 %1, %2, %4 sc1\n\ts_waitcnt vmcnt(0)" : "=&v"(v[0]), "=&v"(v[1]) : "v"(off), "s"(ws), "s"(ws + S) : "memory");
}
template <u32 S> DI void pass(const char* ws, u32 off, u32 need, v4u (&v)[4]) {
    asm volatile(GL_(0, d0, b0, o0) GL_(1, d1, b0, o1) GL_(2, d2, b0, o2) GL_(3, d3, b0, o3) "s_waitcnt vmcnt(0)"
        : [d0] "+v"(v[0]), [d1] "+v"(v[1]), [d2] "+v"(v[2]), [d3] "+v"(v[3])
        : [off] "v"(off), [b0] "s"(ws), [need] "s"(need), [o0] "i"(0), [o1] "i"(S), [o2] "i"(2 * S), [o3] "i"(3 * S) : "memory", "scc");
}
template <u32 S> DI void pass(const char* ws, u32 off, u32 need, v4u (&v)[8]) {
    asm volatile(GL_(0, d0, b0, o0) GL_(1, d1, b0, o1) GL_(2, d2, b0, o2) GL_(3, d3, b0, o3) GL_(4, d4, b1, o0) GL_(5, d5, b1, o1) GL_(6, d6, b1, o2) GL_(7, d7, b1, o3) "s_waitcnt vmcnt(0)"
        : [d0] "+v"(v[0]), [d1] "+v"(v[1]), [d2] "+v"(v[2]), [d3] "+v"(v[3]), [d4] "+v"(v[4]), [d5] "+v"(v[5]), [d6] "+v"(v[6]), [d7] "+v"(v[7])
        : [off] "v"(off), [b0] "s"(ws), [b1] "s"(ws + 4 * S), [need] "s"(need), [o0] "i"(0), [o1] "i"(S), [o2] "i"(2 * S), [o3] "i"(3 * S) : "memory", "scc");
}
template <int N, u32 STRIDE, bool T32 = false, int SLP = POLL_SLEEP> DI void gather(const char* ws, u32 off, u32 tlo, u32 thi, v4u (&v)[N], int& budget) {
    u32 need = (1u << N) - 1u;
    if (N > 2) {
#pragma unroll
        for (int k = 0; k < N; ++k) asm volatile("" : "=v"(v[k]));
    }
#pragma clang loop unroll(disable)
    do {
        pass<STRIDE>(ws, off, need, v);
        need = 0u;
#pragma unroll
        for (int k = 0; k < N; ++k) if (__builtin_amdgcn_ballot_w64(T32 ? v[k].w != tlo : (((u64)v[k].w << 32 | v[k].z) != ((u64)thi << 32 | tlo))) != 0ull) need |= 1u << k;
        if (need == 0u) break;
        __builtin_amdgcn_s_sleep(SLP);
    } while (--budget > 0);
}
DI void lds_wait(u32 addr, u32 target, int& budget) {
#pragma clang loop unroll(disable)
    while (*(volatile LAS u32*)lp<u32>(addr) < target && --budget > 0) { __builtin_amdgcn_s_sleep(1); }
    asm volatile("" ::: "memory");
}
DI void lds_add(u32 addr, int lane) {
    u32 a, one;
    asm volatile("s_mov_b64 exec, 1\n\tv_mov_b32 %0, %2\n\tv_mov_b32 %1, 1\n\tds_add_u32 %0, %1\n\ts_mov_b64 exec, -1" : "=&v"(a), "=&v"(one) : "s"(addr) : "memory");
}
DI u32 lds_inc(u32 addr, int lane) {
    u32 old = 0;
    LGKM0();
    if (lane == 0) old = __hip_atomic_fetch_add(lp<u32>(addr), 1u, __ATOMIC_RELAXED, __HIP_MEMORY_SCOPE_WORKGROUP);
    return rfl(old);
}
#define SWAPBIT(NAME, PERM, MASK) \
DI void NAME(u32* a, u32* b) { \
    u32 n0, n1, n2; \
    asm volatile("s_mov_b64 vcc, %[nc]\n\ts_nop 0\n\t" \
        "v_cndmask_b32_dpp %[n0], %[b0], %[a0], vcc " PERM " row_mask:0xf bank_mask:0xf\n\t" \
        "v_cndmask_b32_dpp %[n1], %[b1], %[a1], vcc " PERM " row_mask:0xf bank_mask:0xf\n\t" \
        "v_cndmask_b32_dpp %[n2], %[b2], %[a2], vcc " PERM " row_mask:0xf bank_mask:0xf\n\t" \
        "s_mov_b64 vcc, %[c]\n\t" \
        "v_cndmask_b32_dpp %[b0], %[a0], %[b0], vcc " PERM " row_mask:0xf bank_mask:0xf\n\t" \
        "v_cndmask_b32_dpp %[b1], %[a1], %[b1], vcc " PERM " row_mask:0xf bank_mask:0xf\n\t" \
        "v_cndmask_b32_dpp %[b2], %[a2], %[b2], vcc " PERM " row_mask:0xf bank_mask:0xf\n\ts_nop 1" \
        : [n0] "=&v"(n0), [n1] "=&v"(n1), [n2] "=&v"(n2), [b0] "+v"(b[0]), [b1] "+v"(b[1]), [b2] "+v"(b[2]) \
        : [a0] "v"(a[0]), [a1] "v"(a[1]), [a2] "v"(a[2]), [c] "s"(MASK), [nc] "s"(~(MASK)) : "vcc"); \
    a[0] = n0; a[1] = n1; a[2] = n2; \
}
SWAPBIT(swap_l0, "quad_perm:[1,0,3,2]", 0xAAAAAAAAAAAAAAAAull)
SWAPBIT(swap_l1, "quad_perm:[2,3,0,1]", 0xCCCCCCCCCCCCCCCCull)
template <int K> DI u32 hint_wait(const char* ws, u32 oh, u32 tlo, int& budget) {
    v4u h[1];
#pragma clang loop unroll(disable)
    do {
        pass<0>(ws, oh, 1u, h);
        if (__builtin_popcount((u32)__builtin_amdgcn_ballot_w64(h[0].z == tlo) & 0xffu) >= K) break;
        __builtin_amdgcn_s_sleep(HINT_SLEEP);
    } while (--budget > 0);
    const u32 now = (u32)__builtin_amdgcn_s_memrealtime();
    u32 age = h[0].z == tlo ? now - h[0].x : 0xffffu;
    age = age > 0xffffu ? 0u : age;
    age = min(age, (u32)__builtin_amdgcn_update_dpp(0, (int)age, 0xB1, 0xf, 0xf, true));
    age = min(age, (u32)__builtin_amdgcn_update_dpp(0, (int)age, 0x4E, 0xf, 0xf, true));
    age = min(age, (u32)__builtin_amdgcn_update_dpp(0, (int)age, 0x124, 0xf, 0xf, true));
    return now - rfl(age);
}
typedef u32 v16u __attribute__((ext_vector_type(16)));
DI u32 time_base(const char* p, u32 t0) {
    v16u h;
    asm volatile("s_load_dwordx16 %0, %1, 0x0\n\ts_waitcnt lgkmcnt(0)" : "=s"(h) : "s"(p) : "memory");
    u32 m = 0, bad = 0;
#pragma unroll
    for (int k = 0; k < 8; ++k) { m = max(m, h[2 * k] - t0 + 150u); bad |= h[2 * k] ^ ~h[2 * k + 1]; }
    return (bad != 0u || m >= 300u) ? t0 + SKEW_MAX : t0 - 150u + m;
}
DI void until_abs(u32 t) { while ((int)((u32)__builtin_amdgcn_s_memrealtime() - t) < 0) __builtin_amdgcn_s_sleep(1); }
DI void until32(u32 t, u32 ticks) { while ((u32)__builtin_amdgcn_s_memrealtime() + 1u - t < ticks) __builtin_amdgcn_s_sleep(1); }
DI void until(u64 t0, u32 ticks) { while (__builtin_amdgcn_s_memrealtime() - t0 < (u64)ticks) __builtin_amdgcn_s_sleep(2); }
DI u32 swz(u32 row) { return ((row & 3u) << 2) | ((row >> 2) & 3u); }

#define EPOCH() do { asm volatile("s_load_dwordx2 %0, %1, 0x0\n\ts_waitcnt lgkmcnt(0)" : "=s"(e64) : "s"(ws) : "memory"); ++e64; } while (0)
#define TAGS() EPOCH(); const u32 tlo = (u32)e64, thi = (u32)(e64 >> 32)
DI void wave_sum2(float s1, float s2, float& t1, float& t2) {
    float z = red32(s1, s2);
    z = red16(z, z); z = rowsum(z);
    u32 a, b;
    both32(asu(z), a, b);
    t1 = asf(a); t2 = asf(b);
}
DI void ln_stats(const v4u& xa, float& mean, float& rstd) {
    const float pil = asf(rfl(xa[0]) << 16);
    float s1 = 0.f, s2 = 0.f;
#pragma unroll
    for (int k = 0; k < 4; ++k) { const float a = bflo(xa[k]) - pil, c = bfhi(xa[k]) - pil; s1 += a; s2 = __builtin_fmaf(a, a, s2); s1 += c; s2 = __builtin_fmaf(c, c, s2); }
    float t1, t2;
    wave_sum2(s1, s2, t1, t2);
    const float dm = t1 * (1.f / 512.f);
    mean = pil + dm;
    rstd = __builtin_amdgcn_rsqf(__builtin_fmaxf(t2 * (1.f / 512.f) - dm * dm, 0.f) + 1e-5f);
}
DI float sigm(float v) { return __builtin_amdgcn_rcpf(1.f + ex2(v * -1.4426950408889634f)); }
DI float silu(float v) { return v * sigm(v); }

template <int NW> DI f32x2 reduce16(const char* ws, u32 base, u32 tlo, u32 thi, int wave, int lane, u32 lds_red, int& budget) {
    float z = 0.f;
    u32 la = lds_red + (u32)(wave * 16 + (lane & 7) * 2 + (lane >> 5)) * 4, ra = lds_red + (u32)lane * 8;
    PIN(la); PIN(ra);
    if (wave < NW) {
        v4u g[1];
        gather<1, 0, false, 1>(ws, base + (u32)(wave * 64 + lane) * 16u, tlo, thi, g, budget);
        z = red32(asf(g[0].x), asf(g[0].y));
        z = red16(z, z); z += dpp<0x128>(z);
    }
    *lp<float>(la) = z;
    barrier();
    f32x2 s = {0.f, 0.f};
    if (wave == 0) {
        s = *lp<f32x2>(ra) + *lp<f32x2>(ra + 512);
        float t = red32(s.x, s.y);
        t = red16(t, t); t += dpp<0x128>(t);
        u32 a, c;
        both32(asu(t), a, c);
        s = (f32x2){asf(a), asf(c)};
    }
    return s;
}

#define QOFF(s, hg) ((u32)(((((s) >> 2) + 1) & 3) * 8192 + (hg) * 4096))
#define KEPT0(s) ((s) < 8 || (s) >= 12)
#define KEPT1(s) ((s) < 4 || (s) >= 12)
DI void qk_first(const v4u (&kr)[16], bf16x8 (&q0)[16], bf16x8 (&q1)[16], const u32 (&bq)[4], f32x4& s0, f32x4& s1, int lane, bool young) {
#pragma unroll
    for (int i = 0; i < 4; ++i) { s0[i] = 0.f; s1[i] = 0.f; }
#pragma unroll
    for (int s = 0; s < 4; ++s) { q0[s] = *lp<bf16x8>(bq[s & 3] + QOFF(s, 0)); q1[s] = *lp<bf16x8>(bq[s & 3] + QOFF(s, 1)); }
    lds_add(C_E0, lane);
    if (young) __builtin_amdgcn_s_setprio(PRIO_Y);
#pragma unroll
    for (int s = 0; s < 16; ++s) {
        if (s >= 4 - QD && s < 16 - QD) { q0[s + QD] = *lp<bf16x8>(bq[(s + QD) & 3] + QOFF(s + QD, 0)); q1[s + QD] = *lp<bf16x8>(bq[(s + QD) & 3] + QOFF(s + QD, 1)); }
        __builtin_amdgcn_sched_barrier(0);
        s0 = __builtin_amdgcn_mfma_f32_16x16x32_bf16(__builtin_bit_cast(bf16x8, kr[s]), q0[s], s0, 0, 0, 0);
        s1 = __builtin_amdgcn_mfma_f32_16x16x32_bf16(__builtin_bit_cast(bf16x8, kr[s]), q1[s], s1, 0, 0, 0);
        __builtin_amdgcn_sched_barrier(0);
        if (s == 5) { lds_add(C_E, lane); __builtin_amdgcn_sched_barrier(0); }
        if (s == 13) { lds_add(C_M, lane); __builtin_amdgcn_sched_barrier(0); }
    }
}
template <int S0, int S1> DI void qk_second(bf16x8 (&q0)[16], bf16x8 (&q1)[16], bf16x8 (&fa)[4], u32 ak, const u32 (&bq)[4], f32x4& s0, f32x4& s1) {
    PIN(ak);
    if (S0 == 0) {
#pragma unroll
        for (int i = 0; i < 4; ++i) { s0[i] = 0.f; s1[i] = 0.f; }
#pragma unroll
        for (int s = 0; s < 3; ++s) fa[s] = *lp<bf16x8>(ak ^ (u32)(s << 4));
    }
#pragma unroll
    for (int s = S0; s < S1; ++s) {
        if (s < 13) {
            fa[(s + 3) & 3] = *lp<bf16x8>(ak ^ (u32)((s + 3) << 4));
            if (!KEPT0(s + 3)) q0[s + 3] = *lp<bf16x8>(bq[(s + 3) & 3] + QOFF(s + 3, 0));
            if (!KEPT1(s + 3)) q1[s + 3] = *lp<bf16x8>(bq[(s + 3) & 3] + QOFF(s + 3, 1));
        }
        __builtin_amdgcn_sched_barrier(0);
        s0 = __builtin_amdgcn_mfma_f32_16x16x32_bf16(fa[s & 3], q0[s], s0, 0, 0, 0);
        s1 = __builtin_amdgcn_mfma_f32_16x16x32_bf16(fa[s & 3], q1[s], s1, 0, 0, 0);
        __builtin_amdgcn_sched_barrier(0);
    }
}
DI bf16x8 pshift(bf16x8 p, u32 d) {
    v4u x = __builtin_bit_cast(v4u, p);
#pragma unroll
    for (int k = 0; k < 4; ++k) { u32 r; asm("v_pk_sub_u16 %0, %1, %2 op_sel_hi:[1,0] clamp" : "=v"(r) : "v"(x[k]), "v"(d)); x[k] = r; }
    return __builtin_bit_cast(bf16x8, x);
}
DI u32 shift_of(float r, float n) { return (u32)__builtin_fminf(r - n, 255.f) << 7; }
DI float tile_max(const f32x4& s0, const f32x4& s1) {
    auto r = __builtin_amdgcn_permlane16_swap(asu(max3(max3(s0.x, s0.y, s0.z), s0.w, s0.x)), asu(max3(max3(s1.x, s1.y, s1.z), s1.w, s1.x)), false, false);
    u32 e, o;
    both32(asu(__builtin_fmaxf(asf((u32)r[0]), asf((u32)r[1]))), e, o);
    return __builtin_ceilf(__builtin_fmaxf(asf(e), asf(o)));
}
DI float tile_exp(const f32x4& s0, const f32x4& s1, u32 pa) {
    float p[8];
#pragma unroll
    for (int k = 0; k < 4; ++k) p[k] = ex2(s0[k]);
    *lp<v2u>(pa) = (v2u){pack2(p[0], p[1]), pack2(p[2], p[3])};
#pragma unroll
    for (int k = 0; k < 4; ++k) p[4 + k] = ex2(s1[k]);
    *lp<v2u>(pa + 4096) = (v2u){pack2(p[4], p[5]), pack2(p[6], p[7])};
    return red16((p[0] + p[1]) + (p[2] + p[3]), (p[4] + p[5]) + (p[6] + p[7]));
}
DI void tile_softmax(const f32x4& s0, const f32x4& s1, u32 pa, u32 mpa, float& ref, float& l) {
    const float a = max3(max3(s0.x, s0.y, s0.z), s0.w, s0.x), b = max3(max3(s1.x, s1.y, s1.z), s1.w, s1.x);
    if (__builtin_expect(__builtin_amdgcn_ballot_w64(!(__builtin_fmaxf(__builtin_fabsf(a), __builtin_fabsf(b)) <= LAZY)) != 0ull, 0)) {
        ref = tile_max(s0, s1);
        u32 e, o;
        both16(asu(ref), e, o);
        l = tile_exp(s0 - asf(e), s1 - asf(o), pa);
    } else {
        ref = 0.f;
        l = tile_exp(s0, s1, pa);
    }
    asm volatile("" ::: "memory");
    *(volatile LAS float*)lp<float>(mpa) = ref;
}

DI u32 mlp_id(int b) { const int xc = b & 7, yy = (b >> 3) - 16; return (yy < 12 || xc < 4) ? (u32)(((yy >> 2) * 8 + xc) * 4 + (yy & 3)) : (u32)(xc * 4 + (yy & 3)); }
DI void mlp_part(const Params& P, int b, int wave, int lane, v4u (&wgu)[4], u32 wo, u64 t0, u32 tlo, u32 thi, int& budget, u32& tl) {
    char* const ws = P.ws;
    const bool mw_ = (wave >> 2) == 2;
    const u32 gu_id = mlp_id(b), w = (u32)wave & 3, i15 = lane & 15, g = lane >> 4;
    barrier();
    STAMP(1);
    until(__builtin_amdgcn_s_memrealtime(), G_DELAY);
    wgu[2] = ld_nt(P.wu, wo); wgu[3] = ld_nt(P.wu + 1792, wo);
    STAMP(2);
    u32 wdr[16];
#pragma unroll
    for (int j = 0; j < 16; ++j) wdr[j] = 0u;
    if (mw_) {
#pragma unroll
        for (int j = 0; j < 16; ++j) wdr[j] = ld_u32_nt(P.wd, (gu_id * 16 + j) * 1024u + (64 * w + (u32)lane) * 4u);
        STAMP(3);
        VMWAIT(0);
#pragma unroll
        for (int j = 0; j < 16; j += 2) {
            PIN(wdr[j]); PIN(wdr[j + 1]);
            const u32 a = wdr[j], bb = wdr[j + 1];
            wdr[j] = perm_lo(a, bb); wdr[j + 1] = perm_hi(a, bb);
            PIN(wdr[j]); PIN(wdr[j + 1]);
        }
    }
    VMWAIT(0);
    PIN(wgu[0]); PIN(wgu[1]); PIN(wgu[2]); PIN(wgu[3]);
    STAMP(8);
    {
        const u32 a = L_W + (u32)((wave & 7) * 64 + (lane >> 1) * 2) * 64u + (u32)(lane & 1) * 16u;
        *lp<v4u>(a) = wgu[0]; *lp<v4u>(a + 64) = wgu[1]; *lp<v4u>(a + 32) = wgu[2]; *lp<v4u>(a + 96) = wgu[3];
    }
    barrier();
    STAMP(9);
    if (!mw_) return;
    bf16x8 bg[4], bp[4];
    {
        const u32 a = L_W + (128 * w + 8 * g + (i15 >> 2)) * 64 + (lane & 3) * 8;
#pragma unroll
        for (int s = 0; s < 4; ++s) {
            const s16x4 x0 = __builtin_amdgcn_ds_read_tr16_b64_v4i16(lp<s16x4>(a + s * 2048)), x1 = __builtin_amdgcn_ds_read_tr16_b64_v4i16(lp<s16x4>(a + s * 2048 + 256));
            const s16x4 x2 = __builtin_amdgcn_ds_read_tr16_b64_v4i16(lp<s16x4>(a + s * 2048 + 32)), x3 = __builtin_amdgcn_ds_read_tr16_b64_v4i16(lp<s16x4>(a + s * 2048 + 288));
            bg[s] = __builtin_shufflevector(x0, x1, 0, 1, 2, 3, 4, 5, 6, 7); bp[s] = __builtin_shufflevector(x2, x3, 0, 1, 2, 3, 4, 5, 6, 7);
        }
    }
    u32 ha = L_H2 + w * 256 + (u32)lane * 4, hr = L_H2 + w * 256 + g * 16, ga = L_GU + w * 128 + (u32)(lane & 31) * 4, gr = L_GU + (u32)lane * 4;
    u32 so = piece(WS_YP, 16384, 8 * w + (u32)(lane >> 3)) + (gu_id * 8 + (lane & 7)) * 16;
    v4u mg[1];
    gather<1, 0>(ws, WS_SC + (64 * w + (u32)lane) * 16u, tlo, thi, mg, budget);
    float cb0 = 1.f + bflo(mg[0].x), cb1 = 1.f + bfhi(mg[0].x), sb0 = bflo(mg[0].y), sb1 = bfhi(mg[0].y);
    PIN(sb0); PIN(sb1); PIN(cb0); PIN(cb1); PIN(ha); PIN(hr); PIN(ga); PIN(gr); PIN(so);
#pragma unroll
    for (int s = 0; s < 4; ++s) { PIN(bg[s]); PIN(bp[s]); }
    STAMP(12);
    v4u xg[4];
    gather<4, 1024, false, 1>(ws, piece(WS_X1, 4096, (u32)(b & 7)) + (u32)lane * 16u, tlo, thi, xg, budget);
    STAMP(13);
    const float pil = asf(rfl(xg[0].x));
    float s1 = 0.f, s2 = 0.f;
#pragma unroll
    for (int k = 0; k < 4; ++k) { const float a = asf(xg[k].x) - pil, c = asf(xg[k].y) - pil; s1 += a; s2 = __builtin_fmaf(a, a, s2); s1 += c; s2 = __builtin_fmaf(c, c, s2); }
    float t1, t2;
    wave_sum2(s1, s2, t1, t2);
    const float dm = t1 * (1.f / 512.f), mean = pil + dm;
    const float rstd = __builtin_amdgcn_rsqf(__builtin_fmaxf(t2 * (1.f / 512.f) - dm * dm, 0.f) + 1e-5f);
    const u32 xa = w == 0 ? xg[0].x : w == 1 ? xg[1].x : w == 2 ? xg[2].x : xg[3].x, xb = w == 0 ? xg[0].y : w == 1 ? xg[1].y : w == 2 ? xg[2].y : xg[3].y;
    *lp<u32>(ha) = pack2((asf(xa) - mean) * cb0 * rstd + sb0, (asf(xb) - mean) * cb1 * rstd + sb1);
    asm volatile("" ::: "memory");
    f32x4 dg = {0.f, 0.f, 0.f, 0.f}, du = {0.f, 0.f, 0.f, 0.f};
    bf16x8 af[4];
#pragma unroll
    for (int s = 0; s < 4; ++s) af[s] = *lp<bf16x8>(hr + s * 64);
    __builtin_amdgcn_sched_barrier(0);
#pragma unroll
    for (int s = 0; s < 4; ++s) {
        dg = __builtin_amdgcn_mfma_f32_16x16x32_bf16(af[s], bg[s], dg, 0, 0, 0);
        du = __builtin_amdgcn_mfma_f32_16x16x32_bf16(af[s], bp[s], du, 0, 0, 0);
    }
    *lp<float>(ga) = (lane & 16) ? du.x : dg.x;
    barrier();
    STAMP(11);
    float s = *lp<float>(gr) + *lp<float>(gr + 256);
    s = red32(s, s);
    u32 gs, us;
    both16(asu(s), gs, us);
    const float gsum = asf(gs), usum = asf(us);
    const float z = 1.5957691216057308f * (gsum + 0.044715f * gsum * gsum * gsum);
    const float act = gsum * usum * sigm(z);
    const u32 actp = pack2(act, dpp<0x101>(act));
    float y0 = 0.f, y1 = 0.f;
#pragma unroll
    for (int j = 0; j < 16; j += 2) { const u32 ap = (u32)__builtin_amdgcn_readlane((int)actp, j); y0 = dot2(wdr[j], ap, y0); y1 = dot2(wdr[j + 1], ap, y1); }
    st_sc(ws, so, (v4u){asu(y0), asu(y1), tlo, thi});
    STAMP(14);
}

DI void stream_pv(const Params& P, int b, int wave, int lane, u64 t0, int& budget, u32& tl) {
    char* const ws = P.ws;
    const int xc = b & 7, yy = (b >> 3) - 16, sidx = yy * 8 + xc;
    const int l31 = lane & 31, hf = lane >> 5;
    const char* ckv = (const char*)P.ckv + (size_t)sidx * 262144;
#define TILE(t) (ckv + (t) * 16384)
    const u32 r0 = 2u * (u32)wave;
    const u32 oa = r0 * 1024u + (((u32)lane ^ swz(r0)) << 4), ob = (r0 + 1) * 1024u + (((u32)lane ^ swz(r0 + 1)) << 4);
#define ISSUE(t) do { const char* tb_ = TILE(t); dma_nt(tb_, oa, L_CKV + ((t) & 7) * 16384u + r0 * 1024u); dma_nt(tb_, ob, L_CKV + ((t) & 7) * 16384u + r0 * 1024u + 1024u); } while (0)
    const u32 jc = (u32)wave & 3, half = (u32)wave >> 2;
    const u32 blk = (lane >> 4) & 1, q4 = (lane & 15) >> 2, p4 = lane & 3;
    const u32 ch = 16 * jc + 8 * half + 2 * blk + (p4 >> 1);
    const u32 k0 = 8 * hf + q4, k1 = k0 + 4;
    u32 a0 = L_CKV + k0 * 1024 + ((ch ^ swz(k0)) << 4) + 8 * (p4 & 1), a1 = L_CKV + k1 * 1024 + ((ch ^ swz(k1)) << 4) + 8 * (p4 & 1);
#define VREAD(t, d0, d1) do { \
        if ((t) == 4) { a0 ^= 65536u; a1 ^= 65536u; } \
        if (((t) & 3) == 0) { PIN(a0); PIN(a1); } \
        const u32 a2 = a0 ^ 64u, a3 = a1 ^ 64u; \
        const s16x4 x0_ = __builtin_amdgcn_ds_read_tr16_b64_v4i16(lp<s16x4>(a0 + ((t) & 3) * 16384)), x1_ = __builtin_amdgcn_ds_read_tr16_b64_v4i16(lp<s16x4>(a1 + ((t) & 3) * 16384)); \
        const s16x4 x2_ = __builtin_amdgcn_ds_read_tr16_b64_v4i16(lp<s16x4>(a2 + ((t) & 3) * 16384)), x3_ = __builtin_amdgcn_ds_read_tr16_b64_v4i16(lp<s16x4>(a3 + ((t) & 3) * 16384)); \
        d0 = __builtin_shufflevector(x0_, x1_, 0, 1, 2, 3, 4, 5, 6, 7); d1 = __builtin_shufflevector(x2_, x3_, 0, 1, 2, 3, 4, 5, 6, 7); } while (0)
#define VREAD2(t, d0, d1) do { \
        const u32 b0_ = a0 + ((t) & 4) * 16384u, b1_ = (t) == 7 ? a7 - 3 * 16384u : a1 + ((t) & 4) * 16384u, b2_ = b0_ ^ 64u, b3_ = b1_ ^ 64u; \
        const s16x4 x0_ = __builtin_amdgcn_ds_read_tr16_b64_v4i16(lp<s16x4>(b0_ + ((t) & 3) * 16384)), x1_ = __builtin_amdgcn_ds_read_tr16_b64_v4i16(lp<s16x4>(b1_ + ((t) & 3) * 16384)); \
        const s16x4 x2_ = __builtin_amdgcn_ds_read_tr16_b64_v4i16(lp<s16x4>(b2_ + ((t) & 3) * 16384)), x3_ = __builtin_amdgcn_ds_read_tr16_b64_v4i16(lp<s16x4>(b3_ + ((t) & 3) * 16384)); \
        d0 = __builtin_shufflevector(x0_, x1_, 0, 1, 2, 3, 4, 5, 6, 7); d1 = __builtin_shufflevector(x2_, x3_, 0, 1, 2, 3, 4, 5, 6, 7); } while (0)
    bf16x8 va[8], vb[8];
#pragma unroll
    for (int t = 0; t < 8; ++t) {
        ISSUE(t + DEPTH);
        asm volatile("s_waitcnt vmcnt(%0)" :: "i"(2 * (upto(t) - t)) : "memory");
        barrier();
        if (t == 1) STAMP(18);
        if (t == 4) STAMP(19);
        VREAD(t, va[t], vb[t]);
    }
    a0 ^= 65536u; a1 ^= 65536u;
    PIN(a0); PIN(a1);
    u64 e64;
    TAGS();
    barrier();
    STAMP(1);
    *lp<u32>(L_RF + (u32)(wave * 64 + lane) * 4) = NOREF;
    if (wave == 0) *lp<u32>(L_CNT + (u32)lane * 4) = 0u;
    barrier();
    if (half == 0) {
        VMWAIT(4); lds_add(C_T, lane); VMWAIT(2); lds_add(C_T + 4, lane); VMWAIT(0); lds_add(C_T + 8, lane);
    } else {
        const u32 r4 = 4 * jc, thold = rfl(*lp<u32>(C_TB)) + (u32)(Q_FIRST + P_SKEW);
        u32 oc = r4 * 1024u + (((u32)lane ^ jc) << 4);
        PIN(oc);
#define ISSUE4(t) do { if ((int)((u32)__builtin_amdgcn_s_memrealtime() - thold) >= 0) lds_wait(HOLD, 4u, budget); _Pragma("unroll") for (int k = 0; k < 4; ++k) dma_nt(TILE(t) + k * 1024, oc ^ (u32)(k << 6), L_CKV + ((t) & 7) * 16384u + (r4 + k) * 1024u); } while (0)
        ISSUE4(11); VMWAIT(8); lds_add(C_T, lane);
        ISSUE4(12); VMWAIT(10); lds_add(C_T + 4, lane);
        ISSUE4(13); VMWAIT(12); lds_add(C_T + 8, lane);
        ISSUE4(14); VMWAIT(12); lds_add(C_T + 12, lane);
        if (jc < 3) { ISSUE4(15); VMWAIT(12); lds_add(C_T + 16, lane); VMWAIT(8); lds_add(C_T + 20, lane); VMWAIT(4); lds_add(C_T + 24, lane); }
        else {
            dma_nt(TILE(15), oc, L_CKV + 7 * 16384u + 12 * 1024u);
            VMWAIT(9); lds_add(C_T + 16, lane);
            lds_wait(C_E, 8u, budget);
#pragma unroll
            for (int k = 1; k < 4; ++k) dma_nt(TILE(15) + k * 1024, oc ^ (u32)(k << 6), L_REL + (k - 1) * 1024u);
            VMWAIT(8); lds_add(C_T + 20, lane); VMWAIT(4); lds_add(C_T + 24, lane);
        }
        VMWAIT(0); lds_add(C_T + 28, lane);
        STAMP(8);
    }
    u32 pbase = L_P + l31 * 256 + ((hf ^ (l31 & 15)) << 4), ra = L_RF + l31 * 8, a7 = k1 >= 13 ? L_REL - 13 * 1024 + a1 : a1 + 7 * 16384;
    PIN(pbase); PIN(ra); PIN(a7);
    if (half == 0) __builtin_amdgcn_s_setprio(PRIO_P0); else __builtin_amdgcn_s_setprio(PRIO_P1);
    CYC0();
    float mrow = 0.f;
    f32x16 o0, o1;
#pragma unroll
    for (int i = 0; i < 16; ++i) { o0[i] = 0.f; o1[i] = 0.f; }
    bf16x8 pb[4];
    v2u rf;
#define LLOAD(k) do { \
        rf = *(volatile LAS v2u*)lp<v2u>(ra + 256 * (k)); \
        asm volatile("" ::: "memory"); \
        pb[(2 * (k)) & 3] = *lp<bf16x8>((pbase ^ (u32)(64 * ((k) & 3))) - ((k) >> 2) * (L_P - L_P2)); pb[(2 * (k) + 1) & 3] = *lp<bf16x8>((pbase ^ (u32)(64 * ((k) & 3) + 32)) - ((k) >> 2) * (L_P - L_P2)); \
        if ((k) >= 4) { VREAD2(2 * (k) - 8, va[(2 * (k)) & 7], vb[(2 * (k)) & 7]); VREAD2(2 * (k) - 7, va[(2 * (k) + 1) & 7], vb[(2 * (k) + 1) & 7]); } } while (0)
    bool moved = false;
    u32 g1, g2, ho;
    LLOAD(0);
#pragma unroll
    for (int k = 0; k < 8; ++k) {
        if (__builtin_amdgcn_ballot_w64(rf.x != asu(mrow) || rf.y != asu(mrow)) != 0ull) {
#pragma clang loop unroll(disable)
            while (__builtin_amdgcn_ballot_w64(rf.x == NOREF || rf.y == NOREF) != 0ull && --budget > 0) {
                __builtin_amdgcn_s_sleep(1);
                rf = *(volatile LAS v2u*)lp<v2u>(ra + 256 * k);
            }
            LLOAD(k);
            if (__builtin_expect(__builtin_amdgcn_ballot_w64(rf.x != asu(mrow) || rf.y != asu(mrow)) != 0ull, 0)) {
                if (k == 0) mrow = __builtin_fmaxf(asf(rf.x), asf(rf.y));
                else {
                    const float m2 = max3(mrow, asf(rf.x), asf(rf.y)), al = ex2(mrow - m2);
                    o0 *= al; o1 *= al; mrow = m2;
                }
                moved = true;
                pb[(2 * k) & 3] = pshift(pb[(2 * k) & 3], shift_of(mrow, asf(rf.x)));
                pb[(2 * k + 1) & 3] = pshift(pb[(2 * k + 1) & 3], shift_of(mrow, asf(rf.y)));
            }
        }
        if (k == 0) STAMP(7);
        if (k < 7) LLOAD(k + 1);
        __builtin_amdgcn_sched_barrier(0);
        o0 = __builtin_amdgcn_mfma_f32_32x32x16_bf16(va[(2 * k) & 7], pb[(2 * k) & 3], o0, 0, 0, 0);
        o1 = __builtin_amdgcn_mfma_f32_32x32x16_bf16(vb[(2 * k) & 7], pb[(2 * k) & 3], o1, 0, 0, 0);
        if (k == 4) {
            u32 ln = (u32)lane; PIN(ln);
            const u32 base = WS_PAY + ((jc * 2 + half) * 128 + (u32)sidx) * PBLK + (ln & 28) * PHD + (ln & 3) * 16u;
            g1 = base + (ln >> 5) * 64u; g2 = base + (ln >> 5) * PHD + 128u;
            ho = WS_HC + (((u32)sidx + 17u * ln) & 127u) * HSTR + ln * 16u;
            PIN(g1); PIN(g2); PIN(ho);
        }
        o0 = __builtin_amdgcn_mfma_f32_32x32x16_bf16(va[(2 * k + 1) & 7], pb[(2 * k + 1) & 3], o0, 0, 0, 0);
        o1 = __builtin_amdgcn_mfma_f32_32x32x16_bf16(vb[(2 * k + 1) & 7], pb[(2 * k + 1) & 3], o1, 0, 0, 0);
        __builtin_amdgcn_sched_barrier(0);
        if (k == 3) STAMP(2);
    }
    PIN(lane);
    __builtin_amdgcn_s_setprio(0);
    if (wave == 0 && lane < 8) st_sc(ws, ho, (v4u){(u32)__builtin_amdgcn_s_memrealtime(), 0u, tlo, thi});
    STAMPC(28);
    {
        u32 d[18];
#pragma unroll
        for (int k = 0; k < 8; ++k) { d[k] = pack2(o0[2 * k], o0[2 * k + 1]); d[8 + k] = pack2(o1[2 * k], o1[2 * k + 1]); }
        d[16] = asu(mrow); d[17] = 0u;
        swap_l0(d, d + 3); swap_l0(d + 6, d + 9);
        swap_l1(d, d + 6);
        st_sc(ws, g1, (v4u){d[0], d[1], d[2], tlo});
        st_sc(ws + 2 * PHD, g1, (v4u){d[6], d[7], d[8], tlo});
        swap_l1(d + 3, d + 9);
        st_sc(ws + PHD, g1, (v4u){d[3], d[4], d[5], tlo});
        st_sc(ws + 3 * PHD, g1, (v4u){d[9], d[10], d[11], tlo});
        if (half) {
            float lrow;
#pragma clang loop unroll(disable)
            do {
                const u32 cnt = *(volatile LAS u32*)lp<u32>(C_L);
                f32x4 la = *(volatile LAS f32x4*)lp<f32x4>(L_LP + (ra - L_RF) * 4), lb = *(volatile LAS f32x4*)lp<f32x4>(L_LP + (ra - L_RF) * 4 + 16);
                if (__builtin_expect(moved, 0)) {
#pragma unroll
                    for (int k = 0; k < 4; ++k) {
                        la[k] *= ex2(__builtin_fmaxf(*lp<float>(ra + (k >> 1) * 256 + (k & 1) * 4), *lp<float>(ra + 1024 + (k >> 1) * 256 + (k & 1) * 4)) - mrow);
                        lb[k] *= ex2(__builtin_fmaxf(*lp<float>(ra + 512 + (k >> 1) * 256 + (k & 1) * 4), *lp<float>(ra + 1536 + (k >> 1) * 256 + (k & 1) * 4)) - mrow);
                    }
                }
                lrow = ((la.x + la.y) + (la.z + la.w)) + ((lb.x + lb.y) + (lb.z + lb.w));
                if (cnt >= 8u) break;
                __builtin_amdgcn_s_sleep(1);
            } while (--budget > 0);
            d[17] = pack2(lrow, lrow - bflo(pack2(lrow, 0.f)));
        }
        swap_l0(d + 12, d + 15);
#pragma unroll
        for (int k = 0; k < 3; ++k) { auto r = __builtin_amdgcn_permlane32_swap(d[12 + k], d[15 + k], false, false); d[12 + k] = (u32)r[0]; d[15 + k] = (u32)r[1]; }
        swap_l1(d + 12, d + 15);
        st_sc(ws, g2, (v4u){d[12], d[13], d[14], tlo});
        st_sc(ws + 2 * PHD, g2, (v4u){d[15], d[16], d[17], tlo});
    }
    STAMPC(29);
    STAMP(9);
}

DI void stream_qk(const Params& P, int b, int wave, int lane, u64 t0, u32 tlo, u32 thi, int& budget, u32& tl) {
    char* const ws = P.ws;
    const int xc = b & 7, yy = (b >> 3) - 16, w = wave - 8;
    const u32 i15 = lane & 15, g = lane >> 4;
    u32 ak = L_CKV + (u32)w * 16384u + i15 * 1024u + g * 256u + (swz(i15) << 4);
    u32 qo = WS_QABS + (u32)w * 2048u + g * 256u + ((i15 ^ g ^ ((w & 1) ? 12u : 0u)) << 4), co = WS_QCHK + (u32)w * 2048u + (u32)lane * 16u;
    PIN(qo); PIN(co);
    v4u chk0, chk1;
    asm volatile("" : "=v"(chk0), "=v"(chk1));
#define REQ_Q() do { \
        _Pragma("unroll") for (int k = 0; k < 8; ++k) dma_sc(ws + (k >> 1) * 8192 + (k & 1) * 1024, qo, L_IMG + (u32)w * 2048u + (k & 1) * 1024u + (((k >> 1) + 1) & 3) * 8192u); \
        ld_sc_into(chk0, ws, co); ld_sc_into(chk1, ws + 1024, co); } while (0)
    if (w < 4) __builtin_amdgcn_s_setprio(3);
    v4u kr[16];
    for (int t = 0; t <= w; ++t) barrier();
#pragma unroll
    for (int s = 0; s < 16; ++s) kr[s] = *lp<v4u>(ak ^ (u32)(s << 4));
    for (int t = w + 1; t <= 7; ++t) barrier();
    const u32 tb = time_base(ws + WS_ST + xc * 128, (u32)t0);
    VMWAIT(0);
    barrier();
    if (w == 0) *lp<u32>(C_TB) = tb;
    barrier();
    if (w < 4) {
        until_abs(tb + Q_FIRST);
        REQ_Q();
        lds_add(C_Q, lane);
        STAMP(20);
        u32 va = L_IMG + ((i15 + 1) & 3) * 8192u + (u32)w * 2048u + g * 256u + (((i15 >> 2) ^ ((w & 1) ? 3u : 0u)) << 6) + ((i15 & 3) << 4);
        PIN(va);
        VMWAIT(0);
        STAMP(4);
#pragma clang loop unroll(disable)
        do {
            PIN(chk0); PIN(chk1);
            const v4u x0 = *(volatile LAS v4u*)lp<v4u>(va), x1 = *(volatile LAS v4u*)lp<v4u>(va ^ 16u), x2 = *(volatile LAS v4u*)lp<v4u>(va ^ 32u), x3 = *(volatile LAS v4u*)lp<v4u>(va ^ 48u);
            const v4u y0 = *(volatile LAS v4u*)lp<v4u>(va + 1024), y1 = *(volatile LAS v4u*)lp<v4u>((va ^ 16u) + 1024), y2 = *(volatile LAS v4u*)lp<v4u>((va ^ 32u) + 1024), y3 = *(volatile LAS v4u*)lp<v4u>((va ^ 48u) + 1024);
            const v4u xs = (x0 + x1) + (x2 + x3), ys = (y0 + y1) + (y2 + y3);
            const u64 b0 = __builtin_amdgcn_ballot_w64(((((xs.x + xs.y) + (xs.z + xs.w)) ^ chk0.x) | (chk0.z ^ tlo) | (chk0.w ^ thi)) != 0u);
            const u64 b1 = __builtin_amdgcn_ballot_w64(((((ys.x + ys.y) + (ys.z + ys.w)) ^ chk1.x) | (chk1.z ^ tlo) | (chk1.w ^ thi)) != 0u);
            if ((b0 | b1) == 0ull) break;
            __builtin_amdgcn_s_sleep(QSLEEP);
#pragma unroll
            for (int k = 0; k < 8; ++k)
                if (((k & 1) ? b1 : b0) & (0x1111111111111111ull << (k >> 1))) dma_sc(ws + (k >> 1) * 8192 + (k & 1) * 1024, qo, L_IMG + (u32)w * 2048u + (k & 1) * 1024u + (((k >> 1) + 1) & 3) * 8192u);
            if (b0) ld_sc_into(chk0, ws, co);
            if (b1) ld_sc_into(chk1, ws + 1024, co);
            VMWAIT(0);
        } while (--budget > 0);
        lds_add(C_Z, lane);
        STAMP(5);
    }
    u32 bq[4];
#pragma unroll
    for (int k = 0; k < 4; ++k) { bq[k] = L_IMG + i15 * 256u + (((g * 4 + k) ^ (i15 & 3) ^ ((i15 & 8) ? 12u : 0u)) << 4); PIN(bq[k]); }
    __builtin_amdgcn_s_setprio(3);
    lds_wait(C_Z, 4u, budget);
    STAMP(6);
    CYC0();
    f32x4 s0, s1;
    bf16x8 q0[16], q1[16];
    qk_first(kr, q0, q1, bq, s0, s1, lane, w >= 4);
    STAMP(21);
    PIN(lane);
    const u32 mpa = L_RF + (u32)(w >> 1) * 256 + ((u32)lane & 31) * 8 + (u32)(w & 1) * 4;
    const u32 pa = L_P + ((u32)lane & 15) * 256 + (((2 * (u32)w + ((u32)lane >> 5)) ^ ((u32)lane & 15)) << 4) + (((u32)lane >> 4) & 1) * 8;
    float n1, l1, n2, l2;
    lds_wait(C_E0, 8u, budget);
    tile_softmax(s0, s1, pa, mpa, n1, l1);
    PIN(n1); PIN(l1);
    STAMP(24);
    STAMPC(29);
    if (w == 7 && (lane & 15) >= 13) ak += L_REL - (7 * 16384 + 13 * 1024);
    lds_wait(C_T + 4 * (u32)w, w < 3 ? 8u : 4u, budget);
    STAMP(25);
    bf16x8 fa[4];
    qk_second<0, 2>(q0, q1, fa, ak, bq, s0, s1);
    v4u wgu[4];
    PIN(lane);
    const u32 wo = (u32)w * (64u * 3584u) + mlp_id(b) * 32u + ((u32)lane >> 1) * 7168u + ((u32)lane & 1) * 16u;
    wgu[0] = ld_nt(P.wg, wo); wgu[1] = ld_nt(P.wg + 1792, wo);
    qk_second<2, 16>(q0, q1, fa, ak, bq, s0, s1);
    STAMP(26);
    lds_wait(C_M, 8u, budget);
    tile_softmax(s0, s1, pa - (L_P - L_P2), mpa + 1024, n2, l2);
    STAMP(27);
    __builtin_amdgcn_s_setprio(0);
    {
        const float nw = __builtin_fmaxf(n1, n2);
        float l = l1 * ex2(n1 - nw) + l2 * ex2(n2 - nw);
        l = red32(l, l);
        *lp<float>(L_LP + ((u32)lane & 31) * 32 + (u32)w * 4) = l;
        lds_add(C_L, lane);
    }
    if (yy < 12 || xc < 4) mlp_part(P, b, wave, lane, wgu, wo, t0, tlo, thi, budget, tl);
    else VMWAIT(0);
}

__global__ void __launch_bounds__(1024, 4) fused_kernel(Params P) {
    extern __shared__ __attribute__((aligned(16))) char lds_raw[];
    const int tid = threadIdx.x, lane = tid & 63;
    const int wave = __builtin_amdgcn_readfirstlane(tid >> 6);
    const int b = blockIdx.x, xc = b & 7, y = b >> 3;
    char* const ws = P.ws;
    int budget = BUDGET;
    asm volatile("" :: "v"(lds_raw));
    u32 tl = 0;
    const u64 t0 = __builtin_amdgcn_s_memrealtime();
    u64 pc0;
    asm volatile("s_getpc_b64 %0" : "=s"(pc0));
    if (y >= 16 && wave < 8) {
        const char* ckv = (const char*)P.ckv + (size_t)(b - 128) * 262144;
        until(t0, S_DELAY);
        const u32 r0 = 2u * (u32)wave;
        const u32 oa = r0 * 1024u + (((u32)lane ^ swz(r0)) << 4), ob = (r0 + 1) * 1024u + (((u32)lane ^ swz(r0 + 1)) << 4);
#pragma unroll
        for (int t = 0; t < DEPTH; ++t) {
            if (t == D0 + 1) until(t0, T_OPEN);
            dma_nt(ckv + t * 16384, oa, L_CKV + t * 16384u + r0 * 1024u); dma_nt(ckv + t * 16384, ob, L_CKV + t * 16384u + r0 * 1024u + 1024u);
        }
    }
    if (wave >= 8 && wave < 12 && (y & 0x13) == 0x10) {
        asm volatile("s_mov_b32 m0, %1\n\ts_nop 0\n\tglobal_load_lds_dword %0, %2" :: "v"((u32)lane * 128u + (wave < 11 ? (u32)(wave - 8) * 8192u : (u32)CODE_LAST)), "s"(L_IMG), "s"(pc0) : "memory");
    }
    const int head = xc * 4 + (y >> 2), j = y & 3;
    const u32 i15 = lane & 15, g = lane >> 4;
    u64 e64 = 0;
    int role = y < 16 ? (wave < 2 ? 0 : wave < 10 ? 1 : 2) : (wave < 8 ? 3 : 2);
    {
        const u32 ks = b & 15, cg = (u32)(b >> 4) * 2 + ((u32)wave & 1), s = (u32)wave - 2;
        const u32 row0 = ks * 32 + 2 * (u32)(lane >> 2), p = lane & 3, m0 = (u32)j * 128 + s * 16 + g * 4;
        v4u wr[4], wv[4];
#pragma unroll
        for (int k = 0; k < 4; ++k) asm volatile("" : "=v"(wv[k]));
        if (y < 16 && wave >= 2) {
            if (b < 8 && wave == 2 && lane < 8) st_sc2(ws, WS_ST + (u32)lane * 128u + (u32)b * 8u, (v2u){(u32)t0, ~(u32)t0});
            const u32 o = m0 * 8192u + (u32)head * 256u + i15 * 16u;
            barrier();
            if (wave < 10) {
                until(t0, WQ_DELAY);
#pragma unroll
                for (int k = 0; k < 4; ++k) ld_nt_into(wv[k], P.wq + k * 4096, o);
                STAMP(20);
            }
        }
        asm volatile("" : "+s"(role));
        if (role == 0) {
            const u32 o = row0 * 6144u + cg * 64u + p * 16u;
            u32 cc = ld_u32(P.c, row0 * 2);
            v4u xa = ld_pl(P.x, lane * 16);
            u32 xs = ld_u32(P.x, cg * 64u + p * 16u + g * 4u);
            wr[0] = ld_nt(P.wmod, o); wr[1] = ld_nt(P.wmod + 3072, o); wr[2] = ld_nt(P.wmod + 512, o); wr[3] = ld_nt(P.wmod + 3584, o);
            STAMP(20);
            barrier();
            TAGS();
            u32 so = piece(WS_HP, 2048, cg) + (ks * 8 + 2 * p + (u32)(lane >> 5)) * 16u;
            VMWAIT(4);
            PIN(xa); PIN(xs); PIN(cc);
            float mean, rstd;
            ln_stats(xa, mean, rstd);
            const u32 sp = pack2(silu(bflo(cc)), silu(bfhi(cc)));
            u32 s0l = sp & 0xffffu, s0h = sp << 16, s1l = sp >> 16, s1h = sp & 0xffff0000u;
            float xn0 = (bflo(xs) - mean) * rstd, xn1 = (bfhi(xs) - mean) * rstd;
            PIN(xn0); PIN(xn1); PIN(s0l); PIN(s0h); PIN(s1l); PIN(s1h); PIN(so);
            VMWAIT(0);
#pragma unroll
            for (int k = 0; k < 4; ++k) PIN(wr[k]);
            STAMP(22);
            float ta[4], tc[4];
#pragma unroll
            for (int k = 0; k < 2; ++k) {
                ta[2 * k] = red32(dot2(wr[1][k], s1l, dot2(wr[0][k], s0l, 0.f)), dot2(wr[1][k + 2], s1l, dot2(wr[0][k + 2], s0l, 0.f)));
                ta[2 * k + 1] = red32(dot2(wr[1][k], s1h, dot2(wr[0][k], s0h, 0.f)), dot2(wr[1][k + 2], s1h, dot2(wr[0][k + 2], s0h, 0.f)));
                tc[2 * k] = red32(dot2(wr[3][k], s1l, dot2(wr[2][k], s0l, 0.f)), dot2(wr[3][k + 2], s1l, dot2(wr[2][k + 2], s0l, 0.f)));
                tc[2 * k + 1] = red32(dot2(wr[3][k], s1h, dot2(wr[2][k], s0h, 0.f)), dot2(wr[3][k + 2], s1h, dot2(wr[2][k + 2], s0h, 0.f)));
            }
            float u0 = xn0 * (red16(tc[0], tc[2]) + 0.015625f) + red16(ta[0], ta[2]), u1 = xn1 * (red16(tc[1], tc[3]) + 0.015625f) + red16(ta[1], ta[3]);
            u0 += dpp<0x124>(u0); u1 += dpp<0x124>(u1); u0 += dpp<0x128>(u0); u1 += dpp<0x128>(u1);
            u32 e, od;
            both16(pack2(u0, u1), e, od);
            if ((lane & 28) == 0) st_sc(ws, so, (v4u){e, od, tlo, thi});
            STAMP(24);
            barrier();
            barrier();
        }
        asm volatile("" : "+s"(role));
        if (role == 1) {
            TAGS();
            u32 so = piece(WS_QP, 2048, (u32)head) + ((u32)j * 32 + (u32)(lane >> 5) * 16 + i15) * 16u, ho = piece(WS_HP, 2048, (u32)j * 4 + (s >> 1)) + (i15 * 8 + (s & 1) * 4 + g) * 16u;
            PIN(so); PIN(ho);
            v4u hg[1];
            until(t0, ST_LOOK);
            const u32 tb = time_base(ws + WS_ST + xc * 128, (u32)t0);
            if (s == 0) *lp<u32>(F_TB) = tb;
            asm volatile("" : "=v"(hg[0]));
            until_abs(tb + A_FIRST);
            ld_sc_into(hg[0], ws, ho);
            barrier();
            VMWAIT(1);
#pragma unroll
            for (int k = 0; k < 4; ++k) { PIN(wv[k]); wr[k] = wv[k]; }
            STAMP(1);
            pair_rows(wr[0], wr[1]); pair_rows(wr[2], wr[3]);
#pragma unroll
            for (int k = 0; k < 4; ++k) PIN(wr[k]);
            u32 la = F_QP + s * 512 + lane * 8;
            PIN(la);
            VMWAIT(0);
            PIN(hg[0]);
            if (__builtin_amdgcn_ballot_w64(hg[0].z != tlo || hg[0].w != thi) != 0ull) gather<1, 0>(ws, ho, tlo, thi, hg, budget);
            STAMP(3);
            const u32 h01 = pack2(rowsum(bflo(hg[0].x)), rowsum(bfhi(hg[0].x))), h23 = pack2(rowsum(bflo(hg[0].y)), rowsum(bfhi(hg[0].y)));
            float acc[8];
#pragma unroll
            for (int k = 0; k < 4; ++k) { acc[2 * k] = dot2(wr[2][k], h23, dot2(wr[0][k], h01, 0.f)); acc[2 * k + 1] = dot2(wr[3][k], h23, dot2(wr[1][k], h01, 0.f)); }
            float t[4];
#pragma unroll
            for (int k = 0; k < 4; ++k) t[k] = red32(acc[k], acc[k + 4]);
            const float u0 = red16(t[0], t[2]), u1 = red16(t[1], t[3]);
            *lp<f32x2>(la) = (f32x2){u0, u1};
            barrier();
            if (s == 2) {
                const LAS f32x2* qp = lp<f32x2>(F_QP + lane * 8);
                const f32x2 q2 = ((qp[0] + qp[64]) + (qp[128] + qp[192])) + ((qp[256] + qp[320]) + (qp[384] + qp[448]));
                u32 e, od;
                both16(pack2(q2.x, q2.y), e, od);
                if ((lane & 16) == 0) st_sc(ws, so, (v4u){e, od, tlo, thi});
            }
            STAMP(2);
        }
    }
    asm volatile("" : "+s"(role));
    if (role == 2) EPOCH();
    const u32 tlo = (u32)e64, thi = (u32)(e64 >> 32);
    if (y >= 16) {
        if (wave < 8) stream_pv(P, b, wave, lane, t0, budget, tl);
        else stream_qk(P, b, wave, lane, t0, tlo, thi, budget, tl);
        STAMP(15);
        STAMP_OUT();
        return;
    }
    const bool is_red = (y >> 2) == 1;
    const u32 rid = (u32)(xc * 4 + j);
    if (wave >= 12) {
        const u32 w = (u32)y & 3, jk = (u32)y >> 2, hk = (u32)xc * 4 + ((u32)wave & 3);
        v4u wk[8];
        const u32 o = ((u32)y * 32 + g * 8) * 8192u + hk * 256u + i15 * 16u;
        u32 warm = ld_u32(P.wuk, (u32)y * 32 * 8192u);
        barrier();
#pragma unroll
        for (int k = 0; k < 8; ++k) wk[k] = ld_nt(P.wuk, o + k * 8192u);
        STAMP(20);
        VMWAIT(8);
        PIN(warm);
        const u32 oq = piece(WS_QP, 2048, hk) + (g * 32 + i15) * 16u;
        u32 so = WS_QABS + w * 8192u + hk * 256u + jk * 64u + g * 16u, sc = WS_QCHK + hk * 256u + (u32)y * 16u;
        PIN(so); PIN(sc);
        barrier();
        v4u qg[2];
        const u64 tb1 = __builtin_amdgcn_s_memrealtime();
        until(tb1, A2_DELAY);
        gather<2, 256>(ws, oq, tlo, thi, qg, budget);
        STAMP(23);
        const float t0_ = red32(bflo(qg[0].x), bflo(qg[1].x)), t1_ = red32(bfhi(qg[0].x), bfhi(qg[1].x));
        const float t2_ = red32(bflo(qg[0].y), bflo(qg[1].y)), t3_ = red32(bfhi(qg[0].y), bfhi(qg[1].y));
        u32 e, od, q0, q1, q2, q3;
        both16(pack2(red16(t0_, t2_), red16(t1_, t3_)), e, od);
        both32(e, q0, q2); both32(od, q1, q3);
        float a[8];
#pragma unroll
        for (int k = 0; k < 8; ++k) { PIN(wk[k]); a[k] = dot2(wk[k][3], q3, dot2(wk[k][2], q2, dot2(wk[k][1], q1, dot2(wk[k][0], q0, 0.f)))); }
        float z[4];
#pragma unroll
        for (int k = 0; k < 4; ++k) {
            const float keep = (lane & 1) ? a[k + 4] : a[k], give = (lane & 1) ? a[k] : a[k + 4];
            float s = keep + dpp<0xB1>(give);
            s += dpp<0x122>(s); s += dpp<0x124>(s); s += dpp<0x128>(s);
            z[k] = s * (0.08838834764831845f * 1.4426950408889634f);
        }
        const u32 y0 = pack2(z[0], z[1]), y1 = pack2(z[2], z[3]);
        const u32 y2 = (u32)__builtin_amdgcn_update_dpp(0, (int)y0, 0xB1, 0xf, 0xf, true), y3 = (u32)__builtin_amdgcn_update_dpp(0, (int)y1, 0xB1, 0xf, 0xf, true);
        if (i15 == 0) st_sc(ws, so, (v4u){y0, y1, y2, y3});
        u32 c0, c1;
        both16((y0 + y1) + (y2 + y3), c0, c1);
        both32(c0 + c1, c0, c1);
        if (lane == 0) st_sc(ws, sc, (v4u){c0 + c1, 0u, tlo, thi});
        STAMP(4);
    } else if (wave >= 10) {
        { u32 w0 = ld_u32(P.wo, (u32)head * 131072u), w1 = ld_u32(P.wuv, (u32)j * 1048576u); VMWAIT(0); PIN(w0); PIN(w1); }
        barrier();
        barrier();
    }
#define WO_ROW(r) dma_nt(P.wo, ((u32)head * 128 + (r)) * 1024u + (((u32)lane ^ swz((r) & 15)) << 4), F_WO + (r) * 1024u)
    const u32 tb = rfl(*lp<u32>(F_TB));
    barrier();
    WO_ROW((u32)j * 32u + (u32)wave * 2); WO_ROW((u32)j * 32u + (u32)wave * 2 + 1);
    VMWAIT(0);
    STAMP(5);
    bf16x8 bo[8], bu[4];
    {
        const u32 kq = (u32)b & 3, cgl = (u32)b >> 2;
        const u32 mrow = kq * 128 + (u32)wave * 8 + (u32)(lane >> 3);
        until_abs(tb + T_MID);
#pragma unroll
        for (int k = 2; k < 8; ++k) WO_ROW((((u32)(k >> 1) + (u32)j) & 3u) * 32u + (u32)wave * 2 + (k & 1));
        STAMP(19);
        barrier();
        until_abs(tb + T_START);
#pragma unroll
        for (int k = 0; k < 2; ++k) {
            const u32 r = (u32)wave * 8 + k * 4 + g;
            dma_nt(P.wuv, ((u32)j * 128 + r) * 8192u + (u32)head * 256u + ((i15 ^ swz(r & 15)) << 4), F_UV + ((u32)wave * 8 + k * 4) * 256u);
        }
        v4u mw = ld_nt(P.wmod, mrow * 6144u + 2048u + cgl * 128u + (u32)(lane & 7) * 16u);
        u32 cv = ld_u16(P.c, mrow * 2);
        VMWAIT(4);
        STAMP(14);
        barrier();
        const u32 q4 = i15 >> 2, p4 = lane & 3;
        const u32 ra = 8 * (g & 1) + q4, rb = ra + 4;
        {
            const u32 c0 = 4 * (u32)wave + (p4 >> 1);
            const u32 x0 = F_WO + (8 * g + q4) * 1024 + 8 * (p4 & 1), x1 = x0 + 4096;
#pragma unroll
            for (int t2 = 0; t2 < 2; ++t2)
#pragma unroll
                for (int s = 0; s < 4; ++s) {
                    const s16x4 lo = __builtin_amdgcn_ds_read_tr16_b64_v4i16(lp<s16x4>(x0 + s * 32768 + (((c0 + 2 * t2) ^ swz(ra)) << 4)));
                    const s16x4 hi = __builtin_amdgcn_ds_read_tr16_b64_v4i16(lp<s16x4>(x1 + s * 32768 + (((c0 + 2 * t2) ^ swz(rb)) << 4)));
                    bo[4 * t2 + s] = __builtin_shufflevector(lo, hi, 0, 1, 2, 3, 4, 5, 6, 7);
                }
#pragma unroll
            for (int k = 0; k < 8; ++k) PIN(bo[k]);
        }
        VMWAIT(0);
        PIN(mw); PIN(cv);
        STAMP(6);
        barrier();
        {
            const u32 c0 = 2 * ((u32)wave & 7) + (p4 >> 1);
            const u32 x0 = F_UV + (8 * g + q4) * 256 + 8 * (p4 & 1) + ((c0 ^ swz(ra)) << 4), x1 = F_UV + (8 * g + q4 + 4) * 256 + 8 * (p4 & 1) + ((c0 ^ swz(rb)) << 4);
#pragma unroll
            for (int s = 0; s < 4; ++s) {
                const s16x4 lo = __builtin_amdgcn_ds_read_tr16_b64_v4i16(lp<s16x4>(x0 + s * 8192));
                const s16x4 hi = __builtin_amdgcn_ds_read_tr16_b64_v4i16(lp<s16x4>(x1 + s * 8192));
                bu[s] = __builtin_shufflevector(lo, hi, 0, 1, 2, 3, 4, 5, 6, 7);
            }
        }
        const u32 sl = pack2(silu(bflo(cv)), 0.f), sh = sl << 16;
        float t[4], u[2];
#pragma unroll
        for (int k = 0; k < 2; ++k) { t[2 * k] = red32(dot2(mw[k], sl, 0.f), dot2(mw[k + 2], sl, 0.f)); t[2 * k + 1] = red32(dot2(mw[k], sh, 0.f), dot2(mw[k + 2], sh, 0.f)); }
#pragma unroll
        for (int k = 0; k < 2; ++k) { float z = red16(t[k], t[k + 2]); z += dpp<0x128>(z); u[k] = z; }
        if ((lane & 8) == 0) *lp<f32x2>(F_ML + wave * 256 + ((lane & 7) * 4 + g) * 8) = (f32x2){u[0], u[1]};
        if (tid == 8) *lp<v2u>(F_GO) = (v2u){0u, 0u};
#pragma unroll
        for (int k = 0; k < 4; ++k) PIN(bu[k]);
        barrier();
        if (wave == 15) {
            const LAS f32x2* q = lp<f32x2>(F_ML) + lane;
            const f32x2 s = ((q[0] + q[64]) + (q[128] + q[192])) + ((q[256] + q[320]) + (q[384] + q[448]));
            const float sx = red32(s.x, s.x), sy = red32(s.y, s.y);
            if (lane < 32) st_sc(ws, WS_ML + (cgl & 7) * 8192u + ((u32)lane >> 4) * 4096u + (cgl >> 3) * 1024u + (kq * 16 + ((u32)lane & 15)) * 16u, (v4u){asu(sx), asu(sy), tlo, thi});
        }
    }
    float ga0 = 0.f, ga1 = 0.f, gb0 = 0.f, gb1 = 0.f, xv0 = 0.f, xv1 = 0.f;
    u32 xo = piece(WS_X1, 4096, (u32)(lane >> 3)) + (rid * 8 + (u32)(lane & 7)) * 16u;
    u32 aa = F_A + (i15 & 7) * 272 + g * 16, cw = F_CX + ((u32)wave & 7) * 32 + i15 * 2, cr = F_CX + g * 16;
    u32 oo = piece(WS_OUTP, 16384, 2 * (u32)wave + (g & 1)) + (u32)b * 128u + (i15 >> 1) * 16u;
    PIN(aa); PIN(cw); PIN(cr); PIN(oo); PIN(xo);
    STAMP(7);
    {
        const u32 sl = i15 < 12 ? i15 : 11u, half = (u32)wave >> 3, w7 = (u32)wave & 7;
        const u32 o = WS_PAY + (u32)head * PHD + (((u32)j * 2 + half) * 128 + w7 * 16 + g) * PBLK + sl * 16u;
        const u32 hfp = sl < 8 ? sl >> 2 : (sl - 8) >> 1, gi = sl < 8 ? sl & 3 : 4 + (sl & 1);
        const u32 ta = 3 * gi + (g >> 1), tz = 3 * gi + 2;
        const u32 cbase = F_A + w7 * 272 + half * 128 + hfp * 8;
        u32 wa = (i15 < 12 && ta < 16) ? cbase + (ta >> 3) * 64 + ((ta >> 1) & 3) * 16 + (ta & 1) * 4 + (g & 1) * 2 : F_A + w7 * 272 + 256;
        u32 wz = (i15 < 12 && tz < 16) ? cbase + (tz >> 3) * 64 + ((tz >> 1) & 3) * 16 + (tz & 1) * 4 + (g >> 1) * 2 : F_A + w7 * 272 + 260;
        v4u v[4];
        PIN(wa); PIN(wz);
        u32 tdet;
        if (wave == 0) {
            tdet = hint_wait<HC_K>(ws, WS_HC + (u32)b * HSTR + (u32)(lane & 7) * 16u, tlo, budget) | 1u;
            if (lane == 0) *lp<u32>(F_GO) = tdet;
            if (is_red) {
                v4u gm[4];
                const u32 om = WS_ML + (rid >> 1) * 4096u + (g * 16 + (rid & 1) * 8 + (u32)(lane & 7)) * 16u;
                u32 xr = ld_u32(P.x, (rid * 8 + (u32)(lane & 7)) * 4);
                gather<4, 1024>(ws, om, tlo, thi, gm, budget);
                PIN(xr);
                xv0 = bflo(xr); xv1 = bfhi(xr);
                {
                    const float s0 = red16(red32(asf(gm[1].x), asf(gm[2].x)), red32(asf(gm[1].y), asf(gm[2].y)));
                    u32 e, od, sbp, cbp;
                    both16(asu(s0), e, od);
                    both32(pack2(asf(e), asf(od)), sbp, cbp);
                    if (lane < 8) st_sc(ws, WS_SC + (rid * 8 + (u32)lane) * 16u, (v4u){cbp, sbp, tlo, thi});
                }
                ga0 = asf(gm[0].x); ga1 = asf(gm[0].y); gb0 = asf(gm[3].x); gb1 = asf(gm[3].y);
                ga0 = red32(ga0, ga0); ga0 = red16(ga0, ga0); ga1 = red32(ga1, ga1); ga1 = red16(ga1, ga1);
                gb0 = red32(gb0, gb0); gb0 = red16(gb0, gb0); gb1 = red32(gb1, gb1); gb1 = red16(gb1, gb1);
            }
        } else {
            lds_wait(F_GO, 1u, budget);
            tdet = rfl(*lp<u32>(F_GO));
        }
        PIN(ga0); PIN(ga1); PIN(gb0); PIN(gb1); PIN(xv0); PIN(xv1);
#pragma unroll
        for (int k = 0; k < 8; ++k) PIN(bo[k]);
        until32(tdet, half ? C_DELAY2 : C_DELAY);
        STAMP(8);
        float M = 0.f;
        gather<4, 4 * PBLK, true>(ws, o, tlo, thi, v, budget);
        STAMP(9);
        float mk[4];
#pragma unroll
        for (int k = 0; k < 4; ++k) mk[k] = dpp<0x159>(asf(v[k].y));
        if (half == 0) {
            const float Mw = max32(max16(max3(max3(mk[0], mk[1], mk[2]), mk[3], mk[0])));
            if (lane == 0) *lp<float>(F_ST + w7 * 4) = Mw;
            lds_inc(F_GO + 4, lane);
        }
#pragma clang loop unroll(disable)
        do {
            const u32 cnt = *(volatile LAS u32*)lp<u32>(F_GO + 4);
            const f32x4 m0 = *(volatile LAS f32x4*)lp<f32x4>(F_ST), m1 = *(volatile LAS f32x4*)lp<f32x4>(F_ST + 16);
            M = max8(m0, m1);
            if (cnt >= 8u) break;
            __builtin_amdgcn_s_sleep(1);
        } while (--budget > 0);
        const u32 w01 = pack2(ex2(mk[0] - M), ex2(mk[1] - M)), w23 = pack2(ex2(mk[2] - M), ex2(mk[3] - M));
        float a[6];
#pragma unroll
        for (int q = 0; q < 3; ++q) {
            a[2 * q] = dot2(perm_lo(v[2][q], v[3][q]), w23, dot2(perm_lo(v[0][q], v[1][q]), w01, 0.f));
            a[2 * q + 1] = dot2(perm_hi(v[2][q], v[3][q]), w23, dot2(perm_hi(v[0][q], v[1][q]), w01, 0.f));
        }
        const float x = red16(red32(a[0], a[2]), red32(a[1], a[3]));
        float z = red32(a[4], a[5]);
        z = red16(z, z);
        const u32 xz = pack2(x, z);
        *lp<u16>(wa) = (u16)xz;
        *lp<u16>(wz) = (u16)(xz >> 16);
        if (half) {
            const float L = red32(z, z);
            if (lane == 9) *lp<float>(F_ST + 32 + w7 * 4) = L;
        }
    }
    barrier();
    STAMP(10);
    if (wave < 8) {
        bf16x8 af[4];
#pragma unroll
        for (int s = 0; s < 4; ++s) af[s] = *lp<bf16x8>(aa + s * 64);
        const f32x4 l0 = *lp<f32x4>(F_ST + 32), l1 = *lp<f32x4>(F_ST + 48);
        __builtin_amdgcn_sched_barrier(0);
        f32x4 d = {0.f, 0.f, 0.f, 0.f}, e = {0.f, 0.f, 0.f, 0.f};
        d = __builtin_amdgcn_mfma_f32_16x16x32_bf16(af[0], bu[0], d, 0, 0, 0);
        e = __builtin_amdgcn_mfma_f32_16x16x32_bf16(af[1], bu[1], e, 0, 0, 0);
        d = __builtin_amdgcn_mfma_f32_16x16x32_bf16(af[2], bu[2], d, 0, 0, 0);
        e = __builtin_amdgcn_mfma_f32_16x16x32_bf16(af[3], bu[3], e, 0, 0, 0);
        const float rl = __builtin_amdgcn_rcpf(((l0.x + l0.y) + (l0.z + l0.w)) + ((l1.x + l1.y) + (l1.z + l1.w)));
        float c = ((d.x + e.x) + (d.y + e.y)) + ((d.z + e.z) + (d.w + e.w));
        c = red16(c, c) * rl;
        *lp<u16>(cw) = (u16)pack2(c, c);
    } else if (b == 0 && tid == 512) st_sc2(ws, WS_EPOCH, (v2u){tlo, thi});
    barrier();
    STAMP(13);
    {
        bf16x8 af[4];
#pragma unroll
        for (int s = 0; s < 4; ++s) af[s] = *lp<bf16x8>(cr + s * 64);
        __builtin_amdgcn_sched_barrier(0);
        f32x4 d0 = {0.f, 0.f, 0.f, 0.f}, d1 = {0.f, 0.f, 0.f, 0.f};
#pragma unroll
        for (int s = 0; s < 4; ++s) { d0 = __builtin_amdgcn_mfma_f32_16x16x32_bf16(af[s], bo[s], d0, 0, 0, 0); d1 = __builtin_amdgcn_mfma_f32_16x16x32_bf16(af[s], bo[4 + s], d1, 0, 0, 0); }
        const float o = (g & 1) ? d1.x : d0.x;
        const float n = dpp<0x101>(o);
        if ((lane & 33) == 0) st_sc(ws, oo, (v4u){asu(o), asu(n), tlo, thi});
    }
    STAMP(11);
    if (is_red) {
        const f32x2 s = reduce16<16>(ws, piece(WS_OUTP, 16384, rid), tlo, thi, wave, lane, F_RD, budget);
        const float x10 = xv0 + ga0 * s.x, x11 = xv1 + ga1 * s.y;
        if (wave == 0) st_sc(ws, xo, (v4u){asu(x10), asu(x11), tlo, thi});
        STAMP(12);
        until(__builtin_amdgcn_s_memrealtime(), F_DELAY);
        const f32x2 s2 = reduce16<14>(ws, piece(WS_YP, 16384, rid), tlo, thi, wave, lane, F_RV, budget);
        if (tid < 8) ((u32*)P.out)[rid * 8 + lane] = pack2(x10 + gb0 * s2.x, x11 + gb1 * s2.y);
    }
    STAMP(15);
    STAMP_OUT();
}

extern "C" void kernel_launch(void* const* d_in, const int* in_sizes, int n_in, void* d_out, int out_size, void* d_ws, size_t ws_size, hipStream_t stream) {
    Params P;
    P.x = (const u16*)d_in[0]; P.ckv = (const u16*)d_in[1]; P.wq = (const u16*)d_in[2]; P.wuk = (const u16*)d_in[3];
    P.wuv = (const u16*)d_in[4]; P.wo = (const u16*)d_in[5]; P.wg = (const u16*)d_in[7]; P.wu = (const u16*)d_in[8];
    P.wd = (const u16*)d_in[9]; P.c = (const u16*)d_in[10]; P.wmod = (const u16*)d_in[11];
    P.out = (u16*)d_out; P.ws = (char*)d_ws + WS_BASE(ws_size);
    (void)hipFuncSetAttribute((const void*)fused_kernel, hipFuncAttributeMaxDynamicSharedMemorySize, LDS_BYTES);
    fused_kernel<<<256, 1024, LDS_BYTES, stream>>>(P);
}
```

```cpp
#include <hip/hip_runtime.h>

typedef unsigned u32;
typedef unsigned long long u64;
typedef unsigned short u16;
typedef u32 v4u __attribute__((ext_vector_type(4)));
typedef u32 v2u __attribute__((ext_vector_type(2)));
typedef float f32x2 __attribute__((ext_vector_type(2)));
typedef float f32x4 __attribute__((ext_vector_type(4)));
typedef float f32x16 __attribute__((ext_vector_type(16)));
typedef short bf16x8 __attribute__((ext_vector_type(8)));
typedef short s16x4 __attribute__((ext_vector_type(4)));
typedef __bf16 bf16x2_t __attribute__((ext_vector_type(2)));
#define LAS __attribute__((address_space(3)))
#define DI __device__ __forceinline__

#define CFG(name, val) constexpr int name = val
CFG(BUDGET, 1 << 20);
CFG(POLL_SLEEP, 4);
CFG(HINT_SLEEP, 4);
#define PACE 0, 65, 65, 240, 65, 600, 150, 560, 800, 110, 20, 150, 150, 45, 10
constexpr int pace_[] = {PACE};
CFG(T_OPEN0, pace_[0]);
CFG(T_OPEN1, pace_[1]);
CFG(T_OPEN2, pace_[2]);
CFG(A_FIRST, pace_[3]);
CFG(A2_DELAY, pace_[4]);
CFG(Q_FIRST, pace_[5]);
CFG(T_OWN, pace_[6]);
CFG(T_MID, pace_[7]);
CFG(T_UV, pace_[8]);
CFG(C_DELAY, pace_[9]);
CFG(G_DELAY, pace_[10]);
CFG(F_DELAY, pace_[11]);
CFG(ST_LOOK, pace_[12]);
CFG(SKEW_MAX, pace_[13]);
CFG(Q_PRE, pace_[14]);
#define KD 6
#define KMAP 0
#define KMUL 41
DI u32 ktile(u32 s, u32 t) {
    if (KMAP == 0) return s * 262144u + t * 16384u;
    if (KMAP == 1) return t * 2097152u + s * 16384u;
    const u32 pos = (s + KMUL * t) & 127u;
    if (KMAP == 3) return t * 2097152u + pos * 16384u;
    if (KMAP == 4) return ((t + pos) & 15u) * 2097152u + pos * 16384u;
    if (KMAP == 5) return ((t + (pos >> 3)) & 15u) * 2097152u + pos * 16384u;
    return pos * 262144u + t * 16384u;
}
#define HW 5
CFG(HC_K, 5);
#define QD 2
#define LAZY 48.f
#define CODE_STR 6912
#define WS_BASE(sz) (((sz) - (32u << 20)) & ~(size_t)((2u << 20) - 1))

constexpr u32 WS_EPOCH = 0;
DI u32 piece(u32 base, u32 size, u32 i) { return base + i * size; }
constexpr u32 WS_ST = 16384;
constexpr u32 WS_HP = 32768;
constexpr u32 WS_QABS = 65536;
constexpr u32 WS_QCHK = 98304;
constexpr u32 WS_QP = 131072;
constexpr u32 WS_ML = 262144;
constexpr u32 WS_X1 = 327680;
constexpr u32 WS_SC = 360448;
constexpr u32 HSTR = 1152, WS_HC = 368640;
constexpr u32 WS_OUTP = 524288;
constexpr u32 WS_YP = 1u << 20;
constexpr u32 WS_CHK = 3u << 19;
constexpr u32 WS_PAY = 2u << 20;
constexpr u32 PHD = 4 * 128 * 256 + 4096;
constexpr u32 WS_TL = 16u << 20;

constexpr u32 L_FL = 114688, L_P = L_FL + 1024, L_IMG = 131072;
constexpr u32 slot(int t) { return t == 15 ? L_IMG + 16384 : (u32)(t % 7) * 16384u; }
constexpr u32 C_LAND = 0;
constexpr u32 C_KRD = 256, C_Z = 264, C_E = 268, C_P = 272, C_Q = 284, C_L = 288, C_BAD = 292;
constexpr u32 L_M = L_FL + 384, L_L = L_FL + 512;
constexpr u32 L_W = 0;
constexpr u32 L_H2 = 32768;
constexpr u32 L_GU = 34816;
constexpr u32 F_QP = 98304;
constexpr u32 F_WO = 0;
constexpr u32 F_UV = 131072;
constexpr u32 F_A = 0;
constexpr u32 F_ST = 8192;
constexpr u32 F_GO = 8384;
constexpr u32 F_CX = 8448;
constexpr u32 F_ML = 12288;
constexpr u32 F_RD = 20480, F_RV = 22528;
constexpr u32 LDS_BYTES = 163840;

struct Params {
    const u16 *wmod, *c, *x;
    char* ws;
    const u16 *wq, *ckv, *wuk, *wo, *wuv, *wg, *wu, *wd;
    u16* out;
};

DI float asf(u32 v) { return __builtin_bit_cast(float, v); }
DI u32 asu(float v) { return __builtin_bit_cast(u32, v); }
DI float bflo(u32 v) { return asf(v << 16); }
DI float bfhi(u32 v) { return asf(v & 0xffff0000u); }
DI u32 pack2(float a, float b) { f32x2 v = {a, b}; return __builtin_bit_cast(u32, __builtin_convertvector(v, bf16x2_t)); }
DI float dot2(u32 w, u32 x, float acc) { return __builtin_amdgcn_fdot2_f32_bf16(__builtin_bit_cast(bf16x2_t, w), __builtin_bit_cast(bf16x2_t, x), acc, false); }
DI void pair_rows(v4u& x, v4u& y) {
#pragma unroll
    for (int k = 0; k < 4; ++k) { const u32 a = x[k], b = y[k]; x[k] = __builtin_amdgcn_perm(b, a, 0x05040100u); y[k] = __builtin_amdgcn_perm(b, a, 0x07060302u); }
}
template <int CTRL> DI u32 dppu(u32 v) { return (u32)__builtin_amdgcn_update_dpp(0, (int)v, CTRL, 0xf, 0xf, true); }
template <int CTRL> DI float dpp(float v) { return asf(dppu<CTRL>(asu(v))); }
DI float red32(float a, float b) { auto r = __builtin_amdgcn_permlane32_swap(asu(a), asu(b), false, false); return asf((u32)r[0]) + asf((u32)r[1]); }
DI float red16(float a, float b) { auto r = __builtin_amdgcn_permlane16_swap(asu(a), asu(b), false, false); return asf((u32)r[0]) + asf((u32)r[1]); }
DI float max3(float a, float b, float c) { return __builtin_fmaxf(__builtin_fmaxf(a, b), c); }
DI float max4(f32x4 a) { return max3(max3(a.x, a.y, a.z), a.w, a.x); }
DI float max32(float a) { auto r = __builtin_amdgcn_permlane32_swap(asu(a), asu(a), false, false); return __builtin_fmaxf(asf((u32)r[0]), asf((u32)r[1])); }
DI float max16(float a) { auto r = __builtin_amdgcn_permlane16_swap(asu(a), asu(a), false, false); return __builtin_fmaxf(asf((u32)r[0]), asf((u32)r[1])); }
DI float max8(const f32x4& a, const f32x4& b) { return max3(max3(max3(a.x, a.y, a.z), a.w, b.x), max3(b.y, b.z, b.w), a.x); }
DI float sum4(f32x4 a) { return (a.x + a.y) + (a.z + a.w); }
DI u32 perm_lo(u32 a, u32 b) { return __builtin_amdgcn_perm(b, a, 0x05040100u); }
DI u32 perm_hi(u32 a, u32 b) { return __builtin_amdgcn_perm(b, a, 0x07060302u); }
DI u32 rfl(u32 v) { return (u32)__builtin_amdgcn_readfirstlane((int)v); }
DI void both16(u32 a, u32& e, u32& o) { auto r = __builtin_amdgcn_permlane16_swap(a, a, false, false); e = (u32)r[0]; o = (u32)r[1]; }
DI void both32(u32 a, u32& l, u32& h) { auto r = __builtin_amdgcn_permlane32_swap(a, a, false, false); l = (u32)r[0]; h = (u32)r[1]; }
DI float rowsum(float v) { v += dpp<0x128>(v); v += dpp<0x124>(v); v += dpp<0x122>(v); v += dpp<0x121>(v); return v; }
DI float ex2(float v) { return __builtin_amdgcn_exp2f(v); }
DI u64 ballot(bool c) { return __builtin_amdgcn_ballot_w64(c); }

DI v4u ld_nt(const void* base, u32 off) { v4u r; asm volatile("global_load_dwordx4 %0, %1, %2 nt" : "=v"(r) : "v"(off), "s"(base) : "memory"); return r; }
DI u32 ld_u32(const void* base, u32 off) { u32 r; asm volatile("global_load_dword %0, %1, %2" : "=v"(r) : "v"(off), "s"(base) : "memory"); return r; }
DI u32 ld_u32_nt(const void* base, u32 off) { u32 r; asm volatile("global_load_dword %0, %1, %2 nt" : "=v"(r) : "v"(off), "s"(base) : "memory"); return r; }
DI u32 ld_u16(const void* base, u32 off) { u32 r; asm volatile("global_load_ushort %0, %1, %2" : "=v"(r) : "v"(off), "s"(base) : "memory"); return r; }
DI void st_sc(void* base, u32 off, v4u v) { asm volatile("global_store_dwordx4 %0, %1, %2 sc1\n\ts_nop 1" :: "v"(off), "v"(v), "s"(base) : "memory"); }
DI void st_sc_keep(void* base, u32 off, v4u v) { asm volatile("global_store_dwordx4 %0, %1, %2 sc1" :: "v"(off), "v"(v), "s"(base) : "memory"); }
DI void st_sc2(void* base, u32 off, v2u v) { asm volatile("global_store_dwordx2 %0, %1, %2 sc1\n\ts_nop 1" :: "v"(off), "v"(v), "s"(base) : "memory"); }
DI void dma_nt(const void* base, u32 off, u32 lds_dst) { asm volatile("s_mov_b32 m0, %1\n\ts_nop 0\n\tglobal_load_lds_dwordx4 %0, %2 nt" :: "v"(off), "s"(lds_dst), "s"(base) : "memory"); }
DI void dma_pl(const void* base, u32 off, u32 lds_dst) { asm volatile("s_mov_b32 m0, %1\n\ts_nop 0\n\tglobal_load_lds_dwordx4 %0, %2" :: "v"(off), "s"(lds_dst), "s"(base) : "memory"); }
DI void dma_sc(const void* base, u32 off, u32 lds_dst) { asm volatile("s_mov_b32 m0, %1\n\ts_nop 0\n\tglobal_load_lds_dwordx4 %0, %2 sc1" :: "v"(off), "s"(lds_dst), "s"(base) : "memory"); }
DI void ld_nt_into(v4u& r, const void* base, u32 off) { asm volatile("global_load_dwordx4 %0, %1, %2 nt" : "+v"(r) : "v"(off), "s"(base) : "memory"); }
DI void ld_sc_into(v4u& r, const void* base, u32 off) { asm volatile("global_load_dwordx4 %0, %1, %2 sc1" : "+v"(r) : "v"(off), "s"(base) : "memory"); }
#define VMWAIT(n) asm volatile("s_waitcnt vmcnt(%0)" :: "i"(n) : "memory")
#define LGKM0() asm volatile("s_waitcnt lgkmcnt(0)" ::: "memory")
#define PIN(x) asm volatile("" : "+v"(x))
#define UNDEF(x) asm volatile("" : "=v"(x))
#define SCHED() __builtin_amdgcn_sched_barrier(0)
DI void barrier() { LGKM0(); __builtin_amdgcn_s_barrier(); asm volatile("" ::: "memory"); }

template <typename T> DI LAS T* lp(u32 off) { return (LAS T*)(size_t)off; }
DI bf16x8 tr2(u32 a, u32 b) {
    const s16x4 lo = __builtin_amdgcn_ds_read_tr16_b64_v4i16(lp<s16x4>(a)), hi = __builtin_amdgcn_ds_read_tr16_b64_v4i16(lp<s16x4>(b));
    return __builtin_shufflevector(lo, hi, 0, 1, 2, 3, 4, 5, 6, 7);
}

#define STAMP(i) do {} while (0)
#define CYC0() do {} while (0)
#define STAMPC(i) do {} while (0)
#define STAMP_OUT() do {} while (0)

#define CDECL() do {} while (0)
#define CS(i) do {} while (0)
#define COUT(n) do {} while (0)
template <u32 S> DI void pass(const char* ws, u32 off, v4u (&v)[1]) {
    asm volatile("global_load_dwordx4 %0, %1, %2 sc1\n\ts_waitcnt vmcnt(0)" : "=v"(v[0]) : "v"(off), "s"(ws) : "memory");
}
template <u32 S> DI void pass(const char* ws, u32 off, v4u (&v)[2]) {
    asm volatile("global_load_dwordx4 %0, %2, %3 sc1\n\tglobal_load_dwordx4 %1, %2, %4 sc1\n\ts_waitcnt vmcnt(0)" : "=&v"(v[0]), "=&v"(v[1]) : "v"(off), "s"(ws), "s"(ws + S) : "memory");
}
template <u32 S> DI void pass(const char* ws, u32 off, v4u (&v)[4]) {
    asm volatile("global_load_dwordx4 %0, %4, %5 sc1\n\tglobal_load_dwordx4 %1, %4, %5 offset:%6 sc1\n\tglobal_load_dwordx4 %2, %4, %5 offset:%7 sc1\n\tglobal_load_dwordx4 %3, %4, %5 offset:%8 sc1\n\ts_waitcnt vmcnt(0)"
        : "=&v"(v[0]), "=&v"(v[1]), "=&v"(v[2]), "=&v"(v[3]) : "v"(off), "s"(ws), "i"(S), "i"(2 * S), "i"(3 * S) : "memory");
}
template <int N, u32 STRIDE, int SLP = POLL_SLEEP> DI void gather(const char* ws, u32 off, u32 tlo, u32 thi, v4u (&v)[N], int& budget) {
#pragma clang loop unroll(disable)
    do {
        pass<STRIDE>(ws, off, v);
        u32 bad = 0;
#pragma unroll
        for (int k = 0; k < N; ++k) bad |= (v[k].z ^ tlo) | (v[k].w ^ thi);
        if (ballot(bad != 0u) == 0ull) break;
        __builtin_amdgcn_s_sleep(SLP);
    } while (--budget > 0);
}
DI void flag_set(u32 fa, u32 off) {
    asm volatile("s_mov_b64 exec, 1\n\tds_write_b8_d16_hi %0, %0 offset:%1\n\ts_mov_b64 exec, -1" :: "v"(fa), "i"(off) : "memory");
}
DI u32 flag_get(u32 off) { return *(volatile LAS u32*)lp<u32>(L_FL + off); }
DI void wait4(u32 fb, u32 off) {
    u32 v, n;
    asm volatile("s_mov_b32 %1, 0x400000\n.Lw%=:\n\tds_read_b32 %0, %2 offset:%3\n\ts_sub_u32 %1, %1, 1\n\ts_waitcnt lgkmcnt(0)\n\tv_cmp_ne_u32 vcc, 0x1010101, %0\n\ts_cbranch_scc1 .Le%=\n\ts_cbranch_vccnz .Lw%=\n.Le%=:"
        : "=&v"(v), "=&s"(n) : "v"(fb), "i"(off) : "memory", "vcc", "scc");
}
DI void wait1(u32 fb, u32 off) {
    u32 v, n;
    asm volatile("s_mov_b32 %1, 0x400000\n.Lw%=:\n\tds_read_u8 %0, %2 offset:%3\n\ts_sub_u32 %1, %1, 1\n\ts_waitcnt lgkmcnt(0)\n\tv_cmp_ne_u32 vcc, 1, %0\n\ts_cbranch_scc1 .Le%=\n\ts_cbranch_vccnz .Lw%=\n.Le%=:"
        : "=&v"(v), "=&s"(n) : "v"(fb), "i"(off) : "memory", "vcc", "scc");
}
#define SPIN_HEAD "s_mov_b32 %[n], 0x400000\n.Lw%=:\n\t"
#define SPIN_TAIL(cmp) "s_sub_u32 %[n], %[n], 1\n\ts_waitcnt lgkmcnt(0)\n\t" cmp ", %[v]\n\ts_cbranch_scc1 .Le%=\n\ts_cbranch_vccnz .Lw%=\n.Le%=:"
DI void land_wait(u32 fl, u32 off) {
    u32 v, n;
    asm volatile(SPIN_HEAD "ds_read_b32 %[v], %[b] offset:%[o]\n\t" SPIN_TAIL("v_cmp_eq_u32 vcc, 0") : [v] "=&v"(v), [n] "=&s"(n) : [b] "v"(fl), [o] "i"(off) : "memory", "vcc", "scc");
}
DI bf16x8 wait1p(u32 fb, u32 off, u32 pa, u32 poff) {
    u32 v, n;
    bf16x8 p;
    asm volatile(SPIN_HEAD "ds_read_u8 %[v], %[b] offset:%[o]\n\tds_read_b128 %[p], %[a] offset:%[q]\n\t" SPIN_TAIL("v_cmp_ne_u32 vcc, 1")
        : [v] "=&v"(v), [p] "=&v"(p), [n] "=&s"(n) : [b] "v"(fb), [o] "i"(off), [a] "v"(pa), [q] "i"(poff) : "memory", "vcc", "scc");
    return p;
}
DI void wait4p(u32 fb, u32 off, u32 pa, u32 poff, bf16x8 (&p)[4]) {
    u32 v, n;
    asm volatile(SPIN_HEAD "ds_read_b32 %[v], %[b] offset:%[o]\n\tds_read_b128 %[p0], %[a] offset:%[q0]\n\tds_read_b128 %[p1], %[a] offset:%[q1]\n\tds_read_b128 %[p2], %[a] offset:%[q2]\n\tds_read_b128 %[p3], %[a] offset:%[q3]\n\t"
        SPIN_TAIL("v_cmp_ne_u32 vcc, 0x1010101")
        : [v] "=&v"(v), [p0] "=&v"(p[0]), [p1] "=&v"(p[1]), [p2] "=&v"(p[2]), [p3] "=&v"(p[3]), [n] "=&s"(n)
        : [b] "v"(fb), [o] "i"(off), [a] "v"(pa), [q0] "i"(poff), [q1] "i"(poff + 1024), [q2] "i"(poff + 2048), [q3] "i"(poff + 3072) : "memory", "vcc", "scc");
}
DI void land_wait_k(u32 fl, u32 off, u32 a0, u32 a1, u32 a2, u32 a3, bf16x8& f0, bf16x8& f1, bf16x8& f2, bf16x8& f3) {
    u32 v, n;
    asm volatile(SPIN_HEAD "ds_read_b32 %[v], %[b] offset:%[o]\n\tds_read_b128 %[p0], %[a0]\n\tds_read_b128 %[p1], %[a1]\n\tds_read_b128 %[p2], %[a2]\n\tds_read_b128 %[p3], %[a3]\n\t"
        SPIN_TAIL("v_cmp_eq_u32 vcc, 0")
        : [v] "=&v"(v), [p0] "=&v"(f0), [p1] "=&v"(f1), [p2] "=&v"(f2), [p3] "=&v"(f3), [n] "=&s"(n)
        : [b] "v"(fl), [o] "i"(off), [a0] "v"(a0), [a1] "v"(a1), [a2] "v"(a2), [a3] "v"(a3) : "memory", "vcc", "scc");
}
DI void dma_a(const char* src, const u32 (&od)[4], u32 dst) {
    asm volatile("s_mov_b32 m0, %3\n\ts_nop 0\n\tglobal_load_lds_dwordx4 %0, %2 nt\n\tglobal_load_lds_dwordx4 %1, %2 offset:1024 nt" :: "v"(od[0]), "v"(od[1]), "s"(src), "s"(dst) : "memory");
}
DI void dma_b(const char* src, const u32 (&od)[4], u32 dst, u64 pc, u32 flag) {
    asm volatile("s_mov_b32 m0, %4\n\ts_nop 0\n\tglobal_load_lds_dwordx4 %1, %3 offset:2048 nt\n\tglobal_load_lds_dwordx4 %2, %3 offset:3072 nt\n\t"
        "s_mov_b32 m0, %6\n\ts_mov_b64 exec, 1\n\tglobal_load_lds_dword %0, %5\n\ts_mov_b64 exec, -1"
        :: "v"(od[0]), "v"(od[2]), "v"(od[3]), "s"(src), "s"(dst), "s"(pc), "s"(flag) : "memory");
}
static_assert(C_LAND == 0, "");
static_assert(((L_FL >> 16) & 0xff) == 1, "flag_set writes bits 23:16 of the address");
template <int K> DI u32 hint_wait(const char* ws, u32 oh, u32 tlo, int& budget) {
    v4u h[1];
#pragma clang loop unroll(disable)
    do {
        pass<0>(ws, oh, h);
        if (__builtin_popcount((u32)ballot(h[0].z == tlo) & 0xffu) >= K) break;
        __builtin_amdgcn_s_sleep(HINT_SLEEP);
    } while (--budget > 0);
    const u32 now = (u32)__builtin_amdgcn_s_memrealtime();
    u32 age = h[0].z == tlo ? now - h[0].x : 0xffffu;
    age = age > 0xffffu ? 0u : age;
    age = min(age, dppu<0xB1>(age));
    age = min(age, dppu<0x4E>(age));
    age = min(age, dppu<0x124>(age));
    return now - rfl(age);
}
DI u32 now32() { return (u32)__builtin_amdgcn_s_memrealtime(); }
DI void until(u32 t) { while ((int)(now32() - t) < 0) __builtin_amdgcn_s_sleep(1); }
typedef u32 v16u __attribute__((ext_vector_type(16)));
DI u32 time_base(const char* p, u32 t0) {
    v16u h;
    asm volatile("s_load_dwordx16 %0, %1, 0x0\n\ts_waitcnt lgkmcnt(0)" : "=s"(h) : "s"(p) : "memory");
    u32 m = 0, bad = 0;
#pragma unroll
    for (int k = 0; k < 8; ++k) { m = max(m, h[2 * k] - t0 + 150u); bad |= h[2 * k] ^ ~h[2 * k + 1]; }
    return (bad != 0u || m >= 300u) ? t0 + SKEW_MAX : t0 - 150u + m;
}
DI u32 swz(u32 row) { return ((row & 3u) << 2) | ((row >> 2) & 3u); }

#define EPOCH() do { asm volatile("s_load_dwordx2 %0, %1, 0x0\n\ts_waitcnt lgkmcnt(0)" : "=s"(e64) : "s"(ws) : "memory"); ++e64; } while (0)
DI void wave_sum2(float s1, float s2, float& t1, float& t2) {
    float z = red32(s1, s2);
    z = red16(z, z); z = rowsum(z);
    u32 a, b;
    both32(asu(z), a, b);
    t1 = asf(a); t2 = asf(b);
}
DI void ln_stats(const v4u& xa, float& mean, float& rstd) {
    const float pil = asf(rfl(xa[0]) << 16);
    float s1 = 0.f, s2 = 0.f;
#pragma unroll
    for (int k = 0; k < 4; ++k) { const float a = bflo(xa[k]) - pil, c = bfhi(xa[k]) - pil; s1 += a; s2 = __builtin_fmaf(a, a, s2); s1 += c; s2 = __builtin_fmaf(c, c, s2); }
    float t1, t2;
    wave_sum2(s1, s2, t1, t2);
    const float dm = t1 * (1.f / 512.f);
    mean = pil + dm;
    rstd = __builtin_amdgcn_rsqf(__builtin_fmaxf(t2 * (1.f / 512.f) - dm * dm, 0.f) + 1e-5f);
}
DI float sigm(float v) { return __builtin_amdgcn_rcpf(1.f + ex2(v * -1.4426950408889634f)); }
DI float silu(float v) { return v * sigm(v); }

template <int NW2> DI f32x2 reduce16(const char* ws, u32 base, u32 tlo, u32 thi, int wave, int lane, u32 lds_red, int& budget) {
    u32 la = lds_red + (u32)(wave * 16 + (lane & 7) * 2 + (lane >> 5)) * 4, ra = lds_red + (u32)lane * 8, go = base + (u32)(wave * 64 + lane) * 16u;
    PIN(la); PIN(ra); PIN(go);
    float z;
    if (NW2 == 8 || wave < NW2) {
        v4u g[2];
        gather<2, 8192, 1>(ws, go, tlo, thi, g, budget);
        z = red32(asf(g[0].x) + asf(g[1].x), asf(g[0].y) + asf(g[1].y));
    } else {
        v4u g[1];
        gather<1, 0, 1>(ws, go, tlo, thi, g, budget);
        z = red32(asf(g[0].x), asf(g[0].y));
    }
    z = red16(z, z); z += dpp<0x128>(z);
    *lp<float>(la) = z;
    barrier();
    f32x2 s = {0.f, 0.f};
    if (wave == 0) {
        s = *lp<f32x2>(ra);
        float t = red32(s.x, s.y);
        t = red16(t, t); t += dpp<0x128>(t);
        u32 a, c;
        both32(asu(t), a, c);
        s = (f32x2){asf(a), asf(c)};
    }
    return s;
}

DI u32 mlp_id(int b) { const int xc = b & 7, yy = (b >> 3) - 16; return (u32)(((yy >> 2) * 8 + xc) * 4 + (yy & 3)); }
DI void mlp_part(const Params& P, int b, u32 w, int lane, u32 tlo, u32 thi, int& budget, u32& tl) {
    char* const ws = P.ws;
    const u32 gu_id = mlp_id(b), i15 = lane & 15, g = lane >> 4;
    u32 wo = (128 * w + ((u32)lane >> 1)) * 3584u + gu_id * 32u + ((u32)lane & 1) * 16u, wdo = gu_id * 16384u + (64 * w + (u32)lane) * 4u;
    PIN(wo); PIN(wdo);
    barrier();
    STAMP(10);
    until(now32() + G_DELAY);
#pragma unroll
    for (int s = 0; s < 4; ++s) { dma_nt(P.wg + s * 32 * 1792, wo, L_W + w * 8192u + s * 1024u); dma_nt(P.wu + s * 32 * 1792, wo, L_W + w * 8192u + 4096u + s * 1024u); }
    u32 wdr[16];
#pragma unroll
    for (int j = 0; j < 16; ++j) wdr[j] = ld_u32_nt(P.wd + j * 512, wdo);
    VMWAIT(0);
#pragma unroll
    for (int j = 0; j < 16; j += 2) {
        PIN(wdr[j]); PIN(wdr[j + 1]);
        const u32 a = wdr[j], bb = wdr[j + 1];
        wdr[j] = perm_lo(a, bb); wdr[j + 1] = perm_hi(a, bb);
        PIN(wdr[j]); PIN(wdr[j + 1]);
    }
    bf16x8 bg[4], bp[4];
    {
        const u32 a = L_W + w * 8192u + (8 * g + (i15 >> 2)) * 32 + (lane & 3) * 8;
#pragma unroll
        for (int s = 0; s < 4; ++s) { bg[s] = tr2(a + s * 1024, a + s * 1024 + 128); bp[s] = tr2(a + 4096 + s * 1024, a + 4096 + s * 1024 + 128); }
    }
    u32 ha = L_H2 + w * 256 + (u32)lane * 4, hr = L_H2 + w * 256 + g * 16, ga = L_GU + w * 128 + (u32)(lane & 31) * 4, gr = L_GU + (u32)lane * 4;
    u32 so = piece(WS_YP, 16384, 8 * w + (u32)(lane >> 3)) + (gu_id * 8 + (lane & 7)) * 16;
    v4u mg[1];
    gather<1, 0>(ws, WS_SC + (64 * w + (u32)lane) * 16u, tlo, thi, mg, budget);
    float cb0 = 1.f + bflo(mg[0].x), cb1 = 1.f + bfhi(mg[0].x), sb0 = bflo(mg[0].y), sb1 = bfhi(mg[0].y);
    PIN(sb0); PIN(sb1); PIN(cb0); PIN(cb1); PIN(ha); PIN(hr); PIN(ga); PIN(gr); PIN(so);
#pragma unroll
    for (int s = 0; s < 4; ++s) { PIN(bg[s]); PIN(bp[s]); }
    STAMP(12);
    v4u xg[4];
    gather<4, 1024, 1>(ws, piece(WS_X1, 8192, (u32)(b & 3)) + w * 1024u + (u32)lane * 16u, tlo, thi, xg, budget);
    STAMP(13);
    const float pil = asf(rfl(xg[0].x));
    float s1 = 0.f, s2 = 0.f;
#pragma unroll
    for (int k = 0; k < 4; ++k) { const float a = asf(xg[k].x) - pil, c = asf(xg[k].y) - pil; s1 += a; s2 = __builtin_fmaf(a, a, s2); s1 += c; s2 = __builtin_fmaf(c, c, s2); }
    float t1, t2;
    wave_sum2(s1, s2, t1, t2);
    const float dm = t1 * (1.f / 512.f), mean = pil + dm;
    const float rstd = __builtin_amdgcn_rsqf(__builtin_fmaxf(t2 * (1.f / 512.f) - dm * dm, 0.f) + 1e-5f);
    *lp<u32>(ha) = pack2((asf(xg[0].x) - mean) * cb0 * rstd + sb0, (asf(xg[0].y) - mean) * cb1 * rstd + sb1);
    asm volatile("" ::: "memory");
    f32x4 dg = {0.f, 0.f, 0.f, 0.f}, du = {0.f, 0.f, 0.f, 0.f};
    bf16x8 af[4];
#pragma unroll
    for (int s = 0; s < 4; ++s) af[s] = *lp<bf16x8>(hr + s * 64);
    SCHED();
#pragma unroll
    for (int s = 0; s < 4; ++s) {
        dg = __builtin_amdgcn_mfma_f32_16x16x32_bf16(af[s], bg[s], dg, 0, 0, 0);
        du = __builtin_amdgcn_mfma_f32_16x16x32_bf16(af[s], bp[s], du, 0, 0, 0);
    }
    *lp<float>(ga) = (lane & 16) ? du.x : dg.x;
    barrier();
    STAMP(11);
    float s = *lp<float>(gr) + *lp<float>(gr + 256);
    s = red32(s, s);
    u32 gs, us;
    both16(asu(s), gs, us);
    const float gsum = asf(gs), usum = asf(us);
    const float z = 1.5957691216057308f * (gsum + 0.044715f * gsum * gsum * gsum);
    const float act = gsum * usum * sigm(z);
    const u32 actp = pack2(act, dpp<0x101>(act));
    float y0 = 0.f, y1 = 0.f;
#pragma unroll
    for (int j = 0; j < 16; j += 2) { const u32 ap = (u32)__builtin_amdgcn_readlane((int)actp, j); y0 = dot2(wdr[j], ap, y0); y1 = dot2(wdr[j + 1], ap, y1); }
    st_sc(ws, so, (v4u){asu(y0), asu(y1), tlo, thi});
    STAMP(14);
}

#define ISSUE(h) do { if ((h) & 1) dma_b(ckv + ktile(sidx, (h) >> 1), od, slot((h) >> 1) + p * 4096u, pcf, L_FL + C_LAND + ((h) >> 1) * 16u + p * 4u); else dma_a(ckv + ktile(sidx, (h) >> 1), od, slot((h) >> 1) + p * 4096u); } while (0)
constexpr int ops_in(int h0, int h1) { int n = 0; for (int h = h0; h < h1; ++h) n += 2 + (h & 1); return n; }
DI void publish(char* ws, u32 g1, const f32x16 (&o)[4], v4u (&d)[8]) {
#pragma unroll
    for (int k = 0; k < 8; k += 2) {
        u32 c[8];
#pragma unroll
        for (int i = 0; i < 4; ++i) { c[i] = pack2(o[0][2 * k + i], o[1][2 * k + i]); c[4 + i] = pack2(o[2][2 * k + i], o[3][2 * k + i]); }
        SCHED();
#pragma unroll
        for (int i = 0; i < 2; ++i) {
            auto r0 = __builtin_amdgcn_permlane16_swap(c[2 * i], c[2 * i + 1], false, false);
            auto r1 = __builtin_amdgcn_permlane16_swap(c[4 + 2 * i], c[5 + 2 * i], false, false);
            d[k + i] = (v4u){(u32)r0[0], (u32)r0[1], (u32)r1[0], (u32)r1[1]};
            st_sc_keep(ws + WS_PAY + (k + i) * PHD, g1, d[k + i]);
        }
        SCHED();
    }
}
DI void stream_pv(const Params& P, int b, u32 p, int lane, const u32 (&od)[4], u64 pcf, u32 t0, u32& tl) {
    const u32 tz = t0; (void)tz;
    char* const ws = P.ws;
    const u32 sidx = (u32)b - 128, i15 = lane & 15, R = lane >> 4;
    const char* ckv = (const char*)P.ckv;
    u32 va[2][4];
    {
        const u32 hf = R >> 1, blk = R & 1, q4 = i15 >> 2, p4 = i15 & 3;
#pragma unroll
        for (int e = 0; e < 2; ++e)
#pragma unroll
            for (int cb = 0; cb < 4; ++cb) {
                const u32 row = 8 * hf + 4 * e + q4;
                va[e][cb] = p * 4096u + row * 256u + (((4 * cb + 2 * blk + (p4 >> 1)) ^ swz(row)) << 4) + 8 * (p4 & 1);
                PIN(va[e][cb]);
            }
    }
    u32 fb = L_FL, fo = L_FL + p * 4, pa = L_P + (u32)lane * 16u;
    PIN(fb); PIN(fo); PIN(pa);
    bf16x8 V[8][4];
    u32 vbase = 0;
#define VBASE(t) do { if ((slot(t) & ~65535u) != vbase) { _Pragma("unroll") for (int i_ = 0; i_ < 8; ++i_) { va[i_ >> 2][i_ & 3] += (slot(t) & ~65535u) - vbase; PIN(va[i_ >> 2][i_ & 3]); } vbase = slot(t) & ~65535u; } } while (0)
#define VREAD(t) do { VBASE(t); _Pragma("unroll") for (int cb_ = 0; cb_ < 4; ++cb_) V[(t) & 7][cb_] = tr2(va[0][cb_] + (slot(t) & 65535u), va[1][cb_] + (slot(t) & 65535u)); } while (0)
    u64 e64;
    EPOCH();
    const u32 tlo = (u32)e64;
    u32 g1, gc, lr;
    {
        const u32 k = 2 * (i15 >> 2) + (i15 & 1);
        g1 = (p * 128u + (sidx ^ ((tlo & 1) << 6))) * 256u + R * (8 * PHD) + i15 * 16u;
        gc = WS_CHK + (sidx * 4 + p) * 512u + (8 * R + k) * 16u;
        lr = L_L + (8 * (k >> 1) + 4 * (R >> 1) + 2 * (k & 1) + (R & 1)) * 16u;
        PIN(g1); PIN(gc); PIN(lr);
    }
    u32 thold = 0;
#pragma unroll
    for (int h = HW; h < 32; ++h) {
        VMWAIT(ops_in(h - HW + 1, h));
        if (h - HW == 1) STAMP(18);
        if (h - HW == 7) STAMP(19);
        if (h - HW == 15) STAMP(1);
        if (h - HW == 23) STAMP(2);
        if (h - HW == 4) thold = time_base(ws + WS_ST + (b & 7) * 128, t0) + (u32)(Q_FIRST - Q_PRE);
        if (h >= 10 && h < 30 && (int)(now32() - thold) >= 0) { wait4(fb, C_Q); thold += 0x40000000u; }
        if (h == 30) wait4(fb, C_E);
        else if (h >= 14 && !(h & 1)) wait1(fb, C_KRD + h / 2 - 7);
        ISSUE(h);
        if (((h - HW) & 1) && h - HW < 16) VREAD((h - HW) >> 1);
    }
    static_assert(HW >= 3 && HW <= 6, "tile 12 must have landed here");
    f32x16 o[4];
#pragma unroll
    for (int cb = 0; cb < 4; ++cb)
#pragma unroll
        for (int i = 0; i < 16; ++i) o[cb][i] = 0.f;
    bf16x8 pb[4], pc[4];
#define PV(t, pp) do { SCHED(); _Pragma("unroll") for (int cb_ = 0; cb_ < 4; ++cb_) o[cb_] = __builtin_amdgcn_mfma_f32_32x32x16_bf16(pp[(t) & 3], V[(t) & 7][cb_], o[cb_], 0, 0, 0); SCHED(); } while (0)
#define PVR(t, u) do { VBASE(u); _Pragma("unroll") for (int cb_ = 0; cb_ < 4; ++cb_) { SCHED(); \
        o[cb_] = __builtin_amdgcn_mfma_f32_32x32x16_bf16(pb[(t) & 3], V[(t) & 7][cb_], o[cb_], 0, 0, 0); SCHED(); \
        V[(u) & 7][cb_] = tr2(va[0][cb_] + (slot(u) & 65535u), va[1][cb_] + (slot(u) & 65535u)); } SCHED(); } while (0)
    STAMP(3);
    wait4p(fb, C_P, pa, 0, pb);
    STAMP(7);
    CYC0();
    CDECL();
    CS(0);
#pragma unroll
    for (int t = 0; t < 5; ++t) { PVR(t, t + 8); if (t < 4) pb[t] = *lp<bf16x8>(pa + (t + 4) * 1024); if (t == 0) CS(1); if (t == 1) CS(2); if (t == 3) CS(3); }
    CS(4);
    PV(5, pb); PV(6, pb);
    u32 pf = flag_get(C_P + 4);
#pragma unroll
    for (int i = 0; i < 4; ++i) pc[i] = *(volatile LAS bf16x8*)lp<bf16x8>(pa + (8 + i) * 1024);
    PV(7, pb);
    STAMP(4);
    CS(5);
    if (__builtin_expect(rfl(pf) != 0x01010101u, 0)) wait4p(fb, C_P + 4, pa, 8192, pc);
    CS(6);
#pragma unroll
    for (int i = 0; i < 4; ++i) pb[i] = pc[i];
    const u32 lf = *(volatile LAS u32*)lp<u32>(fo + C_LAND + 15 * 16);
    PVR(8, 13); PVR(9, 14); PVR(10, 15);
    pf = flag_get(C_P + 8);
#pragma unroll
    for (int i = 0; i < 4; ++i) pc[i] = *(volatile LAS bf16x8*)lp<bf16x8>(pa + (12 + i) * 1024);
    PV(11, pb);
    STAMP(5);
    CS(7);
    if (rfl(lf) != 0u && rfl(pf) == 0x01010101u) {
        PV(12, pc); PV(13, pc); PV(14, pc); PV(15, pc);
    } else {
#pragma unroll
        for (int t = 12; t < 16; ++t) {
            if (t > 12) {
                VMWAIT(5 * (15 - t));
#pragma unroll
                for (int cb = 0; cb < 4; ++cb) V[t & 7][cb] = tr2(va[0][cb] + (slot(t) - vbase), va[1][cb] + (slot(t) - vbase));
            }
            pc[t & 3] = wait1p(fb, C_P + 8 + (t & 3), pa, t * 1024);
            PV(t, pc);
        }
    }
    STAMP(6);
    STAMPC(29);
    CS(8);
    v4u d[8];
    publish(ws, g1, o, d);
    v2u fl = *(volatile LAS v2u*)lp<v2u>(fb + C_L);
    f32x4 lq = *(volatile LAS f32x4*)lp<f32x4>(lr);
    u32 mref = 0;
    STAMP(8);
    CS(9);
    u32 cs[8];
#pragma unroll
    for (int k = 0; k < 8; ++k) { cs[k] = (d[k].x + d[k].y) + (d[k].z + d[k].w); PIN(cs[k]); }
    if (__builtin_expect(rfl(fl.x) != 0x01010101u || rfl(fl.y) != 0u, 0)) {
        wait4(fb, C_L);
        if (rfl(flag_get(C_BAD)) != 0u) {
            barrier();
            barrier();
#pragma unroll
            for (int cb = 0; cb < 4; ++cb)
#pragma unroll
                for (int i = 0; i < 16; ++i) o[cb][i] = 0.f;
#pragma clang loop unroll(disable)
            for (u32 t = 0; t < 16; ++t) {
                dma_a(ckv + ktile(sidx, t), od, slot(15) + p * 4096u); dma_b(ckv + ktile(sidx, t), od, slot(15) + p * 4096u, pcf, L_FL + C_LAND);
                VMWAIT(0);
                VREAD(15);
                pc[3] = *lp<bf16x8>(pa + t * 1024);
                PV(15, pc);
                LGKM0();
            }
            publish(ws, g1, o, d);
            asm volatile("s_nop 1");
#pragma unroll
            for (int k = 0; k < 8; ++k) { cs[k] = (d[k].x + d[k].y) + (d[k].z + d[k].w); PIN(cs[k]); }
            mref = *(volatile LAS u32*)lp<u32>(L_M + (lr - L_L) / 4);
        }
        lq = *(volatile LAS f32x4*)lp<f32x4>(lr);
    }
    {
        u32 t0, t1, t2, t3, u0, u1;
        asm volatile("s_nop 1\n\t"
            "v_add_u32_dpp %0, %6, %6 row_ror:8 row_mask:0xf bank_mask:0x3\n\tv_add_u32_dpp %1, %7, %7 row_ror:8 row_mask:0xf bank_mask:0x3\n\t"
            "v_add_u32_dpp %2, %8, %8 row_ror:8 row_mask:0xf bank_mask:0x3\n\tv_add_u32_dpp %3, %9, %9 row_ror:8 row_mask:0xf bank_mask:0x3\n\t"
            "v_add_u32_dpp %0, %10, %10 row_ror:8 row_mask:0xf bank_mask:0xc\n\tv_add_u32_dpp %1, %11, %11 row_ror:8 row_mask:0xf bank_mask:0xc\n\t"
            "v_add_u32_dpp %2, %12, %12 row_ror:8 row_mask:0xf bank_mask:0xc\n\tv_add_u32_dpp %3, %13, %13 row_ror:8 row_mask:0xf bank_mask:0xc\n\t"
            "v_add_u32_dpp %4, %0, %0 row_half_mirror row_mask:0xf bank_mask:0x5\n\tv_add_u32_dpp %5, %1, %1 row_half_mirror row_mask:0xf bank_mask:0x5\n\t"
            "v_add_u32_dpp %4, %2, %2 row_half_mirror row_mask:0xf bank_mask:0xa\n\tv_add_u32_dpp %5, %3, %3 row_half_mirror row_mask:0xf bank_mask:0xa\n\t"
            "s_nop 0\n\t"
            "v_add_u32_dpp %4, %4, %4 quad_perm:[1,0,3,2] row_mask:0xf bank_mask:0xf\n\ts_nop 0\n\tv_add_u32_dpp %5, %5, %5 quad_perm:[1,0,3,2] row_mask:0xf bank_mask:0xf\n\t"
            "v_add_u32_dpp %4, %4, %4 quad_perm:[2,3,0,1] row_mask:0xf bank_mask:0xf\n\ts_nop 0\n\tv_add_u32_dpp %5, %5, %5 quad_perm:[2,3,0,1] row_mask:0xf bank_mask:0xf\n\ts_nop 0"
            : "=&v"(t0), "=&v"(t1), "=&v"(t2), "=&v"(t3), "=&v"(u0), "=&v"(u1)
            : "v"(cs[0]), "v"(cs[1]), "v"(cs[2]), "v"(cs[3]), "v"(cs[4]), "v"(cs[5]), "v"(cs[6]), "v"(cs[7]));
        st_sc(ws, gc, (v4u){(gc & 16u) ? u1 : u0, asu(sum4(lq)), mref, tlo});
    }
    CS(10);
    STAMP(9);
    COUT(11);
    barrier();
}

DI void p_write(const f32x4& s0, const f32x4& s1, u32 pa, float& l0, float& l1) {
    f32x4 a, c;
#pragma unroll
    for (int k = 0; k < 4; ++k) a[k] = ex2(s0[k]);
    *lp<v2u>(pa) = (v2u){pack2(a.x, a.y), pack2(a.z, a.w)};
#pragma unroll
    for (int k = 0; k < 4; ++k) c[k] = ex2(s1[k]);
    *lp<v2u>(pa + 256) = (v2u){pack2(c.x, c.y), pack2(c.z, c.w)};
    l0 = sum4(a); l1 = sum4(c);
}
DI void qk_second(const bf16x8 (&q0)[16], const bf16x8 (&q1)[16], u32 ak, u32 fw, u32 off, f32x4& s0, f32x4& s1, u64& cw) {
    bf16x8 fa[16];
    u32 a1 = ak ^ 16u, a2 = ak ^ 32u, a3 = ak ^ 48u;
    PIN(ak); PIN(a1); PIN(a2); PIN(a3);
    land_wait_k(fw, off, ak, a1, a2, a3, fa[0], fa[1], fa[2], fa[3]);
#pragma unroll
    for (int i = 0; i < 4; ++i) { s0[i] = 0.f; s1[i] = 0.f; }
#pragma unroll
    for (int s = 0; s < 16; ++s) {
        SCHED();
        s0 = __builtin_amdgcn_mfma_f32_16x16x32_bf16(fa[s], q0[s], s0, 0, 0, 0);
        if (s < KD - 4) { SCHED(); fa[2 * s + 4] = *lp<bf16x8>(ak ^ (u32)((2 * s + 4) << 4)); SCHED(); }
        s1 = __builtin_amdgcn_mfma_f32_16x16x32_bf16(fa[s], q1[s], s1, 0, 0, 0);
        SCHED();
        if (s < KD - 4) fa[2 * s + 5] = *lp<bf16x8>(ak ^ (u32)((2 * s + 5) << 4));
        else if (s + KD < 16) fa[s + KD] = *lp<bf16x8>(ak ^ (u32)((s + KD) << 4));
    }
}
DI void stream_qk(const Params& P, int b, u32 w, int lane, u32 t0, int& budget, u32& tl) {
    char* const ws = P.ws;
    const int xc = b & 7, yy = (b >> 3) - 16;
    const u32 i15 = lane & 15, g = lane >> 4;
    u64 e64;
    EPOCH();
    const u32 tlo = (u32)e64, thi = (u32)(e64 >> 32);
    const u32 ak = g * 4096u + i15 * 256u + (swz(i15) << 4);
    const u32 c16 = i15 ^ g ^ ((w & 1) ? 12u : 0u);
    u32 qo = WS_QABS + ((8 * w + g) ^ ((tlo & 1) << 4)) * 256u + c16 * 16u, co = WS_QCHK + w * 2048u + (u32)lane * 16u;
    u32 va = L_IMG + ((i15 & 3) ^ 2) * 8192u + (8 * w + g) * 256u + (((i15 >> 2) ^ ((w & 1) ? 3u : 0u)) << 6) + ((i15 & 3) << 4);
    u32 fa = L_FL + w, fb = L_FL, fw = L_FL + 16 * w + (i15 & 3) * 4, pa = L_P + w * 1024u + (g >> 1) * 512u + i15 * 16u + (g & 1) * 8u;
    u32 bq[4];
#pragma unroll
    for (int k = 0; k < 4; ++k) { bq[k] = L_IMG + i15 * 256u + (((g * 4 + k) ^ (i15 & 3) ^ ((i15 & 8) ? 12u : 0u)) << 4); PIN(bq[k]); }
    PIN(qo); PIN(co); PIN(va); PIN(fa); PIN(fb); PIN(fw); PIN(pa);
    v4u chk0, chk1;
    UNDEF(chk0); UNDEF(chk1);
#define REQ_Q() do { \
        _Pragma("unroll") for (int k = 0; k < 8; ++k) dma_sc(ws + (k & 1) * 1024 + (k >> 1) * 8192, qo, L_IMG + w * 2048u + (k & 1) * 1024u + ((k >> 1) ^ 2) * 8192u); \
        ld_sc_into(chk0, ws, co); ld_sc_into(chk1, ws + 1024, co); } while (0)
    u32 warm;
    UNDEF(warm);
    if (KMAP && w == 2) asm volatile("global_load_dword %0, %1, %2" : "+v"(warm) : "v"(((u32)lane & 15) * 2097152u + 2097148u), "s"(P.ckv) : "memory");
    if (w == 3) asm volatile("global_load_dword %0, %1, %2\n\tglobal_load_dword %0, %1, %3\n\tglobal_load_dword %0, %1, %4\n\tglobal_load_dword %0, %5, %6" : "+v"(warm)
        : "v"(((u32)lane & 1) * (1792u * 1024u - 4u)), "s"(P.wg), "s"(P.wu), "s"(P.wd), "v"(WS_PAY + ((u32)lane & 31) * PHD + 4 * 128 * 256), "s"(ws) : "memory");
    until(t0 + T_OWN);
#pragma unroll
    for (int k = 0; k < 8; ++k) dma_nt(P.wo + k * 512, (u32)(xc * 16 + yy) * 32768u + w * 8192u + (u32)lane * 16u, L_IMG + w * 2048u);
    __builtin_amdgcn_s_setprio(3);
    bf16x8 k0[16], k1[16];
    land_wait(fw, C_LAND);
    VMWAIT(8);
    PIN(warm);
    {
        u32 a = ak + w * 16384u; PIN(a);
#pragma unroll
        for (int s = 0; s < 16; ++s) k0[s] = *lp<bf16x8>(a ^ (u32)(s << 4));
        flag_set(fa, C_KRD);
    }
    const u32 tb = time_base(ws + WS_ST + xc * 128, t0);
    bool asked = false;
#pragma clang loop unroll(disable)
    while (ballot(*(volatile LAS u32*)lp<u32>(fw + C_LAND + 64) == 0u) != 0ull) {
        if ((int)(now32() - (tb + Q_FIRST)) >= 0) {
            REQ_Q();
            flag_set(fa, C_Q);
            land_wait(fw, C_LAND + 64);
            asked = true;
            break;
        }
    }
    asm volatile("" ::: "memory");
    {
        u32 a = ak + (w == 3 ? 0u : (w + 4) * 16384u); PIN(a);
#pragma unroll
        for (int s = 0; s < 16; ++s) k1[s] = *lp<bf16x8>(a ^ (u32)(s << 4));
        flag_set(fa, C_KRD + 4);
    }
#pragma unroll
    for (int s = 0; s < 16; ++s) { PIN(k0[s]); PIN(k1[s]); }
    STAMP(1);
    until(tb + Q_FIRST);
#pragma clang loop unroll(disable)
    do {
        if (!asked) {
            REQ_Q();
            flag_set(fa, C_Q);
        }
        asked = false;
        STAMP(20);
        VMWAIT(0);
        STAMP(4);
        PIN(chk0); PIN(chk1);
        const v4u x0 = *(volatile LAS v4u*)lp<v4u>(va), x1 = *(volatile LAS v4u*)lp<v4u>(va ^ 16u), x2 = *(volatile LAS v4u*)lp<v4u>(va ^ 32u), x3 = *(volatile LAS v4u*)lp<v4u>(va ^ 48u);
        const v4u y0 = *(volatile LAS v4u*)lp<v4u>(va + 1024), y1 = *(volatile LAS v4u*)lp<v4u>((va ^ 16u) + 1024), y2 = *(volatile LAS v4u*)lp<v4u>((va ^ 32u) + 1024), y3 = *(volatile LAS v4u*)lp<v4u>((va ^ 48u) + 1024);
        const v4u xs = (x0 + x1) + (x2 + x3), ys = (y0 + y1) + (y2 + y3);
        const u32 bad = ((((xs.x + xs.y) + (xs.z + xs.w)) ^ chk0.x) | (chk0.z ^ tlo) | (chk0.w ^ thi)) | ((((ys.x + ys.y) + (ys.z + ys.w)) ^ chk1.x) | (chk1.z ^ tlo) | (chk1.w ^ thi));
        if (ballot(bad != 0u) == 0ull) break;
        __builtin_amdgcn_s_sleep(4);
    } while (--budget > 0);
    flag_set(fa, C_Z);
    STAMP(5);
    wait4(fb, C_Z);
    STAMP(6);
    const u32 tz = t0; (void)tz;
    CYC0();
    u64 cs_[12];
    CS(0);
    f32x4 s[8];
#pragma unroll
    for (int i = 0; i < 4; ++i) s[i] = (f32x4){0.f, 0.f, 0.f, 0.f};
    bf16x8 q0[16], q1[16];
#define QREAD(s_) do { q0[s_] = *lp<bf16x8>(bq[(s_) & 3] + (((s_) >> 2) ^ 2) * 8192); q1[s_] = *lp<bf16x8>(bq[(s_) & 3] + (((s_) >> 2) ^ 2) * 8192 + 4096); } while (0)
#pragma unroll
    for (int i = 0; i < QD; ++i) QREAD(i);
#pragma unroll
    for (int i = 0; i < 16; ++i) {
        if (i + QD < 16) QREAD(i + QD);
        if (i + QD == 7) flag_set(fa, C_E);
        SCHED();
        s[0] = __builtin_amdgcn_mfma_f32_16x16x32_bf16(k0[i], q0[i], s[0], 0, 0, 0);
        s[1] = __builtin_amdgcn_mfma_f32_16x16x32_bf16(k0[i], q1[i], s[1], 0, 0, 0);
        s[2] = __builtin_amdgcn_mfma_f32_16x16x32_bf16(k1[i], q0[i], s[2], 0, 0, 0);
        s[3] = __builtin_amdgcn_mfma_f32_16x16x32_bf16(k1[i], q1[i], s[3], 0, 0, 0);
        SCHED();
    }
    STAMP(21);
    STAMPC(29);
    CS(1);
    float l0, l1, l2, l3;
    p_write(s[0], s[1], pa, l0, l1);
    p_write(s[2], s[3], pa + 4096, l2, l3);
    flag_set(fa, C_P);
    STAMP(24);
    CS(2);
    l0 += l2; l1 += l3;
    PIN(l0); PIN(l1);
    qk_second(q0, q1, ak + (w + 1) * 16384u, fw, C_LAND + 128, s[4], s[5], cs_[3]);
    CS(4);
    p_write(s[4], s[5], pa + 8192, l2, l3);
    flag_set(fa, C_P + 4);
    STAMP(26);
    CS(5);
    l0 += l2; l1 += l3;
    float m0 = __builtin_fmaxf(max8(s[0], s[2]), max4(s[4])), m1 = __builtin_fmaxf(max8(s[1], s[3]), max4(s[5]));
    u32 ln = (u32)lane; PIN(ln);
    u32 la = L_L + (ln & 31) * 16 + w * 4, pl = pa + 12288;
    PIN(l0); PIN(l1); PIN(m0); PIN(m1); PIN(la); PIN(pl);
    if (w == 3) wait4(fb, C_P);
    qk_second(q0, q1, ak + (w == 3 ? slot(15) : w == 2 ? 0u : (w + 5) * 16384u), fw, C_LAND + 192, s[6], s[7], cs_[6]);
    CS(7);
    STAMP(27);
    p_write(s[6], s[7], pl, l2, l3);
    flag_set(fa, C_P + 8);
    STAMP(28);
    CS(8);
    {
        float z = red16(l0 + l2, l1 + l3);
        *lp<float>(la) = red32(z, z);
    }
    m0 = __builtin_fmaxf(m0, max4(s[6])); m1 = __builtin_fmaxf(m1, max4(s[7]));
    if (ballot(!(__builtin_fmaxf(__builtin_fabsf(m0), __builtin_fabsf(m1)) <= LAZY)) != 0ull) flag_set(fa, C_BAD);
    flag_set(fa, C_L);
    CS(9);
    COUT(10);
    __builtin_amdgcn_s_setprio(0);
    if (w == 2) {
        const u32 ho = WS_HC + (((u32)b + 17u * ln) & 127u) * HSTR + ln * 16u;
        land_wait(fw, C_LAND + 208);
        if (ln < 8) st_sc(ws, ho, (v4u){now32(), 0u, tlo, thi});
    }
    wait4(fb, C_L);
    if (__builtin_expect(rfl(flag_get(C_BAD)) != 0u, 0)) {
        m0 = max32(max16(m0)); m1 = max32(max16(m1));
        *lp<float>(la + (L_IMG + 4096 - L_L)) = (ln & 16) ? m1 : m0;
        barrier();
        const float m = max4(*lp<f32x4>((la & ~15u) + (L_IMG + 4096 - L_L)));
        *lp<float>(L_M + (ln & 31) * 4) = m;
        u32 e, od;
        both16(asu(m), e, od);
        l0 = 0.f; l1 = 0.f;
#pragma unroll
        for (int i = 0; i < 4; ++i) { p_write(s[2 * i] - asf(e), s[2 * i + 1] - asf(od), pa + 4096 * i, l2, l3); l0 += l2; l1 += l3; }
        const float z = red16(l0, l1);
        *lp<float>(la) = red32(z, z);
        barrier();
    }
    if (yy < 12 || xc < 4) mlp_part(P, b, w, lane, tlo, thi, budget, tl);
}

#define UKFIRST 1
#define SIBDMA dma_pl
#define OWNDMA dma_nt
DI void dma_none(const void* base, u32 off, u32 lds_dst) { asm volatile("s_mov_b32 m0, %1\n\ts_mov_b64 exec, 1\n\tglobal_load_lds_dword %0, %2\n\ts_mov_b64 exec, -1" :: "v"(0u), "s"(lds_dst), "s"(base) : "memory"); }
DI void dma_s0(const void* base, u32 off, u32 lds_dst) { asm volatile("s_mov_b32 m0, %1\n\ts_nop 0\n\tglobal_load_lds_dwordx4 %0, %2 sc0" :: "v"(off), "s"(lds_dst), "s"(base) : "memory"); }
DI void dma_s01(const void* base, u32 off, u32 lds_dst) { asm volatile("s_mov_b32 m0, %1\n\ts_nop 0\n\tglobal_load_lds_dwordx4 %0, %2 sc0 sc1" :: "v"(off), "s"(lds_dst), "s"(base) : "memory"); }
__global__ void __launch_bounds__(512, 2) fused_kernel(Params P) {
    extern __shared__ __attribute__((aligned(16))) char lds_raw[];
    const int tid = threadIdx.x, lane = tid & 63;
    const int wave = __builtin_amdgcn_readfirstlane(tid >> 6);
    const int b = blockIdx.x, xc = b & 7, y = b >> 3;
    char* const ws = P.ws;
    int budget = BUDGET;
    asm volatile("" :: "v"(lds_raw));
    u32 tl = 0;
    u64 pc0;
    asm volatile("s_getpc_b64 %0\n\ts_nop 0" : "=s"(pc0));
    if (lane < 16 && wave < 4) *lp<u32>(L_FL + C_LAND + (u32)lane * 16 + (u32)wave * 4) = 0u;
    if (lane >= 16 && wave == 4) *lp<v4u>(L_FL + (u32)lane * 16) = (v4u){0u, 0u, 0u, 0u};
    if (wave >= 4 && (y & 0x13) == 0x10) {
        asm volatile("s_mov_b32 m0, %1\n\ts_nop 0\n\tglobal_load_lds_dword %0, %2\n\tglobal_load_lds_dword %0, %3"
            :: "v"((u32)lane * 64u + (u32)(wave - 4) * CODE_STR), "s"(L_IMG + (u32)(wave - 4) * 2048u), "s"(pc0), "s"(pc0 + 4096) : "memory");
    }
    asm volatile("s_waitcnt lgkmcnt(0)" :: "s"(P.ws), "s"(P.out) : "memory");
    const u32 t0 = now32();
    const u32 head = (u32)(xc * 4 + (y >> 2)), j = (u32)y & 3, w = (u32)wave;
    const u32 i15 = lane & 15, g = lane >> 4;
    if (y >= 16) {
        if (wave < 4) {
            const char* ckv = (const char*)P.ckv;
            const u32 sidx = (u32)b - 128;
            const u32 p = w;
            u32 od[4];
#pragma unroll
            for (int k = 0; k < 4; ++k) od[k] = (3 * k + g) * 1024u + p * 256u + ((i15 ^ (g << 2) ^ (u32)k) << 4);
            const u64 pcf = pc0 - p * 256u;
#pragma unroll
            for (int h = 0; h < HW; ++h) {
                if (h == 0 && T_OPEN0) until(t0 + T_OPEN0);
                if (h == 2) { __builtin_amdgcn_s_barrier(); until(t0 + T_OPEN1); }
                if (h == 4 && T_OPEN2 > T_OPEN1) until(t0 + T_OPEN2);
                ISSUE(h);
            }
            stream_pv(P, b, w, lane, od, pcf, t0, tl);
        } else {
            __builtin_amdgcn_s_barrier();
            stream_qk(P, b, w - 4, lane, t0, budget, tl);
        }
        STAMP(15);
        STAMP_OUT();
        return;
    }
    u64 e64;
    u32 tb;
    {
        const u32 ks = b & 15, cg = (u32)(b >> 4) * 2 + (w & 1);
        const u32 row0 = ks * 32 + 2 * (u32)(lane >> 2), p = lane & 3;
        v4u wm[4], wr[4], wk[8], xa;
        u32 cc, xs;
#pragma unroll
        for (int k = 0; k < 4; ++k) UNDEF(wm[k]);
#pragma unroll
        for (int k = 0; k < 8; ++k) UNDEF(wk[k]);
        UNDEF(xa); UNDEF(cc); UNDEF(xs);
        if (wave < 2) {
            const u32 o = row0 * 6144u + cg * 64u + p * 16u;
            asm volatile("global_load_dword %0, %3, %4\n\tglobal_load_dwordx4 %1, %5, %6\n\tglobal_load_dword %2, %7, %6" : "+v"(cc), "+v"(xa), "+v"(xs)
                : "v"(row0 * 2), "s"(P.c), "v"((u32)lane * 16), "s"(P.x), "v"(cg * 64u + p * 16u + g * 4u) : "memory");
            ld_nt_into(wm[0], P.wmod, o); ld_nt_into(wm[1], P.wmod + 3072, o); ld_nt_into(wm[2], P.wmod + 512, o); ld_nt_into(wm[3], P.wmod + 3584, o);
        }
        const u32 oq = (j * 128 + w * 16 + g * 4) * 8192u + head * 256u + i15 * 16u;
        if (b < 8 && wave == 2 && lane < 8) st_sc2(ws, WS_ST + (u32)lane * 128u + (u32)b * 8u, (v2u){t0, ~t0});
        __builtin_amdgcn_s_barrier();
#pragma unroll
        for (int k = 0; k < 4; ++k) wr[k] = ld_nt(P.wq + k * 4096, oq);
        STAMP(20);
        u32 warm;
        UNDEF(warm);
        if (wave == 7) asm volatile("global_load_dword %0, %1, %2\n\tglobal_load_dword %0, %1, %3\n\tglobal_load_dword %0, %1, %4\n\tglobal_load_dword %0, %5, %6" : "+v"(warm)
            : "v"(((u32)lane & 1) * ((4u << 20) - 4u)), "s"(P.wuk), "s"(P.wo), "s"(P.wuv), "v"(WS_PAY + head * PHD + 4 * 128 * 256), "s"(ws) : "memory");
        EPOCH();
        const u32 tlo = (u32)e64, thi = (u32)(e64 >> 32);
        if (wave < 2) {
            u32 so = piece(WS_HP, 2048, cg) + (ks * 8 + 2 * p + (u32)(lane >> 5)) * 16u;
            VMWAIT(8);
            PIN(xa); PIN(xs); PIN(cc);
            float mean, rstd;
            ln_stats(xa, mean, rstd);
            const u32 sp = pack2(silu(bflo(cc)), silu(bfhi(cc)));
            u32 s0l = sp & 0xffffu, s0h = sp << 16, s1l = sp >> 16, s1h = sp & 0xffff0000u;
            float xn0 = (bflo(xs) - mean) * rstd, xn1 = (bfhi(xs) - mean) * rstd;
            PIN(xn0); PIN(xn1); PIN(s0l); PIN(s0h); PIN(s1l); PIN(s1h); PIN(so);
            VMWAIT(4);
#pragma unroll
            for (int k = 0; k < 4; ++k) PIN(wm[k]);
            STAMP(22);
            float ta[4], tc[4];
#pragma unroll
            for (int k = 0; k < 2; ++k) {
                ta[2 * k] = red32(dot2(wm[1][k], s1l, dot2(wm[0][k], s0l, 0.f)), dot2(wm[1][k + 2], s1l, dot2(wm[0][k + 2], s0l, 0.f)));
                ta[2 * k + 1] = red32(dot2(wm[1][k], s1h, dot2(wm[0][k], s0h, 0.f)), dot2(wm[1][k + 2], s1h, dot2(wm[0][k + 2], s0h, 0.f)));
                tc[2 * k] = red32(dot2(wm[3][k], s1l, dot2(wm[2][k], s0l, 0.f)), dot2(wm[3][k + 2], s1l, dot2(wm[2][k + 2], s0l, 0.f)));
                tc[2 * k + 1] = red32(dot2(wm[3][k], s1h, dot2(wm[2][k], s0h, 0.f)), dot2(wm[3][k + 2], s1h, dot2(wm[2][k + 2], s0h, 0.f)));
            }
            float u0 = xn0 * (red16(tc[0], tc[2]) + 0.015625f) + red16(ta[0], ta[2]), u1 = xn1 * (red16(tc[1], tc[3]) + 0.015625f) + red16(ta[1], ta[3]);
            u0 += dpp<0x124>(u0); u1 += dpp<0x124>(u1); u0 += dpp<0x128>(u0); u1 += dpp<0x128>(u1);
            u32 e, od;
            both16(pack2(u0, u1), e, od);
            if ((lane & 28) == 0) st_sc(ws, so, (v4u){e, od, tlo, thi});
            STAMP(24);
        }
        u32 so = piece(WS_QP, 2048, head) + (j * 32 + (u32)(lane >> 5) * 16 + i15) * 16u, ho = piece(WS_HP, 2048, j * 4 + (w >> 1)) + (i15 * 8 + (w & 1) * 4 + g) * 16u;
        u32 la = F_QP + w * 512 + lane * 8, ok = ((u32)y * 32 + g * 8) * 8192u + ((u32)xc * 4 + (w & 3)) * 256u + i15 * 16u;
        PIN(so); PIN(ho); PIN(la); PIN(ok);
#define UK_LOADS() do { _Pragma("unroll") for (int k = 0; k < 8; ++k) ld_nt_into(wk[k], P.wuk + k * 4096, ok); } while (0)
#define OWN_LOADS() do { _Pragma("unroll") for (int k = 0; k < 8; ++k) dma_nt(P.wo, (head * 128 + j * 32 + 8 * w + k) * 1024u + (((u32)lane ^ swz((8 * w + k) & 15)) << 4), F_WO + (8 * w + k) * 1024u); } while (0)
        VMWAIT(0);
#pragma unroll
        for (int k = 0; k < 4; ++k) PIN(wr[k]);
        PIN(warm);
        STAMP(1);
        pair_rows(wr[0], wr[1]); pair_rows(wr[2], wr[3]);
#pragma unroll
        for (int k = 0; k < 4; ++k) PIN(wr[k]);
        v4u hg[1];
        UNDEF(hg[0]);
        until(t0 + ST_LOOK);
        tb = time_base(ws + WS_ST + xc * 128, t0);
        until(tb + A_FIRST);
        ld_sc_into(hg[0], ws, ho);
        __builtin_amdgcn_s_barrier();
        if (wave >= 4) {
            UK_LOADS();
            VMWAIT(8);
        } else VMWAIT(0);
        PIN(hg[0]);
        if (ballot(hg[0].z != tlo || hg[0].w != thi) != 0ull) gather<1, 0>(ws, ho, tlo, thi, hg, budget);
        STAMP(3);
        {
            const u32 h01 = pack2(rowsum(bflo(hg[0].x)), rowsum(bfhi(hg[0].x))), h23 = pack2(rowsum(bflo(hg[0].y)), rowsum(bfhi(hg[0].y)));
            float acc[8];
#pragma unroll
            for (int k = 0; k < 4; ++k) { acc[2 * k] = dot2(wr[2][k], h23, dot2(wr[0][k], h01, 0.f)); acc[2 * k + 1] = dot2(wr[3][k], h23, dot2(wr[1][k], h01, 0.f)); }
            float t[4];
#pragma unroll
            for (int k = 0; k < 4; ++k) t[k] = red32(acc[k], acc[k + 4]);
            *lp<f32x2>(la) = (f32x2){red16(t[0], t[2]), red16(t[1], t[3])};
        }
        barrier();
        if (wave == 0) {
            const LAS f32x2* qp = lp<f32x2>(la);
            const f32x2 q2 = ((qp[0] + qp[64]) + (qp[128] + qp[192])) + ((qp[256] + qp[320]) + (qp[384] + qp[448]));
            u32 e, od;
            both16(pack2(q2.x, q2.y), e, od);
            if ((lane & 16) == 0) st_sc(ws, so, (v4u){e, od, tlo, thi});
            STAMP(2);
        }
        if (wave >= 4) {
            const u32 tb1 = now32(), hk = (u32)xc * 4 + (w & 3);
            const u32 rho = 8 * ((hk & 7) >> 1) + 4 * (hk >> 4) + 2 * (hk & 1) + ((hk >> 3) & 1);
            const u32 og = piece(WS_QP, 2048, hk) + (g * 32 + i15) * 16u;
            u32 sq = WS_QABS + j * 8192u + (rho ^ ((tlo & 1) << 4)) * 256u + (u32)(y >> 2) * 64u + g * 16u, sc = WS_QCHK + (rho * 16 + (u32)y) * 16u;
            PIN(sq); PIN(sc);
            v4u qg[2];
            UNDEF(qg[0]); UNDEF(qg[1]);
            until(tb1 + A2_DELAY);
            ld_sc_into(qg[0], ws, og); ld_sc_into(qg[1], ws + 256, og);
            VMWAIT(0);
            PIN(qg[0]); PIN(qg[1]);
            if (__builtin_expect(ballot((((qg[0].z ^ tlo) | (qg[0].w ^ thi)) | ((qg[1].z ^ tlo) | (qg[1].w ^ thi))) != 0u) != 0ull, 0)) gather<2, 256>(ws, og, tlo, thi, qg, budget);
            STAMP(23);
            const float t0_ = red32(bflo(qg[0].x), bflo(qg[1].x)), t1_ = red32(bfhi(qg[0].x), bfhi(qg[1].x));
            const float t2_ = red32(bflo(qg[0].y), bflo(qg[1].y)), t3_ = red32(bfhi(qg[0].y), bfhi(qg[1].y));
            u32 e, od, q0, q1, q2, q3;
            both16(pack2(red16(t0_, t2_), red16(t1_, t3_)), e, od);
            both32(e, q0, q2); both32(od, q1, q3);
            float a[8];
#pragma unroll
            for (int k = 0; k < 8; ++k) { PIN(wk[k]); a[k] = dot2(wk[k][3], q3, dot2(wk[k][2], q2, dot2(wk[k][1], q1, dot2(wk[k][0], q0, 0.f)))); }
            float z[4];
#pragma unroll
            for (int k = 0; k < 4; ++k) {
                const float keep = (lane & 1) ? a[k + 4] : a[k], give = (lane & 1) ? a[k] : a[k + 4];
                float s = keep + dpp<0xB1>(give);
                s += dpp<0x122>(s); s += dpp<0x124>(s); s += dpp<0x128>(s);
                z[k] = s * (0.08838834764831845f * 1.4426950408889634f);
            }
            const u32 y0 = pack2(z[0], z[1]), y1 = pack2(z[2], z[3]);
            const u32 y2 = dppu<0xB1>(y0), y3 = dppu<0xB1>(y1);
            if (i15 == 0) st_sc(ws, sq, (v4u){y0, y1, y2, y3});
            u32 c0, c1;
            both16((y0 + y1) + (y2 + y3), c0, c1);
            both32(c0 + c1, c0, c1);
            if (lane == 0) st_sc(ws, sc, (v4u){c0 + c1, 0u, tlo, thi});
            STAMP(4);
        }
    }
    const u32 tlo = (u32)e64, thi = (u32)(e64 >> 32);
    const bool is_red = (y >> 2) == 1;
    const u32 rid = (u32)xc * 4 + j;
    barrier();
    bf16x8 bo[16], bu[4];
    u32 xo, aa, cw, cr, op, o, oc, wa;
#define TAIL_ADDR() do { \
        xo = piece(WS_X1, 4096, (u32)(lane >> 3)) + (rid * 8 + (u32)(lane & 7)) * 16u; \
        aa = F_A + (i15 & 7) * 272 + g * 16; cw = F_CX + ((w - 2 * j) & 7) * 32 + i15 * 2; cr = F_CX + g * 16; \
        op = piece(WS_OUTP, 16384, 4 * w + g) + (u32)b * 128u + (i15 >> 1) * 16u; \
        o = WS_PAY + head * PHD + (j * 128 + ((w * 16) ^ ((tlo & 1) << 6))) * 256u + (u32)lane * 16u; \
        oc = WS_CHK + ((w * 16 + (i15 & 3) * 4 + g) * 4 + j) * 512u + head * 16u; \
        wa = F_A + w * 272 + i15 * 16 + g * 4; \
        PIN(xo); PIN(aa); PIN(cw); PIN(cr); PIN(op); PIN(o); PIN(oc); PIN(wa); } while (0)
    {
        const u32 kq = (u32)b & 3, cgl = (u32)b >> 2;
        const u32 mrow = kq * 128 + w * 16 + (u32)(lane >> 3);
        u32 oo[4];
#pragma unroll
        for (int k = 0; k < 4; ++k) oo[k] = (head * 128 + 4 * w + k) * 1024u + (((u32)lane ^ (u32)(k << 2) ^ (w & 3)) << 4);
        const u32 q4 = i15 >> 2, p4 = lane & 3;
        const u32 ra = 8 * (g & 1) + q4, rb = ra + 4;
        u32 xa[4], xb[4];
#pragma unroll
        for (int t2 = 0; t2 < 4; ++t2) {
            const u32 c0 = 8 * w + (p4 >> 1) + 2 * t2, x0 = F_WO + (8 * g + q4) * 1024 + 8 * (p4 & 1);
            xa[t2] = x0 + ((c0 ^ swz(ra)) << 4); xb[t2] = x0 + 4096 + ((c0 ^ swz(rb)) << 4);
            PIN(xa[t2]); PIN(xb[t2]);
        }
        until(tb + T_MID);
#pragma unroll
        for (int k = 0; k < 16; ++k) SIBDMA(P.wo + ((j + (k >> 2)) & 3) * 16384, oo[k & 3], F_WO + ((k >> 2) * 32 + 4 * w + (k & 3)) * 1024u);
        STAMP(19);
        u32 cv0 = ld_u16(P.c, mrow * 2), cv1 = ld_u16(P.c + 8, mrow * 2);
        u32 mo = mrow * 6144u + 2048u + cgl * 128u + (u32)(lane & 7) * 16u;
#pragma unroll
        for (int k = 0; k < 4; ++k) {
            const u32 e = w * 16 + k * 4 + g, c = ((e >> 2) & 1) * 64 + (e & 1) * 32 + ((e >> 1) & 1) * 16 + (e >> 3);
            oo[k] = (j * 128 + c) * 8192u + head * 256u + ((i15 ^ swz(e & 15)) << 4);
            PIN(oo[k]);
        }
        PIN(mo);
        until(tb + T_UV);
        v4u mw0 = ld_nt(P.wmod, mo), mw1 = ld_nt(P.wmod + 8 * 3072, mo);
#pragma unroll
        for (int k = 0; k < 4; ++k) dma_nt(P.wuv, oo[k], F_UV + (w * 16 + k * 4) * 256u);
        VMWAIT(6);
        PIN(cv0); PIN(cv1);
        STAMP(14);
        barrier();
#pragma unroll
        for (int t2 = 0; t2 < 4; ++t2) { bo[4 * t2] = tr2(xa[t2], xb[t2]); bo[4 * t2 + 1] = tr2(xa[t2] + 32768, xb[t2] + 32768); }
#pragma unroll
        for (int t2 = 0; t2 < 4; ++t2) {
            xa[t2] += 65536; xb[t2] += 65536; PIN(xa[t2]); PIN(xb[t2]);
            bo[4 * t2 + 2] = tr2(xa[t2], xb[t2]); bo[4 * t2 + 3] = tr2(xa[t2] + 32768, xb[t2] + 32768);
        }
        u32 sp = pack2(silu(bflo(cv0)), silu(bflo(cv1)));
        u32 x0, x1, ma = F_ML + w * 256 + ((lane & 7) * 4 + g) * 8;
        {
            const u32 c0 = 2 * w + (p4 >> 1);
            x0 = F_UV + (8 * g + q4) * 256 + 8 * (p4 & 1) + ((c0 ^ swz(ra)) << 4); x1 = F_UV + (8 * g + q4 + 4) * 256 + 8 * (p4 & 1) + ((c0 ^ swz(rb)) << 4);
        }
        PIN(sp); PIN(x0); PIN(x1); PIN(ma);
        TAIL_ADDR();
#pragma unroll
        for (int k = 0; k < 16; ++k) PIN(bo[k]);
        barrier();
        VMWAIT(4);
        PIN(mw0); PIN(mw1);
        pair_rows(mw0, mw1);
        float t[4], u[2];
#pragma unroll
        for (int k = 0; k < 2; ++k) { t[2 * k] = red32(dot2(mw0[k], sp, 0.f), dot2(mw0[k + 2], sp, 0.f)); t[2 * k + 1] = red32(dot2(mw1[k], sp, 0.f), dot2(mw1[k + 2], sp, 0.f)); }
#pragma unroll
        for (int k = 0; k < 2; ++k) { float z = red16(t[k], t[k + 2]); z += dpp<0x128>(z); u[k] = z; }
        *lp<f32x2>(ma) = (f32x2){u[0], u[1]};
        if (tid == 8) *lp<u32>(F_GO) = 0u;
        VMWAIT(0);
        STAMP(6);
        barrier();
#pragma unroll
        for (int s = 0; s < 4; ++s) bu[s] = tr2(x0 + s * 8192, x1 + s * 8192);
        if (wave == 7) {
            const LAS f32x2* q = lp<f32x2>(F_ML) + lane;
            const f32x2 s = (q[0] + q[64]) + (q[128] + q[192]);
            const float sx = red32(s.x, s.x), sy = red32(s.y, s.y);
            if (lane < 32) st_sc(ws, WS_ML + (cgl & 7) * 8192u + ((u32)lane >> 4) * 4096u + (cgl >> 3) * 1024u + (kq * 16 + ((u32)lane & 15)) * 16u, (v4u){asu(sx), asu(sy), tlo, thi});
        }
#pragma unroll
        for (int k = 0; k < 4; ++k) PIN(bu[k]);
    }
    float ga0 = 0.f, ga1 = 0.f, gb0 = 0.f, gb1 = 0.f, xv0 = 0.f, xv1 = 0.f;
    STAMP(7);
    {
        u32 tdet;
        if (wave == 0) {
            tdet = hint_wait<HC_K>(ws, WS_HC + (u32)b * HSTR + (u32)(lane & 7) * 16u, tlo, budget) | 1u;
            if (lane == 0) *lp<u32>(F_GO) = tdet;
            if (is_red) {
                v4u gm[4];
                const u32 om = WS_ML + (rid >> 1) * 4096u + (g * 16 + (rid & 1) * 8 + (u32)(lane & 7)) * 16u;
                u32 xr = ld_u32(P.x, (rid * 8 + (u32)(lane & 7)) * 4);
                gather<4, 1024>(ws, om, tlo, thi, gm, budget);
                PIN(xr);
                xv0 = bflo(xr); xv1 = bfhi(xr);
                {
                    const float s0 = red16(red32(asf(gm[1].x), asf(gm[2].x)), red32(asf(gm[1].y), asf(gm[2].y)));
                    u32 e, od, sbp, cbp;
                    both16(asu(s0), e, od);
                    both32(pack2(asf(e), asf(od)), sbp, cbp);
                    if (lane < 8) st_sc(ws, WS_SC + (rid * 8 + (u32)lane) * 16u, (v4u){cbp, sbp, tlo, thi});
                }
                ga0 = asf(gm[0].x); ga1 = asf(gm[0].y); gb0 = asf(gm[3].x); gb1 = asf(gm[3].y);
                ga0 = red32(ga0, ga0); ga0 = red16(ga0, ga0); ga1 = red32(ga1, ga1); ga1 = red16(ga1, ga1);
                gb0 = red32(gb0, gb0); gb0 = red16(gb0, gb0); gb1 = red32(gb1, gb1); gb1 = red16(gb1, gb1);
            }
        } else {
#pragma clang loop unroll(disable)
            do { tdet = rfl(*(volatile LAS u32*)lp<u32>(F_GO)); if (tdet) break; __builtin_amdgcn_s_sleep(1); } while (--budget > 0);
        }
        PIN(ga0); PIN(ga1); PIN(gb0); PIN(gb1); PIN(xv0); PIN(xv1);
        v4u v[4], ck = {0u, 0u, 0u, tlo};
        until(tdet + C_DELAY);
        STAMP(8);
#pragma clang loop unroll(disable)
        do {
            asm volatile("global_load_dwordx4 %0, %5, %7 sc1\n\tglobal_load_dwordx4 %1, %5, %7 offset:1024 sc1\n\tglobal_load_dwordx4 %2, %5, %7 offset:2048 sc1\n\tglobal_load_dwordx4 %3, %5, %7 offset:3072 sc1\n\t"
                "s_mov_b64 exec, %8\n\tglobal_load_dwordx4 %4, %6, %7 sc1\n\ts_mov_b64 exec, -1\n\ts_waitcnt vmcnt(0)"
                : "=&v"(v[0]), "=&v"(v[1]), "=&v"(v[2]), "=&v"(v[3]), "+v"(ck) : "v"(o), "v"(oc), "s"(ws), "s"(0x000f000f000f000full) : "memory");
            const v4u xs = (v[0] + v[1]) + (v[2] + v[3]);
            u32 x = ((xs.x + xs.y) + (xs.z + xs.w)) - ck.x;
            x += dppu<0x128>(x); x += dppu<0x124>(x); x += dppu<0x122>(x); x += dppu<0x121>(x);
            if (ballot((x | (ck.w ^ tlo)) != 0u) == 0ull) break;
            __builtin_amdgcn_s_sleep(POLL_SLEEP);
        } while (--budget > 0);
        STAMP(9);
        u32 wl[4] = {0x3f80u, 0x3f80u, 0x3f80u, 0x3f80u};
        float M = 0.f, L = asf(ck.y);
        if (__builtin_expect(ballot(ck.z != 0u) != 0ull, 0)) {
            float m = i15 < 4 ? asf(ck.z) : -3.0e38f;
            M = __builtin_fmaxf(m, dpp<0xB1>(m)); M = __builtin_fmaxf(M, dpp<0x4E>(M));
            M = asf(rfl(asu(max32(max16(M)))));
            const u32 f = pack2(ex2(m - M), 0.f);
            L *= bflo(f);
            wl[0] = dppu<0x150>(f); wl[1] = dppu<0x151>(f); wl[2] = dppu<0x152>(f); wl[3] = dppu<0x153>(f);
        }
        float a[8];
#pragma unroll
        for (int q = 0; q < 4; ++q) {
            a[2 * q] = dot2(v[3][q], wl[3], dot2(v[2][q], wl[2], dot2(v[1][q], wl[1], dot2(v[0][q], wl[0], 0.f))));
            a[2 * q + 1] = dot2(v[3][q], wl[3] << 16, dot2(v[2][q], wl[2] << 16, dot2(v[1][q], wl[1] << 16, dot2(v[0][q], wl[0] << 16, 0.f))));
        }
        float t[4];
#pragma unroll
        for (int k = 0; k < 4; ++k) t[k] = red32(a[k], a[k + 4]);
        *lp<u32>(wa) = pack2(red16(t[0], t[2]), red16(t[1], t[3]));
        L += dpp<0xB1>(L); L += dpp<0x4E>(L);
        L = red32(L, L); L = red16(L, L);
        if (lane == 0) { *lp<float>(F_ST + w * 4) = M; *lp<float>(F_ST + 32 + w * 4) = L; }
    }
    barrier();
    STAMP(10);
    {
        bf16x8 af[4];
#pragma unroll
        for (int s = 0; s < 4; ++s) af[s] = *lp<bf16x8>(aa + s * 64);
        const f32x4 m0 = *lp<f32x4>(F_ST), m1 = *lp<f32x4>(F_ST + 16);
        f32x4 l0 = *lp<f32x4>(F_ST + 32), l1 = *lp<f32x4>(F_ST + 48);
        SCHED();
        f32x4 d = {0.f, 0.f, 0.f, 0.f}, e = {0.f, 0.f, 0.f, 0.f};
        d = __builtin_amdgcn_mfma_f32_16x16x32_bf16(af[0], bu[0], d, 0, 0, 0);
        e = __builtin_amdgcn_mfma_f32_16x16x32_bf16(af[1], bu[1], e, 0, 0, 0);
        d = __builtin_amdgcn_mfma_f32_16x16x32_bf16(af[2], bu[2], d, 0, 0, 0);
        e = __builtin_amdgcn_mfma_f32_16x16x32_bf16(af[3], bu[3], e, 0, 0, 0);
        d += e;
        if (__builtin_expect(((asu(m0.x) | asu(m0.y)) | (asu(m0.z) | asu(m0.w)) | (asu(m1.x) | asu(m1.y)) | (asu(m1.z) | asu(m1.w))) != 0u, 0)) {
            const float M = max8(m0, m1);
            f32x4 f0, f1;
#pragma unroll
            for (int k = 0; k < 4; ++k) { f0[k] = ex2(m0[k] - M); f1[k] = ex2(m1[k] - M); }
            l0 *= f0; l1 *= f1;
            d *= (g & 1) ? f1 : f0;
        }
        const float rl = __builtin_amdgcn_rcpf(sum4(l0) + sum4(l1));
        float c = sum4(d);
        c = red16(c, c) * rl;
        *lp<u16>(cw) = (u16)pack2(c, c);
        if (b == 0 && tid == 64) st_sc2(ws, WS_EPOCH, (v2u){tlo, thi});
    }
    barrier();
    STAMP(13);
    {
        bf16x8 af[4];
#pragma unroll
        for (int s = 0; s < 4; ++s) af[s] = *lp<bf16x8>(cr + s * 64);
        SCHED();
        f32x4 d[4];
#pragma unroll
        for (int t = 0; t < 4; ++t) d[t] = (f32x4){0.f, 0.f, 0.f, 0.f};
#pragma unroll
        for (int s = 0; s < 4; ++s)
#pragma unroll
            for (int t = 0; t < 4; ++t) d[t] = __builtin_amdgcn_mfma_f32_16x16x32_bf16(af[s], bo[4 * t + s], d[t], 0, 0, 0);
        const float r = (g & 2) ? ((g & 1) ? d[3].x : d[2].x) : ((g & 1) ? d[1].x : d[0].x);
        const float n = dpp<0x101>(r);
        if ((lane & 1) == 0) st_sc(ws, op, (v4u){asu(r), asu(n), tlo, thi});
    }
    STAMP(11);
    if (is_red) {
        const f32x2 s = reduce16<8>(ws, piece(WS_OUTP, 16384, rid), tlo, thi, wave, lane, F_RD, budget);
        const float x10 = xv0 + ga0 * s.x, x11 = xv1 + ga1 * s.y;
        if (wave == 0) st_sc(ws, xo, (v4u){asu(x10), asu(x11), tlo, thi});
        STAMP(12);
        until(now32() + F_DELAY);
        const f32x2 s2 = reduce16<6>(ws, piece(WS_YP, 16384, rid), tlo, thi, wave, lane, F_RV, budget);
        if (tid < 8) ((u32*)P.out)[rid * 8 + lane] = pack2(x10 + gb0 * s2.x, x11 + gb1 * s2.y);
    }
    STAMP(15);
    STAMP_OUT();
}

extern "C" void kernel_launch(void* const* d_in, const int* in_sizes, int n_in, void* d_out, int out_size, void* d_ws, size_t ws_size, hipStream_t stream) {
    Params P;
    P.x = (const u16*)d_in[0]; P.ckv = (const u16*)d_in[1]; P.wq = (const u16*)d_in[2]; P.wuk = (const u16*)d_in[3];
    P.wuv = (const u16*)d_in[4]; P.wo = (const u16*)d_in[5]; P.wg = (const u16*)d_in[7]; P.wu = (const u16*)d_in[8];
    P.wd = (const u16*)d_in[9]; P.c = (const u16*)d_in[10]; P.wmod = (const u16*)d_in[11];
    P.out = (u16*)d_out; P.ws = (char*)d_ws + WS_BASE(ws_size);
    (void)hipFuncSetAttribute((const void*)fused_kernel, hipFuncAttributeMaxDynamicSharedMemorySize, LDS_BYTES);
    fused_kernel<<<256, 512, LDS_BYTES, stream>>>(P);
}
```

```cpp
#include <hip/hip_runtime.h>

typedef unsigned u32;
typedef unsigned long long u64;
typedef unsigned short u16;
typedef u32 v4u __attribute__((ext_vector_type(4)));
typedef u32 v2u __attribute__((ext_vector_type(2)));
typedef u32 v8u __attribute__((ext_vector_type(8)));
typedef u32 v16u __attribute__((ext_vector_type(16)));
typedef float f32x2 __attribute__((ext_vector_type(2)));
typedef float f32x4 __attribute__((ext_vector_type(4)));
typedef float f32x16 __attribute__((ext_vector_type(16)));
typedef short bf16x8 __attribute__((ext_vector_type(8)));
typedef short s16x4 __attribute__((ext_vector_type(4)));
typedef __bf16 bf16x2_t __attribute__((ext_vector_type(2)));
#define LAS __attribute__((address_space(3)))
#define DI __device__ __forceinline__

#define CFG(name, val) constexpr int name = val
CFG(BUDGET, 1 << 20);
CFG(POLL_SLEEP, 4);
CFG(HINT_SLEEP, 4);
#define PACE 50, 70, 70, 160, 60, 360, 40, 460, 800, 120, 0, 140, 110, 45, 180, 60, 40, 40, 900, 0
constexpr int pace_[] = {PACE};
CFG(T_OPEN0, pace_[0]);
CFG(T_OPEN1, pace_[1]);
CFG(T_OPEN2, pace_[2]);
CFG(A_FIRST, pace_[3]);
CFG(A2_DELAY, pace_[4]);
CFG(Q_FIRST, pace_[5]);
CFG(T_STEP, pace_[6]);
CFG(T_MID, pace_[7]);
CFG(T_UV, pace_[8]);
CFG(C_DELAY, pace_[9]);
CFG(G_DELAY, pace_[10]);
CFG(F_DELAY, pace_[11]);
CFG(ST_LOOK, pace_[12]);
CFG(SKEW_MAX, pace_[13]);
CFG(Q_DELAY, pace_[14]);
CFG(A_OWN, pace_[15]);
CFG(Q_STEP, pace_[16]);
CFG(FD_GAP, pace_[17]);
CFG(T_MLP, pace_[19]);
CFG(T_PUB, pace_[18]);
#define KD 10
#define KMAP 0
#define KMUL 41
DI u32 ktile(u32 s, u32 t) {
    if (KMAP == 0) return s * 262144u + t * 16384u;
    if (KMAP == 1) return t * 2097152u + s * 16384u;
    const u32 pos = (s + KMUL * t) & 127u;
    if (KMAP == 3) return t * 2097152u + pos * 16384u;
    if (KMAP == 4) return ((t + pos) & 15u) * 2097152u + pos * 16384u;
    if (KMAP == 5) return ((t + (pos >> 3)) & 15u) * 2097152u + pos * 16384u;
    return pos * 262144u + t * 16384u;
}
#define HW 5
CFG(HC_K, 5);
#define QD 2
#define LAZY 48.f
#define CODE_STR 7680
#define WS_BASE(sz) (((sz) - (32u << 20)) & ~(size_t)((2u << 20) - 1))

constexpr u32 WS_EPOCH = 0;
DI u32 piece(u32 base, u32 size, u32 i) { return base + i * size; }
#define CH 262144u
constexpr u32 WS_ST = 1024;
constexpr u32 WS_QH = WS_ST + 256, NQH = 6;
constexpr u32 WS_QCHK = 8192;
constexpr u32 WS_HP = CH;
constexpr u32 WS_QP = 2 * CH;
constexpr u32 WS_QABS = 3 * CH;
constexpr u32 WS_L = 25u << 18;
constexpr u32 WS_ML = WS_L;
constexpr u32 WS_X1 = WS_L + 65536;
constexpr u32 WS_SC = WS_L + 98304;
constexpr u32 HSTR = 1152, WS_HC = WS_L + 106496;
constexpr u32 WS_HE = WS_HC + 512;
constexpr u32 WS_OUTP = WS_L + 262144;
constexpr u32 WS_YP = WS_L + 786432;
constexpr u32 WS_CHK = WS_L + 1310720;
constexpr u32 WS_PAY = 2u << 20;
constexpr u32 PHD = 4 * 128 * 256 + 4096;
constexpr u32 WS_TL = 16u << 20;

constexpr u32 L_FL = 114688, L_P = L_FL + 1024, L_IMG = 131072;
constexpr u32 slot_no(u32 t) { return (u32)((0x9543621054362105ull >> (4 * t)) & 15u); }
constexpr u32 slot(int t) { return slot_no((u32)t) * 16384u; }
constexpr u32 vwin(int t) { return (slot_no((u32)t) < 3 ? 0u : slot_no((u32)t) < 7 ? 3u : 6u) * 16384u; }
static_assert(slot(15) == L_IMG + 16384, "");
constexpr u32 C_LAND = 0;
constexpr u32 C_KRD = 256, C_Z = 264, C_E = 268, C_P = 272, C_Q = 284, C_L = 288, C_BAD = 292, C_H = 296;
constexpr u32 L_M = L_FL + 384, L_L = L_FL + 512;
constexpr u32 L_W = 0;
constexpr u32 L_H2 = 32768;
constexpr u32 L_GU = 36864;
constexpr u32 F_QP = 98304;
constexpr u32 F_WO = 0;
constexpr u32 F_UV = 131072;
constexpr u32 F_A = 0;
constexpr u32 F_ST = 8192;
constexpr u32 F_GO = F_ST + 40;
constexpr u32 F_LS = 9216;
constexpr u32 F_CX = 8448;
constexpr u32 F_ML = 12288;
constexpr u32 F_RD = 20480, F_RV = 22528;
constexpr u32 F_JUNK = 65536;
constexpr u32 LDS_BYTES = 163840;

struct Params {
    const u16 *wmod, *c, *x;
    char* ws;
    const u16 *wq, *ckv, *wuk, *wo, *wuv, *wg, *wu, *wd;
    u16* out;
};

DI float asf(u32 v) { return __builtin_bit_cast(float, v); }
DI u32 asu(float v) { return __builtin_bit_cast(u32, v); }
DI float bflo(u32 v) { return asf(v << 16); }
DI float bfhi(u32 v) { return asf(v & 0xffff0000u); }
DI u32 pack2(float a, float b) { f32x2 v = {a, b}; return __builtin_bit_cast(u32, __builtin_convertvector(v, bf16x2_t)); }
DI float dot2(u32 w, u32 x, float acc) { return __builtin_amdgcn_fdot2_f32_bf16(__builtin_bit_cast(bf16x2_t, w), __builtin_bit_cast(bf16x2_t, x), acc, false); }
DI void pair_rows(v4u& x, v4u& y) {
#pragma unroll
    for (int k = 0; k < 4; ++k) { const u32 a = x[k], b = y[k]; x[k] = __builtin_amdgcn_perm(b, a, 0x05040100u); y[k] = __builtin_amdgcn_perm(b, a, 0x07060302u); }
}
template <int CTRL> DI u32 dppu(u32 v) { return (u32)__builtin_amdgcn_update_dpp(0, (int)v, CTRL, 0xf, 0xf, true); }
template <int CTRL> DI float dpp(float v) { return asf(dppu<CTRL>(asu(v))); }
DI float red32(float a, float b) { auto r = __builtin_amdgcn_permlane32_swap(asu(a), asu(b), false, false); return asf((u32)r[0]) + asf((u32)r[1]); }
DI float red16(float a, float b) { auto r = __builtin_amdgcn_permlane16_swap(asu(a), asu(b), false, false); return asf((u32)r[0]) + asf((u32)r[1]); }
DI float max3(float a, float b, float c) { return __builtin_fmaxf(__builtin_fmaxf(a, b), c); }
DI float max4(f32x4 a) { return max3(max3(a.x, a.y, a.z), a.w, a.x); }
DI float max32(float a) { auto r = __builtin_amdgcn_permlane32_swap(asu(a), asu(a), false, false); return __builtin_fmaxf(asf((u32)r[0]), asf((u32)r[1])); }
DI float max16(float a) { auto r = __builtin_amdgcn_permlane16_swap(asu(a), asu(a), false, false); return __builtin_fmaxf(asf((u32)r[0]), asf((u32)r[1])); }
DI float max8(const f32x4& a, const f32x4& b) { return max3(max3(max3(a.x, a.y, a.z), a.w, b.x), max3(b.y, b.z, b.w), a.x); }
DI float sum4(f32x4 a) { return (a.x + a.y) + (a.z + a.w); }
DI u32 perm_lo(u32 a, u32 b) { return __builtin_amdgcn_perm(b, a, 0x05040100u); }
DI u32 perm_hi(u32 a, u32 b) { return __builtin_amdgcn_perm(b, a, 0x07060302u); }
DI u32 rfl(u32 v) { return (u32)__builtin_amdgcn_readfirstlane((int)v); }
DI void both16(u32 a, u32& e, u32& o) { auto r = __builtin_amdgcn_permlane16_swap(a, a, false, false); e = (u32)r[0]; o = (u32)r[1]; }
DI void both32(u32 a, u32& l, u32& h) { auto r = __builtin_amdgcn_permlane32_swap(a, a, false, false); l = (u32)r[0]; h = (u32)r[1]; }
DI float rowsum(float v) { v += dpp<0x128>(v); v += dpp<0x124>(v); v += dpp<0x122>(v); v += dpp<0x121>(v); return v; }
DI float ex2(float v) { return __builtin_amdgcn_exp2f(v); }
DI u32 neq3(u32 a, u32 b, u32 t) { return __builtin_amdgcn_bitop3_b32(a, b, t, 0x7e); }
DI u64 ballot(bool c) { return __builtin_amdgcn_ballot_w64(c); }

DI v4u ld_nt(const void* base, u32 off) { v4u r; asm volatile("global_load_dwordx4 %0, %1, %2 nt" : "=v"(r) : "v"(off), "s"(base) : "memory"); return r; }
DI u32 ld_u32(const void* base, u32 off) { u32 r; asm volatile("global_load_dword %0, %1, %2" : "=v"(r) : "v"(off), "s"(base) : "memory"); return r; }
DI u32 ld_u32_nt(const void* base, u32 off) { u32 r; asm volatile("global_load_dword %0, %1, %2 nt" : "=v"(r) : "v"(off), "s"(base) : "memory"); return r; }
DI u32 ld_u16(const void* base, u32 off) { u32 r; asm volatile("global_load_ushort %0, %1, %2" : "=v"(r) : "v"(off), "s"(base) : "memory"); return r; }
DI void st_sc(void* base, u32 off, v4u v) { asm volatile("global_store_dwordx4 %0, %1, %2 sc1\n\ts_nop 1" :: "v"(off), "v"(v), "s"(base) : "memory"); }
DI void st_sc_keep(void* base, u32 off, v4u v) { asm volatile("global_store_dwordx4 %0, %1, %2 sc1" :: "v"(off), "v"(v), "s"(base) : "memory"); }
DI void st_sc2(void* base, u32 off, v2u v) { asm volatile("global_store_dwordx2 %0, %1, %2 sc1\n\ts_nop 1" :: "v"(off), "v"(v), "s"(base) : "memory"); }
DI void dma_nt(const void* base, u32 off, u32 lds_dst) { asm volatile("s_mov_b32 m0, %1\n\ts_nop 0\n\tglobal_load_lds_dwordx4 %0, %2 nt" :: "v"(off), "s"(lds_dst), "s"(base) : "memory"); }
DI void dma_pl(const void* base, u32 off, u32 lds_dst) { asm volatile("s_mov_b32 m0, %1\n\ts_nop 0\n\tglobal_load_lds_dwordx4 %0, %2" :: "v"(off), "s"(lds_dst), "s"(base) : "memory"); }
DI void dma_sc(const void* base, u32 off, u32 lds_dst) { asm volatile("s_mov_b32 m0, %1\n\ts_nop 0\n\tglobal_load_lds_dwordx4 %0, %2 sc1" :: "v"(off), "s"(lds_dst), "s"(base) : "memory"); }
DI void ld_nt_into(v4u& r, const void* base, u32 off) { asm volatile("global_load_dwordx4 %0, %1, %2 nt" : "+v"(r) : "v"(off), "s"(base) : "memory"); }
DI void ld_sc_into(v4u& r, const void* base, u32 off) { asm volatile("global_load_dwordx4 %0, %1, %2 sc1" : "+v"(r) : "v"(off), "s"(base) : "memory"); }
#define VMWAIT(n) asm volatile("s_waitcnt vmcnt(%0)" :: "i"(n) : "memory")
#define LGKM0() asm volatile("s_waitcnt lgkmcnt(0)" ::: "memory")
#define PIN(x) asm volatile("" : "+v"(x))
#define UNDEF(x) asm volatile("" : "=v"(x))
#define SCHED() __builtin_amdgcn_sched_barrier(0)
DI void barrier() { LGKM0(); __builtin_amdgcn_s_barrier(); asm volatile("" ::: "memory"); }

template <typename T> DI LAS T* lp(u32 off) { return (LAS T*)(size_t)off; }
DI bf16x8 tr2(u32 a, u32 b) {
    const s16x4 lo = __builtin_amdgcn_ds_read_tr16_b64_v4i16(lp<s16x4>(a)), hi = __builtin_amdgcn_ds_read_tr16_b64_v4i16(lp<s16x4>(b));
    return __builtin_shufflevector(lo, hi, 0, 1, 2, 3, 4, 5, 6, 7);
}

#define STAMP(i) do {} while (0)
#define CYC0() do {} while (0)
#define STAMPC(i) do {} while (0)
#define STAMP_OUT() do {} while (0)

#define CDECL() do {} while (0)
#define CS(i) do {} while (0)
#define COUT(n) do {} while (0)
template <u32 S> DI void pass(const char* ws, u32 off, v4u (&v)[1]) {
    asm volatile("global_load_dwordx4 %0, %1, %2 sc1\n\ts_waitcnt vmcnt(0)" : "=v"(v[0]) : "v"(off), "s"(ws) : "memory");
}
template <u32 S> DI void pass(const char* ws, u32 off, v4u (&v)[2]) {
    asm volatile("global_load_dwordx4 %0, %2, %3 sc1\n\tglobal_load_dwordx4 %1, %2, %4 sc1\n\ts_waitcnt vmcnt(0)" : "=&v"(v[0]), "=&v"(v[1]) : "v"(off), "s"(ws), "s"(ws + S) : "memory");
}
template <u32 S> DI void pass(const char* ws, u32 off, v4u (&v)[4]) {
    asm volatile("global_load_dwordx4 %0, %4, %5 sc1\n\tglobal_load_dwordx4 %1, %4, %5 offset:%6 sc1\n\tglobal_load_dwordx4 %2, %4, %5 offset:%7 sc1\n\tglobal_load_dwordx4 %3, %4, %5 offset:%8 sc1\n\ts_waitcnt vmcnt(0)"
        : "=&v"(v[0]), "=&v"(v[1]), "=&v"(v[2]), "=&v"(v[3]) : "v"(off), "s"(ws), "i"(S), "i"(2 * S), "i"(3 * S) : "memory");
}
template <int N, u32 STRIDE, int SLP = POLL_SLEEP> DI void gather(const char* ws, u32 off, u32 tlo, u32 thi, v4u (&v)[N], int& budget) {
#pragma clang loop unroll(disable)
    do {
        pass<STRIDE>(ws, off, v);
        u32 bad = 0;
#pragma unroll
        for (int k = 0; k < N; ++k) bad |= (v[k].z ^ tlo) | (v[k].w ^ thi);
        if (ballot(bad != 0u) == 0ull) break;
        __builtin_amdgcn_s_sleep(SLP);
    } while (--budget > 0);
}
template <bool G8> DI void poll2(v4u& g0, v4u& g1, u32 o0, const char* b0, u32 o1, const char* b1, u32 tlo, u32 thi) {
    u32 n, t, u;
#define POLL2(A, B, C, D) asm volatile("s_mov_b32 %2, 0x200000\n.Lp%=:\n\tglobal_load_dwordx4 v[232:235], %5, %6 sc1\n\tglobal_load_dwordx4 v[236:239], %7, %8 sc1\n\ts_sub_u32 %2, %2, 1\n\ts_waitcnt vmcnt(0)\n\t" \
        "v_bitop3_b32 %3, " A ", " B ", %9 bitop3:0x7e\n\tv_bitop3_b32 %4, " C ", " D ", %10 bitop3:0x7e\n\tv_or_b32 %3, %3, %4\n\tv_cmp_ne_u32 vcc, 0, %3\n\ts_cbranch_scc1 .Lq%=\n\ts_cbranch_vccnz .Lp%=\n.Lq%=:" \
        : "=&{v[232:235]}"(g0), "=&{v[236:239]}"(g1), "=&s"(n), "=&v"(t), "=&v"(u) : "v"(o0), "s"(b0), "v"(o1), "s"(b1), "s"(tlo), "s"(G8 ? tlo : thi) : "memory", "vcc", "scc")
    if (G8) POLL2("v233", "v235", "v237", "v239"); else POLL2("v234", "v238", "v235", "v239");
}
DI void flag_set(u32 fa, u32 off) {
    asm volatile("s_mov_b64 exec, 1\n\tds_write_b8_d16_hi %0, %0 offset:%1\n\ts_mov_b64 exec, -1" :: "v"(fa), "i"(off) : "memory");
}
DI u32 flag_get(u32 off) { return *(volatile LAS u32*)lp<u32>(L_FL + off); }
DI void wait4(u32 fb, u32 off) {
    u32 v, n;
    asm volatile("s_mov_b32 %1, 0x400000\n.Lw%=:\n\tds_read_b32 %0, %2 offset:%3\n\ts_sub_u32 %1, %1, 1\n\ts_waitcnt lgkmcnt(0)\n\tv_cmp_ne_u32 vcc, 0x1010101, %0\n\ts_cbranch_scc1 .Le%=\n\ts_cbranch_vccnz .Lw%=\n.Le%=:"
        : "=&v"(v), "=&s"(n) : "v"(fb), "i"(off) : "memory", "vcc", "scc");
}
DI void wait1(u32 fb, u32 off) {
    u32 v, n;
    asm volatile("s_mov_b32 %1, 0x400000\n.Lw%=:\n\tds_read_u8 %0, %2 offset:%3\n\ts_sub_u32 %1, %1, 1\n\ts_waitcnt lgkmcnt(0)\n\tv_cmp_ne_u32 vcc, 1, %0\n\ts_cbranch_scc1 .Le%=\n\ts_cbranch_vccnz .Lw%=\n.Le%=:"
        : "=&v"(v), "=&s"(n) : "v"(fb), "i"(off) : "memory", "vcc", "scc");
}
#define SPIN_HEAD "s_mov_b32 %[n], 0x400000\n.Lw%=:\n\t"
#define SPIN_TAIL(cmp) "s_sub_u32 %[n], %[n], 1\n\ts_waitcnt lgkmcnt(0)\n\t" cmp ", %[v]\n\ts_cbranch_scc1 .Le%=\n\ts_cbranch_vccnz .Lw%=\n.Le%=:"
DI void land_wait(u32 fl, u32 off) {
    u32 v, n;
    asm volatile(SPIN_HEAD "ds_read_b32 %[v], %[b] offset:%[o]\n\t" SPIN_TAIL("v_cmp_eq_u32 vcc, 0") : [v] "=&v"(v), [n] "=&s"(n) : [b] "v"(fl), [o] "i"(off) : "memory", "vcc", "scc");
}
DI bf16x8 wait1p(u32 fb, u32 off, u32 pa, u32 poff) {
    u32 v, n;
    bf16x8 p;
    asm volatile(SPIN_HEAD "ds_read_u8 %[v], %[b] offset:%[o]\n\tds_read_b128 %[p], %[a] offset:%[q]\n\t" SPIN_TAIL("v_cmp_ne_u32 vcc, 1")
        : [v] "=&v"(v), [p] "=&v"(p), [n] "=&s"(n) : [b] "v"(fb), [o] "i"(off), [a] "v"(pa), [q] "i"(poff) : "memory", "vcc", "scc");
    return p;
}
DI void wait4p(u32 fb, u32 off, u32 pa, u32 poff, bf16x8 (&p)[4]) {
    u32 v, n;
    asm volatile(SPIN_HEAD "ds_read_b32 %[v], %[b] offset:%[o]\n\tds_read_b128 %[p0], %[a] offset:%[q0]\n\tds_read_b128 %[p1], %[a] offset:%[q1]\n\tds_read_b128 %[p2], %[a] offset:%[q2]\n\tds_read_b128 %[p3], %[a] offset:%[q3]\n\t"
        SPIN_TAIL("v_cmp_ne_u32 vcc, 0x1010101")
        : [v] "=&v"(v), [p0] "=&v"(p[0]), [p1] "=&v"(p[1]), [p2] "=&v"(p[2]), [p3] "=&v"(p[3]), [n] "=&s"(n)
        : [b] "v"(fb), [o] "i"(off), [a] "v"(pa), [q0] "i"(poff), [q1] "i"(poff + 1024), [q2] "i"(poff + 2048), [q3] "i"(poff + 3072) : "memory", "vcc", "scc");
}
template <u32 KO> DI void land_wait_k(u32 fl, u32 off, u32 a0, u32 a1, u32 a2, u32 a3, bf16x8& f0, bf16x8& f1, bf16x8& f2, bf16x8& f3) {
    u32 v, n;
    asm volatile(SPIN_HEAD "ds_read_b32 %[v], %[b] offset:%[o]\n\tds_read_b128 %[p0], %[a0] offset:%[k]\n\tds_read_b128 %[p1], %[a1] offset:%[k]\n\tds_read_b128 %[p2], %[a2] offset:%[k]\n\tds_read_b128 %[p3], %[a3] offset:%[k]\n\t"
        SPIN_TAIL("v_cmp_eq_u32 vcc, 0")
        : [v] "=&v"(v), [p0] "=&v"(f0), [p1] "=&v"(f1), [p2] "=&v"(f2), [p3] "=&v"(f3), [n] "=&s"(n)
        : [b] "v"(fl), [o] "i"(off), [a0] "v"(a0), [a1] "v"(a1), [a2] "v"(a2), [a3] "v"(a3), [k] "i"(KO) : "memory", "vcc", "scc");
}
DI void dma_a(const char* src, const u32 (&od)[4], u32 dst) {
    asm volatile("s_mov_b32 m0, %3\n\ts_nop 0\n\tglobal_load_lds_dwordx4 %0, %2 nt\n\tglobal_load_lds_dwordx4 %1, %2 offset:1024 nt" :: "v"(od[0]), "v"(od[1]), "s"(src), "s"(dst) : "memory");
}
DI void dma_b(const char* src, const u32 (&od)[4], u32 dst, u64 pc, u32 flag) {
    asm volatile("s_mov_b32 m0, %4\n\ts_nop 0\n\tglobal_load_lds_dwordx4 %1, %3 offset:2048 nt\n\tglobal_load_lds_dwordx4 %2, %3 offset:3072 nt\n\t"
        "s_mov_b32 m0, %6\n\ts_mov_b64 exec, 1\n\tglobal_load_lds_dword %0, %5\n\ts_mov_b64 exec, -1"
        :: "v"(od[0]), "v"(od[2]), "v"(od[3]), "s"(src), "s"(dst), "s"(pc), "s"(flag) : "memory");
}
static_assert(C_LAND == 0, "");
static_assert(((L_FL >> 16) & 0xff) == 1, "flag_set writes bits 23:16 of the address");
template <int K, bool PRE = false> DI u32 hint_wait(const char* ws, u32 oh, u32 tlo, int& budget, v4u h0 = v4u()) {
    v4u h[1] = {h0};
    if (!PRE || __builtin_popcount((u32)ballot(h[0].z == tlo) & 0xffu) < K) {
#pragma clang loop unroll(disable)
        do {
            pass<0>(ws, oh, h);
            if (__builtin_popcount((u32)ballot(h[0].z == tlo) & 0xffu) >= K) break;
            __builtin_amdgcn_s_sleep(HINT_SLEEP);
        } while (--budget > 0);
    }
    const u32 now = (u32)__builtin_amdgcn_s_memrealtime();
    u32 age = h[0].z == tlo ? now - h[0].x : 0xffffu;
    age = age > 0xffffu ? 0u : age;
    age = min(age, dppu<0xB1>(age));
    age = min(age, dppu<0x4E>(age));
    age = min(age, dppu<0x124>(age));
    return now - rfl(age);
}
DI u32 now32() { return (u32)__builtin_amdgcn_s_memrealtime(); }
DI void until(u32 t) { while ((int)(now32() - t) < 0) __builtin_amdgcn_s_sleep(1); }
DI u32 time_base(const char* p, u32 t0) {
    v16u h;
    asm volatile("s_load_dwordx16 %0, %1, 0x0\n\ts_waitcnt lgkmcnt(0)" : "=s"(h) : "s"(p) : "memory");
    u32 m = 0, bad = 0;
#pragma unroll
    for (int k = 0; k < 8; ++k) { m = max(m, h[2 * k] - t0 + 150u); bad |= h[2 * k] ^ ~h[2 * k + 1]; }
    return (bad != 0u || m >= 300u) ? t0 + SKEW_MAX : t0 - 150u + m;
}
struct QTimer { u32 t, n; };
DI bool q_due(QTimer& q, const char* ws, u32 tlo) {
    const u32 now = now32();
    if ((int)(now - q.t) < 0) return false;
    if (q.n >= NQH) return true;
    v16u h0, h1;
    asm volatile("s_load_dwordx16 %0, %2, 0x0\n\ts_load_dwordx16 %1, %2, 0x40\n\ts_waitcnt lgkmcnt(0)" : "=&s"(h0), "=&s"(h1) : "s"(ws + WS_QH + q.n * 128u) : "memory");
    u32 m = 0xffffffffu, bad = 0;
#pragma unroll
    for (int k = 0; k < 8; ++k) { m = min(m, min(now + 1000u - h0[2 * k], now + 1000u - h1[2 * k])); bad |= (h0[2 * k] ^ h0[2 * k + 1] ^ tlo) | (h1[2 * k] ^ h1[2 * k + 1] ^ tlo); }
    if (bad == 0u) { q.n = NQH; q.t = now + 1000u - max(m, 900u) + Q_DELAY; } else { ++q.n; q.t = now + Q_STEP; }
    return false;
}
DI u32 swz(u32 row) { return ((row & 3u) << 2) | ((row >> 2) & 3u); }

#define EPOCH() do { asm volatile("s_load_dwordx2 %0, %1, 0x0\n\ts_waitcnt lgkmcnt(0)" : "=s"(e64) : "s"(ws) : "memory"); ++e64; } while (0)
DI void wave_sum2(float s1, float s2, float& t1, float& t2) {
    float z = red32(s1, s2);
    z = red16(z, z); z = rowsum(z);
    u32 a, b;
    both32(asu(z), a, b);
    t1 = asf(a); t2 = asf(b);
}
DI void ln_stats(const v4u& xa, float& mean, float& rstd) {
    const float pil = asf(rfl(xa[0]) << 16);
    float s1 = 0.f, s2 = 0.f;
#pragma unroll
    for (int k = 0; k < 4; ++k) { const float a = bflo(xa[k]) - pil, c = bfhi(xa[k]) - pil; s1 += a; s2 = __builtin_fmaf(a, a, s2); s1 += c; s2 = __builtin_fmaf(c, c, s2); }
    float t1, t2;
    wave_sum2(s1, s2, t1, t2);
    const float dm = t1 * (1.f / 512.f);
    mean = pil + dm;
    rstd = __builtin_amdgcn_rsqf(__builtin_fmaxf(t2 * (1.f / 512.f) - dm * dm, 0.f) + 1e-5f);
}
DI float sigm(float v) { return __builtin_amdgcn_rcpf(1.f + ex2(v * -1.4426950408889634f)); }
DI float silu(float v) { return v * sigm(v); }

template <int NW2, bool S8> DI f32x2 reduce16(const char* ws, u32 base, u32 tlo, u32 thi, int wave, int lane, u32 lds_red, u32 tgo, int& budget) {
    u32 la = lds_red + (u32)(wave * 16 + (S8 ? (lane & 7) + (lane >> 5) * 8 : (lane & 7) * 2 + (lane >> 5))) * 4, ra = lds_red + (u32)lane * 8, go = base + (u32)(wave * 64 + lane) * 16u;
    const bool two = NW2 == 8 || wave < NW2;
    const char* w2 = ws + (two ? 8192 : 0);
    float f2 = two ? 1.f : 0.f;
    PIN(la); PIN(ra); PIN(go);
    asm volatile("" : "+s"(w2), "+v"(f2));
    until(tgo);
    v4u g0, g1;
    poll2<false>(g0, g1, go, ws, go, w2, tlo, thi);
    float z = red32(__builtin_fmaf(asf(g1.x), f2, asf(g0.x)), __builtin_fmaf(asf(g1.y), f2, asf(g0.y)));
    z = red16(z, z); z += dpp<0x128>(z);
    *lp<float>(la) = z;
    barrier();
    f32x2 s = {0.f, 0.f};
    if (wave == 0) {
        s = *lp<f32x2>(ra);
        float t = red32(s.x, s.y);
        t = red16(t, t); t += dpp<0x128>(t);
        u32 a, c;
        both32(asu(t), a, c);
        s = (f32x2){asf(a), asf(c)};
    }
    return s;
}

#define E_DELAY 175
#define HE_K 8
#define MDMA dma_nt
DI u32 mlp_id(int b) { const int xc = b & 7, yy = (b >> 3) - 16; return (u32)(((yy >> 2) * 8 + xc) * 4 + (yy & 3)); }
DI void mlp_part(const Params& P, int b, u32 w, int lane, u32 tlo, u32 thi, int& budget, u32& tl) {
    char* const ws = P.ws;
    const u32 gu_id = mlp_id(b), i15 = lane & 15, g = lane >> 4;
    u32 wo = (128 * w + ((u32)lane >> 1)) * 3584u + gu_id * 32u + ((u32)lane & 1) * 16u, wdo = gu_id * 16384u + (64 * w + (u32)lane) * 4u;
    PIN(wo); PIN(wdo);
    u32 ha = L_H2 + w * 256 + (u32)lane * 4, hr = L_H2 + w * 256 + g * 16 + min(i15 & 3, 2u) * 1088, ga = L_GU + w * 512 + i15 * 16 + g * 4096, gr = L_GU + (u32)lane * 16;
    u32 sa = L_GU + 2048 + (u32)(lane >> 5) * 16 + w * 4, heo = WS_HE + (u32)(b - 128) * HSTR + (u32)(lane & 7) * 16u, px = piece(WS_X1, 4096, (u32)(b & 7));
    PIN(sa); PIN(heo); PIN(px);
    u32 so = piece(WS_YP, 16384, 8 * w + (u32)(lane >> 3)) + (gu_id * 8 + (lane & 7)) * 16, sco = WS_SC + (64 * w + (u32)lane) * 16u;
    u32 ta = L_W + w * 8192u + (8 * g + (i15 >> 2)) * 32 + (lane & 3) * 8, xo = piece(WS_X1, 4096, (u32)(b & 7)) + w * 1024u + (u32)lane * 16u;
    PIN(ha); PIN(hr); PIN(ga); PIN(gr); PIN(so); PIN(sco); PIN(ta); PIN(xo);
    barrier();
    STAMP(10);
    if (G_DELAY) until(now32() + G_DELAY);
    u32 wdr[16];
#pragma unroll
    for (int j = 0; j < 16; ++j) wdr[j] = ld_u32_nt(P.wd + j * 512, wdo);
#pragma unroll
    for (int s = 0; s < 4; ++s) { MDMA(P.wg + s * 32 * 1792, wo, L_W + w * 8192u + s * 1024u); MDMA(P.wu + s * 32 * 1792, wo, L_W + w * 8192u + 4096u + s * 1024u); }
    v4u mg[1];
    UNDEF(mg[0]);
    ld_sc_into(mg[0], ws, sco);
    v4u hh;
    UNDEF(hh);
    ld_sc_into(hh, ws, heo);
    STAMP(2);
    VMWAIT(10);
#pragma unroll
    for (int j = 0; j < 16; j += 2) {
        PIN(wdr[j]); PIN(wdr[j + 1]);
        const u32 a = wdr[j], bb = wdr[j + 1];
        wdr[j] = perm_lo(a, bb); wdr[j + 1] = perm_hi(a, bb);
        PIN(wdr[j]); PIN(wdr[j + 1]);
    }
    VMWAIT(0);
    PIN(mg[0]); PIN(hh);
    STAMP(3);
    bf16x8 bg[4], bp[4];
#pragma unroll
    for (int s = 0; s < 4; ++s) { bg[s] = tr2(ta + s * 1024, ta + s * 1024 + 128); bp[s] = tr2(ta + 4096 + s * 1024, ta + 4096 + s * 1024 + 128); }
    if (__builtin_expect(ballot(((mg[0].z ^ tlo) | (mg[0].w ^ thi)) != 0u) != 0ull, 0)) gather<1, 0>(ws, sco, tlo, thi, mg, budget);
    float cb0 = 1.f + bflo(mg[0].x), cb1 = 1.f + bfhi(mg[0].x);
    bf16x8 af[4];
    *lp<u32>(ha + 1088) = pack2(cb0, cb1);
    *lp<u32>(ha + 2176) = mg[0].y;
    v4u yo = {0u, 0u, tlo, thi};
    PIN(cb0); PIN(cb1); PIN(yo);
#pragma unroll
    for (int s = 0; s < 4; ++s) { PIN(bg[s]); PIN(bp[s]); }
    STAMP(12);
    until(hint_wait<HE_K, true>(ws, heo, tlo, budget, hh) + E_DELAY);
    STAMP(8);
    v4u xg, xp;
    poll2<false>(xg, xp, xo, ws, px, ws, tlo, thi);
    STAMP(13);
    float d0 = asf(xg.x) - asf(xp.x), d1 = asf(xg.y) - asf(xp.x);
    PIN(d0); PIN(d1);
    {
        float e0 = d0 * cb0;
        PIN(e0);
        *lp<u32>(ha) = pack2(e0, d1 * cb1);
    }
    asm volatile("" ::: "memory");
#pragma unroll
    for (int s = 0; s < 4; ++s) af[s] = *lp<bf16x8>(hr + s * 64);
    SCHED();
    float z = red32(d0 + d1, __builtin_fmaf(d0, d0, d1 * d1));
    z = red16(z, z); z = rowsum(z);
    *lp<float>(sa) = z;
    SCHED();
    f32x4 dg = {0.f, 0.f, 0.f, 0.f}, du = {0.f, 0.f, 0.f, 0.f};
#pragma unroll
    for (int s = 0; s < 4; ++s) {
        dg = __builtin_amdgcn_mfma_f32_16x16x32_bf16(af[s], bg[s], dg, 0, 0, 0);
        du = __builtin_amdgcn_mfma_f32_16x16x32_bf16(af[s], bp[s], du, 0, 0, 0);
    }
    *lp<f32x4>(ga) = dg; *lp<f32x4>(ga + 256) = du;
    barrier();
    STAMP(11);
    const f32x4 q1 = *lp<f32x4>(L_GU + 2048), q2 = *lp<f32x4>(L_GU + 2064);
    const f32x4 p0 = *lp<f32x4>(gr), p1 = *lp<f32x4>(gr + 1024);
    const float dm = sum4(q1) * (1.f / 512.f);
    const float rstd = __builtin_amdgcn_rsqf(__builtin_fmaxf(sum4(q2) * (1.f / 512.f) - dm * dm, 0.f) + 1e-5f);
    float gx = p0.x + p1.x, gy = p0.y + p1.y;
    PIN(gx); PIN(gy);
    float s = rstd * (gx - dm * gy) + (p0.z + p1.z);
    s = red32(s, s);
    u32 gs, us;
    both16(asu(s), gs, us);
    const float gsum = asf(gs), usum = asf(us);
    const float act = gsum * usum * __builtin_amdgcn_rcpf(1.f + ex2(gsum * __builtin_fmaf(gsum * gsum, -0.044715f * 1.5957691216057308f * 1.4426950408889634f, -1.5957691216057308f * 1.4426950408889634f)));
    const u32 actp = pack2(act, dpp<0x101>(act));
    asm volatile("s_nop 1\n\t"
        "v_dot2c_f32_bf16_dpp %0, %2, %3 row_newbcast:0 row_mask:0xf bank_mask:0xf\n\tv_dot2c_f32_bf16_dpp %1, %2, %4 row_newbcast:0 row_mask:0xf bank_mask:0xf\n\t"
        "v_dot2c_f32_bf16_dpp %0, %2, %5 row_newbcast:2 row_mask:0xf bank_mask:0xf\n\tv_dot2c_f32_bf16_dpp %1, %2, %6 row_newbcast:2 row_mask:0xf bank_mask:0xf\n\t"
        "v_dot2c_f32_bf16_dpp %0, %2, %7 row_newbcast:4 row_mask:0xf bank_mask:0xf\n\tv_dot2c_f32_bf16_dpp %1, %2, %8 row_newbcast:4 row_mask:0xf bank_mask:0xf\n\t"
        "v_dot2c_f32_bf16_dpp %0, %2, %9 row_newbcast:6 row_mask:0xf bank_mask:0xf\n\tv_dot2c_f32_bf16_dpp %1, %2, %10 row_newbcast:6 row_mask:0xf bank_mask:0xf\n\t"
        "v_dot2c_f32_bf16_dpp %0, %2, %11 row_newbcast:8 row_mask:0xf bank_mask:0xf\n\tv_dot2c_f32_bf16_dpp %1, %2, %12 row_newbcast:8 row_mask:0xf bank_mask:0xf\n\t"
        "v_dot2c_f32_bf16_dpp %0, %2, %13 row_newbcast:10 row_mask:0xf bank_mask:0xf\n\tv_dot2c_f32_bf16_dpp %1, %2, %14 row_newbcast:10 row_mask:0xf bank_mask:0xf\n\t"
        "v_dot2c_f32_bf16_dpp %0, %2, %15 row_newbcast:12 row_mask:0xf bank_mask:0xf\n\tv_dot2c_f32_bf16_dpp %1, %2, %16 row_newbcast:12 row_mask:0xf bank_mask:0xf\n\t"
        "v_dot2c_f32_bf16_dpp %0, %2, %17 row_newbcast:14 row_mask:0xf bank_mask:0xf\n\tv_dot2c_f32_bf16_dpp %1, %2, %18 row_newbcast:14 row_mask:0xf bank_mask:0xf\n\ts_nop 2"
        : "+v"(yo.x), "+v"(yo.y) : "v"(actp), "v"(wdr[0]), "v"(wdr[1]), "v"(wdr[2]), "v"(wdr[3]), "v"(wdr[4]), "v"(wdr[5]), "v"(wdr[6]), "v"(wdr[7]),
          "v"(wdr[8]), "v"(wdr[9]), "v"(wdr[10]), "v"(wdr[11]), "v"(wdr[12]), "v"(wdr[13]), "v"(wdr[14]), "v"(wdr[15]));
    st_sc(ws, so, yo);
    STAMP(14);
}

#define ISSUE(h) do { if ((h) & 1) dma_b(ckv + ktile(sidx, (h) >> 1), od, slot((h) >> 1) + p * 4096u, pcf, L_FL + C_LAND + ((h) >> 1) * 16u + p * 4u); else dma_a(ckv + ktile(sidx, (h) >> 1), od, slot((h) >> 1) + p * 4096u); } while (0)
#define WPOS 10
constexpr int half_of(int u) { return u < WPOS ? u : u < WPOS + 4 ? -1 : u - 4; }
constexpr int ops_in(int u0, int u1) { int n = 0; for (int u = u0; u < u1; ++u) n += 2 + (half_of(u) >= 0 ? half_of(u) & 1 : 0); return n; }
DI void publish(char* ws, u32 g1, const f32x16 (&o)[4], v4u (&d)[8]) {
#pragma unroll
    for (int k = 0; k < 8; k += 2) {
        u32 c[8];
#pragma unroll
        for (int i = 0; i < 4; ++i) { c[i] = pack2(o[0][2 * k + i], o[1][2 * k + i]); c[4 + i] = pack2(o[2][2 * k + i], o[3][2 * k + i]); }
        SCHED();
#pragma unroll
        for (int i = 0; i < 2; ++i) {
            auto r0 = __builtin_amdgcn_permlane16_swap(c[2 * i], c[2 * i + 1], false, false);
            auto r1 = __builtin_amdgcn_permlane16_swap(c[4 + 2 * i], c[5 + 2 * i], false, false);
            d[k + i] = (v4u){(u32)r0[0], (u32)r0[1], (u32)r1[0], (u32)r1[1]};
            st_sc_keep(ws + WS_PAY + (k + i) * PHD, g1, d[k + i]);
        }
        SCHED();
    }
}
DI void stream_pv(const Params& P, int b, u32 p, int lane, const u32 (&od)[4], u64 pcf, u32 t0, u32& tl) {
    const u32 tz = t0; (void)tz;
    char* const ws = P.ws;
    const u32 sidx = (u32)b - 128, i15 = lane & 15, R = lane >> 4;
    const char* ckv = (const char*)P.ckv;
    u32 va[2][4];
    {
        const u32 hf = R >> 1, blk = R & 1, q4 = i15 >> 2, p4 = i15 & 3;
#pragma unroll
        for (int e = 0; e < 2; ++e)
#pragma unroll
            for (int cb = 0; cb < 4; ++cb) {
                const u32 row = 8 * hf + 4 * e + q4;
                va[e][cb] = p * 4096u + row * 256u + (((4 * cb + 2 * blk + (p4 >> 1)) ^ swz(row)) << 4) + 8 * (p4 & 1);
                PIN(va[e][cb]);
            }
    }
    u32 fb = L_FL, pa = L_P + (u32)lane * 16u;
    PIN(fb); PIN(pa);
    bf16x8 V[8][4];
    u32 vbase = 0;
#define VBASE(t) do { if (vwin(t) != vbase) { _Pragma("unroll") for (int i_ = 0; i_ < 8; ++i_) { va[i_ >> 2][i_ & 3] += vwin(t) - vbase; PIN(va[i_ >> 2][i_ & 3]); } vbase = vwin(t); } } while (0)
#define VREAD(t) do { VBASE(t); _Pragma("unroll") for (int cb_ = 0; cb_ < 4; ++cb_) V[(t) & 7][cb_] = tr2(va[0][cb_] + (slot(t) - vwin(t)), va[1][cb_] + (slot(t) - vwin(t))); } while (0)
    u64 e64;
    EPOCH();
    const u32 tlo = (u32)e64;
    u32 g1, gc, lr;
    {
        const u32 k = 2 * (i15 >> 2) + (i15 & 1);
        g1 = (p * 128u + (sidx ^ ((tlo & 1) << 6))) * 256u + R * (8 * PHD) + i15 * 16u;
        gc = WS_CHK + (sidx * 4 + p) * 512u + (8 * R + k) * 16u;
        lr = L_L + (8 * (k >> 1) + 4 * (R >> 1) + 2 * (k & 1) + (R & 1)) * 16u;
        PIN(g1); PIN(gc); PIN(lr);
    }
    bool held = false;
    u32 tpub = 0;
    u32 wpf = ((sidx & 7) * 16 + (sidx >> 3)) * 32768u + p * 8192u + (u32)lane * 16u;
    PIN(wpf);
#pragma unroll
    for (int u = HW; u < 36; ++u) {
        const int h = half_of(u), l = half_of(u - HW);
        if (h >= 14 && h < 30 && !(h & 1)) wait1(fb, C_KRD + h / 2 - 7);
        if (u >= 12 && h < 30 && !held && rfl(flag_get(C_H)) != 0u) { wait4(fb, C_Q); held = true; }
        if (h == 30) wait4(fb, C_E);
        VMWAIT(ops_in(u - HW + 1, u));
        if (l == 2) tpub = time_base(ws + WS_ST, t0) + T_PUB;
        if (l == 1) STAMP(18);
        if (l == 7) STAMP(19);
        if (l == 15) STAMP(1);
        if (h == 24) STAMP(2);
        if (h >= 0) ISSUE(h);
        else { dma_nt(P.wo + (u - WPOS) * 1024, wpf, L_P + 14 * 1024); dma_nt(P.wo + (u - WPOS) * 1024 + 512, wpf, L_P + 14 * 1024); }
        if (l >= 0 && (l & 1) && l < 16) VREAD(l >> 1);
    }
    static_assert(WPOS >= HW && WPOS <= 24 && HW >= 3 && HW <= 6, "tile 12 must have landed here");
    f32x16 o[4];
#pragma unroll
    for (int cb = 0; cb < 4; ++cb)
#pragma unroll
        for (int i = 0; i < 16; ++i) o[cb][i] = 0.f;
    bf16x8 pb[4], pc[4];
#define PV(t, pp) do { SCHED(); _Pragma("unroll") for (int cb_ = 0; cb_ < 4; ++cb_) o[cb_] = __builtin_amdgcn_mfma_f32_32x32x16_bf16(pp[(t) & 3], V[(t) & 7][cb_], o[cb_], 0, 0, 0); SCHED(); } while (0)
#define PVR(t, pp, u) do { VBASE(u); _Pragma("unroll") for (int cb_ = 0; cb_ < 4; ++cb_) { SCHED(); \
        o[cb_] = __builtin_amdgcn_mfma_f32_32x32x16_bf16(pp[(t) & 3], V[(t) & 7][cb_], o[cb_], 0, 0, 0); SCHED(); \
        V[(u) & 7][cb_] = tr2(va[0][cb_] + (slot(u) - vwin(u)), va[1][cb_] + (slot(u) - vwin(u))); } SCHED(); } while (0)
    STAMP(3);
    wait4p(fb, C_P, pa, 0, pb);
    STAMP(7);
    CYC0();
    CDECL();
    CS(0);
#pragma unroll
    for (int t = 0; t < 5; ++t) { PVR(t, pb, t + 8); if (t < 4) pb[t] = *lp<bf16x8>(pa + (t + 4) * 1024); if (t == 0) CS(1); if (t == 1) CS(2); if (t == 3) CS(3); }
    CS(4);
    PV(5, pb); PV(6, pb);
    u32 pf = flag_get(C_P + 4);
#pragma unroll
    for (int i = 0; i < 4; ++i) pc[i] = *(volatile LAS bf16x8*)lp<bf16x8>(pa + (8 + i) * 1024);
    PV(7, pb);
    STAMP(4);
    CS(5);
    if (__builtin_expect(rfl(pf) != 0x01010101u, 0)) wait4p(fb, C_P + 4, pa, 8192, pc);
    CS(6);
    PV(8, pc);
    VMWAIT(5);
    PVR(9, pc, 13); PVR(10, pc, 14);
    pf = flag_get(C_P + 8);
#pragma unroll
    for (int i = 0; i < 3; ++i) pb[i] = *(volatile LAS bf16x8*)lp<bf16x8>(pa + (12 + i) * 1024);
    PV(11, pc);
    STAMP(5);
    CS(7);
    pf = rfl(pf);
    if (__builtin_expect(!(pf & 0x1u), 0)) pb[0] = wait1p(fb, C_P + 8, pa, 12 * 1024);
    PV(12, pb);
    if (__builtin_expect(!(pf & 0x100u), 0)) pb[1] = wait1p(fb, C_P + 9, pa, 13 * 1024);
    VBASE(15);
    PV(13, pb);
    if (__builtin_expect(!(pf & 0x10000u), 0)) pb[2] = wait1p(fb, C_P + 10, pa, 14 * 1024);
    VMWAIT(0);
    PVR(14, pb, 15);
    pb[3] = wait1p(fb, C_P + 11, pa, 15 * 1024);
    PV(15, pb);
    STAMP(6);
    STAMPC(29);
    CS(8);
    v4u d[8];
    until(tpub);
    publish(ws, g1, o, d);
    v2u fl = *(volatile LAS v2u*)lp<v2u>(fb + C_L);
    f32x4 lq = *(volatile LAS f32x4*)lp<f32x4>(lr);
    u32 mref = 0;
    STAMP(8);
    CS(9);
    u32 cs[8];
#pragma unroll
    for (int k = 0; k < 8; ++k) { cs[k] = (d[k].x + d[k].y) + (d[k].z + d[k].w); PIN(cs[k]); }
    if (__builtin_expect(rfl(fl.x) != 0x01010101u || rfl(fl.y) != 0u, 0)) {
        wait4(fb, C_L);
        if (rfl(flag_get(C_BAD)) != 0u) {
            barrier();
            barrier();
#pragma unroll
            for (int cb = 0; cb < 4; ++cb)
#pragma unroll
                for (int i = 0; i < 16; ++i) o[cb][i] = 0.f;
#pragma clang loop unroll(disable)
            for (u32 t = 0; t < 16; ++t) {
                dma_a(ckv + ktile(sidx, t), od, slot(15) + p * 4096u); dma_b(ckv + ktile(sidx, t), od, slot(15) + p * 4096u, pcf, L_FL + C_LAND);
                VMWAIT(0);
                VREAD(15);
                pc[3] = *lp<bf16x8>(pa + t * 1024);
                PV(15, pc);
                LGKM0();
            }
            publish(ws, g1, o, d);
            asm volatile("s_nop 1");
#pragma unroll
            for (int k = 0; k < 8; ++k) { cs[k] = (d[k].x + d[k].y) + (d[k].z + d[k].w); PIN(cs[k]); }
            mref = *(volatile LAS u32*)lp<u32>(L_M + (lr - L_L) / 4);
        }
        lq = *(volatile LAS f32x4*)lp<f32x4>(lr);
    }
    {
        u32 t0, t1, t2, t3, u0, u1;
        asm volatile("s_nop 1\n\t"
            "v_add_u32_dpp %0, %6, %6 row_ror:8 row_mask:0xf bank_mask:0x3\n\tv_add_u32_dpp %1, %7, %7 row_ror:8 row_mask:0xf bank_mask:0x3\n\t"
            "v_add_u32_dpp %2, %8, %8 row_ror:8 row_mask:0xf bank_mask:0x3\n\tv_add_u32_dpp %3, %9, %9 row_ror:8 row_mask:0xf bank_mask:0x3\n\t"
            "v_add_u32_dpp %0, %10, %10 row_ror:8 row_mask:0xf bank_mask:0xc\n\tv_add_u32_dpp %1, %11, %11 row_ror:8 row_mask:0xf bank_mask:0xc\n\t"
            "v_add_u32_dpp %2, %12, %12 row_ror:8 row_mask:0xf bank_mask:0xc\n\tv_add_u32_dpp %3, %13, %13 row_ror:8 row_mask:0xf bank_mask:0xc\n\t"
            "v_add_u32_dpp %4, %0, %0 row_half_mirror row_mask:0xf bank_mask:0x5\n\tv_add_u32_dpp %5, %1, %1 row_half_mirror row_mask:0xf bank_mask:0x5\n\t"
            "v_add_u32_dpp %4, %2, %2 row_half_mirror row_mask:0xf bank_mask:0xa\n\tv_add_u32_dpp %5, %3, %3 row_half_mirror row_mask:0xf bank_mask:0xa\n\t"
            "s_nop 0\n\t"
            "v_add_u32_dpp %4, %4, %4 quad_perm:[1,0,3,2] row_mask:0xf bank_mask:0xf\n\ts_nop 0\n\tv_add_u32_dpp %5, %5, %5 quad_perm:[1,0,3,2] row_mask:0xf bank_mask:0xf\n\t"
            "v_add_u32_dpp %4, %4, %4 quad_perm:[2,3,0,1] row_mask:0xf bank_mask:0xf\n\ts_nop 0\n\tv_add_u32_dpp %5, %5, %5 quad_perm:[2,3,0,1] row_mask:0xf bank_mask:0xf\n\ts_nop 0"
            : "=&v"(t0), "=&v"(t1), "=&v"(t2), "=&v"(t3), "=&v"(u0), "=&v"(u1)
            : "v"(cs[0]), "v"(cs[1]), "v"(cs[2]), "v"(cs[3]), "v"(cs[4]), "v"(cs[5]), "v"(cs[6]), "v"(cs[7]));
        st_sc(ws, gc, (v4u){(gc & 16u) ? u1 : u0, asu(sum4(lq)), mref, tlo});
    }
    CS(10);
    STAMP(9);
    COUT(11);
    barrier();
}

DI void p_write(const f32x4& s0, const f32x4& s1, u32 pa, f32x4& a, f32x4& c) {
#pragma unroll
    for (int k = 0; k < 4; ++k) a[k] = ex2(s0[k]);
#pragma unroll
    for (int k = 0; k < 4; ++k) c[k] = ex2(s1[k]);
    *lp<v2u>(pa) = (v2u){pack2(a.x, a.y), pack2(a.z, a.w)};
    *lp<v2u>(pa + 256) = (v2u){pack2(c.x, c.y), pack2(c.z, c.w)};
}
template <u32 KO> DI void qk_second(const bf16x8 (&q0)[16], const bf16x8 (&q1)[16], const u32 (&a)[16], u32 fw, u32 off, f32x4& s0, f32x4& s1, u64& cw) {
    bf16x8 fa[16];
    land_wait_k<KO>(fw, off, a[0], a[1], a[2], a[3], fa[0], fa[1], fa[2], fa[3]);
#pragma unroll
    for (int i = 0; i < 4; ++i) { s0[i] = 0.f; s1[i] = 0.f; }
#pragma unroll
    for (int s = 0; s < 16; ++s) {
        SCHED();
        s0 = __builtin_amdgcn_mfma_f32_16x16x32_bf16(fa[s], q0[s], s0, 0, 0, 0);
        if (s < KD - 4) { SCHED(); fa[2 * s + 4] = *lp<bf16x8>(a[2 * s + 4] + KO); SCHED(); }
        s1 = __builtin_amdgcn_mfma_f32_16x16x32_bf16(fa[s], q1[s], s1, 0, 0, 0);
        SCHED();
        if (s < KD - 4) fa[2 * s + 5] = *lp<bf16x8>(a[2 * s + 5] + KO);
        else if (s + KD < 16) fa[s + KD] = *lp<bf16x8>(a[s + KD] + KO);
    }
}
DI void stream_qk(const Params& P, int b, u32 w, int lane, u32 t0, int& budget, u32& tl) {
    char* const ws = P.ws;
    const int xc = b & 7, yy = (b >> 3) - 16;
    const u32 i15 = lane & 15, g = lane >> 4;
    u64 e64;
    EPOCH();
    const u32 tlo = (u32)e64, thi = (u32)(e64 >> 32);
    const u32 ak = g * 4096u + i15 * 256u + (swz(i15) << 4);
    const u32 c16 = i15 ^ g ^ ((w & 1) ? 12u : 0u);
    u32 qo = WS_QABS + (w ^ ((tlo & 1) << 1)) * 2048u + g * 256u + c16 * 16u, co = WS_QCHK + w * 2048u + (u32)lane * 16u;
    u32 va = L_IMG + ((i15 & 3) ^ 2) * 8192u + (8 * w + g) * 256u + (((i15 >> 2) ^ ((w & 1) ? 3u : 0u)) << 6) + ((i15 & 3) << 4);
    u32 fa = L_FL + w, fb = L_FL, fw = L_FL + 16 * w + (i15 & 3) * 4, pa = L_P + w * 1024u + (g >> 1) * 512u + i15 * 16u + (g & 1) * 8u;
    u32 bq[4];
#pragma unroll
    for (int k = 0; k < 4; ++k) { bq[k] = L_IMG + i15 * 256u + (((g * 4 + k) ^ (i15 & 3) ^ ((i15 & 8) ? 12u : 0u)) << 4); PIN(bq[k]); }
    PIN(qo); PIN(co); PIN(va); PIN(fa); PIN(fb); PIN(fw); PIN(pa);
    u32 pa4 = pa + 4096;
    PIN(pa4);
    v4u chk0, chk1;
    UNDEF(chk0); UNDEF(chk1);
#define REQ_Q() do { \
        _Pragma("unroll") for (int k = 0; k < 8; ++k) dma_sc(ws + (k & 1) * 1024 + (k >> 1) * 8192, qo, L_IMG + w * 2048u + (k & 1) * 1024u + ((k >> 1) ^ 2) * 8192u); \
        ld_sc_into(chk0, ws, co); ld_sc_into(chk1, ws + 1024, co); } while (0)
    u32 warm;
    UNDEF(warm);
    if (KMAP && w == 2) asm volatile("global_load_dword %0, %1, %2" : "+v"(warm) : "v"(((u32)lane & 15) * 2097152u + 2097148u), "s"(P.ckv) : "memory");
    if (w == 3) asm volatile("global_load_dword %0, %1, %2\n\tglobal_load_dword %0, %1, %3\n\tglobal_load_dword %0, %1, %4\n\tglobal_load_dword %0, %5, %6" : "+v"(warm)
        : "v"(((u32)lane & 1) * (1792u * 1024u - 4u)), "s"(P.wg), "s"(P.wu), "s"(P.wd), "v"(WS_PAY + (((u32)lane & 3) * 10 + 1) * PHD + 4 * 128 * 256), "s"(ws) : "memory");
    __builtin_amdgcn_s_setprio(3);
    bf16x8 k0[16], k1[16];
    land_wait(fw, C_LAND);
    VMWAIT(0);
    PIN(warm);
    {
        u32 a = ak + slot_no(w) * 16384u; PIN(a);
#pragma unroll
        for (int s = 0; s < 16; ++s) k0[s] = *lp<bf16x8>(a ^ (u32)(s << 4));
        flag_set(fa, C_KRD);
    }
    QTimer qt = {time_base(ws + WS_ST, t0) + Q_FIRST, 0u};
    u32 ka[16];
    {
        u32 a = ak + slot_no(w + 8) * 16384u; PIN(a);
#pragma unroll
        for (int s = 0; s < 16; ++s) { ka[s] = a ^ (u32)(s << 4); PIN(ka[s]); }
    }
    bool asked = false;
#pragma clang loop unroll(disable)
    while (ballot(*(volatile LAS u32*)lp<u32>(fw + C_LAND + 64) == 0u) != 0ull) {
        if (q_due(qt, ws, tlo)) {
            flag_set(fa, C_H);
            REQ_Q();
            flag_set(fa, C_Q);
            land_wait(fw, C_LAND + 64);
            asked = true;
            break;
        }
    }
    asm volatile("" ::: "memory");
    {
        u32 a = ak + slot_no(w + 4) * 16384u; PIN(a);
#pragma unroll
        for (int s = 0; s < 16; ++s) k1[s] = *lp<bf16x8>(a ^ (u32)(s << 4));
        flag_set(fa, C_KRD + 4);
    }
#pragma unroll
    for (int s = 0; s < 16; ++s) { PIN(k0[s]); PIN(k1[s]); }
    STAMP(1);
    if (!asked) {
#pragma clang loop unroll(disable)
        while (!q_due(qt, ws, tlo)) __builtin_amdgcn_s_sleep(1);
        flag_set(fa, C_H);
    }
    STAMP(22);
#pragma clang loop unroll(disable)
    do {
        if (!asked) {
            REQ_Q();
            flag_set(fa, C_Q);
        }
        asked = false;
        STAMP(20);
        VMWAIT(0);
        STAMP(4);
        PIN(chk0); PIN(chk1);
        const v4u x0 = *(volatile LAS v4u*)lp<v4u>(va), x1 = *(volatile LAS v4u*)lp<v4u>(va ^ 16u), x2 = *(volatile LAS v4u*)lp<v4u>(va ^ 32u), x3 = *(volatile LAS v4u*)lp<v4u>(va ^ 48u);
        const v4u y0 = *(volatile LAS v4u*)lp<v4u>(va + 1024), y1 = *(volatile LAS v4u*)lp<v4u>((va ^ 16u) + 1024), y2 = *(volatile LAS v4u*)lp<v4u>((va ^ 32u) + 1024), y3 = *(volatile LAS v4u*)lp<v4u>((va ^ 48u) + 1024);
        const v4u xs = (x0 + x1) + (x2 + x3), ys = (y0 + y1) + (y2 + y3);
        const u32 bad = ((((xs.x + xs.y) + (xs.z + xs.w)) ^ chk0.x) | (chk0.z ^ tlo) | (chk0.w ^ thi)) | ((((ys.x + ys.y) + (ys.z + ys.w)) ^ chk1.x) | (chk1.z ^ tlo) | (chk1.w ^ thi));
        if (ballot(bad != 0u) == 0ull) break;
        __builtin_amdgcn_s_sleep(4);
    } while (--budget > 0);
    flag_set(fa, C_Z);
    STAMP(5);
    wait4(fb, C_Z);
    STAMP(6);
    const u32 tz = t0; (void)tz;
    CYC0();
    u64 cs_[12];
    CS(0);
    f32x4 s[8];
#pragma unroll
    for (int i = 0; i < 4; ++i) s[i] = (f32x4){0.f, 0.f, 0.f, 0.f};
    bf16x8 q0[16], q1[16];
#define QREAD(s_) do { q0[s_] = *lp<bf16x8>(bq[(s_) & 3] + (((s_) >> 2) ^ 2) * 8192); q1[s_] = *lp<bf16x8>(bq[(s_) & 3] + (((s_) >> 2) ^ 2) * 8192 + 4096); } while (0)
#pragma unroll
    for (int i = 0; i < QD; ++i) QREAD(i);
#pragma unroll
    for (int i = 0; i < 16; ++i) {
        if (i + QD < 16) QREAD(i + QD);
        if (i + QD == 7) flag_set(fa, C_E);
        SCHED();
        s[0] = __builtin_amdgcn_mfma_f32_16x16x32_bf16(k0[i], q0[i], s[0], 0, 0, 0);
        s[1] = __builtin_amdgcn_mfma_f32_16x16x32_bf16(k0[i], q1[i], s[1], 0, 0, 0);
        s[2] = __builtin_amdgcn_mfma_f32_16x16x32_bf16(k1[i], q0[i], s[2], 0, 0, 0);
        s[3] = __builtin_amdgcn_mfma_f32_16x16x32_bf16(k1[i], q1[i], s[3], 0, 0, 0);
        SCHED();
    }
    STAMP(21);
    STAMPC(29);
    CS(1);
    float l0, l1;
    f32x4 e0, e1, e2, e3;
    p_write(s[0], s[1], pa, e0, e1);
    p_write(s[2], s[3], pa4, e2, e3);
    flag_set(fa, C_P);
    SCHED();
    STAMP(24);
    CS(2);
    l0 = sum4(e0 + e2); l1 = sum4(e1 + e3);
    pa4 = pa + 8192;
    PIN(l0); PIN(l1); PIN(pa4);
    qk_second<0>(q0, q1, ka, fw, C_LAND + 128, s[4], s[5], cs_[3]);
    CS(4);
    p_write(s[4], s[5], pa4, e0, e1);
    flag_set(fa, C_P + 4);
    SCHED();
    STAMP(26);
    CS(5);
    l0 += sum4(e0); l1 += sum4(e1);
    float m0 = __builtin_fmaxf(max8(s[0], s[2]), max4(s[4])), m1 = __builtin_fmaxf(max8(s[1], s[3]), max4(s[5]));
    u32 ln = (u32)lane; PIN(ln);
    u32 la = L_L + (ln & 31) * 16 + w * 4, pl = pa + 12288;
    PIN(l0); PIN(l1); PIN(m0); PIN(m1); PIN(la); PIN(pl);
    if (w == 3) wait4(fb, C_P);
    qk_second<49152>(q0, q1, ka, fw, C_LAND + 192, s[6], s[7], cs_[6]);
    CS(7);
    STAMP(27);
    p_write(s[6], s[7], pl, e0, e1);
    flag_set(fa, C_P + 8);
    SCHED();
    STAMP(28);
    CS(8);
    {
        float z = red16(l0 + sum4(e0), l1 + sum4(e1));
        *lp<float>(la) = red32(z, z);
    }
    m0 = __builtin_fmaxf(m0, max4(s[6])); m1 = __builtin_fmaxf(m1, max4(s[7]));
    if (ballot(!(__builtin_fmaxf(__builtin_fabsf(m0), __builtin_fabsf(m1)) <= LAZY)) != 0ull) flag_set(fa, C_BAD);
    flag_set(fa, C_L);
    CS(9);
    COUT(10);
    __builtin_amdgcn_s_setprio(0);
    if (w == 2) {
        const u32 ho = WS_HC + (((u32)b + 17u * ln) & 127u) * HSTR + ln * 16u;
        land_wait(fw, C_LAND + 208);
        if (ln < 8) st_sc(ws, ho, (v4u){now32(), 0u, tlo, thi});
    }
    wait4(fb, C_L);
    if (__builtin_expect(rfl(flag_get(C_BAD)) != 0u, 0)) {
        m0 = max32(max16(m0)); m1 = max32(max16(m1));
        *lp<float>(la + (L_IMG + 4096 - L_L)) = (ln & 16) ? m1 : m0;
        barrier();
        const float m = max4(*lp<f32x4>((la & ~15u) + (L_IMG + 4096 - L_L)));
        *lp<float>(L_M + (ln & 31) * 4) = m;
        u32 e, od;
        both16(asu(m), e, od);
        l0 = 0.f; l1 = 0.f;
#pragma unroll
        for (int i = 0; i < 4; ++i) { p_write(s[2 * i] - asf(e), s[2 * i + 1] - asf(od), pa + 4096 * i, e0, e1); l0 += sum4(e0); l1 += sum4(e1); }
        const float z = red16(l0, l1);
        *lp<float>(la) = red32(z, z);
        barrier();
    }
    if (yy < 12 || xc < 4) mlp_part(P, b, w, lane, tlo, thi, budget, tl);
}

#define C_PUB 75
#define D_DELAY 95
#define PROMOTE 1
#define WQL 1
#define GDONE 0
#define CODE_Y 0x13
#define CODE_POL ""
#define WODMA dma_pl
__global__ void __launch_bounds__(512, 2) fused_kernel(Params) {
    v16u ka;
    v8u kb;
    v2u kc;
    asm volatile("s_load_dwordx16 %0, %3, 0x0\n\ts_load_dwordx8 %1, %3, 0x40\n\ts_load_dwordx2 %2, %3, 0x60" : "=&s"(ka), "=&s"(kb), "=&s"(kc) : "s"(__builtin_amdgcn_kernarg_segment_ptr()));
    SCHED();
    const int tid = threadIdx.x, lane = tid & 63;
    const int wave = __builtin_amdgcn_readfirstlane(tid >> 6);
    const int b = blockIdx.x, xc = b & 7, y = b >> 3;
    int budget = BUDGET;
    u32 tl = 0;
    u64 pc0;
    asm volatile("s_getpc_b64 %0\n\ts_nop 0" : "=s"(pc0));
    *lp<u32>(L_FL + (u32)(wave >> 2) * 1024 + (u32)(wave & 3) * 4 + (u32)lane * 16) = 0u;
    if (wave >= 4 && (y & CODE_Y) == 0x10) {
        asm volatile("s_mov_b32 m0, %1\n\ts_nop 0\n\tglobal_load_lds_dword %0, %2" CODE_POL "\n\tglobal_load_lds_dword %0, %3" CODE_POL
            :: "v"((u32)lane * 64u + (u32)(wave - 4) * CODE_STR), "s"(L_IMG + (u32)(wave - 4) * 2048u), "s"(pc0), "s"(pc0 + 4096) : "memory");
    }
    const u32 head = (u32)(xc * 4 + (y >> 2)), j = (u32)y & 3, w = (u32)wave;
    const u32 i15 = lane & 15, g = lane >> 4;
    const u32 ks = b & 15, cg = (u32)(b >> 4) * 2 + (w & 1);
    u32 od[4], om[4], oq = ((j >> 1) * 256 + w * 32 + (lane & 8) * 2 + g) * 8192u + head * 256u + (j & 1) * 128u + (lane & 7) * 16u;
#pragma unroll
    for (int k = 0; k < 4; ++k) { od[k] = (3 * k + g) * 1024u + w * 256u + ((i15 ^ (g << 2) ^ (u32)k) << 4); PIN(od[k]); }
    om[0] = (ks * 32 + 2 * (u32)(lane >> 2)) * 2; om[1] = (u32)lane * 16; om[2] = cg * 64u + (lane & 3) * 16u; om[3] = om[0] * 3072u + om[2]; om[2] += g * 4u;
#pragma unroll
    for (int k = 0; k < 4; ++k) PIN(om[k]);
    PIN(oq);
    v4u wm[4], xa;
    u32 cc, xs;
#pragma unroll
    for (int k = 0; k < 4; ++k) UNDEF(wm[k]);
    UNDEF(xa); UNDEF(cc); UNDEF(xs);
    int role = (0xffaa5550u >> ((y & 16) + 2 * wave)) & 3;
    asm volatile("s_waitcnt lgkmcnt(0)" : "+s"(role), "+s"(ka), "+s"(kb), "+s"(kc) :: "memory");
#define KP(T, v, i) ((T)((u64)v[i + 1] << 32 | v[i]))
    Params P;
    P.wmod = KP(const u16*, ka, 0); P.c = KP(const u16*, ka, 2); P.x = KP(const u16*, ka, 4); P.ws = KP(char*, ka, 6);
    P.wq = KP(const u16*, ka, 8); P.ckv = KP(const u16*, ka, 10); P.wuk = KP(const u16*, ka, 12); P.wo = KP(const u16*, ka, 14);
    P.wuv = KP(const u16*, kb, 0); P.wg = KP(const u16*, kb, 2); P.wu = KP(const u16*, kb, 4); P.wd = KP(const u16*, kb, 6); P.out = KP(u16*, kc, 0);
    char* const ws = P.ws;
    if (role == 0) {
        asm volatile("global_load_dword %0, %3, %4\n\tglobal_load_dwordx4 %1, %5, %6\n\tglobal_load_dword %2, %7, %6" : "+v"(cc), "+v"(xa), "+v"(xs)
            : "v"(om[0]), "s"(P.c), "v"(om[1]), "s"(P.x), "v"(om[2]) : "memory");
        asm volatile("global_load_dwordx4 %0, %4, %5 nt\n\tglobal_load_dwordx4 %1, %4, %6 nt\n\tglobal_load_dwordx4 %2, %4, %5 offset:1024 nt\n\tglobal_load_dwordx4 %3, %4, %6 offset:1024 nt"
            : "+v"(wm[0]), "+v"(wm[1]), "+v"(wm[2]), "+v"(wm[3]) : "v"(om[3]), "s"(P.wmod), "s"(P.wmod + 3072) : "memory");
    }
    const char* const ckv = (const char*)P.ckv;
    const u32 sidx = (u32)b - 128;
    const u64 pcf = pc0 - w * 256u;
    if (T_OPEN0 == 0 && role == 2) { const u32 p = w; ISSUE(0); ISSUE(1); }
    __builtin_amdgcn_s_barrier();
    v4u wr[4];
#pragma unroll
    for (int k = 0; k < 4; ++k) UNDEF(wr[k]);
    if (WQL ? role == 1 : role < 2) {
#pragma unroll
        for (int k = 0; k < 4; ++k) ld_nt_into(wr[k], P.wq + k * 16384, oq);
    }
    const u32 t0 = now32();
    asm volatile("" : "+s"(role));
    if (role >= 2) {
        if (role == 2) {
            const u32 p = w;
#pragma unroll
            for (int h = T_OPEN0 ? 0 : 2; h < HW; ++h) {
                if (h == 0) until(t0 + T_OPEN0);
                if (h == 2) until(t0 + T_OPEN1);
                if (h == 4 && T_OPEN2 > T_OPEN1) until(t0 + T_OPEN2);
                ISSUE(h);
            }
            stream_pv(P, b, w, lane, od, pcf, t0, tl);
        } else stream_qk(P, b, w - 4, lane, t0, budget, tl);
        STAMP(15);
        STAMP_OUT();
        return;
    }
    u64 e64;
    u32 tb;
    {
        const u32 p = lane & 3;
        v4u wk[8];
#pragma unroll
        for (int k = 0; k < 8; ++k) UNDEF(wk[k]);
        if (b < 8 && tid == 128) st_sc2(ws, WS_ST + (u32)b * 8u, (v2u){t0, ~t0});
        u32 warm;
        UNDEF(warm);
        if (wave == 7) asm volatile("global_load_dword %0, %1, %2\n\tglobal_load_dword %0, %1, %3\n\tglobal_load_dword %0, %1, %4\n\tglobal_load_dword %0, %5, %6" : "+v"(warm)
            : "v"(((u32)lane & 1) * ((4u << 20) - 4u)), "s"(P.wuk), "s"(P.wo), "s"(P.wuv), "v"(WS_PAY + (((u32)lane & 3) * 10 + 1) * PHD + 4 * 128 * 256), "s"(ws) : "memory");
        EPOCH();
        const u32 tlo = (u32)e64, thi = (u32)(e64 >> 32);
        if (role == 0) {
            u32 so = piece(WS_HP, 2048, cg) + (ks * 8 + 2 * p + (u32)(lane >> 5)) * 16u;
            VMWAIT(WQL ? 4 : 8);
            PIN(xa); PIN(xs); PIN(cc);
            float mean, rstd;
            ln_stats(xa, mean, rstd);
            const u32 sp = pack2(silu(bflo(cc)), silu(bfhi(cc)));
            u32 s0l = sp & 0xffffu, s0h = sp << 16, s1l = sp >> 16, s1h = sp & 0xffff0000u;
            float xn0 = (bflo(xs) - mean) * rstd, xn1 = (bfhi(xs) - mean) * rstd;
            PIN(xn0); PIN(xn1); PIN(s0l); PIN(s0h); PIN(s1l); PIN(s1h); PIN(so);
            VMWAIT(WQL ? 0 : 4);
#pragma unroll
            for (int k = 0; k < 4; ++k) PIN(wm[k]);
            STAMP(22);
            float ta[4], tc[4];
#pragma unroll
            for (int k = 0; k < 2; ++k) {
                ta[2 * k] = red32(dot2(wm[1][k], s1l, dot2(wm[0][k], s0l, 0.f)), dot2(wm[1][k + 2], s1l, dot2(wm[0][k + 2], s0l, 0.f)));
                ta[2 * k + 1] = red32(dot2(wm[1][k], s1h, dot2(wm[0][k], s0h, 0.f)), dot2(wm[1][k + 2], s1h, dot2(wm[0][k + 2], s0h, 0.f)));
                tc[2 * k] = red32(dot2(wm[3][k], s1l, dot2(wm[2][k], s0l, 0.f)), dot2(wm[3][k + 2], s1l, dot2(wm[2][k + 2], s0l, 0.f)));
                tc[2 * k + 1] = red32(dot2(wm[3][k], s1h, dot2(wm[2][k], s0h, 0.f)), dot2(wm[3][k + 2], s1h, dot2(wm[2][k + 2], s0h, 0.f)));
            }
            float u0 = xn0 * (red16(tc[0], tc[2]) + 0.015625f) + red16(ta[0], ta[2]), u1 = xn1 * (red16(tc[1], tc[3]) + 0.015625f) + red16(ta[1], ta[3]);
            u0 += dpp<0x124>(u0); u1 += dpp<0x124>(u1); u0 += dpp<0x128>(u0); u1 += dpp<0x128>(u1);
            u32 e, od;
            both16(pack2(u0, u1), e, od);
            if ((lane & 28) == 0) st_sc(ws, so, (v4u){e, od, tlo, thi});
            STAMP(24);
            if (WQL) {
#pragma unroll
                for (int k = 0; k < 4; ++k) ld_nt_into(wr[k], P.wq + k * 16384, oq);
            }
        }
        u32 so = piece(WS_QP, 1024, head) + (((j & 2) + (u32)(lane >> 5)) * 16 + (j & 1) * 8 + (lane & 7)) * 16u, ho = piece(WS_HP, 2048, (j >> 1) * 8 + w) + (u32)lane * 16u;
        u32 la = F_QP + w * 256 + (g * 8 + (lane & 7)) * 8, lb = la + (lane & 8) * 128, ok = ((u32)y * 32 + g * 8) * 8192u + ((u32)xc * 4 + (w & 3)) * 256u + i15 * 16u;
        PIN(so); PIN(ho); PIN(la); PIN(lb); PIN(ok);
        u32 onel = 0x3f80u, oneh = 0x3f800000u;
        PIN(onel); PIN(oneh);
        v4u hg[2];
        UNDEF(hg[0]); UNDEF(hg[1]);
        __builtin_amdgcn_s_barrier();
        const u32 tm = now32();
        until(t0 + ST_LOOK);
        tb = time_base(ws + WS_ST, t0);
        until(tb + max((u32)A_FIRST, tm - t0 + A_OWN));
        ld_sc_into(hg[0], ws, ho); ld_sc_into(hg[1], ws + 1024, ho);
        if (!GDONE) {
            __builtin_amdgcn_s_barrier();
            if (wave >= 4) {
#pragma unroll
                for (int k = 0; k < 8; ++k) ld_nt_into(wk[k], P.wuk + k * 4096, ok);
                VMWAIT(10);
            } else VMWAIT(2);
        } else VMWAIT(2);
#pragma unroll
        for (int k = 0; k < 4; ++k) PIN(wr[k]);
        PIN(warm);
        STAMP(1);
        pair_rows(wr[0], wr[1]); pair_rows(wr[2], wr[3]);
#pragma unroll
        for (int k = 0; k < 4; ++k) PIN(wr[k]);
        if (!GDONE && wave >= 4) VMWAIT(8); else VMWAIT(0);
        PIN(hg[0]); PIN(hg[1]);
        if (__builtin_expect(ballot((((hg[0].z ^ tlo) | (hg[0].w ^ thi)) | ((hg[1].z ^ tlo) | (hg[1].w ^ thi))) != 0u) != 0ull, 0)) gather<2, 1024, 2>(ws, ho, tlo, thi, hg, budget);
        if (GDONE) {
            __builtin_amdgcn_s_barrier();
            if (wave >= 4) {
#pragma unroll
                for (int k = 0; k < 8; ++k) ld_nt_into(wk[k], P.wuk + k * 4096, ok);
            }
        }
        STAMP(3);
        {
            float hx = red16(red32(dot2(hg[1].x, onel, bflo(hg[0].x)), dot2(hg[1].y, onel, bflo(hg[0].y))), red32(dot2(hg[1].x, oneh, bfhi(hg[0].x)), dot2(hg[1].y, oneh, bfhi(hg[0].y))));
            hx += dpp<0x128>(hx);
            hx = asf((u32)__builtin_amdgcn_update_dpp((int)asu(hx), (int)asu(hx), 0x114, 0xf, 0x6, false));
            const u32 hp = pack2(hx, dpp<0xB1>(hx)), h01 = dppu<0x00>(hp), h23 = dppu<0xAA>(hp);
            float acc[8];
#pragma unroll
            for (int k = 0; k < 4; ++k) { acc[2 * k] = dot2(wr[2][k], h23, dot2(wr[0][k], h01, 0.f)); acc[2 * k + 1] = dot2(wr[3][k], h23, dot2(wr[1][k], h01, 0.f)); }
            float t[4];
#pragma unroll
            for (int k = 0; k < 4; ++k) t[k] = red32(acc[k], acc[k + 4]);
            float u0 = red16(t[0], t[2]), u1 = red16(t[1], t[3]);
            u0 += dpp<0x128>(u0); PIN(u0); u1 += dpp<0x128>(u1);
            *lp<f32x2>(la) = (f32x2){u0, u1};
        }
        barrier();
        if (wave == 0) {
            const LAS f32x2* qp = lp<f32x2>(lb);
            const f32x2 q2 = (qp[0] + qp[32]) + (qp[64] + qp[96]);
            float u0 = q2.x, u1 = q2.y;
            u0 += dpp<0x128>(u0); PIN(u0); u1 += dpp<0x128>(u1);
            u32 e, od;
            both16(pack2(u0, u1), e, od);
            if ((lane & 24) == 0) st_sc(ws, so, (v4u){e, od, tlo, thi});
            if (y == ((xc * 5) & 7) + (y & 8) && lane < NQH) { const u32 t = now32(); st_sc2(ws, WS_QH + (u32)lane * 128u + (u32)(xc * 2 + (y >> 3)) * 8u, (v2u){t, t ^ tlo}); }
            STAMP(2);
        }
        if (wave >= 4) {
            const u32 tb1 = now32(), hk = (u32)xc * 4 + (w & 3);
            const u32 rho = 8 * ((hk & 7) >> 1) + 4 * (hk >> 4) + 2 * (hk & 1) + ((hk >> 3) & 1);
            const u32 og = piece(WS_QP, 1024, hk) + (u32)lane * 16u;
            u32 sq = WS_QABS + j * 8192u + (rho ^ ((tlo & 1) << 4)) * 256u + (u32)(y >> 2) * 64u + g * 16u, sc = WS_QCHK + (rho * 16 + (u32)y) * 16u;
            PIN(sq); PIN(sc);
            v4u qg[1];
            until(tb1 + A2_DELAY);
            gather<1, 0, 2>(ws, og, tlo, thi, qg, budget);
            STAMP(23);
            u32 e, od, q0, q1, q2, q3;
            both16(pack2(red32(bflo(qg[0].x), bflo(qg[0].y)), red32(bfhi(qg[0].x), bfhi(qg[0].y))), e, od);
            both32(e, q0, q1); both32(od, q2, q3);
            float a[8];
#pragma unroll
            for (int k = 0; k < 8; ++k) { PIN(wk[k]); a[k] = dot2(wk[k][3], q3, dot2(wk[k][2], q2, dot2(wk[k][1], q1, dot2(wk[k][0], q0, 0.f)))); }
            float z[4];
#pragma unroll
            for (int k = 0; k < 4; ++k) {
                const float keep = (lane & 1) ? a[k + 4] : a[k], give = (lane & 1) ? a[k] : a[k + 4];
                float s = keep + dpp<0xB1>(give);
                s += dpp<0x122>(s); s += dpp<0x124>(s); s += dpp<0x128>(s);
                z[k] = s * (0.08838834764831845f * 1.4426950408889634f);
            }
            const u32 y0 = pack2(z[0], z[1]), y1 = pack2(z[2], z[3]);
            const u32 y2 = dppu<0xB1>(y0), y3 = dppu<0xB1>(y1);
            if (i15 == 0) st_sc(ws, sq, (v4u){y0, y1, y2, y3});
            u32 c0, c1;
            both16((y0 + y1) + (y2 + y3), c0, c1);
            both32(c0 + c1, c0, c1);
            if (lane == 0) st_sc(ws, sc, (v4u){c0 + c1, 0u, tlo, thi});
            STAMP(4);
        }
    }
    const u32 tlo = (u32)e64, thi = (u32)(e64 >> 32);
    const bool is_red = (y >> 2) == 1;
    const u32 rid = (u32)xc * 4 + j;
    barrier();
    const u32 tfd = now32();
    bf16x8 bo[16], bu[4];
    u32 xo, aa, cw, cr, op, o, oc, wa, he, wl, rl;
#define TAIL_ADDR() do { \
        xo = piece(WS_X1, 4096, (u32)(lane >> 3)) + (rid * 8 + (u32)(lane & 7)) * 16u; \
        aa = F_A + (i15 & 7) * 272 + g * 16; cw = F_CX + ((w - 2 * j) & 7) * 32 + i15 * 2; cr = F_CX + g * 16; \
        op = piece(WS_OUTP, 16384, 4 * w + g) + (u32)b * 128u + (i15 & 7) * 16u; \
        wl = F_LS + (i15 < 4 ? (w * 16 + g * 4 + i15) * 4 : 512u); rl = F_LS + (u32)lane * 8; PIN(wl); PIN(rl); \
        o = WS_PAY + head * PHD + (j * 128 + ((w * 16) ^ ((tlo & 1) << 6))) * 256u + (u32)lane * 16u; \
        oc = WS_CHK + ((w * 16 + (i15 & 3) * 4 + g) * 4 + j) * 512u + head * 16u; \
        wa = F_A + w * 272 + i15 * 16 + g * 4; \
        he = WS_HE + (((u32)b + 17u * (u32)lane) & 127u) * HSTR + (u32)lane * 16u; PIN(he); \
        PIN(xo); PIN(aa); PIN(cw); PIN(cr); PIN(op); PIN(o); PIN(oc); PIN(wa); } while (0)
    {
        const u32 kq = (u32)b & 3, cgl = (u32)b >> 2;
        const u32 mrow = kq * 128 + w * 16 + (u32)(lane >> 3);
        u32 oo[4];
#pragma unroll
        for (int k = 0; k < 4; ++k) oo[k] = (head * 128 + 4 * w + k) * 1024u + (((u32)lane ^ (u32)(k << 2) ^ (w & 3)) << 4);
        const u32 q4 = i15 >> 2, p4 = lane & 3;
        const u32 ra = 8 * (g & 1) + q4, rb = ra + 4;
        u32 xa[4], xb[4];
#pragma unroll
        for (int t2 = 0; t2 < 4; ++t2) {
            const u32 c0 = 8 * w + (p4 >> 1) + 2 * t2, x0 = F_WO + (8 * g + q4) * 1024 + 8 * (p4 & 1);
            xa[t2] = x0 + ((c0 ^ swz(ra)) << 4); xb[t2] = x0 + 4096 + ((c0 ^ swz(rb)) << 4);
            PIN(xa[t2]); PIN(xb[t2]);
        }
        until(tfd + FD_GAP);
        until(tb + T_MID);
#pragma unroll
        for (int k = 0; k < 16; ++k) WODMA(P.wo + ((j + (k >> 2)) & 3) * 16384, oo[k & 3], F_WO + ((k >> 2) * 32 + 4 * w + (k & 3)) * 1024u);
        STAMP(19);
        u32 cv0 = ld_u16(P.c, mrow * 2), cv1 = ld_u16(P.c + 8, mrow * 2);
        u32 mo = mrow * 6144u + 2048u + cgl * 128u + (u32)(lane & 7) * 16u;
#pragma unroll
        for (int k = 0; k < 4; ++k) {
            const u32 e = w * 16 + k * 4 + g, c = ((e >> 2) & 1) * 64 + (e & 1) * 32 + ((e >> 1) & 1) * 16 + (e >> 3);
            oo[k] = (j * 128 + c) * 8192u + head * 256u + ((i15 ^ swz(e & 15)) << 4);
            PIN(oo[k]);
        }
        PIN(mo);
        v4u mw0 = ld_nt(P.wmod, mo);
        VMWAIT(1);
        PIN(cv0); PIN(cv1);
        STAMP(14);
        barrier();
#pragma unroll
        for (int t2 = 0; t2 < 4; ++t2) { bo[4 * t2] = tr2(xa[t2], xb[t2]); bo[4 * t2 + 1] = tr2(xa[t2] + 32768, xb[t2] + 32768); }
#pragma unroll
        for (int t2 = 0; t2 < 4; ++t2) {
            xa[t2] += 65536; xb[t2] += 65536; PIN(xa[t2]); PIN(xb[t2]);
            bo[4 * t2 + 2] = tr2(xa[t2], xb[t2]); bo[4 * t2 + 3] = tr2(xa[t2] + 32768, xb[t2] + 32768);
        }
        u32 sp = pack2(silu(bflo(cv0)), silu(bflo(cv1)));
        u32 x0, x1, ma = F_ML + w * 256 + ((lane & 7) * 4 + g) * 8;
        {
            const u32 c0 = 2 * w + (p4 >> 1);
            x0 = F_UV + (8 * g + q4) * 256 + 8 * (p4 & 1) + ((c0 ^ swz(ra)) << 4); x1 = F_UV + (8 * g + q4 + 4) * 256 + 8 * (p4 & 1) + ((c0 ^ swz(rb)) << 4);
        }
        PIN(sp); PIN(x0); PIN(x1); PIN(ma);
        TAIL_ADDR();
#pragma unroll
        for (int k = 0; k < 16; ++k) PIN(bo[k]);
        barrier();
        until(tb + T_UV - 4 * T_STEP);
        v4u mw1 = ld_nt(P.wmod + 8 * 3072, mo);
#pragma unroll
        for (int k = 0; k < 4; ++k) { until(tb + T_UV - (3 - k) * T_STEP); dma_nt(P.wuv, oo[k], F_UV + (w * 16 + k * 4) * 256u); }
        const u32 ml = min((u32)(y >> 2) * 8 + (u32)xc, 27u);
        u32 pg = (j * 128 + w * 16 + (u32)(lane >> 3)) * 3584u + ml * 128u + (u32)(lane & 7) * 16u, pd = (ml * 64 + j * 16 + w * 2) * 1024u + (u32)lane * 16u;
        PIN(pg); PIN(pd);
#define MLP_PF(dma) do { dma(P.wg, pg, F_JUNK + w * 1024u); dma(P.wg + 8 * 1792, pg, F_JUNK + w * 1024u); dma(P.wu, pg, F_JUNK + w * 1024u); dma(P.wu + 8 * 1792, pg, F_JUNK + w * 1024u); \
            dma(P.wd, pd, F_JUNK + w * 1024u); dma(P.wd + 512, pd, F_JUNK + w * 1024u); } while (0)
        VMWAIT(4);
        PIN(mw0); PIN(mw1);
        pair_rows(mw0, mw1);
        float t[4], u[2];
#pragma unroll
        for (int k = 0; k < 2; ++k) { t[2 * k] = red32(dot2(mw0[k], sp, 0.f), dot2(mw0[k + 2], sp, 0.f)); t[2 * k + 1] = red32(dot2(mw1[k], sp, 0.f), dot2(mw1[k + 2], sp, 0.f)); }
#pragma unroll
        for (int k = 0; k < 2; ++k) { float z = red16(t[k], t[k + 2]); z += dpp<0x128>(z); u[k] = z; }
        *lp<f32x2>(ma) = (f32x2){u[0], u[1]};
        if (tid < 12) *lp<u32>(F_ST + tid * 4) = 0u;
        const bool pf = T_MLP && (y < 12 || xc < 4);
        if (pf) {
            until(tb + T_MLP);
            MLP_PF(dma_nt);
            VMWAIT(6);
        } else VMWAIT(0);
        STAMP(6);
        barrier();
#pragma unroll
        for (int s = 0; s < 4; ++s) bu[s] = tr2(x0 + s * 8192, x1 + s * 8192);
        if (wave == 7) {
            const LAS f32x2* q = lp<f32x2>(F_ML) + lane;
            const f32x2 s = (q[0] + q[64]) + (q[128] + q[192]);
            const float sx = red32(s.x, s.x), sy = red32(s.y, s.y);
            if (lane < 32) st_sc(ws, WS_ML + (cgl & 7) * 8192u + ((u32)lane >> 4) * 4096u + (cgl >> 3) * 1024u + (kq * 16 + ((u32)lane & 15)) * 16u, (v4u){asu(sx), asu(sy), tlo, thi});
        }
#pragma unroll
        for (int k = 0; k < 4; ++k) PIN(bu[k]);
        if (pf && PROMOTE) {
            VMWAIT(0);
            STAMP(5);
            MLP_PF(dma_sc);
        }
    }
    float ga0 = 0.f, ga1 = 0.f, gb0 = 0.f, gb1 = 0.f, xv0 = 0.f, xv1 = 0.f;
    STAMP(7);
    {
        u32 tdet;
        if (wave == 0) {
            tdet = hint_wait<HC_K>(ws, WS_HC + (u32)b * HSTR + (u32)(lane & 7) * 16u, tlo, budget) | 1u;
            if (lane == 0) *lp<u32>(F_GO) = tdet;
            if (is_red) {
                v4u gm[4];
                const u32 om = WS_ML + (rid >> 1) * 4096u + (g * 16 + (rid & 1) * 8 + (u32)(lane & 7)) * 16u;
                u32 xr = ld_u32(P.x, (rid * 8 + (u32)(lane & 7)) * 4);
                gather<4, 1024>(ws, om, tlo, thi, gm, budget);
                PIN(xr);
                xv0 = bflo(xr); xv1 = bfhi(xr);
                {
                    const float s0 = red16(red32(asf(gm[1].x), asf(gm[2].x)), red32(asf(gm[1].y), asf(gm[2].y)));
                    u32 e, od, sbp, cbp;
                    both16(asu(s0), e, od);
                    both32(pack2(asf(e), asf(od)), sbp, cbp);
                    if (lane < 8) st_sc(ws, WS_SC + (rid * 8 + (u32)lane) * 16u, (v4u){cbp, sbp, tlo, thi});
                }
                ga0 = asf(gm[0].x); ga1 = asf(gm[0].y); gb0 = asf(gm[3].x); gb1 = asf(gm[3].y);
                ga0 = red32(ga0, ga0); ga0 = red16(ga0, ga0); ga1 = red32(ga1, ga1); ga1 = red16(ga1, ga1);
                gb0 = red32(gb0, gb0); gb0 = red16(gb0, gb0); gb1 = red32(gb1, gb1); gb1 = red16(gb1, gb1);
            }
        } else {
#pragma clang loop unroll(disable)
            do { tdet = rfl(*(volatile LAS u32*)lp<u32>(F_GO)); if (tdet) break; __builtin_amdgcn_s_sleep(1); } while (--budget > 0);
        }
        PIN(ga0); PIN(ga1); PIN(gb0); PIN(gb1); PIN(xv0); PIN(xv1);
        v4u v[4], ck = {0u, 0u, 0u, 0u};
        u32 onel = 0x3f80u, oneh = 0x3f800000u;
        PIN(onel); PIN(oneh);
#pragma unroll
        for (int k = 0; k < 4; ++k) UNDEF(v[k]);
        until(tdet + C_DELAY);
        until(tb + T_PUB + C_PUB);
        STAMP(8);
#pragma clang loop unroll(disable)
        for (;;) {
            asm volatile("global_load_dwordx4 %0, %5, %7 sc1\n\tglobal_load_dwordx4 %1, %5, %7 offset:1024 sc1\n\tglobal_load_dwordx4 %2, %5, %7 offset:2048 sc1\n\tglobal_load_dwordx4 %3, %5, %7 offset:3072 sc1\n\t"
                "s_mov_b64 exec, %8\n\tglobal_load_dwordx4 %4, %6, %7 sc1\n\ts_mov_b64 exec, -1\n\ts_waitcnt vmcnt(4)"
                : "+v"(v[0]), "+v"(v[1]), "+v"(v[2]), "+v"(v[3]), "+v"(ck) : "v"(o), "v"(oc), "s"(ws), "s"(0x000f000f000f000full) : "memory");
            float a[8];
#pragma unroll
            for (int q = 0; q < 4; ++q) { a[2 * q] = bflo(v[0][q]); a[2 * q + 1] = bfhi(v[0][q]); }
            u32 x = (v[0].x + v[0].y) + (v[0].z + v[0].w);
#pragma unroll
            for (int k = 1; k < 4; ++k) {
#pragma unroll
                for (int q = 0; q < 8; ++q) PIN(a[q]);
                PIN(x);
                asm volatile("s_waitcnt vmcnt(%1)" : "+v"(v[k]) : "i"(4 - k) : "memory");
                x = (x + v[k].x + v[k].y) + (v[k].z + v[k].w);
#pragma unroll
                for (int q = 0; q < 4; ++q) { a[2 * q] = dot2(v[k][q], onel, a[2 * q]); a[2 * q + 1] = dot2(v[k][q], oneh, a[2 * q + 1]); }
            }
            float t[4];
            x += dppu<0x128>(x);
#pragma unroll
            for (int k = 0; k < 4; ++k) t[k] = red32(a[k], a[k + 4]);
            x += dppu<0x124>(x);
            float u0 = red16(t[0], t[2]); PIN(u0);
            *lp<u32>(wa) = pack2(u0, red16(t[1], t[3]));
            PIN(x);
            asm volatile("s_waitcnt vmcnt(0)" : "+v"(ck) :: "memory");
            *lp<u32>(wl) = ck.y;
            x -= ck.x;
            x += dppu<0xB1>(x); x += dppu<0x4E>(x);
            const u64 bad = ballot((x | (ck.w ^ tlo)) != 0u) & 0x000f000f000f000full;
            if (__builtin_expect((bad | ballot(ck.z != 0u)) == 0ull, 1)) break;
            if (bad == 0ull || --budget <= 0) {
                const float m = i15 < 4 ? asf(ck.z) : -3.0e38f;
                float M = __builtin_fmaxf(m, dpp<0xB1>(m)); M = __builtin_fmaxf(M, dpp<0x4E>(M));
                M = asf(rfl(asu(max32(max16(M)))));
                const u32 f = pack2(ex2(m - M), 0.f);
                const u32 f0 = dppu<0x150>(f), f1 = dppu<0x151>(f), f2 = dppu<0x152>(f), f3 = dppu<0x153>(f);
#pragma unroll
                for (int q = 0; q < 4; ++q) {
                    a[2 * q] = dot2(v[3][q], f3, dot2(v[2][q], f2, dot2(v[1][q], f1, dot2(v[0][q], f0, 0.f))));
                    a[2 * q + 1] = dot2(v[3][q], f3 << 16, dot2(v[2][q], f2 << 16, dot2(v[1][q], f1 << 16, dot2(v[0][q], f0 << 16, 0.f))));
                }
#pragma unroll
                for (int k = 0; k < 4; ++k) t[k] = red32(a[k], a[k + 4]);
                *lp<u32>(wa) = pack2(red16(t[0], t[2]), red16(t[1], t[3]));
                *lp<float>(wl) = asf(ck.y) * bflo(f);
                *lp<float>(F_ST + w * 4) = M;
                *lp<u32>(F_ST + 32) = 1u;
                break;
            }
            __builtin_amdgcn_s_sleep(POLL_SLEEP);
        }
        STAMP(9);
    }
    barrier();
    u64 tc1;
    asm volatile("s_memrealtime %0" : "=s"(tc1));
    STAMP(10);
    float ls;
    {
        bf16x8 af[4];
#pragma unroll
        for (int s = 0; s < 4; ++s) af[s] = *lp<bf16x8>(aa + s * 64);
        const u32 rare = *lp<u32>(F_ST + 32);
        const f32x2 l2 = *lp<f32x2>(rl);
        SCHED();
        f32x4 d = {0.f, 0.f, 0.f, 0.f};
#pragma unroll
        for (int s = 0; s < 4; ++s) d = __builtin_amdgcn_mfma_f32_16x16x32_bf16(af[s], bu[s], d, 0, 0, 0);
        ls = l2.x + l2.y;
        if (__builtin_expect(rfl(rare) != 0u, 0)) {
            const f32x4 m0 = *lp<f32x4>(F_ST), m1 = *lp<f32x4>(F_ST + 16);
            const float M = max8(m0, m1);
#pragma unroll
            for (int k = 0; k < 4; ++k) d[k] *= ex2(((g & 1) ? m1[k] : m0[k]) - M);
            ls *= ex2(*lp<float>(F_ST + (u32)(lane >> 3) * 4) - M);
        }
        float c0 = d.x + d.y, c1 = d.z + d.w;
        PIN(c0); PIN(c1);
        float c = c0 + c1, c2 = c0 + c1;
        PIN(c); PIN(c2);
        c = red16(c, c2);
        *lp<u16>(cw) = (u16)pack2(c, c);
    }
    barrier();
    STAMP(13);
    {
        bf16x8 af[4];
#pragma unroll
        for (int s = 0; s < 4; ++s) af[s] = *lp<bf16x8>(cr + s * 64);
        ls += dpp<0x128>(ls); ls += dpp<0x124>(ls); ls += dpp<0x122>(ls); ls += dpp<0x121>(ls);
        ls = red16(ls, ls); ls = red32(ls, ls);
        ls = __builtin_amdgcn_rcpf(ls);
        v4u r = {0u, 0u, tlo, thi};
        PIN(ls); PIN(r);
        SCHED();
        f32x4 d[4];
#pragma unroll
        for (int t = 0; t < 4; ++t) d[t] = (f32x4){0.f, 0.f, 0.f, 0.f};
#pragma unroll
        for (int s = 0; s < 4; ++s) {
#pragma unroll
            for (int t = 0; t < 4; ++t) d[t] = __builtin_amdgcn_mfma_f32_16x16x32_bf16(af[s], bo[4 * t + s], d[t], 0, 0, 0);
            SCHED();
        }
        r.x = asu(((g & 2) ? ((g & 1) ? d[3].x : d[2].x) : ((g & 1) ? d[1].x : d[0].x)) * ls);
        r.y = dppu<0x128>(r.x);
        asm volatile("s_mov_b64 exec, %3\n\tglobal_store_dwordx4 %0, %1, %2 sc1\n\ts_mov_b64 exec, -1\n\ts_nop 0" :: "v"(op), "v"(r), "s"(ws), "s"(0x00ff00ff00ff00ffull) : "memory");
    }
    STAMP(11);
    asm volatile("" : "+s"(tc1));
    if (wave == 2 && lane < 8) st_sc(ws, he, (v4u){(u32)tc1, 0u, tlo, thi});
    if (b == 0 && tid == 64) st_sc2(ws, WS_EPOCH, (v2u){tlo, thi});
    if (is_red) {
        const f32x2 s = reduce16<8, true>(ws, piece(WS_OUTP, 16384, rid), tlo, thi, wave, lane, F_RD, (u32)tc1 + D_DELAY, budget);
        const float x10 = xv0 + ga0 * s.x, x11 = xv1 + ga1 * s.y;
        if (wave == 0) st_sc(ws, xo, (v4u){asu(x10), asu(x11), tlo, thi});
        STAMP(12);
        u32 oa = (rid * 8 + (u32)lane) * 4u;
        PIN(oa);
        const f32x2 s2 = reduce16<6, false>(ws, piece(WS_YP, 16384, rid), tlo, thi, wave, lane, F_RV, now32() + F_DELAY, budget);
        if (tid < 8) asm volatile("global_store_dword %0, %1, %2" :: "v"(oa), "v"(pack2(x10 + gb0 * s2.x, x11 + gb1 * s2.y)), "s"(P.out) : "memory");
    }
    STAMP(15);
    STAMP_OUT();
}

extern "C" void kernel_launch(void* const* d_in, const int* in_sizes, int n_in, void* d_out, int out_size, void* d_ws, size_t ws_size, hipStream_t stream) {
    Params P;
    P.x = (const u16*)d_in[0]; P.ckv = (const u16*)d_in[1]; P.wq = (const u16*)d_in[2]; P.wuk = (const u16*)d_in[3];
    P.wuv = (const u16*)d_in[4]; P.wo = (const u16*)d_in[5]; P.wg = (const u16*)d_in[7]; P.wu = (const u16*)d_in[8];
    P.wd = (const u16*)d_in[9]; P.c = (const u16*)d_in[10]; P.wmod = (const u16*)d_in[11];
    P.out = (u16*)d_out; P.ws = (char*)d_ws + WS_BASE(ws_size);
    (void)hipFuncSetAttribute((const void*)fused_kernel, hipFuncAttributeMaxDynamicSharedMemorySize, LDS_BYTES);
    fused_kernel<<<256, 512, LDS_BYTES, stream>>>(P);
}
```

```cpp
#include <hip/hip_runtime.h>

typedef unsigned u32;
typedef unsigned long long u64;
typedef unsigned short u16;
typedef u32 v4u __attribute__((ext_vector_type(4)));
typedef u32 v2u __attribute__((ext_vector_type(2)));
typedef float f32x2 __attribute__((ext_vector_type(2)));
typedef float f32x4 __attribute__((ext_vector_type(4)));
typedef float f32x16 __attribute__((ext_vector_type(16)));
typedef short bf16x8 __attribute__((ext_vector_type(8)));
typedef short s16x4 __attribute__((ext_vector_type(4)));
typedef __bf16 bf16x2_t __attribute__((ext_vector_type(2)));
#define LAS __attribute__((address_space(3)))
#define DI __device__ __forceinline__

#define CFG(name, val) constexpr int name = val
CFG(BUDGET, 1 << 20);
CFG(POLL_SLEEP, 4);
CFG(HINT_SLEEP, 4);
#define PACE 155, 245, 65, 555, 10, 595, 100, 140, 20, 195, 555, 0, -70, 0, 65, 45
constexpr int pace_[] = {PACE};
CFG(ST_LOOK, pace_[0]);
CFG(A_FIRST, pace_[1]);
CFG(A2_DELAY, pace_[2]);
CFG(Q_FIRST, pace_[3]);
CFG(T_START1, pace_[4]);
CFG(T_START, pace_[5]);
CFG(C_DELAY, pace_[6]);
CFG(C_DELAY2, pace_[7]);
CFG(G_DELAY, pace_[8]);
CFG(F_DELAY, pace_[9]);
CFG(T_MID, pace_[10]);
CFG(S_DELAY, pace_[11]);
CFG(WQ_DELAY, pace_[13]);
CFG(T_OPEN, pace_[14]);
CFG(SKEW_MAX, pace_[15]);
CFG(P_SKEW, pace_[12]);
#define DEPTH_ 3
#define RAMP_ 0
#define D0_ 0
CFG(RAMP, RAMP_); CFG(D0, D0_);
constexpr int upto(int t) { return t + DEPTH_; }
static_assert(DEPTH_ == 3, "");
CFG(DEPTH, DEPTH_);
CFG(HC_K, 5);
#define QD 2
#define PRIO_Y 3
#define PRIO_P0 2
#define PRIO_P1 1
#define EARLY_W 0
#define HOLD C_Q
#define QSLEEP 4
#define INQK 6
#define KH 13
#define LAZY 48.f
CFG(CODE_LAST, 24576);
#define WS_BASE(sz) (((sz) - (32u << 20)) & ~(size_t)((2u << 20) - 1))

constexpr u32 WS_EPOCH = 0;
DI u32 piece(u32 base, u32 size, u32 i) { return base + i * size; }
constexpr u32 WS_ST = 16384;
constexpr u32 WS_HP = 32768;
constexpr u32 WS_QABS = 65536;
constexpr u32 WS_QCHK = 98304;
constexpr u32 WS_QP = 131072;
constexpr u32 WS_ML = 262144;
constexpr u32 WS_SC = 360448;
constexpr u32 WS_X1 = 327680;
constexpr u32 HSTR = 1152, WS_HC = 368640;
constexpr u32 WS_OUTP = 524288;
constexpr u32 WS_YP = 1u << 20;
constexpr u32 WS_PAY = 2u << 20;
constexpr u32 PBLK = 256, PHD = 4 * 2 * 128 * PBLK + 4096;
constexpr u32 WS_TL = 16u << 20;

constexpr u32 L_CKV = 0;
constexpr u32 L_IMG = 131072;
constexpr u32 L_P2 = L_IMG;
constexpr u32 L_P = L_IMG + 8192;
constexpr u32 L_REL = L_IMG + 16384;
constexpr u32 L_LP = L_IMG + 19456;
constexpr u32 L_RF = 128000;
constexpr u32 L_CNT = 130048;
constexpr u32 C_Z = L_CNT, C_E = L_CNT + 4, C_M = L_CNT + 8, C_L = L_CNT + 12, C_Q = L_CNT + 16, C_E0 = L_CNT + 20, C_TB = L_CNT + 256, C_T = L_CNT + 32;
constexpr u32 NOREF = 0xffc00000u;
constexpr u32 L_W = 0;
constexpr u32 L_H2 = 32768;
constexpr u32 L_GU = 34816;
constexpr u32 F_QP = 131072, F_TB = F_QP + 4096;
constexpr u32 F_WO = 0;
constexpr u32 F_UV = 131072;
constexpr u32 F_A = 0;
constexpr u32 F_ST = 8192;
constexpr u32 F_GO = 8384;
constexpr u32 F_CX = 8448;
constexpr u32 F_ML = 12288;
constexpr u32 F_RD = 20480, F_RV = 22528;
constexpr u32 LDS_BYTES = 163840;

struct Params {
    const u16 *wmod, *c, *x;
    char* ws;
    const u16 *wq, *ckv, *wuk, *wo, *wuv, *wg, *wu, *wd;
    u16* out;
};

DI float asf(u32 v) { return __builtin_bit_cast(float, v); }
DI u32 asu(float v) { return __builtin_bit_cast(u32, v); }
DI float bflo(u32 v) { return asf(v << 16); }
DI float bfhi(u32 v) { return asf(v & 0xffff0000u); }
DI u32 pack2(float a, float b) { f32x2 v = {a, b}; return __builtin_bit_cast(u32, __builtin_convertvector(v, bf16x2_t)); }
DI float dot2(u32 w, u32 x, float acc) { return __builtin_amdgcn_fdot2_f32_bf16(__builtin_bit_cast(bf16x2_t, w), __builtin_bit_cast(bf16x2_t, x), acc, false); }
DI void pair_rows(v4u& x, v4u& y) {
#pragma unroll
    for (int k = 0; k < 4; ++k) { const u32 a = x[k], b = y[k]; x[k] = __builtin_amdgcn_perm(b, a, 0x05040100u); y[k] = __builtin_amdgcn_perm(b, a, 0x07060302u); }
}
template <int CTRL> DI float dpp(float v) { return asf((u32)__builtin_amdgcn_update_dpp(0, (int)asu(v), CTRL, 0xf, 0xf, true)); }
DI float red32(float a, float b) { auto r = __builtin_amdgcn_permlane32_swap(asu(a), asu(b), false, false); return asf((u32)r[0]) + asf((u32)r[1]); }
DI float red16(float a, float b) { auto r = __builtin_amdgcn_permlane16_swap(asu(a), asu(b), false, false); return asf((u32)r[0]) + asf((u32)r[1]); }
DI float max3(float a, float b, float c) { return __builtin_fmaxf(__builtin_fmaxf(a, b), c); }
DI float max32(float a) { auto r = __builtin_amdgcn_permlane32_swap(asu(a), asu(a), false, false); return max3(asf((u32)r[0]), asf((u32)r[1]), a); }
DI float max16(float a) { auto r = __builtin_amdgcn_permlane16_swap(asu(a), asu(a), false, false); return max3(asf((u32)r[0]), asf((u32)r[1]), a); }
DI float max8(const f32x4& a, const f32x4& b) { return max3(max3(max3(a.x, a.y, a.z), a.w, b.x), max3(b.y, b.z, b.w), a.x); }
DI u32 perm_lo(u32 a, u32 b) { return __builtin_amdgcn_perm(b, a, 0x05040100u); }
DI u32 perm_hi(u32 a, u32 b) { return __builtin_amdgcn_perm(b, a, 0x07060302u); }
DI u32 rfl(u32 v) { return (u32)__builtin_amdgcn_readfirstlane((int)v); }
DI void both16(u32 a, u32& e, u32& o) { auto r = __builtin_amdgcn_permlane16_swap(a, a, false, false); e = (u32)r[0]; o = (u32)r[1]; }
DI void both32(u32 a, u32& l, u32& h) { auto r = __builtin_amdgcn_permlane32_swap(a, a, false, false); l = (u32)r[0]; h = (u32)r[1]; }
DI float rowsum(float v) { v += dpp<0x128>(v); v += dpp<0x124>(v); v += dpp<0x122>(v); v += dpp<0x121>(v); return v; }
DI float ex2(float v) { return __builtin_amdgcn_exp2f(v); }

DI v4u ld_nt(const void* base, u32 off) { v4u r; asm volatile("global_load_dwordx4 %0, %1, %2 nt" : "=v"(r) : "v"(off), "s"(base) : "memory"); return r; }
DI v4u ld_pl(const void* base, u32 off) { v4u r; asm volatile("global_load_dwordx4 %0, %1, %2" : "=v"(r) : "v"(off), "s"(base) : "memory"); return r; }
DI v2u ld_sc2(const void* base, u32 off) { v2u r; asm volatile("global_load_dwordx2 %0, %1, %2 sc1" : "=v"(r) : "v"(off), "s"(base) : "memory"); return r; }
DI v2u ld_u64(const void* base, u32 off) { v2u r; asm volatile("global_load_dwordx2 %0, %1, %2" : "=v"(r) : "v"(off), "s"(base) : "memory"); return r; }
DI u32 ld_u32(const void* base, u32 off) { u32 r; asm volatile("global_load_dword %0, %1, %2" : "=v"(r) : "v"(off), "s"(base) : "memory"); return r; }
DI u32 ld_u32_nt(const void* base, u32 off) { u32 r; asm volatile("global_load_dword %0, %1, %2 nt" : "=v"(r) : "v"(off), "s"(base) : "memory"); return r; }
DI u32 ld_u16(const void* base, u32 off) { u32 r; asm volatile("global_load_ushort %0, %1, %2" : "=v"(r) : "v"(off), "s"(base) : "memory"); return r; }
DI void st_sc(void* base, u32 off, v4u v) { asm volatile("global_store_dwordx4 %0, %1, %2 sc1\n\ts_nop 1" :: "v"(off), "v"(v), "s"(base) : "memory"); }
DI void st_sc2(void* base, u32 off, v2u v) { asm volatile("global_store_dwordx2 %0, %1, %2 sc1\n\ts_nop 1" :: "v"(off), "v"(v), "s"(base) : "memory"); }
DI void dma_nt(const void* base, u32 off, u32 lds_dst) { asm volatile("s_mov_b32 m0, %1\n\ts_nop 0\n\tglobal_load_lds_dwordx4 %0, %2 nt" :: "v"(off), "s"(lds_dst), "s"(base) : "memory", "m0"); }
DI void dma_sc(const void* base, u32 off, u32 lds_dst) { asm volatile("s_mov_b32 m0, %1\n\ts_nop 0\n\tglobal_load_lds_dwordx4 %0, %2 sc1" :: "v"(off), "s"(lds_dst), "s"(base) : "memory", "m0"); }
DI void ld_nt_into(v4u& r, const void* base, u32 off) { asm volatile("global_load_dwordx4 %0, %1, %2 nt" : "+v"(r) : "v"(off), "s"(base) : "memory"); }
DI void ld_sc_into(v4u& r, const void* base, u32 off) { asm volatile("global_load_dwordx4 %0, %1, %2 sc1" : "+v"(r) : "v"(off), "s"(base) : "memory"); }
#define VMWAIT_(n) asm volatile("s_waitcnt vmcnt(" #n ")" ::: "memory")
#define VMWAIT(n) VMWAIT_(n)
#define LGKM0() asm volatile("s_waitcnt lgkmcnt(0)" ::: "memory")
#define PIN(x) asm volatile("" : "+v"(x))
DI void barrier() { LGKM0(); __builtin_amdgcn_s_barrier(); asm volatile("" ::: "memory"); }

template <typename T> DI LAS T* lp(u32 off) { return (LAS T*)(size_t)off; }

#define STAMP(i) do { if ((i) == 1 && blockIdx.x >= 128 && threadIdx.x < 512) { tl = (u32)__builtin_amdgcn_s_memrealtime(); } } while (0)
#define STAMP_OUT() do { if (rfl(tl) != 0u) until(t0, 3000u + 8u * min(rfl(tl) - (u32)t0, 2000u)); } while (0)
#define CYC0() do {} while (0)
#define STAMPC(i) do {} while (0)

#define GL_(k, d, base, o) "s_bitcmp1_b32 %[need], " #k "\n\ts_cbranch_scc0 .Lg%=_" #k "\n\tglobal_load_dwordx4 %[" #d "], %[off], %[" #base "] offset:%[" #o "] sc1\n.Lg%=_" #k ":\n\t"
template <u32 S> DI void pass(const char* ws, u32 off, u32 need, v4u (&v)[1]) {
    asm volatile("global_load_dwordx4 %0, %1, %2 sc1\n\ts_waitcnt vmcnt(0)" : "=v"(v[0]) : "v"(off), "s"(ws) : "memory");
}
template <u32 S> DI void pass(const char* ws, u32 off, u32 need, v4u (&v)[2]) {
    asm volatile("global_load_dwordx4 %0, %2, %3 sc1\n\tglobal_load_dwordx4 %1, %2, %4 sc1\n\ts_waitcnt vmcnt(0)" : "=&v"(v[0]), "=&v"(v[1]) : "v"(off), "s"(ws), "s"(ws + S) : "memory");
}
template <u32 S> DI void pass(const char* ws, u32 off, u32 need, v4u (&v)[4]) {
    asm volatile(GL_(0, d0, b0, o0) GL_(1, d1, b0, o1) GL_(2, d2, b0, o2) GL_(3, d3, b0, o3) "s_waitcnt vmcnt(0)"
        : [d0] "+v"(v[0]), [d1] "+v"(v[1]), [d2] "+v"(v[2]), [d3] "+v"(v[3])
        : [off] "v"(off), [b0] "s"(ws), [need] "s"(need), [o0] "i"(0), [o1] "i"(S), [o2] "i"(2 * S), [o3] "i"(3 * S) : "memory", "scc");
}
template <u32 S> DI void pass(const char* ws, u32 off, u32 need, v4u (&v)[8]) {
    asm volatile(GL_(0, d0, b0, o0) GL_(1, d1, b0, o1) GL_(2, d2, b0, o2) GL_(3, d3, b0, o3) GL_(4, d4, b1, o0) GL_(5, d5, b1, o1) GL_(6, d6, b1, o2) GL_(7, d7, b1, o3) "s_waitcnt vmcnt(0)"
        : [d0] "+v"(v[0]), [d1] "+v"(v[1]), [d2] "+v"(v[2]), [d3] "+v"(v[3]), [d4] "+v"(v[4]), [d5] "+v"(v[5]), [d6] "+v"(v[6]), [d7] "+v"(v[7])
        : [off] "v"(off), [b0] "s"(ws), [b1] "s"(ws + 4 * S), [need] "s"(need), [o0] "i"(0), [o1] "i"(S), [o2] "i"(2 * S), [o3] "i"(3 * S) : "memory", "scc");
}
template <int N, u32 STRIDE, bool T32 = false, int SLP = POLL_SLEEP> DI void gather(const char* ws, u32 off, u32 tlo, u32 thi, v4u (&v)[N], int& budget) {
    u32 need = (1u << N) - 1u;
    if (N > 2) {
#pragma unroll
        for (int k = 0; k < N; ++k) asm volatile("" : "=v"(v[k]));
    }
#pragma clang loop unroll(disable)
    do {
        pass<STRIDE>(ws, off, need, v);
        need = 0u;
#pragma unroll
        for (int k = 0; k < N; ++k) if (__builtin_amdgcn_ballot_w64(T32 ? v[k].w != tlo : (((u64)v[k].w << 32 | v[k].z) != ((u64)thi << 32 | tlo))) != 0ull) need |= 1u << k;
        if (need == 0u) break;
        __builtin_amdgcn_s_sleep(SLP);
    } while (--budget > 0);
}
DI void lds_wait(u32 addr, u32 target, int& budget) {
#pragma clang loop unroll(disable)
    while (*(volatile LAS u32*)lp<u32>(addr) < target && --budget > 0) { __builtin_amdgcn_s_sleep(1); }
    asm volatile("" ::: "memory");
}
DI void lds_add(u32 addr, int lane) {
    u32 a, one;
    asm volatile("s_mov_b64 exec, 1\n\tv_mov_b32 %0, %2\n\tv_mov_b32 %1, 1\n\tds_add_u32 %0, %1\n\ts_mov_b64 exec, -1" : "=&v"(a), "=&v"(one) : "s"(addr) : "memory");
}
DI u32 lds_inc(u32 addr, int lane) {
    u32 old = 0;
    LGKM0();
    if (lane == 0) old = __hip_atomic_fetch_add(lp<u32>(addr), 1u, __ATOMIC_RELAXED, __HIP_MEMORY_SCOPE_WORKGROUP);
    return rfl(old);
}
#define SWAPBIT(NAME, PERM, MASK) \
DI void NAME(u32* a, u32* b) { \
    u32 n0, n1, n2; \
    asm volatile("s_mov_b64 vcc, %[nc]\n\ts_nop 0\n\t" \
        "v_cndmask_b32_dpp %[n0], %[b0], %[a0], vcc " PERM " row_mask:0xf bank_mask:0xf\n\t" \
        "v_cndmask_b32_dpp %[n1], %[b1], %[a1], vcc " PERM " row_mask:0xf bank_mask:0xf\n\t" \
        "v_cndmask_b32_dpp %[n2], %[b2], %[a2], vcc " PERM " row_mask:0xf bank_mask:0xf\n\t" \
        "s_mov_b64 vcc, %[c]\n\t" \
        "v_cndmask_b32_dpp %[b0], %[a0], %[b0], vcc " PERM " row_mask:0xf bank_mask:0xf\n\t" \
        "v_cndmask_b32_dpp %[b1], %[a1], %[b1], vcc " PERM " row_mask:0xf bank_mask:0xf\n\t" \
        "v_cndmask_b32_dpp %[b2], %[a2], %[b2], vcc " PERM " row_mask:0xf bank_mask:0xf\n\ts_nop 1" \
        : [n0] "=&v"(n0), [n1] "=&v"(n1), [n2] "=&v"(n2), [b0] "+v"(b[0]), [b1] "+v"(b[1]), [b2] "+v"(b[2]) \
        : [a0] "v"(a[0]), [a1] "v"(a[1]), [a2] "v"(a[2]), [c] "s"(MASK), [nc] "s"(~(MASK)) : "vcc"); \
    a[0] = n0; a[1] = n1; a[2] = n2; \
}
SWAPBIT(swap_l0, "quad_perm:[1,0,3,2]", 0xAAAAAAAAAAAAAAAAull)
SWAPBIT(swap_l1, "quad_perm:[2,3,0,1]", 0xCCCCCCCCCCCCCCCCull)
template <int K> DI u32 hint_wait(const char* ws, u32 oh, u32 tlo, int& budget) {
    v4u h[1];
#pragma clang loop unroll(disable)
    do {
        pass<0>(ws, oh, 1u, h);
        if (__builtin_popcount((u32)__builtin_amdgcn_ballot_w64(h[0].z == tlo) & 0xffu) >= K) break;
        __builtin_amdgcn_s_sleep(HINT_SLEEP);
    } while (--budget > 0);
    const u32 now = (u32)__builtin_amdgcn_s_memrealtime();
    u32 age = h[0].z == tlo ? now - h[0].x : 0xffffu;
    age = age > 0xffffu ? 0u : age;
    age = min(age, (u32)__builtin_amdgcn_update_dpp(0, (int)age, 0xB1, 0xf, 0xf, true));
    age = min(age, (u32)__builtin_amdgcn_update_dpp(0, (int)age, 0x4E, 0xf, 0xf, true));
    age = min(age, (u32)__builtin_amdgcn_update_dpp(0, (int)age, 0x124, 0xf, 0xf, true));
    return now - rfl(age);
}
typedef u32 v16u __attribute__((ext_vector_type(16)));
DI u32 time_base(const char* p, u32 t0) {
    v16u h;
    asm volatile("s_load_dwordx16 %0, %1, 0x0\n\ts_waitcnt lgkmcnt(0)" : "=s"(h) : "s"(p) : "memory");
    u32 m = 0, bad = 0;
#pragma unroll
    for (int k = 0; k < 8; ++k) { m = max(m, h[2 * k] - t0 + 150u); bad |= h[2 * k] ^ ~h[2 * k + 1]; }
    return (bad != 0u || m >= 300u) ? t0 + SKEW_MAX : t0 - 150u + m;
}
DI void until_abs(u32 t) { while ((int)((u32)__builtin_amdgcn_s_memrealtime() - t) < 0) __builtin_amdgcn_s_sleep(1); }
DI void until32(u32 t, u32 ticks) { while ((u32)__builtin_amdgcn_s_memrealtime() + 1u - t < ticks) __builtin_amdgcn_s_sleep(1); }
DI void until(u64 t0, u32 ticks) { while (__builtin_amdgcn_s_memrealtime() - t0 < (u64)ticks) __builtin_amdgcn_s_sleep(2); }
DI u32 swz(u32 row) { return ((row & 3u) << 2) | ((row >> 2) & 3u); }

#define EPOCH() do { asm volatile("s_load_dwordx2 %0, %1, 0x0\n\ts_waitcnt lgkmcnt(0)" : "=s"(e64) : "s"(ws) : "memory"); ++e64; } while (0)
#define TAGS() EPOCH(); const u32 tlo = (u32)e64, thi = (u32)(e64 >> 32)
DI void wave_sum2(float s1, float s2, float& t1, float& t2) {
    float z = red32(s1, s2);
    z = red16(z, z); z = rowsum(z);
    u32 a, b;
    both32(asu(z), a, b);
    t1 = asf(a); t2 = asf(b);
}
DI void ln_stats(const v4u& xa, float& mean, float& rstd) {
    const float pil = asf(rfl(xa[0]) << 16);
    float s1 = 0.f, s2 = 0.f;
#pragma unroll
    for (int k = 0; k < 4; ++k) { const float a = bflo(xa[k]) - pil, c = bfhi(xa[k]) - pil; s1 += a; s2 = __builtin_fmaf(a, a, s2); s1 += c; s2 = __builtin_fmaf(c, c, s2); }
    float t1, t2;
    wave_sum2(s1, s2, t1, t2);
    const float dm = t1 * (1.f / 512.f);
    mean = pil + dm;
    rstd = __builtin_amdgcn_rsqf(__builtin_fmaxf(t2 * (1.f / 512.f) - dm * dm, 0.f) + 1e-5f);
}
DI float sigm(float v) { return __builtin_amdgcn_rcpf(1.f + ex2(v * -1.4426950408889634f)); }
DI float silu(float v) { return v * sigm(v); }

template <int NW> DI f32x2 reduce16(const char* ws, u32 base, u32 tlo, u32 thi, int wave, int lane, u32 lds_red, int& budget) {
    float z = 0.f;
    u32 la = lds_red + (u32)(wave * 16 + (lane & 7) * 2 + (lane >> 5)) * 4, ra = lds_red + (u32)lane * 8;
    PIN(la); PIN(ra);
    if (wave < NW) {
        v4u g[1];
        gather<1, 0, false, 1>(ws, base + (u32)(wave * 64 + lane) * 16u, tlo, thi, g, budget);
        z = red32(asf(g[0].x), asf(g[0].y));
        z = red16(z, z); z += dpp<0x128>(z);
    }
    *lp<float>(la) = z;
    barrier();
    f32x2 s = {0.f, 0.f};
    if (wave == 0) {
        s = *lp<f32x2>(ra) + *lp<f32x2>(ra + 512);
        float t = red32(s.x, s.y);
        t = red16(t, t); t += dpp<0x128>(t);
        u32 a, c;
        both32(asu(t), a, c);
        s = (f32x2){asf(a), asf(c)};
    }
    return s;
}

#define QOFF(s, hg) ((u32)(((((s) >> 2) + 1) & 3) * 8192 + (hg) * 4096))
#define KEPT0(s) ((s) < 8 || (s) >= 12)
#define KEPT1(s) ((s) < 4 || (s) >= 12)
DI void qk_first(const v4u (&kr)[16], bf16x8 (&q0)[16], bf16x8 (&q1)[16], const u32 (&bq)[4], f32x4& s0, f32x4& s1, int lane, bool young) {
#pragma unroll
    for (int i = 0; i < 4; ++i) { s0[i] = 0.f; s1[i] = 0.f; }
#pragma unroll
    for (int s = 0; s < 4; ++s) { q0[s] = *lp<bf16x8>(bq[s & 3] + QOFF(s, 0)); q1[s] = *lp<bf16x8>(bq[s & 3] + QOFF(s, 1)); }
    lds_add(C_E0, lane);
    if (young) __builtin_amdgcn_s_setprio(PRIO_Y);
#pragma unroll
    for (int s = 0; s < 16; ++s) {
        if (s >= 4 - QD && s < 16 - QD) { q0[s + QD] = *lp<bf16x8>(bq[(s + QD) & 3] + QOFF(s + QD, 0)); q1[s + QD] = *lp<bf16x8>(bq[(s + QD) & 3] + QOFF(s + QD, 1)); }
        __builtin_amdgcn_sched_barrier(0);
        s0 = __builtin_amdgcn_mfma_f32_16x16x32_bf16(__builtin_bit_cast(bf16x8, kr[s]), q0[s], s0, 0, 0, 0);
        s1 = __builtin_amdgcn_mfma_f32_16x16x32_bf16(__builtin_bit_cast(bf16x8, kr[s]), q1[s], s1, 0, 0, 0);
        __builtin_amdgcn_sched_barrier(0);
        if (s == 5) { lds_add(C_E, lane); __builtin_amdgcn_sched_barrier(0); }
        if (s == 13) { lds_add(C_M, lane); __builtin_amdgcn_sched_barrier(0); }
    }
}
template <int S0, int S1> DI void qk_second(bf16x8 (&q0)[16], bf16x8 (&q1)[16], bf16x8 (&fa)[4], u32 ak, const u32 (&bq)[4], f32x4& s0, f32x4& s1) {
    PIN(ak);
    if (S0 == 0) {
#pragma unroll
        for (int i = 0; i < 4; ++i) { s0[i] = 0.f; s1[i] = 0.f; }
#pragma unroll
        for (int s = 0; s < 3; ++s) fa[s] = *lp<bf16x8>(ak ^ (u32)(s << 4));
    }
#pragma unroll
    for (int s = S0; s < S1; ++s) {
        if (s < 13) {
            fa[(s + 3) & 3] = *lp<bf16x8>(ak ^ (u32)((s + 3) << 4));
            if (!KEPT0(s + 3)) q0[s + 3] = *lp<bf16x8>(bq[(s + 3) & 3] + QOFF(s + 3, 0));
            if (!KEPT1(s + 3)) q1[s + 3] = *lp<bf16x8>(bq[(s + 3) & 3] + QOFF(s + 3, 1));
        }
        __builtin_amdgcn_sched_barrier(0);
        s0 = __builtin_amdgcn_mfma_f32_16x16x32_bf16(fa[s & 3], q0[s], s0, 0, 0, 0);
        s1 = __builtin_amdgcn_mfma_f32_16x16x32_bf16(fa[s & 3], q1[s], s1, 0, 0, 0);
        __builtin_amdgcn_sched_barrier(0);
    }
}
DI bf16x8 pshift(bf16x8 p, u32 d) {
    v4u x = __builtin_bit_cast(v4u, p);
#pragma unroll
    for (int k = 0; k < 4; ++k) { u32 r; asm("v_pk_sub_u16 %0, %1, %2 op_sel_hi:[1,0] clamp" : "=v"(r) : "v"(x[k]), "v"(d)); x[k] = r; }
    return __builtin_bit_cast(bf16x8, x);
}
DI u32 shift_of(float r, float n) { return (u32)__builtin_fminf(r - n, 255.f) << 7; }
DI float tile_max(const f32x4& s0, const f32x4& s1) {
    auto r = __builtin_amdgcn_permlane16_swap(asu(max3(max3(s0.x, s0.y, s0.z), s0.w, s0.x)), asu(max3(max3(s1.x, s1.y, s1.z), s1.w, s1.x)), false, false);
    u32 e, o;
    both32(asu(__builtin_fmaxf(asf((u32)r[0]), asf((u32)r[1]))), e, o);
    return __builtin_ceilf(__builtin_fmaxf(asf(e), asf(o)));
}
DI float tile_exp(const f32x4& s0, const f32x4& s1, u32 pa) {
    float p[8];
#pragma unroll
    for (int k = 0; k < 4; ++k) p[k] = ex2(s0[k]);
    *lp<v2u>(pa) = (v2u){pack2(p[0], p[1]), pack2(p[2], p[3])};
#pragma unroll
    for (int k = 0; k < 4; ++k) p[4 + k] = ex2(s1[k]);
    *lp<v2u>(pa + 4096) = (v2u){pack2(p[4], p[5]), pack2(p[6], p[7])};
    return red16((p[0] + p[1]) + (p[2] + p[3]), (p[4] + p[5]) + (p[6] + p[7]));
}
DI void tile_softmax(const f32x4& s0, const f32x4& s1, u32 pa, u32 mpa, float& ref, float& l) {
    const float a = max3(max3(s0.x, s0.y, s0.z), s0.w, s0.x), b = max3(max3(s1.x, s1.y, s1.z), s1.w, s1.x);
    if (__builtin_expect(__builtin_amdgcn_ballot_w64(!(__builtin_fmaxf(__builtin_fabsf(a), __builtin_fabsf(b)) <= LAZY)) != 0ull, 0)) {
        ref = tile_max(s0, s1);
        u32 e, o;
        both16(asu(ref), e, o);
        l = tile_exp(s0 - asf(e), s1 - asf(o), pa);
    } else {
        ref = 0.f;
        l = tile_exp(s0, s1, pa);
    }
    asm volatile("" ::: "memory");
    *(volatile LAS float*)lp<float>(mpa) = ref;
}

DI u32 mlp_id(int b) { const int xc = b & 7, yy = (b >> 3) - 16; return (yy < 12 || xc < 4) ? (u32)(((yy >> 2) * 8 + xc) * 4 + (yy & 3)) : (u32)(xc * 4 + (yy & 3)); }
DI void mlp_part(const Params& P, int b, int wave, int lane, v4u (&wgu)[4], u32 wo, u64 t0, u32 tlo, u32 thi, int& budget, u32& tl) {
    char* const ws = P.ws;
    const bool mw_ = (wave >> 2) == 2;
    const u32 gu_id = mlp_id(b), w = (u32)wave & 3, i15 = lane & 15, g = lane >> 4;
    barrier();
    STAMP(1);
    until(__builtin_amdgcn_s_memrealtime(), G_DELAY);
    wgu[2] = ld_nt(P.wu, wo); wgu[3] = ld_nt(P.wu + 1792, wo);
    STAMP(2);
    u32 wdr[16];
#pragma unroll
    for (int j = 0; j < 16; ++j) wdr[j] = 0u;
    if (mw_) {
#pragma unroll
        for (int j = 0; j < 16; ++j) wdr[j] = ld_u32_nt(P.wd, (gu_id * 16 + j) * 1024u + (64 * w + (u32)lane) * 4u);
        STAMP(3);
        VMWAIT(0);
#pragma unroll
        for (int j = 0; j < 16; j += 2) {
            PIN(wdr[j]); PIN(wdr[j + 1]);
            const u32 a = wdr[j], bb = wdr[j + 1];
            wdr[j] = perm_lo(a, bb); wdr[j + 1] = perm_hi(a, bb);
            PIN(wdr[j]); PIN(wdr[j + 1]);
        }
    }
    VMWAIT(0);
    PIN(wgu[0]); PIN(wgu[1]); PIN(wgu[2]); PIN(wgu[3]);
    STAMP(8);
    {
        const u32 a = L_W + (u32)((wave & 7) * 64 + (lane >> 1) * 2) * 64u + (u32)(lane & 1) * 16u;
        *lp<v4u>(a) = wgu[0]; *lp<v4u>(a + 64) = wgu[1]; *lp<v4u>(a + 32) = wgu[2]; *lp<v4u>(a + 96) = wgu[3];
    }
    barrier();
    STAMP(9);
    if (!mw_) return;
    bf16x8 bg[4], bp[4];
    {
        const u32 a = L_W + (128 * w + 8 * g + (i15 >> 2)) * 64 + (lane & 3) * 8;
#pragma unroll
        for (int s = 0; s < 4; ++s) {
            const s16x4 x0 = __builtin_amdgcn_ds_read_tr16_b64_v4i16(lp<s16x4>(a + s * 2048)), x1 = __builtin_amdgcn_ds_read_tr16_b64_v4i16(lp<s16x4>(a + s * 2048 + 256));
            const s16x4 x2 = __builtin_amdgcn_ds_read_tr16_b64_v4i16(lp<s16x4>(a + s * 2048 + 32)), x3 = __builtin_amdgcn_ds_read_tr16_b64_v4i16(lp<s16x4>(a + s * 2048 + 288));
            bg[s] = __builtin_shufflevector(x0, x1, 0, 1, 2, 3, 4, 5, 6, 7); bp[s] = __builtin_shufflevector(x2, x3, 0, 1, 2, 3, 4, 5, 6, 7);
        }
    }
    u32 ha = L_H2 + w * 256 + (u32)lane * 4, hr = L_H2 + w * 256 + g * 16, ga = L_GU + w * 128 + (u32)(lane & 31) * 4, gr = L_GU + (u32)lane * 4;
    u32 so = piece(WS_YP, 16384, 8 * w + (u32)(lane >> 3)) + (gu_id * 8 + (lane & 7)) * 16;
    v4u mg[1];
    gather<1, 0>(ws, WS_SC + (64 * w + (u32)lane) * 16u, tlo, thi, mg, budget);
    float cb0 = 1.f + bflo(mg[0].x), cb1 = 1.f + bfhi(mg[0].x), sb0 = bflo(mg[0].y), sb1 = bfhi(mg[0].y);
    PIN(sb0); PIN(sb1); PIN(cb0); PIN(cb1); PIN(ha); PIN(hr); PIN(ga); PIN(gr); PIN(so);
#pragma unroll
    for (int s = 0; s < 4; ++s) { PIN(bg[s]); PIN(bp[s]); }
    STAMP(12);
    v4u xg[4];
    gather<4, 1024, false, 1>(ws, piece(WS_X1, 4096, (u32)(b & 7)) + (u32)lane * 16u, tlo, thi, xg, budget);
    STAMP(13);
    const float pil = asf(rfl(xg[0].x));
    float s1 = 0.f, s2 = 0.f;
#pragma unroll
    for (int k = 0; k < 4; ++k) { const float a = asf(xg[k].x) - pil, c = asf(xg[k].y) - pil; s1 += a; s2 = __builtin_fmaf(a, a, s2); s1 += c; s2 = __builtin_fmaf(c, c, s2); }
    float t1, t2;
    wave_sum2(s1, s2, t1, t2);
    const float dm = t1 * (1.f / 512.f), mean = pil + dm;
    const float rstd = __builtin_amdgcn_rsqf(__builtin_fmaxf(t2 * (1.f / 512.f) - dm * dm, 0.f) + 1e-5f);
    const u32 xa = w == 0 ? xg[0].x : w == 1 ? xg[1].x : w == 2 ? xg[2].x : xg[3].x, xb = w == 0 ? xg[0].y : w == 1 ? xg[1].y : w == 2 ? xg[2].y : xg[3].y;
    *lp<u32>(ha) = pack2((asf(xa) - mean) * cb0 * rstd + sb0, (asf(xb) - mean) * cb1 * rstd + sb1);
    asm volatile("" ::: "memory");
    f32x4 dg = {0.f, 0.f, 0.f, 0.f}, du = {0.f, 0.f, 0.f, 0.f};
    bf16x8 af[4];
#pragma unroll
    for (int s = 0; s < 4; ++s) af[s] = *lp<bf16x8>(hr + s * 64);
    __builtin_amdgcn_sched_barrier(0);
#pragma unroll
    for (int s = 0; s < 4; ++s) {
        dg = __builtin_amdgcn_mfma_f32_16x16x32_bf16(af[s], bg[s], dg, 0, 0, 0);
        du = __builtin_amdgcn_mfma_f32_16x16x32_bf16(af[s], bp[s], du, 0, 0, 0);
    }
    *lp<float>(ga) = (lane & 16) ? du.x : dg.x;
    barrier();
    STAMP(11);
    float s = *lp<float>(gr) + *lp<float>(gr + 256);
    s = red32(s, s);
    u32 gs, us;
    both16(asu(s), gs, us);
    const float gsum = asf(gs), usum = asf(us);
    const float z = 1.5957691216057308f * (gsum + 0.044715f * gsum * gsum * gsum);
    const float act = gsum * usum * sigm(z);
    const u32 actp = pack2(act, dpp<0x101>(act));
    float y0 = 0.f, y1 = 0.f;
#pragma unroll
    for (int j = 0; j < 16; j += 2) { const u32 ap = (u32)__builtin_amdgcn_readlane((int)actp, j); y0 = dot2(wdr[j], ap, y0); y1 = dot2(wdr[j + 1], ap, y1); }
    st_sc(ws, so, (v4u){asu(y0), asu(y1), tlo, thi});
    STAMP(14);
}

DI void stream_pv(const Params& P, int b, int wave, int lane, u64 t0, int& budget, u32& tl) {
    char* const ws = P.ws;
    const int xc = b & 7, yy = (b >> 3) - 16, sidx = yy * 8 + xc;
    const int l31 = lane & 31, hf = lane >> 5;
    const char* ckv = (const char*)P.ckv + (size_t)sidx * 262144;
#define TILE(t) (ckv + (t) * 16384)
    const u32 r0 = 2u * (u32)wave;
    const u32 oa = r0 * 1024u + (((u32)lane ^ swz(r0)) << 4), ob = (r0 + 1) * 1024u + (((u32)lane ^ swz(r0 + 1)) << 4);
#define ISSUE(t) do { const char* tb_ = TILE(t); dma_nt(tb_, oa, L_CKV + ((t) & 7) * 16384u + r0 * 1024u); dma_nt(tb_, ob, L_CKV + ((t) & 7) * 16384u + r0 * 1024u + 1024u); } while (0)
    const u32 jc = (u32)wave & 3, half = (u32)wave >> 2;
    const u32 blk = (lane >> 4) & 1, q4 = (lane & 15) >> 2, p4 = lane & 3;
    const u32 ch = 16 * jc + 8 * half + 2 * blk + (p4 >> 1);
    const u32 k0 = 8 * hf + q4, k1 = k0 + 4;
    u32 a0 = L_CKV + k0 * 1024 + ((ch ^ swz(k0)) << 4) + 8 * (p4 & 1), a1 = L_CKV + k1 * 1024 + ((ch ^ swz(k1)) << 4) + 8 * (p4 & 1);
#define VREAD(t, d0, d1) do { \
        if ((t) == 4) { a0 ^= 65536u; a1 ^= 65536u; } \
        if (((t) & 3) == 0) { PIN(a0); PIN(a1); } \
        const u32 a2 = a0 ^ 64u, a3 = a1 ^ 64u; \
        const s16x4 x0_ = __builtin_amdgcn_ds_read_tr16_b64_v4i16(lp<s16x4>(a0 + ((t) & 3) * 16384)), x1_ = __builtin_amdgcn_ds_read_tr16_b64_v4i16(lp<s16x4>(a1 + ((t) & 3) * 16384)); \
        const s16x4 x2_ = __builtin_amdgcn_ds_read_tr16_b64_v4i16(lp<s16x4>(a2 + ((t) & 3) * 16384)), x3_ = __builtin_amdgcn_ds_read_tr16_b64_v4i16(lp<s16x4>(a3 + ((t) & 3) * 16384)); \
        d0 = __builtin_shufflevector(x0_, x1_, 0, 1, 2, 3, 4, 5, 6, 7); d1 = __builtin_shufflevector(x2_, x3_, 0, 1, 2, 3, 4, 5, 6, 7); } while (0)
#define VREAD2(t, d0, d1) do { \
        const u32 b0_ = a0 + ((t) & 4) * 16384u, b1_ = (t) == 7 ? a7 - 3 * 16384u : a1 + ((t) & 4) * 16384u, b2_ = b0_ ^ 64u, b3_ = b1_ ^ 64u; \
        const s16x4 x0_ = __builtin_amdgcn_ds_read_tr16_b64_v4i16(lp<s16x4>(b0_ + ((t) & 3) * 16384)), x1_ = __builtin_amdgcn_ds_read_tr16_b64_v4i16(lp<s16x4>(b1_ + ((t) & 3) * 16384)); \
        const s16x4 x2_ = __builtin_amdgcn_ds_read_tr16_b64_v4i16(lp<s16x4>(b2_ + ((t) & 3) * 16384)), x3_ = __builtin_amdgcn_ds_read_tr16_b64_v4i16(lp<s16x4>(b3_ + ((t) & 3) * 16384)); \
        d0 = __builtin_shufflevector(x0_, x1_, 0, 1, 2, 3, 4, 5, 6, 7); d1 = __builtin_shufflevector(x2_, x3_, 0, 1, 2, 3, 4, 5, 6, 7); } while (0)
    bf16x8 va[8], vb[8];
#pragma unroll
    for (int t = 0; t < 8; ++t) {
        ISSUE(t + DEPTH);
        asm volatile("s_waitcnt vmcnt(%0)" :: "i"(2 * (upto(t) - t)) : "memory");
        barrier();
        if (t == 1) STAMP(18);
        if (t == 4) STAMP(19);
        VREAD(t, va[t], vb[t]);
    }
    a0 ^= 65536u; a1 ^= 65536u;
    PIN(a0); PIN(a1);
    u64 e64;
    TAGS();
    barrier();
    STAMP(1);
    *lp<u32>(L_RF + (u32)(wave * 64 + lane) * 4) = NOREF;
    if (wave == 0) *lp<u32>(L_CNT + (u32)lane * 4) = 0u;
    barrier();
    if (half == 0) {
        VMWAIT(4); lds_add(C_T, lane); VMWAIT(2); lds_add(C_T + 4, lane); VMWAIT(0); lds_add(C_T + 8, lane);
    } else {
        const u32 r4 = 4 * jc, thold = rfl(*lp<u32>(C_TB)) + (u32)(Q_FIRST + P_SKEW);
        u32 oc = r4 * 1024u + (((u32)lane ^ jc) << 4);
        PIN(oc);
#define ISSUE4(t) do { if ((int)((u32)__builtin_amdgcn_s_memrealtime() - thold) >= 0) lds_wait(HOLD, 4u, budget); _Pragma("unroll") for (int k = 0; k < 4; ++k) dma_nt(TILE(t) + k * 1024, oc ^ (u32)(k << 6), L_CKV + ((t) & 7) * 16384u + (r4 + k) * 1024u); } while (0)
        ISSUE4(11); VMWAIT(8); lds_add(C_T, lane);
        ISSUE4(12); VMWAIT(10); lds_add(C_T + 4, lane);
        ISSUE4(13); VMWAIT(12); lds_add(C_T + 8, lane);
        ISSUE4(14); VMWAIT(12); lds_add(C_T + 12, lane);
        if (jc < 3) { ISSUE4(15); VMWAIT(12); lds_add(C_T + 16, lane); VMWAIT(8); lds_add(C_T + 20, lane); VMWAIT(4); lds_add(C_T + 24, lane); }
        else {
            dma_nt(TILE(15), oc, L_CKV + 7 * 16384u + 12 * 1024u);
            VMWAIT(9); lds_add(C_T + 16, lane);
            lds_wait(C_E, 8u, budget);
#pragma unroll
            for (int k = 1; k < 4; ++k) dma_nt(TILE(15) + k * 1024, oc ^ (u32)(k << 6), L_REL + (k - 1) * 1024u);
            VMWAIT(8); lds_add(C_T + 20, lane); VMWAIT(4); lds_add(C_T + 24, lane);
        }
        VMWAIT(0); lds_add(C_T + 28, lane);
        STAMP(8);
    }
    u32 pbase = L_P + l31 * 256 + ((hf ^ (l31 & 15)) << 4), ra = L_RF + l31 * 8, a7 = k1 >= 13 ? L_REL - 13 * 1024 + a1 : a1 + 7 * 16384;
    PIN(pbase); PIN(ra); PIN(a7);
    if (half == 0) __builtin_amdgcn_s_setprio(PRIO_P0); else __builtin_amdgcn_s_setprio(PRIO_P1);
    CYC0();
    float mrow = 0.f;
    f32x16 o0, o1;
#pragma unroll
    for (int i = 0; i < 16; ++i) { o0[i] = 0.f; o1[i] = 0.f; }
    bf16x8 pb[4];
    v2u rf;
#define LLOAD(k) do { \
        rf = *(volatile LAS v2u*)lp<v2u>(ra + 256 * (k)); \
        asm volatile("" ::: "memory"); \
        pb[(2 * (k)) & 3] = *lp<bf16x8>((pbase ^ (u32)(64 * ((k) & 3))) - ((k) >> 2) * (L_P - L_P2)); pb[(2 * (k) + 1) & 3] = *lp<bf16x8>((pbase ^ (u32)(64 * ((k) & 3) + 32)) - ((k) >> 2) * (L_P - L_P2)); \
        if ((k) >= 4) { VREAD2(2 * (k) - 8, va[(2 * (k)) & 7], vb[(2 * (k)) & 7]); VREAD2(2 * (k) - 7, va[(2 * (k) + 1) & 7], vb[(2 * (k) + 1) & 7]); } } while (0)
    bool moved = false;
    u32 g1, g2, ho;
    LLOAD(0);
#pragma unroll
    for (int k = 0; k < 8; ++k) {
        if (__builtin_amdgcn_ballot_w64(rf.x != asu(mrow) || rf.y != asu(mrow)) != 0ull) {
#pragma clang loop unroll(disable)
            while (__builtin_amdgcn_ballot_w64(rf.x == NOREF || rf.y == NOREF) != 0ull && --budget > 0) {
                __builtin_amdgcn_s_sleep(1);
                rf = *(volatile LAS v2u*)lp<v2u>(ra + 256 * k);
            }
            LLOAD(k);
            if (__builtin_expect(__builtin_amdgcn_ballot_w64(rf.x != asu(mrow) || rf.y != asu(mrow)) != 0ull, 0)) {
                if (k == 0) mrow = __builtin_fmaxf(asf(rf.x), asf(rf.y));
                else {
                    const float m2 = max3(mrow, asf(rf.x), asf(rf.y)), al = ex2(mrow - m2);
                    o0 *= al; o1 *= al; mrow = m2;
                }
                moved = true;
                pb[(2 * k) & 3] = pshift(pb[(2 * k) & 3], shift_of(mrow, asf(rf.x)));
                pb[(2 * k + 1) & 3] = pshift(pb[(2 * k + 1) & 3], shift_of(mrow, asf(rf.y)));
            }
        }
        if (k == 0) STAMP(7);
        if (k < 7) LLOAD(k + 1);
        __builtin_amdgcn_sched_barrier(0);
        o0 = __builtin_amdgcn_mfma_f32_32x32x16_bf16(va[(2 * k) & 7], pb[(2 * k) & 3], o0, 0, 0, 0);
        o1 = __builtin_amdgcn_mfma_f32_32x32x16_bf16(vb[(2 * k) & 7], pb[(2 * k) & 3], o1, 0, 0, 0);
        if (k == 4) {
            u32 ln = (u32)lane; PIN(ln);
            const u32 base = WS_PAY + ((jc * 2 + half) * 128 + (u32)sidx) * PBLK + (ln & 28) * PHD + (ln & 3) * 16u;
            g1 = base + (ln >> 5) * 64u; g2 = base + (ln >> 5) * PHD + 128u;
            ho = WS_HC + (((u32)sidx + 17u * ln) & 127u) * HSTR + ln * 16u;
            PIN(g1); PIN(g2); PIN(ho);
        }
        o0 = __builtin_amdgcn_mfma_f32_32x32x16_bf16(va[(2 * k + 1) & 7], pb[(2 * k + 1) & 3], o0, 0, 0, 0);
        o1 = __builtin_amdgcn_mfma_f32_32x32x16_bf16(vb[(2 * k + 1) & 7], pb[(2 * k + 1) & 3], o1, 0, 0, 0);
        __builtin_amdgcn_sched_barrier(0);
        if (k == 3) STAMP(2);
    }
    PIN(lane);
    __builtin_amdgcn_s_setprio(0);
    if (wave == 0 && lane < 8) st_sc(ws, ho, (v4u){(u32)__builtin_amdgcn_s_memrealtime(), 0u, tlo, thi});
    STAMPC(28);
    {
        u32 d[18];
#pragma unroll
        for (int k = 0; k < 8; ++k) { d[k] = pack2(o0[2 * k], o0[2 * k + 1]); d[8 + k] = pack2(o1[2 * k], o1[2 * k + 1]); }
        d[16] = asu(mrow); d[17] = 0u;
        swap_l0(d, d + 3); swap_l0(d + 6, d + 9);
        swap_l1(d, d + 6);
        st_sc(ws, g1, (v4u){d[0], d[1], d[2], tlo});
        st_sc(ws + 2 * PHD, g1, (v4u){d[6], d[7], d[8], tlo});
        swap_l1(d + 3, d + 9);
        st_sc(ws + PHD, g1, (v4u){d[3], d[4], d[5], tlo});
        st_sc(ws + 3 * PHD, g1, (v4u){d[9], d[10], d[11], tlo});
        if (half) {
            float lrow;
#pragma clang loop unroll(disable)
            do {
                const u32 cnt = *(volatile LAS u32*)lp<u32>(C_L);
                f32x4 la = *(volatile LAS f32x4*)lp<f32x4>(L_LP + (ra - L_RF) * 4), lb = *(volatile LAS f32x4*)lp<f32x4>(L_LP + (ra - L_RF) * 4 + 16);
                if (__builtin_expect(moved, 0)) {
#pragma unroll
                    for (int k = 0; k < 4; ++k) {
                        la[k] *= ex2(__builtin_fmaxf(*lp<float>(ra + (k >> 1) * 256 + (k & 1) * 4), *lp<float>(ra + 1024 + (k >> 1) * 256 + (k & 1) * 4)) - mrow);
                        lb[k] *= ex2(__builtin_fmaxf(*lp<float>(ra + 512 + (k >> 1) * 256 + (k & 1) * 4), *lp<float>(ra + 1536 + (k >> 1) * 256 + (k & 1) * 4)) - mrow);
                    }
                }
                lrow = ((la.x + la.y) + (la.z + la.w)) + ((lb.x + lb.y) + (lb.z + lb.w));
                if (cnt >= 8u) break;
                __builtin_amdgcn_s_sleep(1);
            } while (--budget > 0);
            d[17] = pack2(lrow, lrow - bflo(pack2(lrow, 0.f)));
        }
        swap_l0(d + 12, d + 15);
#pragma unroll
        for (int k = 0; k < 3; ++k) { auto r = __builtin_amdgcn_permlane32_swap(d[12 + k], d[15 + k], false, false); d[12 + k] = (u32)r[0]; d[15 + k] = (u32)r[1]; }
        swap_l1(d + 12, d + 15);
        st_sc(ws, g2, (v4u){d[12], d[13], d[14], tlo});
        st_sc(ws + 2 * PHD, g2, (v4u){d[15], d[16], d[17], tlo});
    }
    STAMPC(29);
    STAMP(9);
}

DI void stream_qk(const Params& P, int b, int wave, int lane, u64 t0, u32 tlo, u32 thi, int& budget, u32& tl) {
    char* const ws = P.ws;
    const int xc = b & 7, yy = (b >> 3) - 16, w = wave - 8;
    const u32 i15 = lane & 15, g = lane >> 4;
    u32 ak = L_CKV + (u32)w * 16384u + i15 * 1024u + g * 256u + (swz(i15) << 4);
    u32 qo = WS_QABS + (u32)w * 2048u + g * 256u + ((i15 ^ g ^ ((w & 1) ? 12u : 0u)) << 4), co = WS_QCHK + (u32)w * 2048u + (u32)lane * 16u;
    PIN(qo); PIN(co);
    v4u chk0, chk1;
    asm volatile("" : "=v"(chk0), "=v"(chk1));
#define REQ_Q() do { \
        _Pragma("unroll") for (int k = 0; k < 8; ++k) dma_sc(ws + (k >> 1) * 8192 + (k & 1) * 1024, qo, L_IMG + (u32)w * 2048u + (k & 1) * 1024u + (((k >> 1) + 1) & 3) * 8192u); \
        ld_sc_into(chk0, ws, co); ld_sc_into(chk1, ws + 1024, co); } while (0)
    if (w < 4) __builtin_amdgcn_s_setprio(3);
    v4u kr[16];
    for (int t = 0; t <= w; ++t) barrier();
#pragma unroll
    for (int s = 0; s < 16; ++s) kr[s] = *lp<v4u>(ak ^ (u32)(s << 4));
    for (int t = w + 1; t <= 7; ++t) barrier();
    const u32 tb = time_base(ws + WS_ST + xc * 128, (u32)t0);
    VMWAIT(0);
    barrier();
    if (w == 0) *lp<u32>(C_TB) = tb;
    barrier();
    if (w < 4) {
        until_abs(tb + Q_FIRST);
        REQ_Q();
        lds_add(C_Q, lane);
        STAMP(20);
        u32 va = L_IMG + ((i15 + 1) & 3) * 8192u + (u32)w * 2048u + g * 256u + (((i15 >> 2) ^ ((w & 1) ? 3u : 0u)) << 6) + ((i15 & 3) << 4);
        PIN(va);
        VMWAIT(0);
        STAMP(4);
#pragma clang loop unroll(disable)
        do {
            PIN(chk0); PIN(chk1);
            const v4u x0 = *(volatile LAS v4u*)lp<v4u>(va), x1 = *(volatile LAS v4u*)lp<v4u>(va ^ 16u), x2 = *(volatile LAS v4u*)lp<v4u>(va ^ 32u), x3 = *(volatile LAS v4u*)lp<v4u>(va ^ 48u);
            const v4u y0 = *(volatile LAS v4u*)lp<v4u>(va + 1024), y1 = *(volatile LAS v4u*)lp<v4u>((va ^ 16u) + 1024), y2 = *(volatile LAS v4u*)lp<v4u>((va ^ 32u) + 1024), y3 = *(volatile LAS v4u*)lp<v4u>((va ^ 48u) + 1024);
            const v4u xs = (x0 + x1) + (x2 + x3), ys = (y0 + y1) + (y2 + y3);
            const u64 b0 = __builtin_amdgcn_ballot_w64(((((xs.x + xs.y) + (xs.z + xs.w)) ^ chk0.x) | (chk0.z ^ tlo) | (chk0.w ^ thi)) != 0u);
            const u64 b1 = __builtin_amdgcn_ballot_w64(((((ys.x + ys.y) + (ys.z + ys.w)) ^ chk1.x) | (chk1.z ^ tlo) | (chk1.w ^ thi)) != 0u);
            if ((b0 | b1) == 0ull) break;
            __builtin_amdgcn_s_sleep(QSLEEP);
#pragma unroll
            for (int k = 0; k < 8; ++k)
                if (((k & 1) ? b1 : b0) & (0x1111111111111111ull << (k >> 1))) dma_sc(ws + (k >> 1) * 8192 + (k & 1) * 1024, qo, L_IMG + (u32)w * 2048u + (k & 1) * 1024u + (((k >> 1) + 1) & 3) * 8192u);
            if (b0) ld_sc_into(chk0, ws, co);
            if (b1) ld_sc_into(chk1, ws + 1024, co);
            VMWAIT(0);
        } while (--budget > 0);
        lds_add(C_Z, lane);
        STAMP(5);
    }
    u32 bq[4];
#pragma unroll
    for (int k = 0; k < 4; ++k) { bq[k] = L_IMG + i15 * 256u + (((g * 4 + k) ^ (i15 & 3) ^ ((i15 & 8) ? 12u : 0u)) << 4); PIN(bq[k]); }
    __builtin_amdgcn_s_setprio(3);
    lds_wait(C_Z, 4u, budget);
    STAMP(6);
    CYC0();
    f32x4 s0, s1;
    bf16x8 q0[16], q1[16];
    qk_first(kr, q0, q1, bq, s0, s1, lane, w >= 4);
    STAMP(21);
    PIN(lane);
    const u32 mpa = L_RF + (u32)(w >> 1) * 256 + ((u32)lane & 31) * 8 + (u32)(w & 1) * 4;
    const u32 pa = L_P + ((u32)lane & 15) * 256 + (((2 * (u32)w + ((u32)lane >> 5)) ^ ((u32)lane & 15)) << 4) + (((u32)lane >> 4) & 1) * 8;
    float n1, l1, n2, l2;
    lds_wait(C_E0, 8u, budget);
    tile_softmax(s0, s1, pa, mpa, n1, l1);
    PIN(n1); PIN(l1);
    STAMP(24);
    STAMPC(29);
    if (w == 7 && (lane & 15) >= 13) ak += L_REL - (7 * 16384 + 13 * 1024);
    lds_wait(C_T + 4 * (u32)w, w < 3 ? 8u : 4u, budget);
    STAMP(25);
    bf16x8 fa[4];
    qk_second<0, 2>(q0, q1, fa, ak, bq, s0, s1);
    v4u wgu[4];
    PIN(lane);
    const u32 wo = (u32)w * (64u * 3584u) + mlp_id(b) * 32u + ((u32)lane >> 1) * 7168u + ((u32)lane & 1) * 16u;
    wgu[0] = ld_nt(P.wg, wo); wgu[1] = ld_nt(P.wg + 1792, wo);
    qk_second<2, 16>(q0, q1, fa, ak, bq, s0, s1);
    STAMP(26);
    lds_wait(C_M, 8u, budget);
    tile_softmax(s0, s1, pa - (L_P - L_P2), mpa + 1024, n2, l2);
    STAMP(27);
    __builtin_amdgcn_s_setprio(0);
    {
        const float nw = __builtin_fmaxf(n1, n2);
        float l = l1 * ex2(n1 - nw) + l2 * ex2(n2 - nw);
        l = red32(l, l);
        *lp<float>(L_LP + ((u32)lane & 31) * 32 + (u32)w * 4) = l;
        lds_add(C_L, lane);
    }
    if (yy < 12 || xc < 4) mlp_part(P, b, wave, lane, wgu, wo, t0, tlo, thi, budget, tl);
    else VMWAIT(0);
}

__global__ void __launch_bounds__(1024, 4) fused_kernel(Params P) {
    extern __shared__ __attribute__((aligned(16))) char lds_raw[];
    const int tid = threadIdx.x, lane = tid & 63;
    const int wave = __builtin_amdgcn_readfirstlane(tid >> 6);
    const int b = blockIdx.x, xc = b & 7, y = b >> 3;
    char* const ws = P.ws;
    int budget = BUDGET;
    asm volatile("" :: "v"(lds_raw));
    u32 tl = 0;
    u64 pc0;
    asm volatile("s_getpc_b64 %0" : "=s"(pc0));
    if (wave >= 8 && (y & 0x13) == 0x10) {
        asm volatile("s_mov_b32 m0, %1\n\ts_nop 0\n\tglobal_load_lds_dword %0, %2" :: "v"((u32)lane * 64u + (wave < 15 ? (u32)(wave - 8) * 4096u : (u32)CODE_LAST)), "s"(L_IMG), "s"(pc0) : "memory", "m0");
    }
    asm volatile("" :: "s"(P.ws), "s"(P.out) : "memory");
    const u64 t0 = __builtin_amdgcn_s_memrealtime();
    const int head = xc * 4 + (y >> 2), j = y & 3;
    const u32 i15 = lane & 15, g = lane >> 4;
    u64 e64 = 0;
    int role = y < 16 ? (wave < 2 ? 0 : wave < 10 ? 1 : 2) : (wave < 8 ? 3 : 2);
    {
        const u32 ks = b & 15, cg = (u32)(b >> 4) * 2 + ((u32)wave & 1), s = (u32)wave - 2;
        const u32 row0 = ks * 32 + 2 * (u32)(lane >> 2), p = lane & 3, m0 = (u32)j * 128 + s * 16 + g * 4;
        v4u wr[4], xa;
        u32 cc, xs;
#pragma unroll
        for (int k = 0; k < 4; ++k) asm volatile("" : "=v"(wr[k]));
        asm volatile("" : "=v"(xa), "=v"(cc), "=v"(xs));
        if (role == 0) {
            const u32 o = row0 * 6144u + cg * 64u + p * 16u;
            asm volatile("global_load_dword %0, %3, %4\n\tglobal_load_dwordx4 %1, %5, %6\n\tglobal_load_dword %2, %7, %6" : "+v"(cc), "+v"(xa), "+v"(xs)
                : "v"(row0 * 2), "s"(P.c), "v"((u32)lane * 16), "s"(P.x), "v"(cg * 64u + p * 16u + g * 4u) : "memory");
            ld_nt_into(wr[0], P.wmod, o); ld_nt_into(wr[1], P.wmod + 3072, o); ld_nt_into(wr[2], P.wmod + 512, o); ld_nt_into(wr[3], P.wmod + 3584, o);
            STAMP(20);
        }
        if (y < 16) {
            if (b < 8 && wave == 2 && lane < 8) st_sc2(ws, WS_ST + (u32)lane * 128u + (u32)b * 8u, (v2u){(u32)t0, ~(u32)t0});
            const u32 o = m0 * 8192u + (u32)head * 256u + i15 * 16u;
            __builtin_amdgcn_s_barrier();
            if (role == 1) {
#pragma unroll
                for (int k = 0; k < 4; ++k) ld_nt_into(wr[k], P.wq + k * 4096, o);
                STAMP(20);
            }
        }
    if (y >= 16 && wave < 8) {
        const char* ckv = (const char*)P.ckv + (size_t)(b - 128) * 262144;
        const u32 r0 = 2u * (u32)wave;
        const u32 oa = r0 * 1024u + (((u32)lane ^ swz(r0)) << 4), ob = (r0 + 1) * 1024u + (((u32)lane ^ swz(r0 + 1)) << 4);
#pragma unroll
        for (int t = 0; t < DEPTH; ++t) {
            if (t == D0 + 1) until(t0, T_OPEN);
            dma_nt(ckv + t * 16384, oa, L_CKV + t * 16384u + r0 * 1024u); dma_nt(ckv + t * 16384, ob, L_CKV + t * 16384u + r0 * 1024u + 1024u);
        }
    }
        asm volatile("" : "+s"(role));
        if (role == 0) {
            u32 so = piece(WS_HP, 2048, cg) + (ks * 8 + 2 * p + (u32)(lane >> 5)) * 16u;
            TAGS();
            VMWAIT(4);
            PIN(xa); PIN(xs); PIN(cc);
            float mean, rstd;
            ln_stats(xa, mean, rstd);
            const u32 sp = pack2(silu(bflo(cc)), silu(bfhi(cc)));
            u32 s0l = sp & 0xffffu, s0h = sp << 16, s1l = sp >> 16, s1h = sp & 0xffff0000u;
            float xn0 = (bflo(xs) - mean) * rstd, xn1 = (bfhi(xs) - mean) * rstd;
            PIN(xn0); PIN(xn1); PIN(s0l); PIN(s0h); PIN(s1l); PIN(s1h); PIN(so);
            VMWAIT(0);
#pragma unroll
            for (int k = 0; k < 4; ++k) PIN(wr[k]);
            STAMP(22);
            float ta[4], tc[4];
#pragma unroll
            for (int k = 0; k < 2; ++k) {
                ta[2 * k] = red32(dot2(wr[1][k], s1l, dot2(wr[0][k], s0l, 0.f)), dot2(wr[1][k + 2], s1l, dot2(wr[0][k + 2], s0l, 0.f)));
                ta[2 * k + 1] = red32(dot2(wr[1][k], s1h, dot2(wr[0][k], s0h, 0.f)), dot2(wr[1][k + 2], s1h, dot2(wr[0][k + 2], s0h, 0.f)));
                tc[2 * k] = red32(dot2(wr[3][k], s1l, dot2(wr[2][k], s0l, 0.f)), dot2(wr[3][k + 2], s1l, dot2(wr[2][k + 2], s0l, 0.f)));
                tc[2 * k + 1] = red32(dot2(wr[3][k], s1h, dot2(wr[2][k], s0h, 0.f)), dot2(wr[3][k + 2], s1h, dot2(wr[2][k + 2], s0h, 0.f)));
            }
            float u0 = xn0 * (red16(tc[0], tc[2]) + 0.015625f) + red16(ta[0], ta[2]), u1 = xn1 * (red16(tc[1], tc[3]) + 0.015625f) + red16(ta[1], ta[3]);
            u0 += dpp<0x124>(u0); u1 += dpp<0x124>(u1); u0 += dpp<0x128>(u0); u1 += dpp<0x128>(u1);
            u32 e, od;
            both16(pack2(u0, u1), e, od);
            if ((lane & 28) == 0) st_sc(ws, so, (v4u){e, od, tlo, thi});
            STAMP(24);
            barrier();
            barrier();
        }
        asm volatile("" : "+s"(role));
        if (role == 1) {
            TAGS();
            u32 so = piece(WS_QP, 2048, (u32)head) + ((u32)j * 32 + (u32)(lane >> 5) * 16 + i15) * 16u, ho = piece(WS_HP, 2048, (u32)j * 4 + (s >> 1)) + (i15 * 8 + (s & 1) * 4 + g) * 16u;
            PIN(so); PIN(ho);
            v4u hg[1];
            until(t0, ST_LOOK);
            const u32 tb = time_base(ws + WS_ST + xc * 128, (u32)t0);
            if (s == 0) *lp<u32>(F_TB) = tb;
            asm volatile("" : "=v"(hg[0]));
            until_abs(tb + A_FIRST);
            ld_sc_into(hg[0], ws, ho);
            barrier();
            VMWAIT(1);
#pragma unroll
            for (int k = 0; k < 4; ++k) PIN(wr[k]);
            STAMP(1);
            pair_rows(wr[0], wr[1]); pair_rows(wr[2], wr[3]);
#pragma unroll
            for (int k = 0; k < 4; ++k) PIN(wr[k]);
            u32 la = F_QP + s * 512 + lane * 8;
            PIN(la);
            VMWAIT(0);
            PIN(hg[0]);
            if (__builtin_amdgcn_ballot_w64(hg[0].z != tlo || hg[0].w != thi) != 0ull) gather<1, 0>(ws, ho, tlo, thi, hg, budget);
            STAMP(3);
            const u32 h01 = pack2(rowsum(bflo(hg[0].x)), rowsum(bfhi(hg[0].x))), h23 = pack2(rowsum(bflo(hg[0].y)), rowsum(bfhi(hg[0].y)));
            float acc[8];
#pragma unroll
            for (int k = 0; k < 4; ++k) { acc[2 * k] = dot2(wr[2][k], h23, dot2(wr[0][k], h01, 0.f)); acc[2 * k + 1] = dot2(wr[3][k], h23, dot2(wr[1][k], h01, 0.f)); }
            float t[4];
#pragma unroll
            for (int k = 0; k < 4; ++k) t[k] = red32(acc[k], acc[k + 4]);
            const float u0 = red16(t[0], t[2]), u1 = red16(t[1], t[3]);
            *lp<f32x2>(la) = (f32x2){u0, u1};
            barrier();
            if (s == 2) {
                const LAS f32x2* qp = lp<f32x2>(F_QP + lane * 8);
                const f32x2 q2 = ((qp[0] + qp[64]) + (qp[128] + qp[192])) + ((qp[256] + qp[320]) + (qp[384] + qp[448]));
                u32 e, od;
                both16(pack2(q2.x, q2.y), e, od);
                if ((lane & 16) == 0) st_sc(ws, so, (v4u){e, od, tlo, thi});
            }
            STAMP(2);
        }
    }
    asm volatile("" : "+s"(role));
    if (role == 2) EPOCH();
    const u32 tlo = (u32)e64, thi = (u32)(e64 >> 32);
    if (y >= 16) {
        if (wave < 8) stream_pv(P, b, wave, lane, t0, budget, tl);
        else stream_qk(P, b, wave, lane, t0, tlo, thi, budget, tl);
        STAMP(15);
        STAMP_OUT();
        return;
    }
    const bool is_red = (y >> 2) == 1;
    const u32 rid = (u32)(xc * 4 + j);
    if (wave >= 12) {
        const u32 w = (u32)y & 3, jk = (u32)y >> 2, hk = (u32)xc * 4 + ((u32)wave & 3);
        v4u wk[8];
        const u32 o = ((u32)y * 32 + g * 8) * 8192u + hk * 256u + i15 * 16u;
        u32 warm = ld_u32(P.wuk, (u32)y * 32 * 8192u);
        barrier();
#pragma unroll
        for (int k = 0; k < 8; ++k) wk[k] = ld_nt(P.wuk, o + k * 8192u);
        STAMP(20);
        VMWAIT(8);
        PIN(warm);
        const u32 oq = piece(WS_QP, 2048, hk) + (g * 32 + i15) * 16u;
        u32 so = WS_QABS + w * 8192u + hk * 256u + jk * 64u + g * 16u, sc = WS_QCHK + hk * 256u + (u32)y * 16u;
        PIN(so); PIN(sc);
        barrier();
        v4u qg[2];
        const u64 tb1 = __builtin_amdgcn_s_memrealtime();
        until(tb1, A2_DELAY);
        gather<2, 256>(ws, oq, tlo, thi, qg, budget);
        STAMP(23);
        const float t0_ = red32(bflo(qg[0].x), bflo(qg[1].x)), t1_ = red32(bfhi(qg[0].x), bfhi(qg[1].x));
        const float t2_ = red32(bflo(qg[0].y), bflo(qg[1].y)), t3_ = red32(bfhi(qg[0].y), bfhi(qg[1].y));
        u32 e, od, q0, q1, q2, q3;
        both16(pack2(red16(t0_, t2_), red16(t1_, t3_)), e, od);
        both32(e, q0, q2); both32(od, q1, q3);
        float a[8];
#pragma unroll
        for (int k = 0; k < 8; ++k) { PIN(wk[k]); a[k] = dot2(wk[k][3], q3, dot2(wk[k][2], q2, dot2(wk[k][1], q1, dot2(wk[k][0], q0, 0.f)))); }
        float z[4];
#pragma unroll
        for (int k = 0; k < 4; ++k) {
            const float keep = (lane & 1) ? a[k + 4] : a[k], give = (lane & 1) ? a[k] : a[k + 4];
            float s = keep + dpp<0xB1>(give);
            s += dpp<0x122>(s); s += dpp<0x124>(s); s += dpp<0x128>(s);
            z[k] = s * (0.08838834764831845f * 1.4426950408889634f);
        }
        const u32 y0 = pack2(z[0], z[1]), y1 = pack2(z[2], z[3]);
        const u32 y2 = (u32)__builtin_amdgcn_update_dpp(0, (int)y0, 0xB1, 0xf, 0xf, true), y3 = (u32)__builtin_amdgcn_update_dpp(0, (int)y1, 0xB1, 0xf, 0xf, true);
        if (i15 == 0) st_sc(ws, so, (v4u){y0, y1, y2, y3});
        u32 c0, c1;
        both16((y0 + y1) + (y2 + y3), c0, c1);
        both32(c0 + c1, c0, c1);
        if (lane == 0) st_sc(ws, sc, (v4u){c0 + c1, 0u, tlo, thi});
        STAMP(4);
    } else if (wave >= 10) {
        { u32 w0 = ld_u32(P.wo, (u32)head * 131072u), w1 = ld_u32(P.wuv, (u32)j * 1048576u); VMWAIT(0); PIN(w0); PIN(w1); }
        barrier();
        barrier();
    }
#define WO_ROW(r) dma_nt(P.wo, ((u32)head * 128 + (r)) * 1024u + (((u32)lane ^ swz((r) & 15)) << 4), F_WO + (r) * 1024u)
    const u32 tb = rfl(*lp<u32>(F_TB));
    barrier();
    WO_ROW((u32)j * 32u + (u32)wave * 2); WO_ROW((u32)j * 32u + (u32)wave * 2 + 1);
    VMWAIT(0);
    STAMP(5);
    bf16x8 bo[8], bu[4];
    {
        const u32 kq = (u32)b & 3, cgl = (u32)b >> 2;
        const u32 mrow = kq * 128 + (u32)wave * 8 + (u32)(lane >> 3);
        until_abs(tb + T_MID);
#pragma unroll
        for (int k = 2; k < 8; ++k) WO_ROW((((u32)(k >> 1) + (u32)j) & 3u) * 32u + (u32)wave * 2 + (k & 1));
        STAMP(19);
        barrier();
        until_abs(tb + T_START);
#pragma unroll
        for (int k = 0; k < 2; ++k) {
            const u32 r = (u32)wave * 8 + k * 4 + g;
            dma_nt(P.wuv, ((u32)j * 128 + r) * 8192u + (u32)head * 256u + ((i15 ^ swz(r & 15)) << 4), F_UV + ((u32)wave * 8 + k * 4) * 256u);
        }
        v4u mw = ld_nt(P.wmod, mrow * 6144u + 2048u + cgl * 128u + (u32)(lane & 7) * 16u);
        u32 cv = ld_u16(P.c, mrow * 2);
        VMWAIT(4);
        STAMP(14);
        barrier();
        const u32 q4 = i15 >> 2, p4 = lane & 3;
        const u32 ra = 8 * (g & 1) + q4, rb = ra + 4;
        {
            const u32 c0 = 4 * (u32)wave + (p4 >> 1);
            const u32 x0 = F_WO + (8 * g + q4) * 1024 + 8 * (p4 & 1), x1 = x0 + 4096;
#pragma unroll
            for (int t2 = 0; t2 < 2; ++t2)
#pragma unroll
                for (int s = 0; s < 4; ++s) {
                    const s16x4 lo = __builtin_amdgcn_ds_read_tr16_b64_v4i16(lp<s16x4>(x0 + s * 32768 + (((c0 + 2 * t2) ^ swz(ra)) << 4)));
                    const s16x4 hi = __builtin_amdgcn_ds_read_tr16_b64_v4i16(lp<s16x4>(x1 + s * 32768 + (((c0 + 2 * t2) ^ swz(rb)) << 4)));
                    bo[4 * t2 + s] = __builtin_shufflevector(lo, hi, 0, 1, 2, 3, 4, 5, 6, 7);
                }
#pragma unroll
            for (int k = 0; k < 8; ++k) PIN(bo[k]);
        }
        VMWAIT(0);
        PIN(mw); PIN(cv);
        STAMP(6);
        barrier();
        {
            const u32 c0 = 2 * ((u32)wave & 7) + (p4 >> 1);
            const u32 x0 = F_UV + (8 * g + q4) * 256 + 8 * (p4 & 1) + ((c0 ^ swz(ra)) << 4), x1 = F_UV + (8 * g + q4 + 4) * 256 + 8 * (p4 & 1) + ((c0 ^ swz(rb)) << 4);
#pragma unroll
            for (int s = 0; s < 4; ++s) {
                const s16x4 lo = __builtin_amdgcn_ds_read_tr16_b64_v4i16(lp<s16x4>(x0 + s * 8192));
                const s16x4 hi = __builtin_amdgcn_ds_read_tr16_b64_v4i16(lp<s16x4>(x1 + s * 8192));
                bu[s] = __builtin_shufflevector(lo, hi, 0, 1, 2, 3, 4, 5, 6, 7);
            }
        }
        const u32 sl = pack2(silu(bflo(cv)), 0.f), sh = sl << 16;
        float t[4], u[2];
#pragma unroll
        for (int k = 0; k < 2; ++k) { t[2 * k] = red32(dot2(mw[k], sl, 0.f), dot2(mw[k + 2], sl, 0.f)); t[2 * k + 1] = red32(dot2(mw[k], sh, 0.f), dot2(mw[k + 2], sh, 0.f)); }
#pragma unroll
        for (int k = 0; k < 2; ++k) { float z = red16(t[k], t[k + 2]); z += dpp<0x128>(z); u[k] = z; }
        if ((lane & 8) == 0) *lp<f32x2>(F_ML + wave * 256 + ((lane & 7) * 4 + g) * 8) = (f32x2){u[0], u[1]};
        if (tid == 8) *lp<v2u>(F_GO) = (v2u){0u, 0u};
#pragma unroll
        for (int k = 0; k < 4; ++k) PIN(bu[k]);
        barrier();
        if (wave == 15) {
            const LAS f32x2* q = lp<f32x2>(F_ML) + lane;
            const f32x2 s = ((q[0] + q[64]) + (q[128] + q[192])) + ((q[256] + q[320]) + (q[384] + q[448]));
            const float sx = red32(s.x, s.x), sy = red32(s.y, s.y);
            if (lane < 32) st_sc(ws, WS_ML + (cgl & 7) * 8192u + ((u32)lane >> 4) * 4096u + (cgl >> 3) * 1024u + (kq * 16 + ((u32)lane & 15)) * 16u, (v4u){asu(sx), asu(sy), tlo, thi});
        }
    }
    float ga0 = 0.f, ga1 = 0.f, gb0 = 0.f, gb1 = 0.f, xv0 = 0.f, xv1 = 0.f;
    u32 xo = piece(WS_X1, 4096, (u32)(lane >> 3)) + (rid * 8 + (u32)(lane & 7)) * 16u;
    u32 aa = F_A + (i15 & 7) * 272 + g * 16, cw = F_CX + ((u32)wave & 7) * 32 + i15 * 2, cr = F_CX + g * 16;
    u32 oo = piece(WS_OUTP, 16384, 2 * (u32)wave + (g & 1)) + (u32)b * 128u + (i15 >> 1) * 16u;
    PIN(aa); PIN(cw); PIN(cr); PIN(oo); PIN(xo);
    STAMP(7);
    {
        const u32 sl = i15 < 12 ? i15 : 11u, half = (u32)wave >> 3, w7 = (u32)wave & 7;
        const u32 o = WS_PAY + (u32)head * PHD + (((u32)j * 2 + half) * 128 + w7 * 16 + g) * PBLK + sl * 16u;
        const u32 hfp = sl < 8 ? sl >> 2 : (sl - 8) >> 1, gi = sl < 8 ? sl & 3 : 4 + (sl & 1);
        const u32 ta = 3 * gi + (g >> 1), tz = 3 * gi + 2;
        const u32 cbase = F_A + w7 * 272 + half * 128 + hfp * 8;
        u32 wa = (i15 < 12 && ta < 16) ? cbase + (ta >> 3) * 64 + ((ta >> 1) & 3) * 16 + (ta & 1) * 4 + (g & 1) * 2 : F_A + w7 * 272 + 256;
        u32 wz = (i15 < 12 && tz < 16) ? cbase + (tz >> 3) * 64 + ((tz >> 1) & 3) * 16 + (tz & 1) * 4 + (g >> 1) * 2 : F_A + w7 * 272 + 260;
        v4u v[4];
        PIN(wa); PIN(wz);
        u32 tdet;
        if (wave == 0) {
            tdet = hint_wait<HC_K>(ws, WS_HC + (u32)b * HSTR + (u32)(lane & 7) * 16u, tlo, budget) | 1u;
            if (lane == 0) *lp<u32>(F_GO) = tdet;
            if (is_red) {
                v4u gm[4];
                const u32 om = WS_ML + (rid >> 1) * 4096u + (g * 16 + (rid & 1) * 8 + (u32)(lane & 7)) * 16u;
                u32 xr = ld_u32(P.x, (rid * 8 + (u32)(lane & 7)) * 4);
                gather<4, 1024>(ws, om, tlo, thi, gm, budget);
                PIN(xr);
                xv0 = bflo(xr); xv1 = bfhi(xr);
                {
                    const float s0 = red16(red32(asf(gm[1].x), asf(gm[2].x)), red32(asf(gm[1].y), asf(gm[2].y)));
                    u32 e, od, sbp, cbp;
                    both16(asu(s0), e, od);
                    both32(pack2(asf(e), asf(od)), sbp, cbp);
                    if (lane < 8) st_sc(ws, WS_SC + (rid * 8 + (u32)lane) * 16u, (v4u){cbp, sbp, tlo, thi});
                }
                ga0 = asf(gm[0].x); ga1 = asf(gm[0].y); gb0 = asf(gm[3].x); gb1 = asf(gm[3].y);
                ga0 = red32(ga0, ga0); ga0 = red16(ga0, ga0); ga1 = red32(ga1, ga1); ga1 = red16(ga1, ga1);
                gb0 = red32(gb0, gb0); gb0 = red16(gb0, gb0); gb1 = red32(gb1, gb1); gb1 = red16(gb1, gb1);
            }
        } else {
            lds_wait(F_GO, 1u, budget);
            tdet = rfl(*lp<u32>(F_GO));
        }
        PIN(ga0); PIN(ga1); PIN(gb0); PIN(gb1); PIN(xv0); PIN(xv1);
#pragma unroll
        for (int k = 0; k < 8; ++k) PIN(bo[k]);
        until32(tdet, half ? C_DELAY2 : C_DELAY);
        STAMP(8);
        float M = 0.f;
        gather<4, 4 * PBLK, true>(ws, o, tlo, thi, v, budget);
        STAMP(9);
        float mk[4];
#pragma unroll
        for (int k = 0; k < 4; ++k) mk[k] = dpp<0x159>(asf(v[k].y));
        if (half == 0) {
            const float Mw = max32(max16(max3(max3(mk[0], mk[1], mk[2]), mk[3], mk[0])));
            if (lane == 0) *lp<float>(F_ST + w7 * 4) = Mw;
            lds_inc(F_GO + 4, lane);
        }
#pragma clang loop unroll(disable)
        do {
            const u32 cnt = *(volatile LAS u32*)lp<u32>(F_GO + 4);
            const f32x4 m0 = *(volatile LAS f32x4*)lp<f32x4>(F_ST), m1 = *(volatile LAS f32x4*)lp<f32x4>(F_ST + 16);
            M = max8(m0, m1);
            if (cnt >= 8u) break;
            __builtin_amdgcn_s_sleep(1);
        } while (--budget > 0);
        const u32 w01 = pack2(ex2(mk[0] - M), ex2(mk[1] - M)), w23 = pack2(ex2(mk[2] - M), ex2(mk[3] - M));
        float a[6];
#pragma unroll
        for (int q = 0; q < 3; ++q) {
            a[2 * q] = dot2(perm_lo(v[2][q], v[3][q]), w23, dot2(perm_lo(v[0][q], v[1][q]), w01, 0.f));
            a[2 * q + 1] = dot2(perm_hi(v[2][q], v[3][q]), w23, dot2(perm_hi(v[0][q], v[1][q]), w01, 0.f));
        }
        const float x = red16(red32(a[0], a[2]), red32(a[1], a[3]));
        float z = red32(a[4], a[5]);
        z = red16(z, z);
        const u32 xz = pack2(x, z);
        *lp<u16>(wa) = (u16)xz;
        *lp<u16>(wz) = (u16)(xz >> 16);
        if (half) {
            const float L = red32(z, z);
            if (lane == 9) *lp<float>(F_ST + 32 + w7 * 4) = L;
        }
    }
    barrier();
    STAMP(10);
    if (wave < 8) {
        bf16x8 af[4];
#pragma unroll
        for (int s = 0; s < 4; ++s) af[s] = *lp<bf16x8>(aa + s * 64);
        const f32x4 l0 = *lp<f32x4>(F_ST + 32), l1 = *lp<f32x4>(F_ST + 48);
        __builtin_amdgcn_sched_barrier(0);
        f32x4 d = {0.f, 0.f, 0.f, 0.f}, e = {0.f, 0.f, 0.f, 0.f};
        d = __builtin_amdgcn_mfma_f32_16x16x32_bf16(af[0], bu[0], d, 0, 0, 0);
        e = __builtin_amdgcn_mfma_f32_16x16x32_bf16(af[1], bu[1], e, 0, 0, 0);
        d = __builtin_amdgcn_mfma_f32_16x16x32_bf16(af[2], bu[2], d, 0, 0, 0);
        e = __builtin_amdgcn_mfma_f32_16x16x32_bf16(af[3], bu[3], e, 0, 0, 0);
        const float rl = __builtin_amdgcn_rcpf(((l0.x + l0.y) + (l0.z + l0.w)) + ((l1.x + l1.y) + (l1.z + l1.w)));
        float c = ((d.x + e.x) + (d.y + e.y)) + ((d.z + e.z) + (d.w + e.w));
        c = red16(c, c) * rl;
        *lp<u16>(cw) = (u16)pack2(c, c);
    } else if (b == 0 && tid == 512) st_sc2(ws, WS_EPOCH, (v2u){tlo, thi});
    barrier();
    STAMP(13);
    {
        bf16x8 af[4];
#pragma unroll
        for (int s = 0; s < 4; ++s) af[s] = *lp<bf16x8>(cr + s * 64);
        __builtin_amdgcn_sched_barrier(0);
        f32x4 d0 = {0.f, 0.f, 0.f, 0.f}, d1 = {0.f, 0.f, 0.f, 0.f};
#pragma unroll
        for (int s = 0; s < 4; ++s) { d0 = __builtin_amdgcn_mfma_f32_16x16x32_bf16(af[s], bo[s], d0, 0, 0, 0); d1 = __builtin_amdgcn_mfma_f32_16x16x32_bf16(af[s], bo[4 + s], d1, 0, 0, 0); }
        const float o = (g & 1) ? d1.x : d0.x;
        const float n = dpp<0x101>(o);
        if ((lane & 33) == 0) st_sc(ws, oo, (v4u){asu(o), asu(n), tlo, thi});
    }
    STAMP(11);
    if (is_red) {
        const f32x2 s = reduce16<16>(ws, piece(WS_OUTP, 16384, rid), tlo, thi, wave, lane, F_RD, budget);
        const float x10 = xv0 + ga0 * s.x, x11 = xv1 + ga1 * s.y;
        if (wave == 0) st_sc(ws, xo, (v4u){asu(x10), asu(x11), tlo, thi});
        STAMP(12);
        until(__builtin_amdgcn_s_memrealtime(), F_DELAY);
        const f32x2 s2 = reduce16<14>(ws, piece(WS_YP, 16384, rid), tlo, thi, wave, lane, F_RV, budget);
        if (tid < 8) ((u32*)P.out)[rid * 8 + lane] = pack2(x10 + gb0 * s2.x, x11 + gb1 * s2.y);
    }
    STAMP(15);
    STAMP_OUT();
}

extern "C" void kernel_launch(void* const* d_in, const int* in_sizes, int n_in, void* d_out, int out_size, void* d_ws, size_t ws_size, hipStream_t stream) {
    Params P;
    P.x = (const u16*)d_in[0]; P.ckv = (const u16*)d_in[1]; P.wq = (const u16*)d_in[2]; P.wuk = (const u16*)d_in[3];
    P.wuv = (const u16*)d_in[4]; P.wo = (const u16*)d_in[5]; P.wg = (const u16*)d_in[7]; P.wu = (const u16*)d_in[8];
    P.wd = (const u16*)d_in[9]; P.c = (const u16*)d_in[10]; P.wmod = (const u16*)d_in[11];
    P.out = (u16*)d_out; P.ws = (char*)d_ws + WS_BASE(ws_size);
    (void)hipFuncSetAttribute((const void*)fused_kernel, hipFuncAttributeMaxDynamicSharedMemorySize, LDS_BYTES);
    fused_kernel<<<256, 1024, LDS_BYTES, stream>>>(P);
}
```

```cpp
#include <hip/hip_runtime.h>

typedef unsigned u32;
typedef unsigned long long u64;
typedef unsigned short u16;
typedef u32 v4u __attribute__((ext_vector_type(4)));
typedef u32 v2u __attribute__((ext_vector_type(2)));
typedef u32 v8u __attribute__((ext_vector_type(8)));
typedef u32 v16u __attribute__((ext_vector_type(16)));
typedef float f32x2 __attribute__((ext_vector_type(2)));
typedef float f32x4 __attribute__((ext_vector_type(4)));
typedef float f32x16 __attribute__((ext_vector_type(16)));
typedef short bf16x8 __attribute__((ext_vector_type(8)));
typedef short s16x4 __attribute__((ext_vector_type(4)));
typedef __bf16 bf16x2_t __attribute__((ext_vector_type(2)));
#define LAS __attribute__((address_space(3)))
#define DI __device__ __forceinline__

#define CFG(name, val) constexpr int name = val
CFG(BUDGET, 1 << 20);
CFG(POLL_SLEEP, 4);
CFG(HINT_SLEEP, 4);
#define T_OPEN0 50
#define T_OPEN1 70
#define T_OPEN2 70
#define A_FIRST 168
#define A2_DELAY 45
#define Q_FIRST 350
#define G_DELAY 0
#define F_DELAY 0
#define ST_LOOK 110
#define SKEW_MAX 45
#define Q_DELAY 118
#define A_OWN 60
#define Q_STEP 40
#define T_MLP 0
#define T_OWN 40
#define O_STEP 35
#define Q_PRE 60
#define KD 10
#define KMAP 0
#define KMUL 41
DI u32 ktile(u32 s, u32 t) {
    if (KMAP == 0) return s * 262144u + t * 16384u;
    if (KMAP == 1) return t * 2097152u + s * 16384u;
    const u32 pos = (s + KMUL * t) & 127u;
    if (KMAP == 3) return t * 2097152u + pos * 16384u;
    if (KMAP == 4) return ((t + pos) & 15u) * 2097152u + pos * 16384u;
    if (KMAP == 5) return ((t + (pos >> 3)) & 15u) * 2097152u + pos * 16384u;
    return pos * 262144u + t * 16384u;
}
#define HW 5
#define QD 2
#define LAZY 48.f
#define CODE_STR 7936
#define WS_BASE(sz) (((sz) - (32u << 20)) & ~(size_t)((2u << 20) - 1))

constexpr u32 WS_EPOCH = 0;
DI u32 piece(u32 base, u32 size, u32 i) { return base + i * size; }
#define CH 262144u
constexpr u32 WS_ST = 1024;
constexpr u32 WS_QH = 2048, NQH = 6;
constexpr u32 WS_QCHK = 8192;
constexpr u32 WS_HP = CH;
constexpr u32 WS_QP = 2 * CH;
constexpr u32 WS_QABS = 3 * CH;
constexpr u32 WS_L = 25u << 18;
constexpr u32 WS_ML = WS_L;
constexpr u32 WS_X1 = WS_L + 65536;
constexpr u32 WS_SC = WS_L + 98304;
constexpr u32 HSTR = 1152, WS_HC = WS_L + 106496;
constexpr u32 WS_FC = WS_SC + 4096;
constexpr u32 WS_HE = WS_HC + 512;
constexpr u32 WS_OUTP = WS_L + 262144;
constexpr u32 WS_YP = WS_L + 786432;
constexpr u32 WS_CHK = WS_L + 1310720;
constexpr u32 WS_PAY = 2u << 20;
constexpr u32 PHD = 4 * 128 * 256 + 4096;
constexpr u32 WS_TL = 16u << 20;

constexpr u32 L_FL = 114688, L_P = L_FL + 1024, L_IMG = 131072;
constexpr u32 slot_no(u32 t) { return (u32)((0x9543621054362105ull >> (4 * t)) & 15u); }
constexpr u32 slot(int t) { return slot_no((u32)t) * 16384u; }
constexpr u32 vwin(int t) { return (slot_no((u32)t) < 3 ? 0u : slot_no((u32)t) < 7 ? 3u : 6u) * 16384u; }
static_assert(slot(15) == L_IMG + 16384, "");
constexpr u32 C_LAND = 0;
constexpr u32 C_KRD = 256, C_Z = 264, C_E = 268, C_P = 272, C_Q = 284, C_L = 288, C_BAD = 292, C_H = 296, C_TZ = 304;
constexpr u32 L_M = L_FL + 384, L_L = L_FL + 512;
constexpr u32 L_W = 0;
constexpr u32 L_H2 = 32768;
constexpr u32 L_GU = 36864;
constexpr u32 F_QP = 0;
constexpr u32 F_WO = 0;
constexpr u32 F_UV = 131072;
constexpr u32 F_A = 0;
constexpr u32 F_ST = 4096;
constexpr u32 F_GO = F_ST + 40;
constexpr u32 F_CX = 4352;
constexpr u32 F_LS = 5120;
constexpr u32 F_ML = 6144;
constexpr u32 F_RD = 8192, F_RV = 9216;
constexpr u32 F_JUNK = 16384;
constexpr u32 LDS_BYTES = 163840;

struct Params {
    const u16 *wmod, *c, *x;
    char* ws;
    const u16 *wq, *ckv, *wuk, *wo, *wuv, *wg, *wu, *wd;
    u16* out;
};

DI float asf(u32 v) { return __builtin_bit_cast(float, v); }
DI u32 asu(float v) { return __builtin_bit_cast(u32, v); }
DI float bflo(u32 v) { return asf(v << 16); }
DI float bfhi(u32 v) { return asf(v & 0xffff0000u); }
DI u32 pack2(float a, float b) { f32x2 v = {a, b}; return __builtin_bit_cast(u32, __builtin_convertvector(v, bf16x2_t)); }
DI float dot2(u32 w, u32 x, float acc) { return __builtin_amdgcn_fdot2_f32_bf16(__builtin_bit_cast(bf16x2_t, w), __builtin_bit_cast(bf16x2_t, x), acc, false); }
DI void pair_rows(v4u& x, v4u& y) {
#pragma unroll
    for (int k = 0; k < 4; ++k) { const u32 a = x[k], b = y[k]; x[k] = __builtin_amdgcn_perm(b, a, 0x05040100u); y[k] = __builtin_amdgcn_perm(b, a, 0x07060302u); }
}
template <int CTRL> DI u32 dppu(u32 v) { return (u32)__builtin_amdgcn_update_dpp(0, (int)v, CTRL, 0xf, 0xf, true); }
template <int CTRL> DI float dpp(float v) { return asf(dppu<CTRL>(asu(v))); }
DI float red32(float a, float b) { auto r = __builtin_amdgcn_permlane32_swap(asu(a), asu(b), false, false); return asf((u32)r[0]) + asf((u32)r[1]); }
DI float red16(float a, float b) { auto r = __builtin_amdgcn_permlane16_swap(asu(a), asu(b), false, false); return asf((u32)r[0]) + asf((u32)r[1]); }
DI float max3(float a, float b, float c) { return __builtin_fmaxf(__builtin_fmaxf(a, b), c); }
DI float max4(f32x4 a) { return max3(max3(a.x, a.y, a.z), a.w, a.x); }
DI float max32(float a) { auto r = __builtin_amdgcn_permlane32_swap(asu(a), asu(a), false, false); return __builtin_fmaxf(asf((u32)r[0]), asf((u32)r[1])); }
DI float max16(float a) { auto r = __builtin_amdgcn_permlane16_swap(asu(a), asu(a), false, false); return __builtin_fmaxf(asf((u32)r[0]), asf((u32)r[1])); }
DI float max8(const f32x4& a, const f32x4& b) { return max3(max3(max3(a.x, a.y, a.z), a.w, b.x), max3(b.y, b.z, b.w), a.x); }
DI float sum4(f32x4 a) { return (a.x + a.y) + (a.z + a.w); }
DI u32 perm_lo(u32 a, u32 b) { return __builtin_amdgcn_perm(b, a, 0x05040100u); }
DI u32 perm_hi(u32 a, u32 b) { return __builtin_amdgcn_perm(b, a, 0x07060302u); }
DI u32 rfl(u32 v) { return (u32)__builtin_amdgcn_readfirstlane((int)v); }
DI void both16(u32 a, u32& e, u32& o) { auto r = __builtin_amdgcn_permlane16_swap(a, a, false, false); e = (u32)r[0]; o = (u32)r[1]; }
DI void both32(u32 a, u32& l, u32& h) { auto r = __builtin_amdgcn_permlane32_swap(a, a, false, false); l = (u32)r[0]; h = (u32)r[1]; }
DI float rowsum(float v) { v += dpp<0x128>(v); v += dpp<0x124>(v); v += dpp<0x122>(v); v += dpp<0x121>(v); return v; }
DI float ex2(float v) { return __builtin_amdgcn_exp2f(v); }
DI u32 neq3(u32 a, u32 b, u32 t) { return __builtin_amdgcn_bitop3_b32(a, b, t, 0x7e); }
DI u64 ballot(bool c) { return __builtin_amdgcn_ballot_w64(c); }

DI v4u ld_nt(const void* base, u32 off) { v4u r; asm volatile("global_load_dwordx4 %0, %1, %2 nt" : "=v"(r) : "v"(off), "s"(base) : "memory"); return r; }
DI u32 ld_u32(const void* base, u32 off) { u32 r; asm volatile("global_load_dword %0, %1, %2" : "=v"(r) : "v"(off), "s"(base) : "memory"); return r; }
DI u32 ld_u32_nt(const void* base, u32 off) { u32 r; asm volatile("global_load_dword %0, %1, %2 nt" : "=v"(r) : "v"(off), "s"(base) : "memory"); return r; }
DI u32 ld_u16(const void* base, u32 off) { u32 r; asm volatile("global_load_ushort %0, %1, %2" : "=v"(r) : "v"(off), "s"(base) : "memory"); return r; }
DI void st_sc(void* base, u32 off, v4u v) { asm volatile("global_store_dwordx4 %0, %1, %2 sc1\n\ts_nop 1" :: "v"(off), "v"(v), "s"(base) : "memory"); }
DI void st_sc_keep(void* base, u32 off, v4u v) { asm volatile("global_store_dwordx4 %0, %1, %2 sc1" :: "v"(off), "v"(v), "s"(base) : "memory"); }
DI void st_sc2(void* base, u32 off, v2u v) { asm volatile("global_store_dwordx2 %0, %1, %2 sc1\n\ts_nop 1" :: "v"(off), "v"(v), "s"(base) : "memory"); }
DI void dma_nt(const void* base, u32 off, u32 lds_dst) { asm volatile("s_mov_b32 m0, %1\n\ts_nop 0\n\tglobal_load_lds_dwordx4 %0, %2 nt" :: "v"(off), "s"(lds_dst), "s"(base) : "memory"); }
DI void dma_pl(const void* base, u32 off, u32 lds_dst) { asm volatile("s_mov_b32 m0, %1\n\ts_nop 0\n\tglobal_load_lds_dwordx4 %0, %2" :: "v"(off), "s"(lds_dst), "s"(base) : "memory"); }
DI void dma_sc(const void* base, u32 off, u32 lds_dst) { asm volatile("s_mov_b32 m0, %1\n\ts_nop 0\n\tglobal_load_lds_dwordx4 %0, %2 sc1" :: "v"(off), "s"(lds_dst), "s"(base) : "memory"); }
DI void ld_nt_into(v4u& r, const void* base, u32 off) { asm volatile("global_load_dwordx4 %0, %1, %2 nt" : "+v"(r) : "v"(off), "s"(base) : "memory"); }
DI void ld_sc_into(v4u& r, const void* base, u32 off) { asm volatile("global_load_dwordx4 %0, %1, %2 sc1" : "+v"(r) : "v"(off), "s"(base) : "memory"); }
#define VMWAIT(n) asm volatile("s_waitcnt vmcnt(%0)" :: "i"(n) : "memory")
#define LGKM0() asm volatile("s_waitcnt lgkmcnt(0)" ::: "memory")
#define PIN(x) asm volatile("" : "+v"(x))
#define UNDEF(x) asm volatile("" : "=v"(x))
#define SCHED() __builtin_amdgcn_sched_barrier(0)
DI void barrier() { LGKM0(); __builtin_amdgcn_s_barrier(); asm volatile("" ::: "memory"); }

template <typename T> DI LAS T* lp(u32 off) { return (LAS T*)(size_t)off; }
DI bf16x8 tr2(u32 a, u32 b) {
    const s16x4 lo = __builtin_amdgcn_ds_read_tr16_b64_v4i16(lp<s16x4>(a)), hi = __builtin_amdgcn_ds_read_tr16_b64_v4i16(lp<s16x4>(b));
    return __builtin_shufflevector(lo, hi, 0, 1, 2, 3, 4, 5, 6, 7);
}

#define STAMP(i) do {} while (0)
#define CYC0() do {} while (0)
#define STAMPC(i) do {} while (0)
#define STAMP_OUT() do {} while (0)

#define CDECL() do {} while (0)
#define CS(i) do {} while (0)
#define COUT(n) do {} while (0)
template <u32 S> DI void pass(const char* ws, u32 off, v4u (&v)[1]) {
    asm volatile("global_load_dwordx4 %0, %1, %2 sc1\n\ts_waitcnt vmcnt(0)" : "=v"(v[0]) : "v"(off), "s"(ws) : "memory");
}
template <u32 S> DI void pass(const char* ws, u32 off, v4u (&v)[2]) {
    asm volatile("global_load_dwordx4 %0, %2, %3 sc1\n\tglobal_load_dwordx4 %1, %2, %4 sc1\n\ts_waitcnt vmcnt(0)" : "=&v"(v[0]), "=&v"(v[1]) : "v"(off), "s"(ws), "s"(ws + S) : "memory");
}
template <u32 S> DI void pass(const char* ws, u32 off, v4u (&v)[4]) {
    asm volatile("global_load_dwordx4 %0, %4, %5 sc1\n\tglobal_load_dwordx4 %1, %4, %5 offset:%6 sc1\n\tglobal_load_dwordx4 %2, %4, %5 offset:%7 sc1\n\tglobal_load_dwordx4 %3, %4, %5 offset:%8 sc1\n\ts_waitcnt vmcnt(0)"
        : "=&v"(v[0]), "=&v"(v[1]), "=&v"(v[2]), "=&v"(v[3]) : "v"(off), "s"(ws), "i"(S), "i"(2 * S), "i"(3 * S) : "memory");
}
template <int N, u32 STRIDE, int SLP = POLL_SLEEP> DI void gather(const char* ws, u32 off, u32 tlo, u32 thi, v4u (&v)[N], int& budget) {
#pragma clang loop unroll(disable)
    do {
        pass<STRIDE>(ws, off, v);
        u32 bad = 0;
#pragma unroll
        for (int k = 0; k < N; ++k) bad |= (v[k].z ^ tlo) | (v[k].w ^ thi);
        if (ballot(bad != 0u) == 0ull) break;
        __builtin_amdgcn_s_sleep(SLP);
    } while (--budget > 0);
}
#define PSLP "0"
template <bool G8> DI void poll2(v4u& g0, v4u& g1, u32 o0, const char* b0, u32 o1, const char* b1, u32 tlo, u32 thi) {
    u32 n, t, u;
#define POLL2(A, B, C, D) asm volatile("s_mov_b32 %2, 0x200000\n.Lp%=:\n\ts_sleep " PSLP "\n\tglobal_load_dwordx4 v[232:235], %5, %6 sc1\n\tglobal_load_dwordx4 v[236:239], %7, %8 sc1\n\ts_sub_u32 %2, %2, 1\n\ts_waitcnt vmcnt(0)\n\t" \
        "v_bitop3_b32 %3, " A ", " B ", %9 bitop3:0x7e\n\tv_bitop3_b32 %4, " C ", " D ", %10 bitop3:0x7e\n\tv_or_b32 %3, %3, %4\n\tv_cmp_ne_u32 vcc, 0, %3\n\ts_cbranch_scc1 .Lq%=\n\ts_cbranch_vccnz .Lp%=\n.Lq%=:" \
        : "=&{v[232:235]}"(g0), "=&{v[236:239]}"(g1), "=&s"(n), "=&v"(t), "=&v"(u) : "v"(o0), "s"(b0), "v"(o1), "s"(b1), "s"(tlo), "s"(G8 ? tlo : thi) : "memory", "vcc", "scc")
    if (G8) POLL2("v233", "v235", "v237", "v239"); else POLL2("v234", "v238", "v235", "v239");
}
DI void flag_set(u32 fa, u32 off) {
    asm volatile("s_mov_b64 exec, 1\n\tds_write_b8_d16_hi %0, %0 offset:%1\n\ts_mov_b64 exec, -1" :: "v"(fa), "i"(off) : "memory");
}
DI u32 flag_get(u32 off) { return *(volatile LAS u32*)lp<u32>(L_FL + off); }
DI void wait4(u32 fb, u32 off) {
    u32 v, n;
    asm volatile("s_mov_b32 %1, 0x400000\n.Lw%=:\n\tds_read_b32 %0, %2 offset:%3\n\ts_sub_u32 %1, %1, 1\n\ts_waitcnt lgkmcnt(0)\n\tv_cmp_ne_u32 vcc, 0x1010101, %0\n\ts_cbranch_scc1 .Le%=\n\ts_cbranch_vccnz .Lw%=\n.Le%=:"
        : "=&v"(v), "=&s"(n) : "v"(fb), "i"(off) : "memory", "vcc", "scc");
}
DI void wait1(u32 fb, u32 off) {
    u32 v, n;
    asm volatile("s_mov_b32 %1, 0x400000\n.Lw%=:\n\tds_read_u8 %0, %2 offset:%3\n\ts_sub_u32 %1, %1, 1\n\ts_waitcnt lgkmcnt(0)\n\tv_cmp_ne_u32 vcc, 1, %0\n\ts_cbranch_scc1 .Le%=\n\ts_cbranch_vccnz .Lw%=\n.Le%=:"
        : "=&v"(v), "=&s"(n) : "v"(fb), "i"(off) : "memory", "vcc", "scc");
}
#define SPIN_HEAD "s_mov_b32 %[n], 0x400000\n.Lw%=:\n\t"
#define SPIN_TAIL(cmp) "s_sub_u32 %[n], %[n], 1\n\ts_waitcnt lgkmcnt(0)\n\t" cmp ", %[v]\n\ts_cbranch_scc1 .Le%=\n\ts_cbranch_vccnz .Lw%=\n.Le%=:"
DI void land_wait(u32 fl, u32 off) {
    u32 v, n;
    asm volatile(SPIN_HEAD "ds_read_b32 %[v], %[b] offset:%[o]\n\t" SPIN_TAIL("v_cmp_eq_u32 vcc, 0") : [v] "=&v"(v), [n] "=&s"(n) : [b] "v"(fl), [o] "i"(off) : "memory", "vcc", "scc");
}
DI bf16x8 wait1p(u32 fb, u32 off, u32 pa, u32 poff) {
    u32 v, n;
    bf16x8 p;
    asm volatile(SPIN_HEAD "ds_read_u8 %[v], %[b] offset:%[o]\n\tds_read_b128 %[p], %[a] offset:%[q]\n\t" SPIN_TAIL("v_cmp_ne_u32 vcc, 1")
        : [v] "=&v"(v), [p] "=&v"(p), [n] "=&s"(n) : [b] "v"(fb), [o] "i"(off), [a] "v"(pa), [q] "i"(poff) : "memory", "vcc", "scc");
    return p;
}
DI void wait4p(u32 fb, u32 off, u32 pa, u32 poff, bf16x8 (&p)[4]) {
    u32 v, n;
    asm volatile(SPIN_HEAD "ds_read_b32 %[v], %[b] offset:%[o]\n\tds_read_b128 %[p0], %[a] offset:%[q0]\n\tds_read_b128 %[p1], %[a] offset:%[q1]\n\tds_read_b128 %[p2], %[a] offset:%[q2]\n\tds_read_b128 %[p3], %[a] offset:%[q3]\n\t"
        SPIN_TAIL("v_cmp_ne_u32 vcc, 0x1010101")
        : [v] "=&v"(v), [p0] "=&v"(p[0]), [p1] "=&v"(p[1]), [p2] "=&v"(p[2]), [p3] "=&v"(p[3]), [n] "=&s"(n)
        : [b] "v"(fb), [o] "i"(off), [a] "v"(pa), [q0] "i"(poff), [q1] "i"(poff + 1024), [q2] "i"(poff + 2048), [q3] "i"(poff + 3072) : "memory", "vcc", "scc");
}
template <u32 KO> DI void land_wait_k(u32 fl, u32 off, u32 a0, u32 a1, u32 a2, u32 a3, bf16x8& f0, bf16x8& f1, bf16x8& f2, bf16x8& f3) {
    u32 v, n;
    asm volatile(SPIN_HEAD "ds_read_b32 %[v], %[b] offset:%[o]\n\tds_read_b128 %[p0], %[a0] offset:%[k]\n\tds_read_b128 %[p1], %[a1] offset:%[k]\n\tds_read_b128 %[p2], %[a2] offset:%[k]\n\tds_read_b128 %[p3], %[a3] offset:%[k]\n\t"
        SPIN_TAIL("v_cmp_eq_u32 vcc, 0")
        : [v] "=&v"(v), [p0] "=&v"(f0), [p1] "=&v"(f1), [p2] "=&v"(f2), [p3] "=&v"(f3), [n] "=&s"(n)
        : [b] "v"(fl), [o] "i"(off), [a0] "v"(a0), [a1] "v"(a1), [a2] "v"(a2), [a3] "v"(a3), [k] "i"(KO) : "memory", "vcc", "scc");
}
DI void dma_a(const char* src, const u32 (&od)[4], u32 dst) {
    asm volatile("s_mov_b32 m0, %3\n\ts_nop 0\n\tglobal_load_lds_dwordx4 %0, %2 nt\n\tglobal_load_lds_dwordx4 %1, %2 offset:1024 nt" :: "v"(od[0]), "v"(od[1]), "s"(src), "s"(dst) : "memory");
}
DI void dma_b(const char* src, const u32 (&od)[4], u32 dst, u64 pc, u32 flag) {
    asm volatile("s_mov_b32 m0, %4\n\ts_nop 0\n\tglobal_load_lds_dwordx4 %1, %3 offset:2048 nt\n\tglobal_load_lds_dwordx4 %2, %3 offset:3072 nt\n\t"
        "s_mov_b32 m0, %6\n\ts_mov_b64 exec, 1\n\tglobal_load_lds_dword %0, %5\n\ts_mov_b64 exec, -1"
        :: "v"(od[0]), "v"(od[2]), "v"(od[3]), "s"(src), "s"(dst), "s"(pc), "s"(flag) : "memory");
}
static_assert(C_LAND == 0, "");
static_assert(((L_FL >> 16) & 0xff) == 1, "flag_set writes bits 23:16 of the address");
template <int K, bool PRE = false> DI u32 hint_wait(const char* ws, u32 oh, u32 tlo, int& budget, v4u h0 = v4u()) {
    v4u h[1] = {h0};
    if (!PRE || __builtin_popcount((u32)ballot(h[0].z == tlo) & 0xffu) < K) {
#pragma clang loop unroll(disable)
        do {
            pass<0>(ws, oh, h);
            if (__builtin_popcount((u32)ballot(h[0].z == tlo) & 0xffu) >= K) break;
            __builtin_amdgcn_s_sleep(HINT_SLEEP);
        } while (--budget > 0);
    }
    const u32 now = (u32)__builtin_amdgcn_s_memrealtime();
    u32 age = h[0].z == tlo ? now - h[0].x : 0xffffu;
    age = age > 0xffffu ? 0u : age;
    age = min(age, dppu<0xB1>(age));
    age = min(age, dppu<0x4E>(age));
    age = min(age, dppu<0x124>(age));
    return now - rfl(age);
}
DI u32 now32() { return (u32)__builtin_amdgcn_s_memrealtime(); }
DI void until(u32 t) { while ((int)(now32() - t) < 0) __builtin_amdgcn_s_sleep(1); }
DI u32 time_base(const char* p, u32 t0) {
    v16u h;
    asm volatile("s_load_dwordx16 %0, %1, 0x0\n\ts_waitcnt lgkmcnt(0)" : "=s"(h) : "s"(p) : "memory");
    u32 m = 0, bad = 0;
#pragma unroll
    for (int k = 0; k < 8; ++k) { m = max(m, h[2 * k] - t0 + 150u); bad |= h[2 * k] ^ ~h[2 * k + 1]; }
    return (bad != 0u || m >= 300u) ? t0 + SKEW_MAX : t0 - 150u + m;
}
struct QTimer { u32 t, n; };
DI bool q_due(QTimer& q, const char* ws, u32 tlo) {
    const u32 now = now32();
    if ((int)(now - q.t) < 0) return false;
    if (q.n >= NQH) return true;
    v16u h0, h1;
    asm volatile("s_load_dwordx16 %0, %2, 0x0\n\ts_load_dwordx16 %1, %2, 0x40\n\ts_waitcnt lgkmcnt(0)" : "=&s"(h0), "=&s"(h1) : "s"(ws + WS_QH + q.n * 128u) : "memory");
    u32 m = 0xffffffffu, bad = 0;
#pragma unroll
    for (int k = 0; k < 8; ++k) { m = min(m, min(now + 1000u - h0[2 * k], now + 1000u - h1[2 * k])); bad |= (h0[2 * k] ^ h0[2 * k + 1] ^ tlo) | (h1[2 * k] ^ h1[2 * k + 1] ^ tlo); }
    if (bad == 0u) { q.n = NQH; q.t = now + 1000u - max(m, 900u) + Q_DELAY - Q_PRE; } else { ++q.n; q.t = now + Q_STEP; }
    return false;
}
DI u32 swz(u32 row) { return ((row & 3u) << 2) | ((row >> 2) & 3u); }

#define EPOCH() do { asm volatile("s_load_dwordx2 %0, %1, 0x0\n\ts_waitcnt lgkmcnt(0)" : "=s"(e64) : "s"(ws) : "memory"); ++e64; } while (0)
DI void wave_sum2(float s1, float s2, float& t1, float& t2) {
    float z = red32(s1, s2);
    z = red16(z, z); z = rowsum(z);
    u32 a, b;
    both32(asu(z), a, b);
    t1 = asf(a); t2 = asf(b);
}
DI void ln_stats(const v4u& xa, float& mean, float& rstd) {
    const float pil = asf(rfl(xa[0]) << 16);
    float s1 = 0.f, s2 = 0.f;
#pragma unroll
    for (int k = 0; k < 4; ++k) { const float a = bflo(xa[k]) - pil, c = bfhi(xa[k]) - pil; s1 += a; s2 = __builtin_fmaf(a, a, s2); s1 += c; s2 = __builtin_fmaf(c, c, s2); }
    float t1, t2;
    wave_sum2(s1, s2, t1, t2);
    const float dm = t1 * (1.f / 512.f);
    mean = pil + dm;
    rstd = __builtin_amdgcn_rsqf(__builtin_fmaxf(t2 * (1.f / 512.f) - dm * dm, 0.f) + 1e-5f);
}
DI float sigm(float v) { return __builtin_amdgcn_rcpf(1.f + ex2(v * -1.4426950408889634f)); }
DI float silu(float v) { return v * sigm(v); }

template <int NW2, bool S8> DI f32x2 reduce16(const char* ws, u32 base, u32 tlo, u32 thi, int wave, int lane, u32 lds_red, u32 tgo, int& budget) {
    u32 la = lds_red + (u32)(wave * 16 + (S8 ? (lane & 7) + (lane >> 5) * 8 : (lane & 7) * 2 + (lane >> 5))) * 4, ra = lds_red + (u32)lane * 8, go = base + (u32)(wave * 64 + lane) * 16u;
    const bool two = NW2 == 8 || wave < NW2;
    const char* w2 = ws + (two ? 8192 : 0);
    float f2 = two ? 1.f : 0.f;
    PIN(la); PIN(ra); PIN(go);
    asm volatile("" : "+s"(w2), "+v"(f2));
    until(tgo);
    v4u g0, g1;
    poll2<false>(g0, g1, go, ws, go, w2, tlo, thi);
    float z = red32(__builtin_fmaf(asf(g1.x), f2, asf(g0.x)), __builtin_fmaf(asf(g1.y), f2, asf(g0.y)));
    z = red16(z, z); z += dpp<0x128>(z);
    *lp<float>(la) = z;
    barrier();
    f32x2 s = {0.f, 0.f};
    if (wave == 0) {
        s = *lp<f32x2>(ra);
        float t = red32(s.x, s.y);
        t = red16(t, t); t += dpp<0x128>(t);
        u32 a, c;
        both32(asu(t), a, c);
        s = (f32x2){asf(a), asf(c)};
    }
    return s;
}

#define E_DELAY 182
#define HE_K 8
#define MDMA dma_pl
DI u32 mlp_id(int b) { const int xc = b & 7, yy = (b >> 3) - 16; return (u32)(((yy >> 2) * 8 + xc) * 4 + (yy & 3)); }
DI void mlp_part(const Params& P, int b, u32 w, int lane, u32 tlo, u32 thi, int& budget, u32& tl) {
    char* const ws = P.ws;
    const u32 gu_id = mlp_id(b), i15 = lane & 15, g = lane >> 4;
    u32 wo = (128 * w + ((u32)lane >> 1)) * 3584u + gu_id * 32u + ((u32)lane & 1) * 16u, wdo = gu_id * 16384u + (64 * w + (u32)lane) * 4u;
    PIN(wo); PIN(wdo);
    u32 ha = L_H2 + w * 256 + (u32)lane * 4, hr = L_H2 + w * 256 + g * 16 + min(i15 & 3, 2u) * 1088, ga = L_GU + w * 512 + i15 * 16 + g * 4096, gr = L_GU + (u32)lane * 16;
    u32 sa = L_GU + 2048 + (u32)(lane >> 5) * 16 + w * 4, heo = WS_HE + (u32)(b - 128) * HSTR + (u32)(lane & 7) * 16u, px = piece(WS_X1, 4096, (u32)(b & 7)) + (u32)(lane & 7) * 16u;
    PIN(sa); PIN(heo); PIN(px);
    u32 so = piece(WS_YP, 16384, 8 * w + (u32)(lane >> 3)) + (gu_id * 8 + (lane & 7)) * 16, sco = WS_SC + (64 * w + (u32)lane) * 16u;
    u32 ta = L_W + w * 8192u + (8 * g + (i15 >> 2)) * 32 + (lane & 3) * 8, xo = piece(WS_X1, 4096, (u32)(b & 7)) + w * 1024u + (u32)lane * 16u;
    PIN(ha); PIN(hr); PIN(ga); PIN(gr); PIN(so); PIN(sco); PIN(ta); PIN(xo);
    barrier();
    STAMP(10);
    if (G_DELAY) until(now32() + G_DELAY);
    u32 wdr[16];
#pragma unroll
    for (int j = 0; j < 16; ++j) wdr[j] = ld_u32_nt(P.wd + j * 512, wdo);
#pragma unroll
    for (int s = 0; s < 4; ++s) { MDMA(P.wg + s * 32 * 1792, wo, L_W + w * 8192u + s * 1024u); MDMA(P.wu + s * 32 * 1792, wo, L_W + w * 8192u + 4096u + s * 1024u); }
    v4u mg[1], hh;
    UNDEF(mg[0]); UNDEF(hh);
    ld_sc_into(mg[0], ws, sco);
    ld_sc_into(hh, ws, heo);
    STAMP(2);
    VMWAIT(10);
#pragma unroll
    for (int j = 0; j < 16; j += 2) {
        PIN(wdr[j]); PIN(wdr[j + 1]);
        const u32 a = wdr[j], bb = wdr[j + 1];
        wdr[j] = perm_lo(a, bb); wdr[j + 1] = perm_hi(a, bb);
        PIN(wdr[j]); PIN(wdr[j + 1]);
    }
    VMWAIT(0);
    PIN(mg[0]); PIN(hh);
    STAMP(3);
    bf16x8 bg[4], bp[4];
#pragma unroll
    for (int s = 0; s < 4; ++s) { bg[s] = tr2(ta + s * 1024, ta + s * 1024 + 128); bp[s] = tr2(ta + 4096 + s * 1024, ta + 4096 + s * 1024 + 128); }
    if (__builtin_expect(ballot(((mg[0].z ^ tlo) | (mg[0].w ^ thi)) != 0u) != 0ull, 0)) gather<1, 0>(ws, sco, tlo, thi, mg, budget);
    float cb0 = 1.f + bflo(mg[0].x), cb1 = 1.f + bfhi(mg[0].x);
    *lp<u32>(ha + 1088) = pack2(cb0, cb1);
    *lp<u32>(ha + 2176) = mg[0].y;
    PIN(cb0); PIN(cb1);
    bf16x8 af[4];
    v4u yo = {0u, 0u, tlo, thi};
    PIN(yo);
#pragma unroll
    for (int s = 0; s < 4; ++s) { PIN(bg[s]); PIN(bp[s]); }
    STAMP(12);
    until(hint_wait<HE_K, true>(ws, heo, tlo, budget, hh) + E_DELAY);
    STAMP(8);
    v4u xg, xp;
    poll2<false>(xg, xp, xo, ws, px, ws, tlo, thi);
    STAMP(13);
    float pil = asf(xp.x) + asf(xp.y);
    pil += dpp<0xB1>(pil); pil += dpp<0x4E>(pil); pil += dpp<0x141>(pil);
    float d0 = __builtin_fmaf(pil, -0.0625f, asf(xg.x)), d1 = __builtin_fmaf(pil, -0.0625f, asf(xg.y));
    PIN(d0); PIN(d1);
    {
        float e0 = d0 * cb0;
        PIN(e0);
        *lp<u32>(ha) = pack2(e0, d1 * cb1);
    }
    asm volatile("" ::: "memory");
#pragma unroll
    for (int s = 0; s < 4; ++s) af[s] = *lp<bf16x8>(hr + s * 64);
    SCHED();
    float z = red32(d0 + d1, __builtin_fmaf(d0, d0, d1 * d1));
    z = red16(z, z); z = rowsum(z);
    *lp<float>(sa) = z;
    SCHED();
    f32x4 dg = {0.f, 0.f, 0.f, 0.f}, du = {0.f, 0.f, 0.f, 0.f};
#pragma unroll
    for (int s = 0; s < 4; ++s) {
        dg = __builtin_amdgcn_mfma_f32_16x16x32_bf16(af[s], bg[s], dg, 0, 0, 0);
        du = __builtin_amdgcn_mfma_f32_16x16x32_bf16(af[s], bp[s], du, 0, 0, 0);
    }
    *lp<f32x4>(ga) = dg; *lp<f32x4>(ga + 256) = du;
    barrier();
    STAMP(11);
    const f32x4 q1 = *lp<f32x4>(L_GU + 2048), q2 = *lp<f32x4>(L_GU + 2064);
    const f32x4 p0 = *lp<f32x4>(gr), p1 = *lp<f32x4>(gr + 1024);
    const float dm = sum4(q1) * (1.f / 512.f);
    const float rstd = __builtin_amdgcn_rsqf(__builtin_fmaxf(sum4(q2) * (1.f / 512.f) - dm * dm, 0.f) + 1e-5f);
    float gx = p0.x + p1.x, gy = p0.y + p1.y;
    PIN(gx); PIN(gy);
    float s = rstd * (gx - dm * gy) + (p0.z + p1.z);
    s = red32(s, s);
    u32 gs, us;
    both16(asu(s), gs, us);
    const float gsum = asf(gs), usum = asf(us);
    const float act = gsum * usum * __builtin_amdgcn_rcpf(1.f + ex2(gsum * __builtin_fmaf(gsum * gsum, -0.044715f * 1.5957691216057308f * 1.4426950408889634f, -1.5957691216057308f * 1.4426950408889634f)));
    const u32 actp = pack2(act, dpp<0x101>(act));
    asm volatile("s_nop 1\n\t"
        "v_dot2c_f32_bf16_dpp %0, %2, %3 row_newbcast:0 row_mask:0xf bank_mask:0xf\n\tv_dot2c_f32_bf16_dpp %1, %2, %4 row_newbcast:0 row_mask:0xf bank_mask:0xf\n\t"
        "v_dot2c_f32_bf16_dpp %0, %2, %5 row_newbcast:2 row_mask:0xf bank_mask:0xf\n\tv_dot2c_f32_bf16_dpp %1, %2, %6 row_newbcast:2 row_mask:0xf bank_mask:0xf\n\t"
        "v_dot2c_f32_bf16_dpp %0, %2, %7 row_newbcast:4 row_mask:0xf bank_mask:0xf\n\tv_dot2c_f32_bf16_dpp %1, %2, %8 row_newbcast:4 row_mask:0xf bank_mask:0xf\n\t"
        "v_dot2c_f32_bf16_dpp %0, %2, %9 row_newbcast:6 row_mask:0xf bank_mask:0xf\n\tv_dot2c_f32_bf16_dpp %1, %2, %10 row_newbcast:6 row_mask:0xf bank_mask:0xf\n\t"
        "v_dot2c_f32_bf16_dpp %0, %2, %11 row_newbcast:8 row_mask:0xf bank_mask:0xf\n\tv_dot2c_f32_bf16_dpp %1, %2, %12 row_newbcast:8 row_mask:0xf bank_mask:0xf\n\t"
        "v_dot2c_f32_bf16_dpp %0, %2, %13 row_newbcast:10 row_mask:0xf bank_mask:0xf\n\tv_dot2c_f32_bf16_dpp %1, %2, %14 row_newbcast:10 row_mask:0xf bank_mask:0xf\n\t"
        "v_dot2c_f32_bf16_dpp %0, %2, %15 row_newbcast:12 row_mask:0xf bank_mask:0xf\n\tv_dot2c_f32_bf16_dpp %1, %2, %16 row_newbcast:12 row_mask:0xf bank_mask:0xf\n\t"
        "v_dot2c_f32_bf16_dpp %0, %2, %17 row_newbcast:14 row_mask:0xf bank_mask:0xf\n\tv_dot2c_f32_bf16_dpp %1, %2, %18 row_newbcast:14 row_mask:0xf bank_mask:0xf\n\ts_nop 2"
        : "+v"(yo.x), "+v"(yo.y) : "v"(actp), "v"(wdr[0]), "v"(wdr[1]), "v"(wdr[2]), "v"(wdr[3]), "v"(wdr[4]), "v"(wdr[5]), "v"(wdr[6]), "v"(wdr[7]),
          "v"(wdr[8]), "v"(wdr[9]), "v"(wdr[10]), "v"(wdr[11]), "v"(wdr[12]), "v"(wdr[13]), "v"(wdr[14]), "v"(wdr[15]));
    st_sc(ws, so, yo);
    STAMP(14);
}

#define ISSUE(h) do { if ((h) & 1) dma_b(ckv + ktile(sidx, (h) >> 1), od, slot((h) >> 1) + p * 4096u, pcf, L_FL + C_LAND + ((h) >> 1) * 16u + p * 4u); else dma_a(ckv + ktile(sidx, (h) >> 1), od, slot((h) >> 1) + p * 4096u); } while (0)
#define FC_A 195
#define FC_B 332
#define WPOS 10
#define SPF 0
constexpr int NU = SPF ? 36 : 32;
constexpr int half_of(int u) { return !SPF || u < WPOS ? u : u < WPOS + 4 ? -1 : u - 4; }
constexpr int ops_in(int u0, int u1) { int n = 0; for (int u = u0; u < u1; ++u) n += 2 + (half_of(u) >= 0 ? half_of(u) & 1 : 0); return n; }
DI void publish(char* ws, u32 g1, const f32x16 (&o)[4], v4u (&d)[8]) {
#pragma unroll
    for (int k = 0; k < 8; k += 2) {
        u32 c[8];
#pragma unroll
        for (int i = 0; i < 4; ++i) { c[i] = pack2(o[0][2 * k + i], o[1][2 * k + i]); c[4 + i] = pack2(o[2][2 * k + i], o[3][2 * k + i]); }
        SCHED();
#pragma unroll
        for (int i = 0; i < 2; ++i) {
            auto r0 = __builtin_amdgcn_permlane16_swap(c[2 * i], c[2 * i + 1], false, false);
            auto r1 = __builtin_amdgcn_permlane16_swap(c[4 + 2 * i], c[5 + 2 * i], false, false);
            d[k + i] = (v4u){(u32)r0[0], (u32)r0[1], (u32)r1[0], (u32)r1[1]};
            st_sc_keep(ws + WS_PAY + (k + i) * PHD, g1, d[k + i]);
        }
        SCHED();
    }
}
DI void stream_pv(const Params& P, int b, u32 p, int lane, const u32 (&od)[4], u64 pcf, u32 t0, u32& tl) {
    const u32 tz = t0; (void)tz;
    char* const ws = P.ws;
    const u32 sidx = (u32)b - 128, i15 = lane & 15, R = lane >> 4;
    const char* ckv = (const char*)P.ckv;
    u32 va[2][4];
    {
        const u32 hf = R >> 1, blk = R & 1, q4 = i15 >> 2, p4 = i15 & 3;
#pragma unroll
        for (int e = 0; e < 2; ++e)
#pragma unroll
            for (int cb = 0; cb < 4; ++cb) {
                const u32 row = 8 * hf + 4 * e + q4;
                va[e][cb] = p * 4096u + row * 256u + (((4 * cb + 2 * blk + (p4 >> 1)) ^ swz(row)) << 4) + 8 * (p4 & 1);
                PIN(va[e][cb]);
            }
    }
    u32 fb = L_FL, pa = L_P + (u32)lane * 16u;
    PIN(fb); PIN(pa);
    bf16x8 V[8][4];
    u32 vbase = 0;
#define VBASE(t) do { if (vwin(t) != vbase) { _Pragma("unroll") for (int i_ = 0; i_ < 8; ++i_) { va[i_ >> 2][i_ & 3] += vwin(t) - vbase; PIN(va[i_ >> 2][i_ & 3]); } vbase = vwin(t); } } while (0)
#define VREAD(t) do { VBASE(t); _Pragma("unroll") for (int cb_ = 0; cb_ < 4; ++cb_) V[(t) & 7][cb_] = tr2(va[0][cb_] + (slot(t) - vwin(t)), va[1][cb_] + (slot(t) - vwin(t))); } while (0)
    u64 e64;
    EPOCH();
    const u32 tlo = (u32)e64;
    u32 g1, gc, lr;
    {
        const u32 k = 4 * (i15 >> 3) + (i15 & 3);
        g1 = (p * 128u + (sidx ^ ((tlo & 1) << 6))) * 256u + R * (8 * PHD) + i15 * 16u;
        gc = WS_CHK + (sidx * 4 + p) * 512u + (8 * R + k) * 16u;
        lr = L_L + (8 * (k >> 1) + 4 * (R >> 1) + 2 * (k & 1) + (R & 1)) * 16u;
        PIN(g1); PIN(gc); PIN(lr);
    }
    bool held = false;
    u32 wpf = ((sidx & 7) * 16 + (sidx >> 3)) * 32768u + p * 8192u + (u32)lane * 16u;
    PIN(wpf);
#pragma unroll
    for (int u = HW; u < NU; ++u) {
        const int h = half_of(u), l = half_of(u - HW);
        if (h >= 14 && h < 30 && !(h & 1)) wait1(fb, C_KRD + h / 2 - 7);
        if (u >= 12 && h < 30 && !held && rfl(flag_get(C_H)) != 0u) { wait4(fb, C_Q); held = true; }
        if (h == 30) wait4(fb, C_E);
        VMWAIT(ops_in(u - HW + 1, u));
        if (l == 1) STAMP(18);
        if (l == 7) STAMP(19);
        if (l == 15) STAMP(1);
        if (h == 24) STAMP(2);
        if (h >= 0) ISSUE(h);
        else { dma_pl(P.wo + (u - WPOS) * 1024, wpf, L_P + 14 * 1024); dma_pl(P.wo + (u - WPOS) * 1024 + 512, wpf, L_P + 14 * 1024); }
        if (l >= 0 && (l & 1) && l < 16) VREAD(l >> 1);
    }
    static_assert(WPOS >= HW && WPOS <= 24 && HW >= 3 && HW <= 6, "tile 12 must have landed here");
    f32x16 o[4];
#pragma unroll
    for (int cb = 0; cb < 4; ++cb)
#pragma unroll
        for (int i = 0; i < 16; ++i) o[cb][i] = 0.f;
    bf16x8 pb[4], pc[4];
#define PV(t, pp) do { SCHED(); _Pragma("unroll") for (int cb_ = 0; cb_ < 4; ++cb_) o[cb_] = __builtin_amdgcn_mfma_f32_32x32x16_bf16(pp[(t) & 3], V[(t) & 7][cb_], o[cb_], 0, 0, 0); SCHED(); } while (0)
#define PVR(t, pp, u) do { VBASE(u); _Pragma("unroll") for (int cb_ = 0; cb_ < 4; ++cb_) { SCHED(); \
        o[cb_] = __builtin_amdgcn_mfma_f32_32x32x16_bf16(pp[(t) & 3], V[(t) & 7][cb_], o[cb_], 0, 0, 0); SCHED(); \
        V[(u) & 7][cb_] = tr2(va[0][cb_] + (slot(u) - vwin(u)), va[1][cb_] + (slot(u) - vwin(u))); } SCHED(); } while (0)
    STAMP(3);
    if (p == 0) {
        const v4u z = *lp<v4u>(fb + C_TZ);
        const u32 t = now32();
        if (lane == 0) st_sc2(ws, WS_FC + sidx * 8u, (v2u){t + max(FC_A, FC_B - (int)min(min(t - z.x, t - z.y), min(t - z.z, t - z.w))), tlo});
    }
    wait4p(fb, C_P, pa, 0, pb);
    STAMP(7);
    CYC0();
    CDECL();
    CS(0);
#pragma unroll
    for (int t = 0; t < 5; ++t) { PVR(t, pb, t + 8); if (t < 4) pb[t] = *lp<bf16x8>(pa + (t + 4) * 1024); if (t == 0) CS(1); if (t == 1) CS(2); if (t == 3) CS(3); }
    CS(4);
    PV(5, pb); PV(6, pb);
    u32 pf = flag_get(C_P + 4);
#pragma unroll
    for (int i = 0; i < 4; ++i) pc[i] = *(volatile LAS bf16x8*)lp<bf16x8>(pa + (8 + i) * 1024);
    PV(7, pb);
    STAMP(4);
    CS(5);
    if (__builtin_expect(rfl(pf) != 0x01010101u, 0)) wait4p(fb, C_P + 4, pa, 8192, pc);
    CS(6);
    PV(8, pc);
    VMWAIT(5);
    PVR(9, pc, 13); PVR(10, pc, 14);
    pf = flag_get(C_P + 8);
#pragma unroll
    for (int i = 0; i < 3; ++i) pb[i] = *(volatile LAS bf16x8*)lp<bf16x8>(pa + (12 + i) * 1024);
    PV(11, pc);
    STAMP(5);
    CS(7);
    pf = rfl(pf);
    if (__builtin_expect(!(pf & 0x1u), 0)) pb[0] = wait1p(fb, C_P + 8, pa, 12 * 1024);
    PV(12, pb);
    if (__builtin_expect(!(pf & 0x100u), 0)) pb[1] = wait1p(fb, C_P + 9, pa, 13 * 1024);
    VBASE(15);
    PV(13, pb);
    if (__builtin_expect(!(pf & 0x10000u), 0)) pb[2] = wait1p(fb, C_P + 10, pa, 14 * 1024);
    VMWAIT(0);
    PVR(14, pb, 15);
    pb[3] = wait1p(fb, C_P + 11, pa, 15 * 1024);
    PV(15, pb);
    STAMP(6);
    STAMPC(29);
    CS(8);
    v4u d[8];
    publish(ws, g1, o, d);
    v2u fl = *(volatile LAS v2u*)lp<v2u>(fb + C_L);
    f32x4 lq = *(volatile LAS f32x4*)lp<f32x4>(lr);
    u32 mref = 0;
    STAMP(8);
    CS(9);
    u32 cs[8];
#pragma unroll
    for (int k = 0; k < 8; ++k) { cs[k] = (d[k].x + d[k].y) + (d[k].z + d[k].w); PIN(cs[k]); }
    if (__builtin_expect(rfl(fl.x) != 0x01010101u || rfl(fl.y) != 0u, 0)) {
        wait4(fb, C_L);
        if (rfl(flag_get(C_BAD)) != 0u) {
            barrier();
            barrier();
#pragma unroll
            for (int cb = 0; cb < 4; ++cb)
#pragma unroll
                for (int i = 0; i < 16; ++i) o[cb][i] = 0.f;
#pragma clang loop unroll(disable)
            for (u32 t = 0; t < 16; ++t) {
                dma_a(ckv + ktile(sidx, t), od, slot(15) + p * 4096u); dma_b(ckv + ktile(sidx, t), od, slot(15) + p * 4096u, pcf, L_FL + C_LAND);
                VMWAIT(0);
                VREAD(15);
                pc[3] = *lp<bf16x8>(pa + t * 1024);
                PV(15, pc);
                LGKM0();
            }
            publish(ws, g1, o, d);
            asm volatile("s_nop 1");
#pragma unroll
            for (int k = 0; k < 8; ++k) { cs[k] = (d[k].x + d[k].y) + (d[k].z + d[k].w); PIN(cs[k]); }
            mref = *(volatile LAS u32*)lp<u32>(L_M + (lr - L_L) / 4);
        }
        lq = *(volatile LAS f32x4*)lp<f32x4>(lr);
    }
    {
        u32 t0, t1, t2, t3, u0, u1;
        asm volatile("s_nop 1\n\t"
            "v_add_u32_dpp %0, %6, %6 row_ror:8 row_mask:0xf bank_mask:0x3\n\tv_add_u32_dpp %1, %7, %7 row_ror:8 row_mask:0xf bank_mask:0x3\n\t"
            "v_add_u32_dpp %2, %8, %8 row_ror:8 row_mask:0xf bank_mask:0x3\n\tv_add_u32_dpp %3, %9, %9 row_ror:8 row_mask:0xf bank_mask:0x3\n\t"
            "v_add_u32_dpp %0, %10, %10 row_ror:8 row_mask:0xf bank_mask:0xc\n\tv_add_u32_dpp %1, %11, %11 row_ror:8 row_mask:0xf bank_mask:0xc\n\t"
            "v_add_u32_dpp %2, %12, %12 row_ror:8 row_mask:0xf bank_mask:0xc\n\tv_add_u32_dpp %3, %13, %13 row_ror:8 row_mask:0xf bank_mask:0xc\n\t"
            "v_add_u32_dpp %4, %0, %0 row_half_mirror row_mask:0xf bank_mask:0x5\n\tv_add_u32_dpp %5, %1, %1 row_half_mirror row_mask:0xf bank_mask:0x5\n\t"
            "v_add_u32_dpp %4, %2, %2 row_half_mirror row_mask:0xf bank_mask:0xa\n\tv_add_u32_dpp %5, %3, %3 row_half_mirror row_mask:0xf bank_mask:0xa\n\t"
            "s_nop 0\n\t"
            "v_add_u32_dpp %4, %4, %4 quad_perm:[1,0,3,2] row_mask:0xf bank_mask:0xf\n\ts_nop 0\n\tv_add_u32_dpp %5, %5, %5 quad_perm:[1,0,3,2] row_mask:0xf bank_mask:0xf\n\t"
            "v_add_u32_dpp %4, %4, %4 quad_perm:[2,3,0,1] row_mask:0xf bank_mask:0xf\n\ts_nop 0\n\tv_add_u32_dpp %5, %5, %5 quad_perm:[2,3,0,1] row_mask:0xf bank_mask:0xf\n\ts_nop 0"
            : "=&v"(t0), "=&v"(t1), "=&v"(t2), "=&v"(t3), "=&v"(u0), "=&v"(u1)
            : "v"(cs[0]), "v"(cs[1]), "v"(cs[2]), "v"(cs[3]), "v"(cs[4]), "v"(cs[5]), "v"(cs[6]), "v"(cs[7]));
        const u32 x = (gc & 16u) ? u1 : u0;
        const v4u cg = {(gc & 32u) ? dppu<0x104>(x) : x, asu(sum4(lq)), mref, tlo};
        asm volatile("s_mov_b64 exec, %3\n\tglobal_store_dwordx4 %0, %1, %2 sc1\n\ts_mov_b64 exec, -1\n\ts_nop 0" :: "v"(gc), "v"(cg), "s"(ws), "s"(0x0f0f0f0f0f0f0f0full) : "memory");
    }
    CS(10);
    STAMP(9);
    COUT(11);
    barrier();
}

DI void p_write(const f32x4& s0, const f32x4& s1, u32 pa, f32x4& a, f32x4& c) {
#pragma unroll
    for (int k = 0; k < 4; ++k) a[k] = ex2(s0[k]);
#pragma unroll
    for (int k = 0; k < 4; ++k) c[k] = ex2(s1[k]);
    *lp<v2u>(pa) = (v2u){pack2(a.x, a.y), pack2(a.z, a.w)};
    *lp<v2u>(pa + 256) = (v2u){pack2(c.x, c.y), pack2(c.z, c.w)};
}
template <u32 KO> DI void qk_second(const bf16x8 (&q0)[16], const bf16x8 (&q1)[16], const u32 (&a)[16], u32 fw, u32 off, f32x4& s0, f32x4& s1, u64& cw) {
    bf16x8 fa[16];
    land_wait_k<KO>(fw, off, a[0], a[1], a[2], a[3], fa[0], fa[1], fa[2], fa[3]);
#pragma unroll
    for (int i = 0; i < 4; ++i) { s0[i] = 0.f; s1[i] = 0.f; }
#pragma unroll
    for (int s = 0; s < 16; ++s) {
        SCHED();
        s0 = __builtin_amdgcn_mfma_f32_16x16x32_bf16(fa[s], q0[s], s0, 0, 0, 0);
        if (s < KD - 4) { SCHED(); fa[2 * s + 4] = *lp<bf16x8>(a[2 * s + 4] + KO); SCHED(); }
        s1 = __builtin_amdgcn_mfma_f32_16x16x32_bf16(fa[s], q1[s], s1, 0, 0, 0);
        SCHED();
        if (s < KD - 4) fa[2 * s + 5] = *lp<bf16x8>(a[2 * s + 5] + KO);
        else if (s + KD < 16) fa[s + KD] = *lp<bf16x8>(a[s + KD] + KO);
    }
}
DI void stream_qk(const Params& P, int b, u32 w, int lane, u32 t0, int& budget, u32& tl) {
    char* const ws = P.ws;
    const int xc = b & 7, yy = (b >> 3) - 16;
    const u32 i15 = lane & 15, g = lane >> 4;
    u64 e64;
    EPOCH();
    const u32 tlo = (u32)e64, thi = (u32)(e64 >> 32);
    const u32 ak = g * 4096u + i15 * 256u + (swz(i15) << 4);
    const u32 c16 = i15 ^ g ^ ((w & 1) ? 12u : 0u);
    u32 qo = WS_QABS + (w ^ ((tlo & 1) << 1)) * 2048u + g * 256u + c16 * 16u, co = WS_QCHK + w * 2048u + (u32)lane * 16u;
    u32 va = L_IMG + ((i15 & 3) ^ 2) * 8192u + (8 * w + g) * 256u + (((i15 >> 2) ^ ((w & 1) ? 3u : 0u)) << 6) + ((i15 & 3) << 4);
    u32 fa = L_FL + w, fb = L_FL, fw = L_FL + 16 * w + (i15 & 3) * 4, pa = L_P + w * 1024u + (g >> 1) * 512u + i15 * 16u + (g & 1) * 8u;
    u32 bq[4];
#pragma unroll
    for (int k = 0; k < 4; ++k) { bq[k] = L_IMG + i15 * 256u + (((g * 4 + k) ^ (i15 & 3) ^ ((i15 & 8) ? 12u : 0u)) << 4); PIN(bq[k]); }
    PIN(qo); PIN(co); PIN(va); PIN(fa); PIN(fb); PIN(fw); PIN(pa);
    u32 pa4 = pa + 4096;
    PIN(pa4);
    v4u chk0, chk1;
    UNDEF(chk0); UNDEF(chk1);
#define REQ_Q() do { \
        _Pragma("unroll") for (int k = 0; k < 8; ++k) dma_sc(ws + (k & 1) * 1024 + (k >> 1) * 8192, qo, L_IMG + w * 2048u + (k & 1) * 1024u + ((k >> 1) ^ 2) * 8192u); \
        ld_sc_into(chk0, ws, co); ld_sc_into(chk1, ws + 1024, co); } while (0)
    u32 warm;
    UNDEF(warm);
    if (KMAP && w == 2) asm volatile("global_load_dword %0, %1, %2" : "+v"(warm) : "v"(((u32)lane & 15) * 2097152u + 2097148u), "s"(P.ckv) : "memory");
    if (w == 3) asm volatile("global_load_dword %0, %1, %2\n\tglobal_load_dword %0, %1, %3\n\tglobal_load_dword %0, %1, %4\n\tglobal_load_dword %0, %5, %6" : "+v"(warm)
        : "v"(((u32)lane & 1) * (1792u * 1024u - 4u)), "s"(P.wg), "s"(P.wu), "s"(P.wd), "v"(WS_PAY + (((u32)lane & 3) * 10 + 1) * PHD + 4 * 128 * 256), "s"(ws) : "memory");
    __builtin_amdgcn_s_setprio(3);
    bf16x8 k0[16], k1[16];
    land_wait(fw, C_LAND);
    VMWAIT(0);
    PIN(warm);
    {
        u32 a = ak + slot_no(w) * 16384u; PIN(a);
#pragma unroll
        for (int s = 0; s < 16; ++s) k0[s] = *lp<bf16x8>(a ^ (u32)(s << 4));
        flag_set(fa, C_KRD);
    }
    QTimer qt = {time_base(ws + WS_ST, t0) + Q_FIRST, 0u};
    u32 ka[16];
    {
        u32 a = ak + slot_no(w + 8) * 16384u; PIN(a);
#pragma unroll
        for (int s = 0; s < 16; ++s) { ka[s] = a ^ (u32)(s << 4); PIN(ka[s]); }
    }
    bool asked = false;
#pragma clang loop unroll(disable)
    while (ballot(*(volatile LAS u32*)lp<u32>(fw + C_LAND + 64) == 0u) != 0ull) {
        if (q_due(qt, ws, tlo)) {
            flag_set(fa, C_H);
            until(qt.t + Q_PRE);
            REQ_Q();
            flag_set(fa, C_Q);
            land_wait(fw, C_LAND + 64);
            asked = true;
            break;
        }
    }
    asm volatile("" ::: "memory");
    {
        u32 a = ak + slot_no(w + 4) * 16384u; PIN(a);
#pragma unroll
        for (int s = 0; s < 16; ++s) k1[s] = *lp<bf16x8>(a ^ (u32)(s << 4));
        flag_set(fa, C_KRD + 4);
    }
#pragma unroll
    for (int s = 0; s < 16; ++s) { PIN(k0[s]); PIN(k1[s]); }
    STAMP(1);
    if (!asked) {
#pragma clang loop unroll(disable)
        while (!q_due(qt, ws, tlo)) __builtin_amdgcn_s_sleep(1);
        flag_set(fa, C_H);
        until(qt.t + Q_PRE);
    }
    STAMP(22);
    u32 tz_;
#pragma clang loop unroll(disable)
    do {
        if (!asked) {
            REQ_Q();
            flag_set(fa, C_Q);
        }
        asked = false;
        STAMP(20);
        VMWAIT(0);
        tz_ = now32();
        STAMP(4);
        PIN(chk0); PIN(chk1);
        const v4u x0 = *(volatile LAS v4u*)lp<v4u>(va), x1 = *(volatile LAS v4u*)lp<v4u>(va ^ 16u), x2 = *(volatile LAS v4u*)lp<v4u>(va ^ 32u), x3 = *(volatile LAS v4u*)lp<v4u>(va ^ 48u);
        const v4u y0 = *(volatile LAS v4u*)lp<v4u>(va + 1024), y1 = *(volatile LAS v4u*)lp<v4u>((va ^ 16u) + 1024), y2 = *(volatile LAS v4u*)lp<v4u>((va ^ 32u) + 1024), y3 = *(volatile LAS v4u*)lp<v4u>((va ^ 48u) + 1024);
        const v4u xs = (x0 + x1) + (x2 + x3), ys = (y0 + y1) + (y2 + y3);
        const u32 bad = ((((xs.x + xs.y) + (xs.z + xs.w)) ^ chk0.x) | (chk0.z ^ tlo) | (chk0.w ^ thi)) | ((((ys.x + ys.y) + (ys.z + ys.w)) ^ chk1.x) | (chk1.z ^ tlo) | (chk1.w ^ thi));
        if (ballot(bad != 0u) == 0ull) break;
        __builtin_amdgcn_s_sleep(4);
    } while (--budget > 0);
    flag_set(fa, C_Z);
    *lp<u32>(L_FL + C_TZ + w * 4) = tz_;
    STAMP(5);
    wait4(fb, C_Z);
    STAMP(6);
    const u32 tz = t0; (void)tz;
    CYC0();
    u64 cs_[12];
    CS(0);
    f32x4 s[8];
#pragma unroll
    for (int i = 0; i < 4; ++i) s[i] = (f32x4){0.f, 0.f, 0.f, 0.f};
    bf16x8 q0[16], q1[16];
#define QREAD(s_) do { q0[s_] = *lp<bf16x8>(bq[(s_) & 3] + (((s_) >> 2) ^ 2) * 8192); q1[s_] = *lp<bf16x8>(bq[(s_) & 3] + (((s_) >> 2) ^ 2) * 8192 + 4096); } while (0)
#pragma unroll
    for (int i = 0; i < QD; ++i) QREAD(i);
#pragma unroll
    for (int i = 0; i < 16; ++i) {
        if (i + QD < 16) QREAD(i + QD);
        if (i + QD == 7) flag_set(fa, C_E);
        SCHED();
        s[0] = __builtin_amdgcn_mfma_f32_16x16x32_bf16(k0[i], q0[i], s[0], 0, 0, 0);
        s[1] = __builtin_amdgcn_mfma_f32_16x16x32_bf16(k0[i], q1[i], s[1], 0, 0, 0);
        s[2] = __builtin_amdgcn_mfma_f32_16x16x32_bf16(k1[i], q0[i], s[2], 0, 0, 0);
        s[3] = __builtin_amdgcn_mfma_f32_16x16x32_bf16(k1[i], q1[i], s[3], 0, 0, 0);
        SCHED();
    }
    STAMP(21);
    STAMPC(29);
    CS(1);
    float l0, l1;
    f32x4 e0, e1, e2, e3;
    p_write(s[0], s[1], pa, e0, e1);
    p_write(s[2], s[3], pa4, e2, e3);
    flag_set(fa, C_P);
    SCHED();
    STAMP(24);
    CS(2);
    l0 = sum4(e0 + e2); l1 = sum4(e1 + e3);
    pa4 = pa + 8192;
    PIN(l0); PIN(l1); PIN(pa4);
    qk_second<0>(q0, q1, ka, fw, C_LAND + 128, s[4], s[5], cs_[3]);
    CS(4);
    p_write(s[4], s[5], pa4, e0, e1);
    flag_set(fa, C_P + 4);
    SCHED();
    STAMP(26);
    CS(5);
    l0 += sum4(e0); l1 += sum4(e1);
    float m0 = __builtin_fmaxf(max8(s[0], s[2]), max4(s[4])), m1 = __builtin_fmaxf(max8(s[1], s[3]), max4(s[5]));
    u32 ln = (u32)lane; PIN(ln);
    u32 la = L_L + (ln & 31) * 16 + w * 4, pl = pa + 12288;
    PIN(l0); PIN(l1); PIN(m0); PIN(m1); PIN(la); PIN(pl);
    if (w == 3) wait4(fb, C_P);
    qk_second<49152>(q0, q1, ka, fw, C_LAND + 192, s[6], s[7], cs_[6]);
    CS(7);
    STAMP(27);
    p_write(s[6], s[7], pl, e0, e1);
    flag_set(fa, C_P + 8);
    SCHED();
    STAMP(28);
    CS(8);
    {
        float z = red16(l0 + sum4(e0), l1 + sum4(e1));
        *lp<float>(la) = red32(z, z);
    }
    m0 = __builtin_fmaxf(m0, max4(s[6])); m1 = __builtin_fmaxf(m1, max4(s[7]));
    if (ballot(!(__builtin_fmaxf(__builtin_fabsf(m0), __builtin_fabsf(m1)) <= LAZY)) != 0ull) flag_set(fa, C_BAD);
    flag_set(fa, C_L);
    CS(9);
    COUT(10);
    __builtin_amdgcn_s_setprio(0);
    wait4(fb, C_L);
    if (__builtin_expect(rfl(flag_get(C_BAD)) != 0u, 0)) {
        m0 = max32(max16(m0)); m1 = max32(max16(m1));
        *lp<float>(la + (L_IMG + 4096 - L_L)) = (ln & 16) ? m1 : m0;
        barrier();
        const float m = max4(*lp<f32x4>((la & ~15u) + (L_IMG + 4096 - L_L)));
        *lp<float>(L_M + (ln & 31) * 4) = m;
        u32 e, od;
        both16(asu(m), e, od);
        l0 = 0.f; l1 = 0.f;
#pragma unroll
        for (int i = 0; i < 4; ++i) { p_write(s[2 * i] - asf(e), s[2 * i + 1] - asf(od), pa + 4096 * i, e0, e1); l0 += sum4(e0); l1 += sum4(e1); }
        const float z = red16(l0, l1);
        *lp<float>(la) = red32(z, z);
        barrier();
    }
    if (yy < 12 || xc < 4) mlp_part(P, b, w, lane, tlo, thi, budget, tl);
}

#define D_DELAY 100
#define A2_T 165
#define PREW 1
#define ML_LEAD 60
#define FC_STEP 30
#define U_GAP 90
#define U_STEP 30
#define T_MID 220
#define WQL 1
#define CODE_Y 0x13
#define CODE_POL ""
__global__ void __launch_bounds__(512, 2) fused_kernel(Params) {
    v16u ka;
    v8u kb;
    v2u kc;
    asm volatile("s_load_dwordx16 %0, %3, 0x0\n\ts_load_dwordx8 %1, %3, 0x40\n\ts_load_dwordx2 %2, %3, 0x60" : "=&s"(ka), "=&s"(kb), "=&s"(kc) : "s"(__builtin_amdgcn_kernarg_segment_ptr()));
    SCHED();
    const int tid = threadIdx.x, lane = tid & 63;
    const int wave = __builtin_amdgcn_readfirstlane(tid >> 6);
    const int b = blockIdx.x, xc = b & 7, y = b >> 3;
    int budget = BUDGET;
    u32 tl = 0;
    u64 pc0;
    asm volatile("s_getpc_b64 %0\n\ts_nop 0" : "=s"(pc0));
    *lp<u32>(L_FL + (u32)(wave >> 2) * 1024 + (u32)(wave & 3) * 4 + (u32)lane * 16) = 0u;
    if (wave >= 4 && (y & CODE_Y) == 0x10) {
        asm volatile("s_mov_b32 m0, %1\n\ts_nop 0\n\tglobal_load_lds_dword %0, %2" CODE_POL "\n\tglobal_load_lds_dword %0, %3" CODE_POL
            :: "v"((u32)lane * 64u + (u32)(wave - 4) * CODE_STR), "s"(L_IMG + (u32)(wave - 4) * 2048u), "s"(pc0), "s"(pc0 + 4096) : "memory");
    }
    const u32 head = (u32)(xc * 4 + (y >> 2)), j = (u32)y & 3, w = (u32)wave;
    const u32 i15 = lane & 15, g = lane >> 4;
    const u32 ks = b & 15, cg = (u32)(b >> 4) * 2 + (w & 1);
    u32 od[4], oi[4], oq = ((j >> 1) * 256 + w * 32 + (lane & 8) * 2 + g) * 8192u + head * 256u + (j & 1) * 128u + (lane & 7) * 16u;
#pragma unroll
    for (int k = 0; k < 4; ++k) { od[k] = (3 * k + g) * 1024u + w * 256u + ((i15 ^ (g << 2) ^ (u32)k) << 4); PIN(od[k]); }
    oi[0] = (ks * 32 + 2 * (u32)(lane >> 2)) * 2; oi[1] = (u32)lane * 16; oi[2] = cg * 64u + (lane & 3) * 16u; oi[3] = oi[0] * 3072u + oi[2]; oi[2] += g * 4u;
#pragma unroll
    for (int k = 0; k < 4; ++k) PIN(oi[k]);
    PIN(oq);
    v4u wm[4], xa;
    u32 cc, xs;
#pragma unroll
    for (int k = 0; k < 4; ++k) UNDEF(wm[k]);
    UNDEF(xa); UNDEF(cc); UNDEF(xs);
    int role = (0xffaa5550u >> ((y & 16) + 2 * wave)) & 3;
    asm volatile("s_waitcnt lgkmcnt(0)" : "+s"(role), "+s"(ka), "+s"(kb), "+s"(kc) :: "memory");
#define KP(T, v, i) ((T)((u64)v[i + 1] << 32 | v[i]))
    Params P;
    P.wmod = KP(const u16*, ka, 0); P.c = KP(const u16*, ka, 2); P.x = KP(const u16*, ka, 4); P.ws = KP(char*, ka, 6);
    P.wq = KP(const u16*, ka, 8); P.ckv = KP(const u16*, ka, 10); P.wuk = KP(const u16*, ka, 12); P.wo = KP(const u16*, ka, 14);
    P.wuv = KP(const u16*, kb, 0); P.wg = KP(const u16*, kb, 2); P.wu = KP(const u16*, kb, 4); P.wd = KP(const u16*, kb, 6); P.out = KP(u16*, kc, 0);
    char* const ws = P.ws;
    if (role == 0) {
        asm volatile("global_load_dword %0, %3, %4\n\tglobal_load_dwordx4 %1, %5, %6\n\tglobal_load_dword %2, %7, %6" : "+v"(cc), "+v"(xa), "+v"(xs)
            : "v"(oi[0]), "s"(P.c), "v"(oi[1]), "s"(P.x), "v"(oi[2]) : "memory");
        asm volatile("global_load_dwordx4 %0, %4, %5 nt\n\tglobal_load_dwordx4 %1, %4, %6 nt\n\tglobal_load_dwordx4 %2, %4, %5 offset:1024 nt\n\tglobal_load_dwordx4 %3, %4, %6 offset:1024 nt"
            : "+v"(wm[0]), "+v"(wm[1]), "+v"(wm[2]), "+v"(wm[3]) : "v"(oi[3]), "s"(P.wmod), "s"(P.wmod + 3072) : "memory");
    }
    const char* const ckv = (const char*)P.ckv;
    const u32 sidx = (u32)b - 128;
    const u64 pcf = pc0 - w * 256u;
    if (T_OPEN0 == 0 && role == 2) { const u32 p = w; ISSUE(0); ISSUE(1); }
    __builtin_amdgcn_s_barrier();
    u64 t0_;
    asm volatile("s_memrealtime %0" : "=s"(t0_));
    v4u wr[4];
#pragma unroll
    for (int k = 0; k < 4; ++k) UNDEF(wr[k]);
    if (WQL ? role == 1 : role < 2) {
#pragma unroll
        for (int k = 0; k < 4; ++k) ld_nt_into(wr[k], P.wq + k * 16384, oq);
    }
    asm volatile("s_waitcnt lgkmcnt(0)" : "+s"(t0_));
    const u32 t0 = (u32)t0_;
    STAMP(20);
    asm volatile("" : "+s"(role));
    if (role >= 2) {
        if (role == 2) {
            const u32 p = w;
#pragma unroll
            for (int h = T_OPEN0 ? 0 : 2; h < HW; ++h) {
                if (h == 0) until(t0 + T_OPEN0);
                if (h == 2) until(t0 + T_OPEN1);
                if (h == 4 && T_OPEN2 > T_OPEN1) until(t0 + T_OPEN2);
                ISSUE(h);
            }
            stream_pv(P, b, w, lane, od, pcf, t0, tl);
        } else stream_qk(P, b, w - 4, lane, t0, budget, tl);
        STAMP(15);
        STAMP_OUT();
        return;
    }
    u64 e64;
    u32 tb, tq;
    {
        const u32 p = lane & 3;
        v4u wk[8];
#pragma unroll
        for (int k = 0; k < 8; ++k) UNDEF(wk[k]);
        if (b < 8 && tid == 128) st_sc2(ws, WS_ST + (u32)b * 8u, (v2u){t0, ~t0});
        u32 warm;
        UNDEF(warm);
        if (wave == 7) asm volatile("global_load_dword %0, %1, %2\n\tglobal_load_dword %0, %1, %3\n\tglobal_load_dword %0, %1, %4\n\tglobal_load_dword %0, %5, %6" : "+v"(warm)
            : "v"(((u32)lane & 1) * ((4u << 20) - 4u)), "s"(P.wuk), "s"(P.wo), "s"(P.wuv), "v"(WS_PAY + (((u32)lane & 3) * 10 + 1) * PHD + 4 * 128 * 256), "s"(ws) : "memory");
        EPOCH();
        const u32 tlo = (u32)e64, thi = (u32)(e64 >> 32);
        if (PREW && wave == 3) {
            const u32 rh = 8 * ((head & 7) >> 1) + 4 * (head >> 4) + 2 * (head & 1) + ((head >> 3) & 1);
            if (j == 0 && lane < 16) st_sc(ws, WS_QCHK + rh * 256u + (u32)lane * 16u, (v4u){0u, 0u, ~tlo, 0u});
            if (b == 9 && lane < 8 * NQH) st_sc(ws, WS_QH + (u32)lane * 16u, (v4u){0u, ~tlo, 0u, ~tlo});
            if (b == 10) st_sc(ws, WS_FC + (u32)lane * 16u, (v4u){0u, ~tlo, 0u, ~tlo});
        }
        if (role == 0) {
            u32 so = piece(WS_HP, 2048, cg) + (ks * 8 + 2 * p + (u32)(lane >> 5)) * 16u;
            VMWAIT(WQL ? 4 : 8);
            PIN(xa); PIN(xs); PIN(cc);
            float mean, rstd;
            ln_stats(xa, mean, rstd);
            const u32 sp = pack2(silu(bflo(cc)), silu(bfhi(cc)));
            u32 s0l = sp & 0xffffu, s0h = sp << 16, s1l = sp >> 16, s1h = sp & 0xffff0000u;
            float xn0 = (bflo(xs) - mean) * rstd, xn1 = (bfhi(xs) - mean) * rstd;
            PIN(xn0); PIN(xn1); PIN(s0l); PIN(s0h); PIN(s1l); PIN(s1h); PIN(so);
            VMWAIT(WQL ? 0 : 4);
#pragma unroll
            for (int k = 0; k < 4; ++k) PIN(wm[k]);
            STAMP(22);
            float ta[4], tc[4];
#pragma unroll
            for (int k = 0; k < 2; ++k) {
                ta[2 * k] = red32(dot2(wm[1][k], s1l, dot2(wm[0][k], s0l, 0.f)), dot2(wm[1][k + 2], s1l, dot2(wm[0][k + 2], s0l, 0.f)));
                ta[2 * k + 1] = red32(dot2(wm[1][k], s1h, dot2(wm[0][k], s0h, 0.f)), dot2(wm[1][k + 2], s1h, dot2(wm[0][k + 2], s0h, 0.f)));
                tc[2 * k] = red32(dot2(wm[3][k], s1l, dot2(wm[2][k], s0l, 0.f)), dot2(wm[3][k + 2], s1l, dot2(wm[2][k + 2], s0l, 0.f)));
                tc[2 * k + 1] = red32(dot2(wm[3][k], s1h, dot2(wm[2][k], s0h, 0.f)), dot2(wm[3][k + 2], s1h, dot2(wm[2][k + 2], s0h, 0.f)));
            }
            float u0 = xn0 * (red16(tc[0], tc[2]) + 0.015625f) + red16(ta[0], ta[2]), u1 = xn1 * (red16(tc[1], tc[3]) + 0.015625f) + red16(ta[1], ta[3]);
            u0 += dpp<0x124>(u0); u1 += dpp<0x124>(u1); u0 += dpp<0x128>(u0); u1 += dpp<0x128>(u1);
            u32 e, od;
            both16(pack2(u0, u1), e, od);
            if ((lane & 28) == 0) st_sc(ws, so, (v4u){e, od, tlo, thi});
            STAMP(24);
            if (WQL) {
#pragma unroll
                for (int k = 0; k < 4; ++k) ld_nt_into(wr[k], P.wq + k * 16384, oq);
            }
        }
        u32 so = piece(WS_QP, 1024, head) + (((j & 2) + (u32)(lane >> 5)) * 16 + (j & 1) * 8 + (lane & 7)) * 16u, ho = piece(WS_HP, 2048, (j >> 1) * 8 + w) + (u32)lane * 16u;
        u32 la = F_QP + w * 256 + (g * 8 + (lane & 7)) * 8, lb = la + (lane & 8) * 128, ok = ((u32)y * 32 + g * 8) * 8192u + ((u32)xc * 4 + (w & 3)) * 256u + i15 * 16u;
        PIN(so); PIN(ho); PIN(la); PIN(lb); PIN(ok);
        u32 onel = 0x3f80u, oneh = 0x3f800000u;
        PIN(onel); PIN(oneh);
        v4u hg[2];
        UNDEF(hg[0]); UNDEF(hg[1]);
        __builtin_amdgcn_s_barrier();
        const u32 tm = now32();
        until(t0 + ST_LOOK);
        tb = time_base(ws + WS_ST, t0);
        const u32 ta = tb + max((u32)A_FIRST, tm - t0 + A_OWN);
        until(ta);
        ld_sc_into(hg[0], ws, ho); ld_sc_into(hg[1], ws + 1024, ho);
        __builtin_amdgcn_s_barrier();
        if (wave >= 4) {
#pragma unroll
            for (int k = 0; k < 8; ++k) ld_nt_into(wk[k], P.wuk + k * 4096, ok);
            VMWAIT(10);
        } else VMWAIT(2);
#pragma unroll
        for (int k = 0; k < 4; ++k) PIN(wr[k]);
        PIN(warm);
        STAMP(1);
        pair_rows(wr[0], wr[1]); pair_rows(wr[2], wr[3]);
#pragma unroll
        for (int k = 0; k < 4; ++k) PIN(wr[k]);
        if (wave >= 4) VMWAIT(8); else VMWAIT(0);
        PIN(hg[0]); PIN(hg[1]);
        if (__builtin_expect(ballot(neq3(hg[0].z, hg[1].z, tlo) | neq3(hg[0].w, hg[1].w, thi)) != 0ull, 0)) gather<2, 1024, 1>(ws, ho, tlo, thi, hg, budget);
        STAMP(3);
        {
            float hx = red16(red32(dot2(hg[1].x, onel, bflo(hg[0].x)), dot2(hg[1].y, onel, bflo(hg[0].y))), red32(dot2(hg[1].x, oneh, bfhi(hg[0].x)), dot2(hg[1].y, oneh, bfhi(hg[0].y))));
            hx += dpp<0x128>(hx);
            hx = asf((u32)__builtin_amdgcn_update_dpp((int)asu(hx), (int)asu(hx), 0x114, 0xf, 0x6, false));
            const u32 hp = pack2(hx, dpp<0xB1>(hx)), h01 = dppu<0x00>(hp), h23 = dppu<0xAA>(hp);
            float acc[8];
#pragma unroll
            for (int k = 0; k < 4; ++k) { acc[2 * k] = dot2(wr[2][k], h23, dot2(wr[0][k], h01, 0.f)); acc[2 * k + 1] = dot2(wr[3][k], h23, dot2(wr[1][k], h01, 0.f)); }
            float t[4];
#pragma unroll
            for (int k = 0; k < 4; ++k) t[k] = red32(acc[k], acc[k + 4]);
            float u0 = red16(t[0], t[2]), u1 = red16(t[1], t[3]);
            u0 += dpp<0x128>(u0); PIN(u0); u1 += dpp<0x128>(u1);
            *lp<f32x2>(la) = (f32x2){u0, u1};
        }
        barrier();
        if (wave == 0) {
            const LAS f32x2* qp = lp<f32x2>(lb);
            const f32x2 q2 = (qp[0] + qp[32]) + (qp[64] + qp[96]);
            float u0 = q2.x, u1 = q2.y;
            u0 += dpp<0x128>(u0); PIN(u0); u1 += dpp<0x128>(u1);
            u32 e, od;
            both16(pack2(u0, u1), e, od);
            if ((lane & 24) == 0) st_sc(ws, so, (v4u){e, od, tlo, thi});
            STAMP(2);
        }
        tq = ta + max((u32)A2_T, now32() - ta + A2_DELAY);
        if (y == ((xc * 5) & 7) + (y & 8) && tid < NQH) st_sc2(ws, WS_QH + (u32)lane * 128u + (u32)(xc * 2 + (y >> 3)) * 8u, (v2u){tq, tq ^ tlo});
        if (wave >= 4) {
            const u32 hk = (u32)xc * 4 + (w & 3);
            const u32 rho = 8 * ((hk & 7) >> 1) + 4 * (hk >> 4) + 2 * (hk & 1) + ((hk >> 3) & 1);
            const u32 og = piece(WS_QP, 1024, hk) + (u32)lane * 16u;
            u32 sq = WS_QABS + j * 8192u + (rho ^ ((tlo & 1) << 4)) * 256u + (u32)(y >> 2) * 64u + g * 16u, sc = WS_QCHK + (rho * 16 + (u32)y) * 16u;
            PIN(sq); PIN(sc);
            v4u qg[1];
            until(tq);
            gather<1, 0, 1>(ws, og, tlo, thi, qg, budget);
            __builtin_amdgcn_s_barrier();
            STAMP(23);
            u32 e, od, q0, q1, q2, q3;
            both16(pack2(red32(bflo(qg[0].x), bflo(qg[0].y)), red32(bfhi(qg[0].x), bfhi(qg[0].y))), e, od);
            both32(e, q0, q1); both32(od, q2, q3);
            float a[8];
#pragma unroll
            for (int k = 0; k < 8; ++k) { PIN(wk[k]); a[k] = dot2(wk[k][3], q3, dot2(wk[k][2], q2, dot2(wk[k][1], q1, dot2(wk[k][0], q0, 0.f)))); }
            float z[4];
#pragma unroll
            for (int k = 0; k < 4; ++k) {
                const float keep = (lane & 1) ? a[k + 4] : a[k], give = (lane & 1) ? a[k] : a[k + 4];
                float s = keep + dpp<0xB1>(give);
                s += dpp<0x122>(s); s += dpp<0x124>(s); s += dpp<0x128>(s);
                z[k] = s * (0.08838834764831845f * 1.4426950408889634f);
            }
            const u32 y0 = pack2(z[0], z[1]), y1 = pack2(z[2], z[3]);
            const u32 y2 = dppu<0xB1>(y0), y3 = dppu<0xB1>(y1);
            if (i15 == 0) st_sc(ws, sq, (v4u){y0, y1, y2, y3});
            u32 c0, c1;
            both16((y0 + y1) + (y2 + y3), c0, c1);
            both32(c0 + c1, c0, c1);
            if (lane == 0) st_sc(ws, sc, (v4u){c0 + c1, 0u, tlo, thi});
            STAMP(4);
        } else __builtin_amdgcn_s_barrier();
    }
    const u32 tlo = (u32)e64, thi = (u32)(e64 >> 32);
    const bool is_red = (y >> 2) == 1;
    const u32 rid = (u32)xc * 4 + j;
    bf16x8 bo[16], bu[4];
    u32 xo, aa, cw, cr, op, o, oc, wa, he, wl, rl, om, xr;
#define TAIL_ADDR() do { \
        xo = piece(WS_X1, 4096, (u32)(lane >> 3)) + (rid * 8 + (u32)(lane & 7)) * 16u; \
        aa = F_A + (i15 & 7) * 272 + g * 16; cw = F_CX + ((w - 2 * j) & 7) * 32 + i15 * 2; cr = F_CX + g * 16; \
        op = piece(WS_OUTP, 16384, 4 * w + g) + (u32)b * 128u + (i15 & 7) * 16u; \
        wl = F_LS + (i15 < 4 ? (w * 16 + g * 4 + i15) * 4 : 512u); rl = F_LS + (u32)lane * 8; PIN(wl); PIN(rl); \
        o = WS_PAY + head * PHD + (j * 128 + ((w * 16) ^ ((tlo & 1) << 6))) * 256u + (u32)lane * 16u; \
        oc = WS_CHK + ((w * 16 + (i15 & 3) * 4 + g) * 4 + j) * 512u + head * 16u; \
        wa = F_A + w * 272 + i15 * 16 + g * 4; \
        om = WS_ML + (rid >> 1) * 4096u + (g * 16 + (rid & 1) * 8 + (u32)(lane & 7)) * 16u; PIN(om); \
        he = WS_HE + (((u32)b + 17u * (u32)lane) & 127u) * HSTR + (u32)lane * 16u; PIN(he); \
        PIN(xo); PIN(aa); PIN(cw); PIN(cr); PIN(op); PIN(o); PIN(oc); PIN(wa); } while (0)
    {
        const u32 kq = (u32)b & 3, cgl = (u32)b >> 2;
        const u32 w4 = w & 3, mrow = kq * 128 + w4 * 32 + (u32)(lane >> 3);
        const u32 q4 = i15 >> 2, p4 = lane & 3;
        const u32 ra = 8 * (g & 1) + q4, rb = ra + 4;
        u32 xa[4], xb[4];
#pragma unroll
        for (int t2 = 0; t2 < 4; ++t2) {
            const u32 c0 = 8 * w + (p4 >> 1) + 2 * t2, x0 = F_WO + (8 * g + q4) * 1024 + 8 * (p4 & 1);
            xa[t2] = x0 + ((c0 ^ swz(ra)) << 4); xb[t2] = x0 + 4096 + ((c0 ^ swz(rb)) << 4);
            PIN(xa[t2]); PIN(xb[t2]);
        }
        u32 x0, x1, ma = F_ML + w * 256 + ((lane & 7) * 4 + g) * 8;
        {
            const u32 c0 = 2 * w + (p4 >> 1);
            x0 = F_UV + (8 * g + q4) * 256 + 8 * (p4 & 1) + ((c0 ^ swz(ra)) << 4); x1 = F_UV + (8 * g + q4 + 4) * 256 + 8 * (p4 & 1) + ((c0 ^ swz(rb)) << 4);
        }
        PIN(x0); PIN(x1); PIN(ma);
#define PIECE(k) until(tq + T_OWN + (k) * O_STEP)
#define WO_ROW(s, k) dma_pl(P.wo + ((j + (s)) & 3) * 16384, (head * 128 + 8 * w4 + (k)) * 1024u + (((u32)lane ^ swz(8 * (w4 & 1) + (k))) << 4), F_WO + ((s) * 32 + 8 * w4 + (k)) * 1024u)
#define UV_ROWS(i) do { const u32 e_ = w4 * 32 + (i) * 4 + g, c_ = ((e_ >> 2) & 1) * 64 + (e_ & 1) * 32 + ((e_ >> 1) & 1) * 16 + (e_ >> 3); \
            dma_nt(P.wuv, (j * 128 + c_) * 8192u + head * 256u + ((i15 ^ swz(e_ & 15)) << 4), F_UV + (w4 * 32 + (i) * 4) * 256u); } while (0)
        v4u mw[4];
        u32 cv[4];
#pragma unroll
        for (int k = 0; k < 4; ++k) { UNDEF(mw[k]); UNDEF(cv[k]); }
        u32 mo = mrow * 6144u + 2048u + cgl * 128u + (u32)(lane & 7) * 16u;
        PIN(mo);
        if (wave < 4) {
#pragma unroll
            for (int k = 0; k < 4; ++k) { PIECE(k); WO_ROW(0, 2 * k); WO_ROW(0, 2 * k + 1); }
            asm volatile("global_load_ushort %0, %4, %5\n\tglobal_load_ushort %1, %4, %5 offset:16\n\tglobal_load_ushort %2, %4, %5 offset:32\n\tglobal_load_ushort %3, %4, %5 offset:48"
                : "+v"(cv[0]), "+v"(cv[1]), "+v"(cv[2]), "+v"(cv[3]) : "v"(mrow * 2), "s"(P.c) : "memory");
            PIECE(4);
            ld_nt_into(mw[0], P.wmod, mo); ld_nt_into(mw[1], P.wmod + 8 * 3072, mo);
            VMWAIT(2);
        }
        TAIL_ADDR();
        until(tq + T_MID);
        STAMP(5);
        barrier();
        if (wave >= 4) {
#pragma unroll
            for (int k = 8; k < 32; ++k) WO_ROW(k >> 3, k & 7);
            STAMP(19);
        }
#pragma unroll
        for (int t2 = 0; t2 < 4; ++t2) { bo[4 * t2] = tr2(xa[t2], xb[t2]); xa[t2] += 32768; xb[t2] += 32768; PIN(xa[t2]); PIN(xb[t2]); }
        float u[2] = {0.f, 0.f};
        if (wave < 4) {
            const u32 t2 = now32() + U_GAP;
            until(t2);
            ld_nt_into(mw[2], P.wmod + 16 * 3072, mo); ld_nt_into(mw[3], P.wmod + 24 * 3072, mo);
#pragma unroll
            for (int k = 0; k < 4; ++k) { until(t2 + (k + 1) * U_STEP); UV_ROWS(2 * k); UV_ROWS(2 * k + 1); }
            VMWAIT(8);
#pragma unroll
            for (int k = 0; k < 4; ++k) { PIN(mw[k]); PIN(cv[k]); }
            const u32 sp = pack2(silu(bflo(cv[0])), silu(bflo(cv[1]))), sr = pack2(silu(bflo(cv[2])), silu(bflo(cv[3])));
            pair_rows(mw[0], mw[1]); pair_rows(mw[2], mw[3]);
            float t[4];
#pragma unroll
            for (int k = 0; k < 2; ++k) {
                t[2 * k] = red32(dot2(mw[2][k], sr, dot2(mw[0][k], sp, 0.f)), dot2(mw[2][k + 2], sr, dot2(mw[0][k + 2], sp, 0.f)));
                t[2 * k + 1] = red32(dot2(mw[3][k], sr, dot2(mw[1][k], sp, 0.f)), dot2(mw[3][k + 2], sr, dot2(mw[1][k + 2], sp, 0.f)));
            }
#pragma unroll
            for (int k = 0; k < 2; ++k) { float z = red16(t[k], t[k + 2]); z += dpp<0x128>(z); u[k] = z; }
        } else VMWAIT(0);
        PIN(u[0]); PIN(u[1]);
#pragma unroll
        for (int t2 = 0; t2 < 4; ++t2) PIN(bo[4 * t2]);
        STAMP(14);
        barrier();
        *lp<f32x2>(ma) = (f32x2){u[0], u[1]};
        if (tid < 12) *lp<u32>(F_ST + tid * 4) = 0u;
#pragma unroll
        for (int t2 = 0; t2 < 4; ++t2) { bo[4 * t2 + 1] = tr2(xa[t2], xb[t2]); bo[4 * t2 + 2] = tr2(xa[t2] + 32768, xb[t2] + 32768); bo[4 * t2 + 3] = tr2(xa[t2] + 65536, xb[t2] + 65536); }
        xr = ld_u32(P.x, (rid * 8 + (u32)(lane & 7)) * 4);
        VMWAIT(0);
        PIN(xr);
        if (wave >= 4 && T_MLP && (y < 12 || xc < 4)) {
            const u32 pg = (j * 128 + w4 * 32 + (u32)(lane >> 3)) * 3584u + ((u32)(y >> 2) * 8 + (u32)xc) * 128u + (u32)(lane & 7) * 16u;
#pragma unroll
            for (int k = 0; k < 4; ++k) { dma_pl(P.wg + k * 8 * 1792, pg, F_JUNK + w * 1024u); dma_pl(P.wu + k * 8 * 1792, pg, F_JUNK + w * 1024u); }
        }
#pragma unroll
        for (int k = 0; k < 16; ++k) PIN(bo[k]);
        STAMP(6);
        barrier();
#pragma unroll
        for (int s = 0; s < 4; ++s) bu[s] = tr2(x0 + s * 8192, x1 + s * 8192);
        if (wave == 3) {
            const LAS f32x2* q = lp<f32x2>(F_ML) + lane;
            const f32x2 s = q[0] + q[64];
            const float sx = red32(s.x, s.x), sy = red32(s.y, s.y);
            if (lane < 32) st_sc(ws, WS_ML + (cgl & 7) * 8192u + ((u32)lane >> 4) * 4096u + (cgl >> 3) * 1024u + (kq * 16 + ((u32)lane & 15)) * 16u, (v4u){asu(sx), asu(sy), tlo, thi});
        }
#pragma unroll
        for (int k = 0; k < 4; ++k) PIN(bu[k]);
    }
    float ga0 = 0.f, ga1 = 0.f, gb0 = 0.f, gb1 = 0.f, xv0 = 0.f, xv1 = 0.f;
    STAMP(7);
    {
        v4u v[4], ck = {0u, 0u, 0u, 0u};
        u32 onel = 0x3f80u, oneh = 0x3f800000u;
        PIN(onel); PIN(oneh);
#pragma unroll
        for (int k = 0; k < 4; ++k) UNDEF(v[k]);
        u32 of = WS_FC + (u32)lane * 16u;
        PIN(of);
        u32 tlook;
        {
            v4u fc[1];
#pragma clang loop unroll(disable)
            do {
                pass<0>(ws, of, fc);
                if (ballot(neq3(fc[0].y, fc[0].w, tlo)) == 0ull) break;
                until(now32() + FC_STEP);
            } while (--budget > 0);
            const u32 now = now32();
            u32 m = max(fc[0].x + 1000u - now, fc[0].z + 1000u - now), e, od;
            m = max(m, dppu<0x128>(m)); m = max(m, dppu<0x124>(m)); m = max(m, dppu<0x122>(m)); m = max(m, dppu<0x121>(m));
            both16(m, e, od); both32(max(e, od), e, od);
            m = rfl(max(e, od));
            tlook = m < 2000u ? now - 1000u + m : now;
        }
        if (is_red && wave == 0) {
            until(tlook - ML_LEAD);
            v4u gm[4];
            gather<4, 1024, 1>(ws, om, tlo, thi, gm, budget);
            xv0 = bflo(xr); xv1 = bfhi(xr);
            {
                const float s0 = red16(red32(asf(gm[1].x), asf(gm[2].x)), red32(asf(gm[1].y), asf(gm[2].y)));
                u32 e, od, sbp, cbp;
                both16(asu(s0), e, od);
                both32(pack2(asf(e), asf(od)), sbp, cbp);
                if (lane < 8) st_sc(ws, WS_SC + (rid * 8 + (u32)lane) * 16u, (v4u){cbp, sbp, tlo, thi});
            }
            ga0 = asf(gm[0].x); ga1 = asf(gm[0].y); gb0 = asf(gm[3].x); gb1 = asf(gm[3].y);
            ga0 = red32(ga0, ga0); ga0 = red16(ga0, ga0); ga1 = red32(ga1, ga1); ga1 = red16(ga1, ga1);
            gb0 = red32(gb0, gb0); gb0 = red16(gb0, gb0); gb1 = red32(gb1, gb1); gb1 = red16(gb1, gb1);
        }
        PIN(ga0); PIN(ga1); PIN(gb0); PIN(gb1); PIN(xv0); PIN(xv1);
        until(tlook);
        STAMP(8);
#pragma clang loop unroll(disable)
        for (;;) {
            asm volatile("global_load_dwordx4 %0, %5, %7 sc1\n\tglobal_load_dwordx4 %1, %5, %7 offset:1024 sc1\n\tglobal_load_dwordx4 %2, %5, %7 offset:2048 sc1\n\tglobal_load_dwordx4 %3, %5, %7 offset:3072 sc1\n\t"
                "s_mov_b64 exec, %8\n\ts_waitcnt vmcnt(3)\n\tglobal_load_dwordx4 %4, %6, %7 sc1\n\ts_mov_b64 exec, -1"
                : "+v"(v[0]), "+v"(v[1]), "+v"(v[2]), "+v"(v[3]), "+v"(ck) : "v"(o), "v"(oc), "s"(ws), "s"(0x000f000f000f000full) : "memory");
            float a[8];
#pragma unroll
            for (int q = 0; q < 4; ++q) { a[2 * q] = bflo(v[0][q]); a[2 * q + 1] = bfhi(v[0][q]); }
            u32 x = (v[0].x + v[0].y) + (v[0].z + v[0].w);
#pragma unroll
            for (int k = 1; k < 4; ++k) {
#pragma unroll
                for (int q = 0; q < 8; ++q) PIN(a[q]);
                PIN(x);
                asm volatile("s_waitcnt vmcnt(%1)" : "+v"(v[k]) : "i"(4 - k) : "memory");
                x = (x + v[k].x + v[k].y) + (v[k].z + v[k].w);
#pragma unroll
                for (int q = 0; q < 4; ++q) { a[2 * q] = dot2(v[k][q], onel, a[2 * q]); a[2 * q + 1] = dot2(v[k][q], oneh, a[2 * q + 1]); }
            }
            float t[4];
            x += dppu<0x128>(x);
#pragma unroll
            for (int k = 0; k < 4; ++k) t[k] = red32(a[k], a[k + 4]);
            x += dppu<0x124>(x);
            float u0 = red16(t[0], t[2]); PIN(u0);
            *lp<u32>(wa) = pack2(u0, red16(t[1], t[3]));
            PIN(x);
            asm volatile("s_waitcnt vmcnt(0)" : "+v"(ck) :: "memory");
            *lp<u32>(wl) = ck.y;
            x -= ck.x;
            x += dppu<0xB1>(x); x += dppu<0x4E>(x);
            const u64 bad = ballot((x | (ck.w ^ tlo)) != 0u) & 0x000f000f000f000full;
            if (__builtin_expect((bad | ballot(ck.z != 0u)) == 0ull, 1)) break;
            if (bad == 0ull || --budget <= 0) {
                const float m = i15 < 4 ? asf(ck.z) : -3.0e38f;
                float M = __builtin_fmaxf(m, dpp<0xB1>(m)); M = __builtin_fmaxf(M, dpp<0x4E>(M));
                M = asf(rfl(asu(max32(max16(M)))));
                const u32 f = pack2(ex2(m - M), 0.f);
                const u32 f0 = dppu<0x150>(f), f1 = dppu<0x151>(f), f2 = dppu<0x152>(f), f3 = dppu<0x153>(f);
#pragma unroll
                for (int q = 0; q < 4; ++q) {
                    a[2 * q] = dot2(v[3][q], f3, dot2(v[2][q], f2, dot2(v[1][q], f1, dot2(v[0][q], f0, 0.f))));
                    a[2 * q + 1] = dot2(v[3][q], f3 << 16, dot2(v[2][q], f2 << 16, dot2(v[1][q], f1 << 16, dot2(v[0][q], f0 << 16, 0.f))));
                }
#pragma unroll
                for (int k = 0; k < 4; ++k) t[k] = red32(a[k], a[k + 4]);
                *lp<u32>(wa) = pack2(red16(t[0], t[2]), red16(t[1], t[3]));
                *lp<float>(wl) = asf(ck.y) * bflo(f);
                *lp<float>(F_ST + w * 4) = M;
                *lp<u32>(F_ST + 32) = 1u;
                break;
            }
            __builtin_amdgcn_s_sleep(POLL_SLEEP);
        }
        STAMP(9);
    }
    barrier();
    u64 tc1;
    asm volatile("s_memrealtime %0" : "=s"(tc1));
    STAMP(10);
    float ls;
    {
        bf16x8 af[4];
#pragma unroll
        for (int s = 0; s < 4; ++s) af[s] = *lp<bf16x8>(aa + s * 64);
        const u32 rare = *lp<u32>(F_ST + 32);
        const f32x2 l2 = *lp<f32x2>(rl);
        SCHED();
        f32x4 d = {0.f, 0.f, 0.f, 0.f};
#pragma unroll
        for (int s = 0; s < 4; ++s) d = __builtin_amdgcn_mfma_f32_16x16x32_bf16(af[s], bu[s], d, 0, 0, 0);
        ls = l2.x + l2.y;
        if (__builtin_expect(rfl(rare) != 0u, 0)) {
            const f32x4 m0 = *lp<f32x4>(F_ST), m1 = *lp<f32x4>(F_ST + 16);
            const float M = max8(m0, m1);
#pragma unroll
            for (int k = 0; k < 4; ++k) d[k] *= ex2(((g & 1) ? m1[k] : m0[k]) - M);
            ls *= ex2(*lp<float>(F_ST + (u32)(lane >> 3) * 4) - M);
        }
        float c0 = d.x + d.y, c1 = d.z + d.w;
        PIN(c0); PIN(c1);
        float c = c0 + c1, c2 = c0 + c1;
        PIN(c); PIN(c2);
        c = red16(c, c2);
        *lp<u16>(cw) = (u16)pack2(c, c);
    }
    barrier();
    STAMP(13);
    {
        bf16x8 af[4];
#pragma unroll
        for (int s = 0; s < 4; ++s) af[s] = *lp<bf16x8>(cr + s * 64);
        ls += dpp<0x128>(ls); ls += dpp<0x124>(ls); ls += dpp<0x122>(ls); ls += dpp<0x121>(ls);
        ls = red16(ls, ls); ls = red32(ls, ls);
        ls = __builtin_amdgcn_rcpf(ls);
        v4u r = {0u, 0u, tlo, thi};
        PIN(ls); PIN(r);
        SCHED();
        f32x4 d[4];
#pragma unroll
        for (int t = 0; t < 4; ++t) d[t] = (f32x4){0.f, 0.f, 0.f, 0.f};
#pragma unroll
        for (int s = 0; s < 4; ++s) {
#pragma unroll
            for (int t = 0; t < 4; ++t) d[t] = __builtin_amdgcn_mfma_f32_16x16x32_bf16(af[s], bo[4 * t + s], d[t], 0, 0, 0);
            SCHED();
        }
        r.x = asu(((g & 2) ? ((g & 1) ? d[3].x : d[2].x) : ((g & 1) ? d[1].x : d[0].x)) * ls);
        r.y = dppu<0x128>(r.x);
        asm volatile("s_mov_b64 exec, %3\n\tglobal_store_dwordx4 %0, %1, %2 sc1\n\ts_mov_b64 exec, -1\n\ts_nop 0" :: "v"(op), "v"(r), "s"(ws), "s"(0x00ff00ff00ff00ffull) : "memory");
    }
    STAMP(11);
    asm volatile("" : "+s"(tc1));
    if (wave == 2 && lane < 8) st_sc(ws, he, (v4u){(u32)tc1, 0u, tlo, thi});
    if (b == 0 && tid == 64) st_sc2(ws, WS_EPOCH, (v2u){tlo, thi});
    if (is_red) {
        const f32x2 s = reduce16<8, true>(ws, piece(WS_OUTP, 16384, rid), tlo, thi, wave, lane, F_RD, (u32)tc1 + D_DELAY, budget);
        const float x10 = xv0 + ga0 * s.x, x11 = xv1 + ga1 * s.y;
        if (wave == 0) st_sc(ws, xo, (v4u){asu(x10), asu(x11), tlo, thi});
        STAMP(12);
        u32 oa = (rid * 8 + (u32)lane) * 4u;
        PIN(oa);
        const f32x2 s2 = reduce16<6, false>(ws, piece(WS_YP, 16384, rid), tlo, thi, wave, lane, F_RV, now32() + F_DELAY, budget);
        if (tid < 8) asm volatile("global_store_dword %0, %1, %2" :: "v"(oa), "v"(pack2(x10 + gb0 * s2.x, x11 + gb1 * s2.y)), "s"(P.out) : "memory");
    }
    STAMP(15);
    STAMP_OUT();
}

extern "C" void kernel_launch(void* const* d_in, const int* in_sizes, int n_in, void* d_out, int out_size, void* d_ws, size_t ws_size, hipStream_t stream) {
    Params P;
    P.x = (const u16*)d_in[0]; P.ckv = (const u16*)d_in[1]; P.wq = (const u16*)d_in[2]; P.wuk = (const u16*)d_in[3];
    P.wuv = (const u16*)d_in[4]; P.wo = (const u16*)d_in[5]; P.wg = (const u16*)d_in[7]; P.wu = (const u16*)d_in[8];
    P.wd = (const u16*)d_in[9]; P.c = (const u16*)d_in[10]; P.wmod = (const u16*)d_in[11];
    P.out = (u16*)d_out; P.ws = (char*)d_ws + WS_BASE(ws_size);
    (void)hipFuncSetAttribute((const void*)fused_kernel, hipFuncAttributeMaxDynamicSharedMemorySize, LDS_BYTES);
    fused_kernel<<<256, 512, LDS_BYTES, stream>>>(P);
}
```

```cpp
#include <hip/hip_runtime.h>

typedef unsigned u32;
typedef unsigned long long u64;
typedef unsigned short u16;
typedef u32 v4u __attribute__((ext_vector_type(4)));
typedef u32 v2u __attribute__((ext_vector_type(2)));
typedef u32 v8u __attribute__((ext_vector_type(8)));
typedef u32 v16u __attribute__((ext_vector_type(16)));
typedef float f32x2 __attribute__((ext_vector_type(2)));
typedef float f32x4 __attribute__((ext_vector_type(4)));
typedef float f32x16 __attribute__((ext_vector_type(16)));
typedef short bf16x8 __attribute__((ext_vector_type(8)));
typedef short s16x4 __attribute__((ext_vector_type(4)));
typedef __bf16 bf16x2_t __attribute__((ext_vector_type(2)));
#define LAS __attribute__((address_space(3)))
#define DI __device__ __forceinline__

#define CFG(name, val) constexpr int name = val
CFG(BUDGET, 1 << 20);
CFG(POLL_SLEEP, 4);
CFG(HINT_SLEEP, 4);
#define T_OPEN0 50
#define T_OPEN1 70
#define T_OPEN2 70
#define A_FIRST 168
#define A2_DELAY 45
#define Q_FIRST 350
#define F_DELAY 0
#define ST_LOOK 110
#define SKEW_MAX 45
#define Q_DELAY 118
#define A_OWN 60
#define Q_STEP 40
#define T_MLP 0
#define T_OWN 40
#define O_STEP 35
#define Q_PRE 60
#define KD 10
#define KMAP 0
#define KMUL 41
DI u32 ktile(u32 s, u32 t) {
    if (KMAP == 0) return s * 262144u + t * 16384u;
    if (KMAP == 1) return t * 2097152u + s * 16384u;
    const u32 pos = (s + KMUL * t) & 127u;
    if (KMAP == 3) return t * 2097152u + pos * 16384u;
    if (KMAP == 4) return ((t + pos) & 15u) * 2097152u + pos * 16384u;
    if (KMAP == 5) return ((t + (pos >> 3)) & 15u) * 2097152u + pos * 16384u;
    return pos * 262144u + t * 16384u;
}
#define HW 5
#define QD 2
#define LAZY 48.f
#define CODE_STR 6976
#define WS_BASE(sz) (((sz) - (32u << 20)) & ~(size_t)((2u << 20) - 1))

constexpr u32 WS_EPOCH = 0;
DI u32 piece(u32 base, u32 size, u32 i) { return base + i * size; }
#define CH 262144u
constexpr u32 WS_ST = 1024;
constexpr u32 WS_QH = 2048, NQH = 6;
constexpr u32 WS_QCHK = 8192;
constexpr u32 WS_HP = CH;
constexpr u32 WS_QP = 2 * CH;
constexpr u32 WS_QABS = 3 * CH;
constexpr u32 WS_L = 25u << 18;
constexpr u32 WS_ML = WS_L;
constexpr u32 WS_X1 = WS_L + 65536;
constexpr u32 WS_SC = WS_L + 98304;
constexpr u32 HSTR = 1152, WS_HC = WS_L + 106496;
constexpr u32 WS_FC = WS_SC + 4096;
constexpr u32 WS_HE = WS_HC + 512;
constexpr u32 WS_OUTP = WS_L + 262144;
constexpr u32 WS_YP = WS_L + 786432;
constexpr u32 WS_CHK = WS_L + 1310720;
constexpr u32 WS_PAY = 2u << 20;
constexpr u32 PHD = 4 * 128 * 256 + 4096;
constexpr u32 WS_TL = 16u << 20;

constexpr u32 L_FL = 114688, L_P = L_FL + 1024, L_IMG = 131072;
constexpr u32 slot_no(u32 t) { return (u32)((0x9543621054362105ull >> (4 * t)) & 15u); }
constexpr u32 slot(int t) { return slot_no((u32)t) * 16384u; }
constexpr u32 vwin(int t) { return (slot_no((u32)t) < 3 ? 0u : slot_no((u32)t) < 7 ? 3u : 6u) * 16384u; }
static_assert(slot(15) == L_IMG + 16384, "");
constexpr u32 C_LAND = 0;
constexpr u32 C_KRD = 256, C_Z = 264, C_E = 268, C_P = 272, C_Q = 284, C_L = 288, C_BAD = 292, C_H = 296, C_TZ = 304;
constexpr u32 L_M = L_FL + 384, L_L = L_FL + 512;
constexpr u32 L_W = 0;
constexpr u32 L_H2 = 32768;
constexpr u32 L_GU = 36864;
constexpr u32 F_QP = 0;
constexpr u32 F_WO = 0;
constexpr u32 F_UV = 131072;
constexpr u32 F_A = 0;
constexpr u32 F_ST = 4096;
constexpr u32 F_GO = F_ST + 40;
constexpr u32 F_CX = 4352;
constexpr u32 F_LS = 5120;
constexpr u32 F_ML = 6144;
constexpr u32 F_RD = 8192, F_RV = 9216;
constexpr u32 F_JUNK = 16384;
constexpr u32 LDS_BYTES = 163840;

struct Params {
    const u16 *wmod, *c, *x;
    char* ws;
    const u16 *wq, *ckv, *wuk, *wo, *wuv, *wg, *wu, *wd;
    u16* out;
};

DI float asf(u32 v) { return __builtin_bit_cast(float, v); }
DI u32 asu(float v) { return __builtin_bit_cast(u32, v); }
DI float bflo(u32 v) { return asf(v << 16); }
DI float bfhi(u32 v) { return asf(v & 0xffff0000u); }
DI u32 pack2(float a, float b) { f32x2 v = {a, b}; return __builtin_bit_cast(u32, __builtin_convertvector(v, bf16x2_t)); }
DI float dot2(u32 w, u32 x, float acc) { return __builtin_amdgcn_fdot2_f32_bf16(__builtin_bit_cast(bf16x2_t, w), __builtin_bit_cast(bf16x2_t, x), acc, false); }
DI void pair_rows(v4u& x, v4u& y) {
#pragma unroll
    for (int k = 0; k < 4; ++k) { const u32 a = x[k], b = y[k]; x[k] = __builtin_amdgcn_perm(b, a, 0x05040100u); y[k] = __builtin_amdgcn_perm(b, a, 0x07060302u); }
}
template <int CTRL> DI u32 dppu(u32 v) { return (u32)__builtin_amdgcn_update_dpp(0, (int)v, CTRL, 0xf, 0xf, true); }
template <int CTRL> DI float dpp(float v) { return asf(dppu<CTRL>(asu(v))); }
DI float red32(float a, float b) { auto r = __builtin_amdgcn_permlane32_swap(asu(a), asu(b), false, false); return asf((u32)r[0]) + asf((u32)r[1]); }
DI float red16(float a, float b) { auto r = __builtin_amdgcn_permlane16_swap(asu(a), asu(b), false, false); return asf((u32)r[0]) + asf((u32)r[1]); }
DI float max3(float a, float b, float c) { return __builtin_fmaxf(__builtin_fmaxf(a, b), c); }
DI float max4(f32x4 a) { return max3(max3(a.x, a.y, a.z), a.w, a.x); }
DI float max32(float a) { auto r = __builtin_amdgcn_permlane32_swap(asu(a), asu(a), false, false); return __builtin_fmaxf(asf((u32)r[0]), asf((u32)r[1])); }
DI float max16(float a) { auto r = __builtin_amdgcn_permlane16_swap(asu(a), asu(a), false, false); return __builtin_fmaxf(asf((u32)r[0]), asf((u32)r[1])); }
DI float max8(const f32x4& a, const f32x4& b) { return max3(max3(max3(a.x, a.y, a.z), a.w, b.x), max3(b.y, b.z, b.w), a.x); }
DI float sum4(f32x4 a) { return (a.x + a.y) + (a.z + a.w); }
DI u32 perm_lo(u32 a, u32 b) { return __builtin_amdgcn_perm(b, a, 0x05040100u); }
DI u32 perm_hi(u32 a, u32 b) { return __builtin_amdgcn_perm(b, a, 0x07060302u); }
DI u32 rfl(u32 v) { return (u32)__builtin_amdgcn_readfirstlane((int)v); }
DI void both16(u32 a, u32& e, u32& o) { auto r = __builtin_amdgcn_permlane16_swap(a, a, false, false); e = (u32)r[0]; o = (u32)r[1]; }
DI void both32(u32 a, u32& l, u32& h) { auto r = __builtin_amdgcn_permlane32_swap(a, a, false, false); l = (u32)r[0]; h = (u32)r[1]; }
DI float rowsum(float v) { v += dpp<0x128>(v); v += dpp<0x124>(v); v += dpp<0x122>(v); v += dpp<0x121>(v); return v; }
DI float ex2(float v) { return __builtin_amdgcn_exp2f(v); }
DI u32 neq3(u32 a, u32 b, u32 t) { return __builtin_amdgcn_bitop3_b32(a, b, t, 0x7e); }
DI u64 ballot(bool c) { return __builtin_amdgcn_ballot_w64(c); }

DI v4u ld_nt(const void* base, u32 off) { v4u r; asm volatile("global_load_dwordx4 %0, %1, %2 nt" : "=v"(r) : "v"(off), "s"(base) : "memory"); return r; }
DI u32 ld_u32(const void* base, u32 off) { u32 r; asm volatile("global_load_dword %0, %1, %2" : "=v"(r) : "v"(off), "s"(base) : "memory"); return r; }
DI u32 ld_u32_nt(const void* base, u32 off) { u32 r; asm volatile("global_load_dword %0, %1, %2 nt" : "=v"(r) : "v"(off), "s"(base) : "memory"); return r; }
DI u32 ld_u16(const void* base, u32 off) { u32 r; asm volatile("global_load_ushort %0, %1, %2" : "=v"(r) : "v"(off), "s"(base) : "memory"); return r; }
DI void st_sc(void* base, u32 off, v4u v) { asm volatile("global_store_dwordx4 %0, %1, %2 sc1\n\ts_nop 1" :: "v"(off), "v"(v), "s"(base) : "memory"); }
DI void st_sc_keep(void* base, u32 off, v4u v) { asm volatile("global_store_dwordx4 %0, %1, %2 sc1" :: "v"(off), "v"(v), "s"(base) : "memory"); }
DI void st_sc2(void* base, u32 off, v2u v) { asm volatile("global_store_dwordx2 %0, %1, %2 sc1\n\ts_nop 1" :: "v"(off), "v"(v), "s"(base) : "memory"); }
DI void dma_nt(const void* base, u32 off, u32 lds_dst) { asm volatile("s_mov_b32 m0, %1\n\ts_nop 0\n\tglobal_load_lds_dwordx4 %0, %2 nt" :: "v"(off), "s"(lds_dst), "s"(base) : "memory"); }
DI void dma_pl(const void* base, u32 off, u32 lds_dst) { asm volatile("s_mov_b32 m0, %1\n\ts_nop 0\n\tglobal_load_lds_dwordx4 %0, %2" :: "v"(off), "s"(lds_dst), "s"(base) : "memory"); }
DI void dma_sc(const void* base, u32 off, u32 lds_dst) { asm volatile("s_mov_b32 m0, %1\n\ts_nop 0\n\tglobal_load_lds_dwordx4 %0, %2 sc1" :: "v"(off), "s"(lds_dst), "s"(base) : "memory"); }
DI void ld_nt_into(v4u& r, const void* base, u32 off) { asm volatile("global_load_dwordx4 %0, %1, %2 nt" : "+v"(r) : "v"(off), "s"(base) : "memory"); }
DI void ld_sc_into(v4u& r, const void* base, u32 off) { asm volatile("global_load_dwordx4 %0, %1, %2 sc1" : "+v"(r) : "v"(off), "s"(base) : "memory"); }
#define VMWAIT(n) asm volatile("s_waitcnt vmcnt(%0)" :: "i"(n) : "memory")
#define LGKM0() asm volatile("s_waitcnt lgkmcnt(0)" ::: "memory")
#define PIN(x) asm volatile("" : "+v"(x))
#define UNDEF(x) asm volatile("" : "=v"(x))
#define SCHED() __builtin_amdgcn_sched_barrier(0)
DI void barrier() { LGKM0(); __builtin_amdgcn_s_barrier(); asm volatile("" ::: "memory"); }

template <typename T> DI LAS T* lp(u32 off) { return (LAS T*)(size_t)off; }
DI bf16x8 tr2(u32 a, u32 b) {
    const s16x4 lo = __builtin_amdgcn_ds_read_tr16_b64_v4i16(lp<s16x4>(a)), hi = __builtin_amdgcn_ds_read_tr16_b64_v4i16(lp<s16x4>(b));
    return __builtin_shufflevector(lo, hi, 0, 1, 2, 3, 4, 5, 6, 7);
}

#define STAMP(i) do {} while (0)
#define CYC0() do {} while (0)
#define STAMPC(i) do {} while (0)
#define STAMP_OUT() do {} while (0)

#define CDECL() do {} while (0)
#define CS(i) do {} while (0)
#define COUT(n) do {} while (0)
template <u32 S> DI void pass(const char* ws, u32 off, v4u (&v)[1]) {
    asm volatile("global_load_dwordx4 %0, %1, %2 sc1\n\ts_waitcnt vmcnt(0)" : "=v"(v[0]) : "v"(off), "s"(ws) : "memory");
}
template <u32 S> DI void pass(const char* ws, u32 off, v4u (&v)[2]) {
    asm volatile("global_load_dwordx4 %0, %2, %3 sc1\n\tglobal_load_dwordx4 %1, %2, %4 sc1\n\ts_waitcnt vmcnt(0)" : "=&v"(v[0]), "=&v"(v[1]) : "v"(off), "s"(ws), "s"(ws + S) : "memory");
}
template <u32 S> DI void pass(const char* ws, u32 off, v4u (&v)[4]) {
    asm volatile("global_load_dwordx4 %0, %4, %5 sc1\n\tglobal_load_dwordx4 %1, %4, %5 offset:%6 sc1\n\tglobal_load_dwordx4 %2, %4, %5 offset:%7 sc1\n\tglobal_load_dwordx4 %3, %4, %5 offset:%8 sc1\n\ts_waitcnt vmcnt(0)"
        : "=&v"(v[0]), "=&v"(v[1]), "=&v"(v[2]), "=&v"(v[3]) : "v"(off), "s"(ws), "i"(S), "i"(2 * S), "i"(3 * S) : "memory");
}
template <int N, u32 STRIDE, int SLP = POLL_SLEEP> DI void gather(const char* ws, u32 off, u32 tlo, u32 thi, v4u (&v)[N], int& budget) {
#pragma clang loop unroll(disable)
    do {
        pass<STRIDE>(ws, off, v);
        u32 bad = 0;
#pragma unroll
        for (int k = 0; k < N; ++k) bad |= (v[k].z ^ tlo) | (v[k].w ^ thi);
        if (ballot(bad != 0u) == 0ull) break;
        __builtin_amdgcn_s_sleep(SLP);
    } while (--budget > 0);
}
#define PSLP "0"
template <bool G8> DI void poll2(v4u& g0, v4u& g1, u32 o0, const char* b0, u32 o1, const char* b1, u32 tlo, u32 thi) {
    u32 n, t, u;
#define POLL2(A, B, C, D) asm volatile("s_mov_b32 %2, 0x200000\n.Lp%=:\n\ts_sleep " PSLP "\n\tglobal_load_dwordx4 v[232:235], %5, %6 sc1\n\tglobal_load_dwordx4 v[236:239], %7, %8 sc1\n\ts_sub_u32 %2, %2, 1\n\ts_waitcnt vmcnt(0)\n\t" \
        "v_bitop3_b32 %3, " A ", " B ", %9 bitop3:0x7e\n\tv_bitop3_b32 %4, " C ", " D ", %10 bitop3:0x7e\n\tv_or_b32 %3, %3, %4\n\tv_cmp_ne_u32 vcc, 0, %3\n\ts_cbranch_scc1 .Lq%=\n\ts_cbranch_vccnz .Lp%=\n.Lq%=:" \
        : "=&{v[232:235]}"(g0), "=&{v[236:239]}"(g1), "=&s"(n), "=&v"(t), "=&v"(u) : "v"(o0), "s"(b0), "v"(o1), "s"(b1), "s"(tlo), "s"(G8 ? tlo : thi) : "memory", "vcc", "scc")
    if (G8) POLL2("v233", "v235", "v237", "v239"); else POLL2("v234", "v238", "v235", "v239");
}
DI void flag_set(u32 fa, u32 off) {
    asm volatile("s_mov_b64 exec, 1\n\tds_write_b8_d16_hi %0, %0 offset:%1\n\ts_mov_b64 exec, -1" :: "v"(fa), "i"(off) : "memory");
}
DI u32 flag_get(u32 off) { return *(volatile LAS u32*)lp<u32>(L_FL + off); }
DI void wait4(u32 fb, u32 off) {
    u32 v, n;
    asm volatile("s_mov_b32 %1, 0x400000\n.Lw%=:\n\tds_read_b32 %0, %2 offset:%3\n\ts_sub_u32 %1, %1, 1\n\ts_waitcnt lgkmcnt(0)\n\tv_cmp_ne_u32 vcc, 0x1010101, %0\n\ts_cbranch_scc1 .Le%=\n\ts_cbranch_vccnz .Lw%=\n.Le%=:"
        : "=&v"(v), "=&s"(n) : "v"(fb), "i"(off) : "memory", "vcc", "scc");
}
DI void wait1(u32 fb, u32 off) {
    u32 v, n;
    asm volatile("s_mov_b32 %1, 0x400000\n.Lw%=:\n\tds_read_u8 %0, %2 offset:%3\n\ts_sub_u32 %1, %1, 1\n\ts_waitcnt lgkmcnt(0)\n\tv_cmp_ne_u32 vcc, 1, %0\n\ts_cbranch_scc1 .Le%=\n\ts_cbranch_vccnz .Lw%=\n.Le%=:"
        : "=&v"(v), "=&s"(n) : "v"(fb), "i"(off) : "memory", "vcc", "scc");
}
#define SPIN_HEAD "s_mov_b32 %[n], 0x400000\n.Lw%=:\n\t"
#define SPIN_TAIL(cmp) "s_sub_u32 %[n], %[n], 1\n\ts_waitcnt lgkmcnt(0)\n\t" cmp ", %[v]\n\ts_cbranch_scc1 .Le%=\n\ts_cbranch_vccnz .Lw%=\n.Le%=:"
DI void land_wait(u32 fl, u32 off) {
    u32 v, n;
    asm volatile(SPIN_HEAD "ds_read_b32 %[v], %[b] offset:%[o]\n\t" SPIN_TAIL("v_cmp_eq_u32 vcc, 0") : [v] "=&v"(v), [n] "=&s"(n) : [b] "v"(fl), [o] "i"(off) : "memory", "vcc", "scc");
}
DI bf16x8 wait1p(u32 fb, u32 off, u32 pa, u32 poff) {
    u32 v, n;
    bf16x8 p;
    asm volatile(SPIN_HEAD "ds_read_u8 %[v], %[b] offset:%[o]\n\tds_read_b128 %[p], %[a] offset:%[q]\n\t" SPIN_TAIL("v_cmp_ne_u32 vcc, 1")
        : [v] "=&v"(v), [p] "=&v"(p), [n] "=&s"(n) : [b] "v"(fb), [o] "i"(off), [a] "v"(pa), [q] "i"(poff) : "memory", "vcc", "scc");
    return p;
}
DI void wait4p(u32 fb, u32 off, u32 pa, u32 poff, bf16x8 (&p)[4]) {
    u32 v, n;
    asm volatile(SPIN_HEAD "ds_read_b32 %[v], %[b] offset:%[o]\n\tds_read_b128 %[p0], %[a] offset:%[q0]\n\tds_read_b128 %[p1], %[a] offset:%[q1]\n\tds_read_b128 %[p2], %[a] offset:%[q2]\n\tds_read_b128 %[p3], %[a] offset:%[q3]\n\t"
        SPIN_TAIL("v_cmp_ne_u32 vcc, 0x1010101")
        : [v] "=&v"(v), [p0] "=&v"(p[0]), [p1] "=&v"(p[1]), [p2] "=&v"(p[2]), [p3] "=&v"(p[3]), [n] "=&s"(n)
        : [b] "v"(fb), [o] "i"(off), [a] "v"(pa), [q0] "i"(poff), [q1] "i"(poff + 1024), [q2] "i"(poff + 2048), [q3] "i"(poff + 3072) : "memory", "vcc", "scc");
}
template <u32 KO> DI void land_wait_k(u32 fl, u32 off, u32 a0, u32 a1, u32 a2, u32 a3, bf16x8& f0, bf16x8& f1, bf16x8& f2, bf16x8& f3) {
    u32 v, n;
    asm volatile(SPIN_HEAD "ds_read_b32 %[v], %[b] offset:%[o]\n\tds_read_b128 %[p0], %[a0] offset:%[k]\n\tds_read_b128 %[p1], %[a1] offset:%[k]\n\tds_read_b128 %[p2], %[a2] offset:%[k]\n\tds_read_b128 %[p3], %[a3] offset:%[k]\n\t"
        SPIN_TAIL("v_cmp_eq_u32 vcc, 0")
        : [v] "=&v"(v), [p0] "=&v"(f0), [p1] "=&v"(f1), [p2] "=&v"(f2), [p3] "=&v"(f3), [n] "=&s"(n)
        : [b] "v"(fl), [o] "i"(off), [a0] "v"(a0), [a1] "v"(a1), [a2] "v"(a2), [a3] "v"(a3), [k] "i"(KO) : "memory", "vcc", "scc");
}
DI void dma_a(const char* src, const u32 (&od)[4], u32 dst) {
    asm volatile("s_mov_b32 m0, %3\n\ts_nop 0\n\tglobal_load_lds_dwordx4 %0, %2 nt\n\tglobal_load_lds_dwordx4 %1, %2 offset:1024 nt" :: "v"(od[0]), "v"(od[1]), "s"(src), "s"(dst) : "memory");
}
DI void dma_b(const char* src, const u32 (&od)[4], u32 dst, u64 pc, u32 flag) {
    asm volatile("s_mov_b32 m0, %4\n\ts_nop 0\n\tglobal_load_lds_dwordx4 %1, %3 offset:2048 nt\n\tglobal_load_lds_dwordx4 %2, %3 offset:3072 nt\n\t"
        "s_mov_b32 m0, %6\n\ts_mov_b64 exec, 1\n\tglobal_load_lds_dword %0, %5\n\ts_mov_b64 exec, -1"
        :: "v"(od[0]), "v"(od[2]), "v"(od[3]), "s"(src), "s"(dst), "s"(pc), "s"(flag) : "memory");
}
static_assert(C_LAND == 0, "");
static_assert(((L_FL >> 16) & 0xff) == 1, "flag_set writes bits 23:16 of the address");
template <int K, bool PRE = false> DI u32 hint_wait(const char* ws, u32 oh, u32 tlo, int& budget, v4u h0 = v4u()) {
    v4u h[1] = {h0};
    if (!PRE || __builtin_popcount((u32)ballot(h[0].z == tlo) & 0xffu) < K) {
#pragma clang loop unroll(disable)
        do {
            pass<0>(ws, oh, h);
            if (__builtin_popcount((u32)ballot(h[0].z == tlo) & 0xffu) >= K) break;
            __builtin_amdgcn_s_sleep(HINT_SLEEP);
        } while (--budget > 0);
    }
    const u32 now = (u32)__builtin_amdgcn_s_memrealtime();
    u32 age = h[0].z == tlo ? now - h[0].x : 0xffffu;
    age = age > 0xffffu ? 0u : age;
    age = min(age, dppu<0xB1>(age));
    age = min(age, dppu<0x4E>(age));
    age = min(age, dppu<0x124>(age));
    return now - rfl(age);
}
DI u32 now32() { return (u32)__builtin_amdgcn_s_memrealtime(); }
DI void until(u32 t) { while ((int)(now32() - t) < 0) __builtin_amdgcn_s_sleep(1); }
DI u32 time_base(const char* p, u32 t0) {
    v16u h;
    asm volatile("s_load_dwordx16 %0, %1, 0x0\n\ts_waitcnt lgkmcnt(0)" : "=s"(h) : "s"(p) : "memory");
    u32 m = 0, bad = 0;
#pragma unroll
    for (int k = 0; k < 8; ++k) { m = max(m, h[2 * k] - t0 + 150u); bad |= h[2 * k] ^ ~h[2 * k + 1]; }
    return (bad != 0u || m >= 300u) ? t0 + SKEW_MAX : t0 - 150u + m;
}
struct QTimer { u32 t, n; };
DI bool q_due(QTimer& q, const char* ws, u32 tlo) {
    const u32 now = now32();
    if ((int)(now - q.t) < 0) return false;
    if (q.n >= NQH) return true;
    v16u h0, h1;
    asm volatile("s_load_dwordx16 %0, %2, 0x0\n\ts_load_dwordx16 %1, %2, 0x40\n\ts_waitcnt lgkmcnt(0)" : "=&s"(h0), "=&s"(h1) : "s"(ws + WS_QH + q.n * 128u) : "memory");
    u32 m = 0xffffffffu, bad = 0;
#pragma unroll
    for (int k = 0; k < 8; ++k) { m = min(m, min(now + 1000u - h0[2 * k], now + 1000u - h1[2 * k])); bad |= (h0[2 * k] ^ h0[2 * k + 1] ^ tlo) | (h1[2 * k] ^ h1[2 * k + 1] ^ tlo); }
    if (bad == 0u) { q.n = NQH; q.t = now + 1000u - max(m, 900u) + Q_DELAY - Q_PRE; } else { ++q.n; q.t = now + Q_STEP; }
    return false;
}
#define FC_C 62
DI void look_times(const char* ws, int lane, u32 tlo, int& budget, u32& t1, u32& t2) {
    v4u fc[1];
#pragma clang loop unroll(disable)
    do {
        pass<0>(ws, WS_FC + (u32)lane * 16u, fc);
        if (ballot(neq3(fc[0].y, fc[0].w, tlo)) == 0ull) break;
        until(now32() + 30);
    } while (--budget > 0);
    const u32 now = now32(), x = min(fc[0].x + 1000u - now, 2000u), z = min(fc[0].z + 1000u - now, 2000u);
    u32 m = max(x, z), a = x + z, e, od;
    m = max(m, dppu<0x128>(m)); m = max(m, dppu<0x124>(m)); m = max(m, dppu<0x122>(m)); m = max(m, dppu<0x121>(m));
    a += dppu<0x128>(a); a += dppu<0x124>(a); a += dppu<0x122>(a); a += dppu<0x121>(a);
    both16(m, e, od); both32(max(e, od), e, od);
    m = rfl(max(e, od));
    both16(a, e, od); both32(e + od, e, od);
    a = (rfl(e + od) >> 7) + FC_C;
    t1 = m < 2000u ? now - 1000u + a : now;
    t2 = m < 2000u ? now - 1000u + max(m, a) : now;
}
DI u32 swz(u32 row) { return ((row & 3u) << 2) | ((row >> 2) & 3u); }

#define EPOCH() do { asm volatile("s_load_dwordx2 %0, %1, 0x0\n\ts_waitcnt lgkmcnt(0)" : "=s"(e64) : "s"(ws) : "memory"); ++e64; } while (0)
DI void wave_sum2(float s1, float s2, float& t1, float& t2) {
    float z = red32(s1, s2);
    z = red16(z, z); z = rowsum(z);
    u32 a, b;
    both32(asu(z), a, b);
    t1 = asf(a); t2 = asf(b);
}
DI void ln_stats(const v4u& xa, float& mean, float& rstd) {
    const float pil = asf(rfl(xa[0]) << 16);
    float s1 = 0.f, s2 = 0.f;
#pragma unroll
    for (int k = 0; k < 4; ++k) { const float a = bflo(xa[k]) - pil, c = bfhi(xa[k]) - pil; s1 += a; s2 = __builtin_fmaf(a, a, s2); s1 += c; s2 = __builtin_fmaf(c, c, s2); }
    float t1, t2;
    wave_sum2(s1, s2, t1, t2);
    const float dm = t1 * (1.f / 512.f);
    mean = pil + dm;
    rstd = __builtin_amdgcn_rsqf(__builtin_fmaxf(t2 * (1.f / 512.f) - dm * dm, 0.f) + 1e-5f);
}
DI float sigm(float v) { return __builtin_amdgcn_rcpf(1.f + ex2(v * -1.4426950408889634f)); }
DI float silu(float v) { return v * sigm(v); }

template <int NW2, bool S8> DI f32x2 reduce16(const char* ws, u32 base, u32 tlo, u32 thi, int wave, int lane, u32 lds_red, u32 tgo, int& budget) {
    u32 la = lds_red + (u32)(wave * 16 + (S8 ? (lane & 7) + (lane >> 5) * 8 : (lane & 7) * 2 + (lane >> 5))) * 4, ra = lds_red + (u32)lane * 8, go = base + (u32)(wave * 64 + lane) * 16u;
    const bool two = NW2 == 8 || wave < NW2;
    const char* w2 = ws + (two ? 8192 : 0);
    float f2 = two ? 1.f : 0.f;
    PIN(la); PIN(ra); PIN(go);
    asm volatile("" : "+s"(w2), "+v"(f2));
    until(tgo);
    v4u g0, g1;
    poll2<false>(g0, g1, go, ws, go, w2, tlo, thi);
    float z = red32(__builtin_fmaf(asf(g1.x), f2, asf(g0.x)), __builtin_fmaf(asf(g1.y), f2, asf(g0.y)));
    z = red16(z, z); z += dpp<0x128>(z);
    *lp<float>(la) = z;
    barrier();
    f32x2 s = {0.f, 0.f};
    if (wave == 0) {
        s = *lp<f32x2>(ra);
        float t = red32(s.x, s.y);
        t = red16(t, t); t += dpp<0x128>(t);
        u32 a, c;
        both32(asu(t), a, c);
        s = (f32x2){asf(a), asf(c)};
    }
    return s;
}

#define E_DELAY 160
#define HE_K 8
#define E_T 285
#define WDE 0
#define G_T 60
#define G_2 90
#define MDMA dma_pl
DI u32 mlp_id(int b) { const int xc = b & 7, yy = (b >> 3) - 16; return (u32)(((yy >> 2) * 8 + xc) * 4 + (yy & 3)); }
DI void mlp_part(const Params& P, int b, u32 w, int lane, u32 tlo, u32 thi, int& budget, u32& tl) {
    char* const ws = P.ws;
    const u32 gu_id = mlp_id(b), i15 = lane & 15, g = lane >> 4;
    u32 wo = (128 * w + ((u32)lane >> 1)) * 3584u + gu_id * 32u + ((u32)lane & 1) * 16u, wdo = gu_id * 16384u + (64 * w + (u32)lane) * 4u;
    PIN(wo); PIN(wdo);
    u32 ha = L_H2 + w * 256 + (u32)lane * 4, hr = L_H2 + w * 256 + g * 16 + min(i15 & 3, 2u) * 1088, ga = L_GU + w * 512 + i15 * 16 + g * 4096, gr = L_GU + (u32)lane * 16;
    u32 sa = L_GU + 2048 + (u32)(lane >> 5) * 16 + w * 4, heo = WS_HE + (u32)(b - 128) * HSTR + (u32)(lane & 7) * 16u, px = piece(WS_X1, 4096, (u32)(b & 7)) + (u32)(lane & 7) * 16u;
    PIN(sa); PIN(heo); PIN(px);
    u32 so = piece(WS_YP, 16384, 8 * w + (u32)(lane >> 3)) + (gu_id * 8 + (lane & 7)) * 16, sco = WS_SC + (64 * w + (u32)lane) * 16u;
    u32 ta = L_W + w * 8192u + (8 * g + (i15 >> 2)) * 32 + (lane & 3) * 8, xo = piece(WS_X1, 4096, (u32)(b & 7)) + w * 1024u + (u32)lane * 16u;
    PIN(ha); PIN(hr); PIN(ga); PIN(gr); PIN(so); PIN(sco); PIN(ta); PIN(xo);
    barrier();
    STAMP(10);
    u32 wdr[16], tk1, tk2;
    {
        look_times(ws, lane, tlo, budget, tk1, tk2);
        const u32 tg = tk2 + G_T;
        const u32 pg = ((gu_id & 3) * 128 + w * 32 + ((u32)lane >> 3)) * 3584u + (gu_id >> 2) * 128u + ((u32)lane & 7) * 16u;
        if (WDE) {
#pragma unroll
            for (int j = 0; j < 16; ++j) wdr[j] = ld_u32_nt(P.wd + j * 512, wdo);
        }
        until(tg);
#pragma unroll
        for (int k = 0; k < 4; ++k) { dma_pl(P.wg + k * 8 * 1792, pg, 65536u + w * 1024u); dma_pl(P.wu + k * 8 * 1792, pg, 65536u + w * 1024u); }
        if (!WDE) {
#pragma unroll
            for (int j = 0; j < 16; ++j) wdr[j] = ld_u32_nt(P.wd + j * 512, wdo);
        }
        VMWAIT(WDE ? 0 : 16);
        until(tg + G_2);
    }
#pragma unroll
    for (int s = 0; s < 4; ++s) { MDMA(P.wg + s * 32 * 1792, wo, L_W + w * 8192u + s * 1024u); MDMA(P.wu + s * 32 * 1792, wo, L_W + w * 8192u + 4096u + s * 1024u); }
    v4u mg[1], hh;
    UNDEF(mg[0]); UNDEF(hh);
    ld_sc_into(mg[0], ws, sco);
    ld_sc_into(hh, ws, heo);
    STAMP(2);
    VMWAIT(10);
#pragma unroll
    for (int j = 0; j < 16; j += 2) {
        PIN(wdr[j]); PIN(wdr[j + 1]);
        const u32 a = wdr[j], bb = wdr[j + 1];
        wdr[j] = perm_lo(a, bb); wdr[j + 1] = perm_hi(a, bb);
        PIN(wdr[j]); PIN(wdr[j + 1]);
    }
    VMWAIT(0);
    PIN(mg[0]); PIN(hh);
    STAMP(3);
    bf16x8 bg[4], bp[4];
#pragma unroll
    for (int s = 0; s < 4; ++s) { bg[s] = tr2(ta + s * 1024, ta + s * 1024 + 128); bp[s] = tr2(ta + 4096 + s * 1024, ta + 4096 + s * 1024 + 128); }
    if (__builtin_expect(ballot(((mg[0].z ^ tlo) | (mg[0].w ^ thi)) != 0u) != 0ull, 0)) gather<1, 0>(ws, sco, tlo, thi, mg, budget);
    float cb0 = 1.f + bflo(mg[0].x), cb1 = 1.f + bfhi(mg[0].x);
    *lp<u32>(ha + 1088) = pack2(cb0, cb1);
    *lp<u32>(ha + 2176) = mg[0].y;
    PIN(cb0); PIN(cb1);
    bf16x8 af[4];
    v4u yo = {0u, 0u, tlo, thi};
    PIN(yo);
#pragma unroll
    for (int s = 0; s < 4; ++s) { PIN(bg[s]); PIN(bp[s]); }
    STAMP(12);
    until(hint_wait<HE_K, true>(ws, heo, tlo, budget, hh) + E_DELAY);
    until(tk2 + E_T);
    STAMP(8);
    v4u xg, xp;
    poll2<false>(xg, xp, xo, ws, px, ws, tlo, thi);
    STAMP(13);
    float pil = asf(xp.x) + asf(xp.y);
    pil += dpp<0xB1>(pil); pil += dpp<0x4E>(pil); pil += dpp<0x141>(pil);
    float d0 = __builtin_fmaf(pil, -0.0625f, asf(xg.x)), d1 = __builtin_fmaf(pil, -0.0625f, asf(xg.y));
    PIN(d0); PIN(d1);
    {
        float e0 = d0 * cb0;
        PIN(e0);
        *lp<u32>(ha) = pack2(e0, d1 * cb1);
    }
    asm volatile("" ::: "memory");
#pragma unroll
    for (int s = 0; s < 4; ++s) af[s] = *lp<bf16x8>(hr + s * 64);
    SCHED();
    float z = red32(d0 + d1, __builtin_fmaf(d0, d0, d1 * d1));
    z = red16(z, z); z = rowsum(z);
    *lp<float>(sa) = z;
    SCHED();
    f32x4 dg = {0.f, 0.f, 0.f, 0.f}, du = {0.f, 0.f, 0.f, 0.f};
#pragma unroll
    for (int s = 0; s < 4; ++s) {
        dg = __builtin_amdgcn_mfma_f32_16x16x32_bf16(af[s], bg[s], dg, 0, 0, 0);
        du = __builtin_amdgcn_mfma_f32_16x16x32_bf16(af[s], bp[s], du, 0, 0, 0);
    }
    *lp<f32x4>(ga) = dg; *lp<f32x4>(ga + 256) = du;
    barrier();
    STAMP(11);
    const f32x4 q1 = *lp<f32x4>(L_GU + 2048), q2 = *lp<f32x4>(L_GU + 2064);
    const f32x4 p0 = *lp<f32x4>(gr), p1 = *lp<f32x4>(gr + 1024);
    const float dm = sum4(q1) * (1.f / 512.f);
    const float rstd = __builtin_amdgcn_rsqf(__builtin_fmaxf(sum4(q2) * (1.f / 512.f) - dm * dm, 0.f) + 1e-5f);
    float gx = p0.x + p1.x, gy = p0.y + p1.y;
    PIN(gx); PIN(gy);
    float s = rstd * (gx - dm * gy) + (p0.z + p1.z);
    s = red32(s, s);
    u32 gs, us;
    both16(asu(s), gs, us);
    const float gsum = asf(gs), usum = asf(us);
    const float act = gsum * usum * __builtin_amdgcn_rcpf(1.f + ex2(gsum * __builtin_fmaf(gsum * gsum, -0.044715f * 1.5957691216057308f * 1.4426950408889634f, -1.5957691216057308f * 1.4426950408889634f)));
    const u32 actp = pack2(act, dpp<0x101>(act));
    asm volatile("s_nop 1\n\t"
        "v_dot2c_f32_bf16_dpp %0, %2, %3 row_newbcast:0 row_mask:0xf bank_mask:0xf\n\tv_dot2c_f32_bf16_dpp %1, %2, %4 row_newbcast:0 row_mask:0xf bank_mask:0xf\n\t"
        "v_dot2c_f32_bf16_dpp %0, %2, %5 row_newbcast:2 row_mask:0xf bank_mask:0xf\n\tv_dot2c_f32_bf16_dpp %1, %2, %6 row_newbcast:2 row_mask:0xf bank_mask:0xf\n\t"
        "v_dot2c_f32_bf16_dpp %0, %2, %7 row_newbcast:4 row_mask:0xf bank_mask:0xf\n\tv_dot2c_f32_bf16_dpp %1, %2, %8 row_newbcast:4 row_mask:0xf bank_mask:0xf\n\t"
        "v_dot2c_f32_bf16_dpp %0, %2, %9 row_newbcast:6 row_mask:0xf bank_mask:0xf\n\tv_dot2c_f32_bf16_dpp %1, %2, %10 row_newbcast:6 row_mask:0xf bank_mask:0xf\n\t"
        "v_dot2c_f32_bf16_dpp %0, %2, %11 row_newbcast:8 row_mask:0xf bank_mask:0xf\n\tv_dot2c_f32_bf16_dpp %1, %2, %12 row_newbcast:8 row_mask:0xf bank_mask:0xf\n\t"
        "v_dot2c_f32_bf16_dpp %0, %2, %13 row_newbcast:10 row_mask:0xf bank_mask:0xf\n\tv_dot2c_f32_bf16_dpp %1, %2, %14 row_newbcast:10 row_mask:0xf bank_mask:0xf\n\t"
        "v_dot2c_f32_bf16_dpp %0, %2, %15 row_newbcast:12 row_mask:0xf bank_mask:0xf\n\tv_dot2c_f32_bf16_dpp %1, %2, %16 row_newbcast:12 row_mask:0xf bank_mask:0xf\n\t"
        "v_dot2c_f32_bf16_dpp %0, %2, %17 row_newbcast:14 row_mask:0xf bank_mask:0xf\n\tv_dot2c_f32_bf16_dpp %1, %2, %18 row_newbcast:14 row_mask:0xf bank_mask:0xf\n\ts_nop 2"
        : "+v"(yo.x), "+v"(yo.y) : "v"(actp), "v"(wdr[0]), "v"(wdr[1]), "v"(wdr[2]), "v"(wdr[3]), "v"(wdr[4]), "v"(wdr[5]), "v"(wdr[6]), "v"(wdr[7]),
          "v"(wdr[8]), "v"(wdr[9]), "v"(wdr[10]), "v"(wdr[11]), "v"(wdr[12]), "v"(wdr[13]), "v"(wdr[14]), "v"(wdr[15]));
    st_sc(ws, so, yo);
    STAMP(14);
}

#define ISSUE(h) do { if ((h) & 1) dma_b(ckv + ktile(sidx, (h) >> 1), od, slot((h) >> 1) + p * 4096u, pcf, L_FL + C_LAND + ((h) >> 1) * 16u + p * 4u); else dma_a(ckv + ktile(sidx, (h) >> 1), od, slot((h) >> 1) + p * 4096u); } while (0)
#define FC_A 203
#define FC_B 342
#define WPOS 10
#define SPF 0
constexpr int NU = SPF ? 36 : 32;
constexpr int half_of(int u) { return !SPF || u < WPOS ? u : u < WPOS + 4 ? -1 : u - 4; }
constexpr int ops_in(int u0, int u1) { int n = 0; for (int u = u0; u < u1; ++u) n += 2 + (half_of(u) >= 0 ? half_of(u) & 1 : 0); return n; }
DI void publish(char* ws, u32 g1, const f32x16 (&o)[4], v4u (&d)[8]) {
#pragma unroll
    for (int k = 0; k < 8; k += 2) {
        u32 c[8];
#pragma unroll
        for (int i = 0; i < 4; ++i) { c[i] = pack2(o[0][2 * k + i], o[1][2 * k + i]); c[4 + i] = pack2(o[2][2 * k + i], o[3][2 * k + i]); }
        SCHED();
#pragma unroll
        for (int i = 0; i < 2; ++i) {
            auto r0 = __builtin_amdgcn_permlane16_swap(c[2 * i], c[2 * i + 1], false, false);
            auto r1 = __builtin_amdgcn_permlane16_swap(c[4 + 2 * i], c[5 + 2 * i], false, false);
            d[k + i] = (v4u){(u32)r0[0], (u32)r0[1], (u32)r1[0], (u32)r1[1]};
            st_sc_keep(ws + WS_PAY + (k + i) * PHD, g1, d[k + i]);
        }
        SCHED();
    }
}
DI void stream_pv(const Params& P, int b, u32 p, int lane, const u32 (&od)[4], u64 pcf, u32 t0, u32& tl) {
    const u32 tz = t0; (void)tz;
    char* const ws = P.ws;
    const u32 sidx = (u32)b - 128, i15 = lane & 15, R = lane >> 4;
    const char* ckv = (const char*)P.ckv;
    u32 va[2][4];
    {
        const u32 hf = R >> 1, blk = R & 1, q4 = i15 >> 2, p4 = i15 & 3;
#pragma unroll
        for (int e = 0; e < 2; ++e)
#pragma unroll
            for (int cb = 0; cb < 4; ++cb) {
                const u32 row = 8 * hf + 4 * e + q4;
                va[e][cb] = p * 4096u + row * 256u + (((4 * cb + 2 * blk + (p4 >> 1)) ^ swz(row)) << 4) + 8 * (p4 & 1);
                PIN(va[e][cb]);
            }
    }
    u32 fb = L_FL, pa = L_P + (u32)lane * 16u;
    PIN(fb); PIN(pa);
    bf16x8 V[8][4];
    u32 vbase = 0;
#define VBASE(t) do { if (vwin(t) != vbase) { _Pragma("unroll") for (int i_ = 0; i_ < 8; ++i_) { va[i_ >> 2][i_ & 3] += vwin(t) - vbase; PIN(va[i_ >> 2][i_ & 3]); } vbase = vwin(t); } } while (0)
#define VREAD(t) do { VBASE(t); _Pragma("unroll") for (int cb_ = 0; cb_ < 4; ++cb_) V[(t) & 7][cb_] = tr2(va[0][cb_] + (slot(t) - vwin(t)), va[1][cb_] + (slot(t) - vwin(t))); } while (0)
    u64 e64;
    EPOCH();
    const u32 tlo = (u32)e64;
    u32 g1, gc, lr;
    {
        const u32 k = 4 * (i15 >> 3) + (i15 & 3);
        g1 = (p * 128u + (sidx ^ ((tlo & 1) << 6))) * 256u + R * (8 * PHD) + i15 * 16u;
        gc = WS_CHK + (sidx * 4 + p) * 512u + (8 * R + k) * 16u;
        lr = L_L + (8 * (k >> 1) + 4 * (R >> 1) + 2 * (k & 1) + (R & 1)) * 16u;
        PIN(g1); PIN(gc); PIN(lr);
    }
    bool held = false;
    u32 wpf = ((sidx & 7) * 16 + (sidx >> 3)) * 32768u + p * 8192u + (u32)lane * 16u;
    PIN(wpf);
#pragma unroll
    for (int u = HW; u < NU; ++u) {
        const int h = half_of(u), l = half_of(u - HW);
        if (h >= 14 && h < 30 && !(h & 1)) wait1(fb, C_KRD + h / 2 - 7);
        if (u >= 12 && h < 30 && !held && rfl(flag_get(C_H)) != 0u) { wait4(fb, C_Q); held = true; }
        if (h == 30) wait4(fb, C_E);
        VMWAIT(ops_in(u - HW + 1, u));
        if (l == 1) STAMP(18);
        if (l == 7) STAMP(19);
        if (l == 15) STAMP(1);
        if (h == 24) STAMP(2);
        if (h >= 0) ISSUE(h);
        else { dma_pl(P.wo + (u - WPOS) * 1024, wpf, L_P + 14 * 1024); dma_pl(P.wo + (u - WPOS) * 1024 + 512, wpf, L_P + 14 * 1024); }
        if (l >= 0 && (l & 1) && l < 16) VREAD(l >> 1);
    }
    static_assert(WPOS >= HW && WPOS <= 24 && HW >= 3 && HW <= 6, "tile 12 must have landed here");
    f32x16 o[4];
#pragma unroll
    for (int cb = 0; cb < 4; ++cb)
#pragma unroll
        for (int i = 0; i < 16; ++i) o[cb][i] = 0.f;
    bf16x8 pb[4], pc[4];
#define PV(t, pp) do { SCHED(); _Pragma("unroll") for (int cb_ = 0; cb_ < 4; ++cb_) o[cb_] = __builtin_amdgcn_mfma_f32_32x32x16_bf16(pp[(t) & 3], V[(t) & 7][cb_], o[cb_], 0, 0, 0); SCHED(); } while (0)
#define PVR(t, pp, u) do { VBASE(u); _Pragma("unroll") for (int cb_ = 0; cb_ < 4; ++cb_) { SCHED(); \
        o[cb_] = __builtin_amdgcn_mfma_f32_32x32x16_bf16(pp[(t) & 3], V[(t) & 7][cb_], o[cb_], 0, 0, 0); SCHED(); \
        V[(u) & 7][cb_] = tr2(va[0][cb_] + (slot(u) - vwin(u)), va[1][cb_] + (slot(u) - vwin(u))); } SCHED(); } while (0)
    STAMP(3);
    if (p == 0) {
        const v4u z = *lp<v4u>(fb + C_TZ);
        const u32 t = now32();
        if (lane == 0) st_sc2(ws, WS_FC + sidx * 8u, (v2u){t + max(FC_A, FC_B - (int)min(min(t - z.x, t - z.y), min(t - z.z, t - z.w))), tlo});
    }
    wait4p(fb, C_P, pa, 0, pb);
    STAMP(7);
    CYC0();
    CDECL();
    CS(0);
#pragma unroll
    for (int t = 0; t < 5; ++t) { PVR(t, pb, t + 8); if (t < 4) pb[t] = *lp<bf16x8>(pa + (t + 4) * 1024); if (t == 0) CS(1); if (t == 1) CS(2); if (t == 3) CS(3); }
    CS(4);
    PV(5, pb); PV(6, pb);
    u32 pf = flag_get(C_P + 4);
#pragma unroll
    for (int i = 0; i < 4; ++i) pc[i] = *(volatile LAS bf16x8*)lp<bf16x8>(pa + (8 + i) * 1024);
    PV(7, pb);
    STAMP(4);
    CS(5);
    if (__builtin_expect(rfl(pf) != 0x01010101u, 0)) wait4p(fb, C_P + 4, pa, 8192, pc);
    CS(6);
    PV(8, pc);
    VMWAIT(5);
    PVR(9, pc, 13); PVR(10, pc, 14);
    pf = flag_get(C_P + 8);
#pragma unroll
    for (int i = 0; i < 3; ++i) pb[i] = *(volatile LAS bf16x8*)lp<bf16x8>(pa + (12 + i) * 1024);
    PV(11, pc);
    STAMP(5);
    CS(7);
    pf = rfl(pf);
    if (__builtin_expect(!(pf & 0x1u), 0)) pb[0] = wait1p(fb, C_P + 8, pa, 12 * 1024);
    PV(12, pb);
    if (__builtin_expect(!(pf & 0x100u), 0)) pb[1] = wait1p(fb, C_P + 9, pa, 13 * 1024);
    VBASE(15);
    PV(13, pb);
    if (__builtin_expect(!(pf & 0x10000u), 0)) pb[2] = wait1p(fb, C_P + 10, pa, 14 * 1024);
    VMWAIT(0);
    PVR(14, pb, 15);
    pb[3] = wait1p(fb, C_P + 11, pa, 15 * 1024);
    PV(15, pb);
    STAMP(6);
    STAMPC(29);
    CS(8);
    v4u d[8];
    publish(ws, g1, o, d);
    v2u fl = *(volatile LAS v2u*)lp<v2u>(fb + C_L);
    f32x4 lq = *(volatile LAS f32x4*)lp<f32x4>(lr);
    u32 mref = 0;
    STAMP(8);
    CS(9);
    u32 cs[8];
#pragma unroll
    for (int k = 0; k < 8; ++k) { cs[k] = (d[k].x + d[k].y) + (d[k].z + d[k].w); PIN(cs[k]); }
    if (__builtin_expect(rfl(fl.x) != 0x01010101u || rfl(fl.y) != 0u, 0)) {
        wait4(fb, C_L);
        if (rfl(flag_get(C_BAD)) != 0u) {
            barrier();
            barrier();
#pragma unroll
            for (int cb = 0; cb < 4; ++cb)
#pragma unroll
                for (int i = 0; i < 16; ++i) o[cb][i] = 0.f;
#pragma clang loop unroll(disable)
            for (u32 t = 0; t < 16; ++t) {
                dma_a(ckv + ktile(sidx, t), od, slot(15) + p * 4096u); dma_b(ckv + ktile(sidx, t), od, slot(15) + p * 4096u, pcf, L_FL + C_LAND);
                VMWAIT(0);
                VREAD(15);
                pc[3] = *lp<bf16x8>(pa + t * 1024);
                PV(15, pc);
                LGKM0();
            }
            publish(ws, g1, o, d);
            asm volatile("s_nop 1");
#pragma unroll
            for (int k = 0; k < 8; ++k) { cs[k] = (d[k].x + d[k].y) + (d[k].z + d[k].w); PIN(cs[k]); }
            mref = *(volatile LAS u32*)lp<u32>(L_M + (lr - L_L) / 4);
        }
        lq = *(volatile LAS f32x4*)lp<f32x4>(lr);
    }
    {
        u32 t0, t1, t2, t3, u0, u1;
        asm volatile("s_nop 1\n\t"
            "v_add_u32_dpp %0, %6, %6 row_ror:8 row_mask:0xf bank_mask:0x3\n\tv_add_u32_dpp %1, %7, %7 row_ror:8 row_mask:0xf bank_mask:0x3\n\t"
            "v_add_u32_dpp %2, %8, %8 row_ror:8 row_mask:0xf bank_mask:0x3\n\tv_add_u32_dpp %3, %9, %9 row_ror:8 row_mask:0xf bank_mask:0x3\n\t"
            "v_add_u32_dpp %0, %10, %10 row_ror:8 row_mask:0xf bank_mask:0xc\n\tv_add_u32_dpp %1, %11, %11 row_ror:8 row_mask:0xf bank_mask:0xc\n\t"
            "v_add_u32_dpp %2, %12, %12 row_ror:8 row_mask:0xf bank_mask:0xc\n\tv_add_u32_dpp %3, %13, %13 row_ror:8 row_mask:0xf bank_mask:0xc\n\t"
            "v_add_u32_dpp %4, %0, %0 row_half_mirror row_mask:0xf bank_mask:0x5\n\tv_add_u32_dpp %5, %1, %1 row_half_mirror row_mask:0xf bank_mask:0x5\n\t"
            "v_add_u32_dpp %4, %2, %2 row_half_mirror row_mask:0xf bank_mask:0xa\n\tv_add_u32_dpp %5, %3, %3 row_half_mirror row_mask:0xf bank_mask:0xa\n\t"
            "s_nop 0\n\t"
            "v_add_u32_dpp %4, %4, %4 quad_perm:[1,0,3,2] row_mask:0xf bank_mask:0xf\n\ts_nop 0\n\tv_add_u32_dpp %5, %5, %5 quad_perm:[1,0,3,2] row_mask:0xf bank_mask:0xf\n\t"
            "v_add_u32_dpp %4, %4, %4 quad_perm:[2,3,0,1] row_mask:0xf bank_mask:0xf\n\ts_nop 0\n\tv_add_u32_dpp %5, %5, %5 quad_perm:[2,3,0,1] row_mask:0xf bank_mask:0xf\n\ts_nop 0"
            : "=&v"(t0), "=&v"(t1), "=&v"(t2), "=&v"(t3), "=&v"(u0), "=&v"(u1)
            : "v"(cs[0]), "v"(cs[1]), "v"(cs[2]), "v"(cs[3]), "v"(cs[4]), "v"(cs[5]), "v"(cs[6]), "v"(cs[7]));
        const u32 x = (gc & 16u) ? u1 : u0;
        const v4u cg = {(gc & 32u) ? dppu<0x104>(x) : x, asu(sum4(lq)), mref, tlo};
        asm volatile("s_mov_b64 exec, %3\n\tglobal_store_dwordx4 %0, %1, %2 sc1\n\ts_mov_b64 exec, -1\n\ts_nop 0" :: "v"(gc), "v"(cg), "s"(ws), "s"(0x0f0f0f0f0f0f0f0full) : "memory");
    }
    CS(10);
    STAMP(9);
    COUT(11);
    barrier();
}

DI void p_write(const f32x4& s0, const f32x4& s1, u32 pa, f32x4& a, f32x4& c) {
#pragma unroll
    for (int k = 0; k < 4; ++k) a[k] = ex2(s0[k]);
#pragma unroll
    for (int k = 0; k < 4; ++k) c[k] = ex2(s1[k]);
    *lp<v2u>(pa) = (v2u){pack2(a.x, a.y), pack2(a.z, a.w)};
    *lp<v2u>(pa + 256) = (v2u){pack2(c.x, c.y), pack2(c.z, c.w)};
}
template <u32 KO> DI void qk_second(const bf16x8 (&q0)[16], const bf16x8 (&q1)[16], const u32 (&a)[16], u32 fw, u32 off, f32x4& s0, f32x4& s1, u64& cw) {
    bf16x8 fa[16];
    land_wait_k<KO>(fw, off, a[0], a[1], a[2], a[3], fa[0], fa[1], fa[2], fa[3]);
#pragma unroll
    for (int i = 0; i < 4; ++i) { s0[i] = 0.f; s1[i] = 0.f; }
#pragma unroll
    for (int s = 0; s < 16; ++s) {
        SCHED();
        s0 = __builtin_amdgcn_mfma_f32_16x16x32_bf16(fa[s], q0[s], s0, 0, 0, 0);
        if (s < KD - 4) { SCHED(); fa[2 * s + 4] = *lp<bf16x8>(a[2 * s + 4] + KO); SCHED(); }
        s1 = __builtin_amdgcn_mfma_f32_16x16x32_bf16(fa[s], q1[s], s1, 0, 0, 0);
        SCHED();
        if (s < KD - 4) fa[2 * s + 5] = *lp<bf16x8>(a[2 * s + 5] + KO);
        else if (s + KD < 16) fa[s + KD] = *lp<bf16x8>(a[s + KD] + KO);
    }
}
DI void stream_qk(const Params& P, int b, u32 w, int lane, u32 t0, int& budget, u32& tl) {
    char* const ws = P.ws;
    const int xc = b & 7, yy = (b >> 3) - 16;
    const u32 i15 = lane & 15, g = lane >> 4;
    u64 e64;
    EPOCH();
    const u32 tlo = (u32)e64, thi = (u32)(e64 >> 32);
    const u32 ak = g * 4096u + i15 * 256u + (swz(i15) << 4);
    const u32 c16 = i15 ^ g ^ ((w & 1) ? 12u : 0u);
    u32 qo = WS_QABS + (w ^ ((tlo & 1) << 1)) * 2048u + g * 256u + c16 * 16u, co = WS_QCHK + w * 2048u + (u32)lane * 16u;
    u32 va = L_IMG + ((i15 & 3) ^ 2) * 8192u + (8 * w + g) * 256u + (((i15 >> 2) ^ ((w & 1) ? 3u : 0u)) << 6) + ((i15 & 3) << 4);
    u32 fa = L_FL + w, fb = L_FL, fw = L_FL + 16 * w + (i15 & 3) * 4, pa = L_P + w * 1024u + (g >> 1) * 512u + i15 * 16u + (g & 1) * 8u;
    u32 bq[4];
#pragma unroll
    for (int k = 0; k < 4; ++k) { bq[k] = L_IMG + i15 * 256u + (((g * 4 + k) ^ (i15 & 3) ^ ((i15 & 8) ? 12u : 0u)) << 4); PIN(bq[k]); }
    PIN(qo); PIN(co); PIN(va); PIN(fa); PIN(fb); PIN(fw); PIN(pa);
    u32 pa4 = pa + 4096;
    PIN(pa4);
    v4u chk0, chk1;
    UNDEF(chk0); UNDEF(chk1);
#define REQ_Q() do { \
        _Pragma("unroll") for (int k = 0; k < 8; ++k) dma_sc(ws + (k & 1) * 1024 + (k >> 1) * 8192, qo, L_IMG + w * 2048u + (k & 1) * 1024u + ((k >> 1) ^ 2) * 8192u); \
        ld_sc_into(chk0, ws, co); ld_sc_into(chk1, ws + 1024, co); } while (0)
    u32 warm;
    UNDEF(warm);
    if (KMAP && w == 2) asm volatile("global_load_dword %0, %1, %2" : "+v"(warm) : "v"(((u32)lane & 15) * 2097152u + 2097148u), "s"(P.ckv) : "memory");
    if (w == 3) asm volatile("global_load_dword %0, %1, %2\n\tglobal_load_dword %0, %1, %3\n\tglobal_load_dword %0, %1, %4\n\tglobal_load_dword %0, %5, %6" : "+v"(warm)
        : "v"(((u32)lane & 1) * (1792u * 1024u - 4u)), "s"(P.wg), "s"(P.wu), "s"(P.wd), "v"(WS_PAY + (((u32)lane & 3) * 10 + 1) * PHD + 4 * 128 * 256), "s"(ws) : "memory");
    __builtin_amdgcn_s_setprio(3);
    bf16x8 k0[16], k1[16];
    land_wait(fw, C_LAND);
    VMWAIT(0);
    PIN(warm);
    {
        u32 a = ak + slot_no(w) * 16384u; PIN(a);
#pragma unroll
        for (int s = 0; s < 16; ++s) k0[s] = *lp<bf16x8>(a ^ (u32)(s << 4));
        flag_set(fa, C_KRD);
    }
    QTimer qt = {time_base(ws + WS_ST, t0) + Q_FIRST, 0u};
    u32 ka[16];
    {
        u32 a = ak + slot_no(w + 8) * 16384u; PIN(a);
#pragma unroll
        for (int s = 0; s < 16; ++s) { ka[s] = a ^ (u32)(s << 4); PIN(ka[s]); }
    }
    bool asked = false;
#pragma clang loop unroll(disable)
    while (ballot(*(volatile LAS u32*)lp<u32>(fw + C_LAND + 64) == 0u) != 0ull) {
        if (q_due(qt, ws, tlo)) {
            flag_set(fa, C_H);
            until(qt.t + Q_PRE);
            REQ_Q();
            flag_set(fa, C_Q);
            land_wait(fw, C_LAND + 64);
            asked = true;
            break;
        }
    }
    asm volatile("" ::: "memory");
    {
        u32 a = ak + slot_no(w + 4) * 16384u; PIN(a);
#pragma unroll
        for (int s = 0; s < 16; ++s) k1[s] = *lp<bf16x8>(a ^ (u32)(s << 4));
        flag_set(fa, C_KRD + 4);
    }
#pragma unroll
    for (int s = 0; s < 16; ++s) { PIN(k0[s]); PIN(k1[s]); }
    STAMP(1);
    if (!asked) {
#pragma clang loop unroll(disable)
        while (!q_due(qt, ws, tlo)) __builtin_amdgcn_s_sleep(1);
        flag_set(fa, C_H);
        until(qt.t + Q_PRE);
    }
    STAMP(22);
    u32 tz_;
#pragma clang loop unroll(disable)
    do {
        if (!asked) {
            REQ_Q();
            flag_set(fa, C_Q);
        }
        asked = false;
        STAMP(20);
        VMWAIT(0);
        tz_ = now32();
        STAMP(4);
        PIN(chk0); PIN(chk1);
        const v4u x0 = *(volatile LAS v4u*)lp<v4u>(va), x1 = *(volatile LAS v4u*)lp<v4u>(va ^ 16u), x2 = *(volatile LAS v4u*)lp<v4u>(va ^ 32u), x3 = *(volatile LAS v4u*)lp<v4u>(va ^ 48u);
        const v4u y0 = *(volatile LAS v4u*)lp<v4u>(va + 1024), y1 = *(volatile LAS v4u*)lp<v4u>((va ^ 16u) + 1024), y2 = *(volatile LAS v4u*)lp<v4u>((va ^ 32u) + 1024), y3 = *(volatile LAS v4u*)lp<v4u>((va ^ 48u) + 1024);
        const v4u xs = (x0 + x1) + (x2 + x3), ys = (y0 + y1) + (y2 + y3);
        const u32 bad = ((((xs.x + xs.y) + (xs.z + xs.w)) ^ chk0.x) | (chk0.z ^ tlo) | (chk0.w ^ thi)) | ((((ys.x + ys.y) + (ys.z + ys.w)) ^ chk1.x) | (chk1.z ^ tlo) | (chk1.w ^ thi));
        if (ballot(bad != 0u) == 0ull) break;
        __builtin_amdgcn_s_sleep(4);
    } while (--budget > 0);
    flag_set(fa, C_Z);
    *lp<u32>(L_FL + C_TZ + w * 4) = tz_;
    STAMP(5);
    wait4(fb, C_Z);
    STAMP(6);
    const u32 tz = t0; (void)tz;
    CYC0();
    u64 cs_[12];
    CS(0);
    f32x4 s[8];
#pragma unroll
    for (int i = 0; i < 4; ++i) s[i] = (f32x4){0.f, 0.f, 0.f, 0.f};
    bf16x8 q0[16], q1[16];
#define QREAD(s_) do { q0[s_] = *lp<bf16x8>(bq[(s_) & 3] + (((s_) >> 2) ^ 2) * 8192); q1[s_] = *lp<bf16x8>(bq[(s_) & 3] + (((s_) >> 2) ^ 2) * 8192 + 4096); } while (0)
#pragma unroll
    for (int i = 0; i < QD; ++i) QREAD(i);
#pragma unroll
    for (int i = 0; i < 16; ++i) {
        if (i + QD < 16) QREAD(i + QD);
        if (i + QD == 7) flag_set(fa, C_E);
        SCHED();
        s[0] = __builtin_amdgcn_mfma_f32_16x16x32_bf16(k0[i], q0[i], s[0], 0, 0, 0);
        s[1] = __builtin_amdgcn_mfma_f32_16x16x32_bf16(k0[i], q1[i], s[1], 0, 0, 0);
        s[2] = __builtin_amdgcn_mfma_f32_16x16x32_bf16(k1[i], q0[i], s[2], 0, 0, 0);
        s[3] = __builtin_amdgcn_mfma_f32_16x16x32_bf16(k1[i], q1[i], s[3], 0, 0, 0);
        SCHED();
    }
    STAMP(21);
    STAMPC(29);
    CS(1);
    float l0, l1;
    f32x4 e0, e1, e2, e3;
    p_write(s[0], s[1], pa, e0, e1);
    p_write(s[2], s[3], pa4, e2, e3);
    flag_set(fa, C_P);
    SCHED();
    STAMP(24);
    CS(2);
    l0 = sum4(e0 + e2); l1 = sum4(e1 + e3);
    pa4 = pa + 8192;
    PIN(l0); PIN(l1); PIN(pa4);
    qk_second<0>(q0, q1, ka, fw, C_LAND + 128, s[4], s[5], cs_[3]);
    CS(4);
    p_write(s[4], s[5], pa4, e0, e1);
    flag_set(fa, C_P + 4);
    SCHED();
    STAMP(26);
    CS(5);
    l0 += sum4(e0); l1 += sum4(e1);
    float m0 = __builtin_fmaxf(max8(s[0], s[2]), max4(s[4])), m1 = __builtin_fmaxf(max8(s[1], s[3]), max4(s[5]));
    u32 ln = (u32)lane; PIN(ln);
    u32 la = L_L + (ln & 31) * 16 + w * 4, pl = pa + 12288;
    PIN(l0); PIN(l1); PIN(m0); PIN(m1); PIN(la); PIN(pl);
    if (w == 3) wait4(fb, C_P);
    qk_second<49152>(q0, q1, ka, fw, C_LAND + 192, s[6], s[7], cs_[6]);
    CS(7);
    STAMP(27);
    p_write(s[6], s[7], pl, e0, e1);
    flag_set(fa, C_P + 8);
    SCHED();
    STAMP(28);
    CS(8);
    {
        float z = red16(l0 + sum4(e0), l1 + sum4(e1));
        *lp<float>(la) = red32(z, z);
    }
    m0 = __builtin_fmaxf(m0, max4(s[6])); m1 = __builtin_fmaxf(m1, max4(s[7]));
    if (ballot(!(__builtin_fmaxf(__builtin_fabsf(m0), __builtin_fabsf(m1)) <= LAZY)) != 0ull) flag_set(fa, C_BAD);
    flag_set(fa, C_L);
    CS(9);
    COUT(10);
    __builtin_amdgcn_s_setprio(0);
    wait4(fb, C_L);
    if (__builtin_expect(rfl(flag_get(C_BAD)) != 0u, 0)) {
        m0 = max32(max16(m0)); m1 = max32(max16(m1));
        *lp<float>(la + (L_IMG + 4096 - L_L)) = (ln & 16) ? m1 : m0;
        barrier();
        const float m = max4(*lp<f32x4>((la & ~15u) + (L_IMG + 4096 - L_L)));
        *lp<float>(L_M + (ln & 31) * 4) = m;
        u32 e, od;
        both16(asu(m), e, od);
        l0 = 0.f; l1 = 0.f;
#pragma unroll
        for (int i = 0; i < 4; ++i) { p_write(s[2 * i] - asf(e), s[2 * i + 1] - asf(od), pa + 4096 * i, e0, e1); l0 += sum4(e0); l1 += sum4(e1); }
        const float z = red16(l0, l1);
        *lp<float>(la) = red32(z, z);
        barrier();
    }
    if (yy < 12 || xc < 4) mlp_part(P, b, w, lane, tlo, thi, budget, tl);
}

#define D_DELAY 78
#define A2_T 165
#define D_T 205
#define PREW 1
#define U_GAP 90
#define U_STEP 30
#define T_MID 220
#define WQL 1
#define CODE_Y 0x13
__global__ void __launch_bounds__(512, 2) fused_kernel(Params) {
    v16u ka;
    v8u kb;
    v2u kc;
    asm volatile("s_load_dwordx16 %0, %3, 0x0\n\ts_load_dwordx8 %1, %3, 0x40\n\ts_load_dwordx2 %2, %3, 0x60" : "=&s"(ka), "=&s"(kb), "=&s"(kc) : "s"(__builtin_amdgcn_kernarg_segment_ptr()));
    SCHED();
    const int tid = threadIdx.x, lane = tid & 63;
    const int wave = __builtin_amdgcn_readfirstlane(tid >> 6);
    const int b = blockIdx.x, xc = b & 7, y = b >> 3;
    int budget = BUDGET;
    u32 tl = 0;
    u64 pc0;
    asm volatile("s_getpc_b64 %0\n\ts_nop 0" : "=s"(pc0));
    *lp<u32>(L_FL + (u32)(wave >> 2) * 1024 + (u32)(wave & 3) * 4 + (u32)lane * 16) = 0u;
    if (wave >= 4 && (y & CODE_Y) == 0x10) {
        asm volatile("s_mov_b32 m0, %1\n\ts_nop 0\n\tglobal_load_lds_dword %0, %2\n\tglobal_load_lds_dword %0, %3\n\tglobal_load_lds_dword %0, %4"
            :: "v"((u32)lane * 64u + (u32)(wave - 4) * CODE_STR), "s"(L_IMG + (u32)(wave - 4) * 2048u), "s"(pc0), "s"(pc0 + 4096), "s"(pc0 + 8192) : "memory");
    }
    const u32 head = (u32)(xc * 4 + (y >> 2)), j = (u32)y & 3, w = (u32)wave;
    const u32 i15 = lane & 15, g = lane >> 4;
    const u32 ks = b & 15, cg = (u32)(b >> 4) * 2 + (w & 1);
    u32 od[4], oi[4], oq = ((j >> 1) * 256 + w * 32 + (lane & 8) * 2 + g) * 8192u + head * 256u + (j & 1) * 128u + (lane & 7) * 16u;
#pragma unroll
    for (int k = 0; k < 4; ++k) { od[k] = (3 * k + g) * 1024u + w * 256u + ((i15 ^ (g << 2) ^ (u32)k) << 4); PIN(od[k]); }
    oi[0] = (ks * 32 + 2 * (u32)(lane >> 2)) * 2; oi[1] = (u32)lane * 16; oi[2] = cg * 64u + (lane & 3) * 16u; oi[3] = oi[0] * 3072u + oi[2]; oi[2] += g * 4u;
#pragma unroll
    for (int k = 0; k < 4; ++k) PIN(oi[k]);
    PIN(oq);
    v4u wm[4], xa;
    u32 cc, xs;
#pragma unroll
    for (int k = 0; k < 4; ++k) UNDEF(wm[k]);
    UNDEF(xa); UNDEF(cc); UNDEF(xs);
    int role = (0xffaa5550u >> ((y & 16) + 2 * wave)) & 3;
    asm volatile("s_waitcnt lgkmcnt(0)" : "+s"(role), "+s"(ka), "+s"(kb), "+s"(kc) :: "memory");
#define KP(T, v, i) ((T)((u64)v[i + 1] << 32 | v[i]))
    Params P;
    P.wmod = KP(const u16*, ka, 0); P.c = KP(const u16*, ka, 2); P.x = KP(const u16*, ka, 4); P.ws = KP(char*, ka, 6);
    P.wq = KP(const u16*, ka, 8); P.ckv = KP(const u16*, ka, 10); P.wuk = KP(const u16*, ka, 12); P.wo = KP(const u16*, ka, 14);
    P.wuv = KP(const u16*, kb, 0); P.wg = KP(const u16*, kb, 2); P.wu = KP(const u16*, kb, 4); P.wd = KP(const u16*, kb, 6); P.out = KP(u16*, kc, 0);
    char* const ws = P.ws;
    if (role == 0) {
        asm volatile("global_load_dword %0, %3, %4\n\tglobal_load_dwordx4 %1, %5, %6\n\tglobal_load_dword %2, %7, %6" : "+v"(cc), "+v"(xa), "+v"(xs)
            : "v"(oi[0]), "s"(P.c), "v"(oi[1]), "s"(P.x), "v"(oi[2]) : "memory");
        asm volatile("global_load_dwordx4 %0, %4, %5 nt\n\tglobal_load_dwordx4 %1, %4, %6 nt\n\tglobal_load_dwordx4 %2, %4, %5 offset:1024 nt\n\tglobal_load_dwordx4 %3, %4, %6 offset:1024 nt"
            : "+v"(wm[0]), "+v"(wm[1]), "+v"(wm[2]), "+v"(wm[3]) : "v"(oi[3]), "s"(P.wmod), "s"(P.wmod + 3072) : "memory");
    }
    const char* const ckv = (const char*)P.ckv;
    const u32 sidx = (u32)b - 128;
    const u64 pcf = pc0 - w * 256u;
    if (T_OPEN0 == 0 && role == 2) { const u32 p = w; ISSUE(0); ISSUE(1); }
    __builtin_amdgcn_s_barrier();
    u64 t0_;
    asm volatile("s_memrealtime %0" : "=s"(t0_));
    v4u wr[4];
#pragma unroll
    for (int k = 0; k < 4; ++k) UNDEF(wr[k]);
    if (WQL ? role == 1 : role < 2) {
#pragma unroll
        for (int k = 0; k < 4; ++k) ld_nt_into(wr[k], P.wq + k * 16384, oq);
    }
    asm volatile("s_waitcnt lgkmcnt(0)" : "+s"(t0_));
    const u32 t0 = (u32)t0_;
    STAMP(20);
    asm volatile("" : "+s"(role));
    if (role >= 2) {
        if (role == 2) {
            const u32 p = w;
#pragma unroll
            for (int h = T_OPEN0 ? 0 : 2; h < HW; ++h) {
                if (h == 0) until(t0 + T_OPEN0);
                if (h == 2) until(t0 + T_OPEN1);
                if (h == 4 && T_OPEN2 > T_OPEN1) until(t0 + T_OPEN2);
                ISSUE(h);
            }
            stream_pv(P, b, w, lane, od, pcf, t0, tl);
        } else stream_qk(P, b, w - 4, lane, t0, budget, tl);
        STAMP(15);
        STAMP_OUT();
        return;
    }
    u64 e64;
    u32 tb, tq;
    {
        const u32 p = lane & 3;
        v4u wk[8];
#pragma unroll
        for (int k = 0; k < 8; ++k) UNDEF(wk[k]);
        if (b < 8 && tid == 128) st_sc2(ws, WS_ST + (u32)b * 8u, (v2u){t0, ~t0});
        u32 warm;
        UNDEF(warm);
        if (wave == 7) asm volatile("global_load_dword %0, %1, %2\n\tglobal_load_dword %0, %1, %3\n\tglobal_load_dword %0, %1, %4\n\tglobal_load_dword %0, %5, %6" : "+v"(warm)
            : "v"(((u32)lane & 1) * ((4u << 20) - 4u)), "s"(P.wuk), "s"(P.wo), "s"(P.wuv), "v"(WS_PAY + (((u32)lane & 3) * 10 + 1) * PHD + 4 * 128 * 256), "s"(ws) : "memory");
        EPOCH();
        const u32 tlo = (u32)e64, thi = (u32)(e64 >> 32);
        if (PREW && wave == 3) {
            const u32 rh = 8 * ((head & 7) >> 1) + 4 * (head >> 4) + 2 * (head & 1) + ((head >> 3) & 1);
            if (j == 0 && lane < 16) st_sc(ws, WS_QCHK + rh * 256u + (u32)lane * 16u, (v4u){0u, 0u, ~tlo, 0u});
            if (b == 9 && lane < 8 * NQH) st_sc(ws, WS_QH + (u32)lane * 16u, (v4u){0u, ~tlo, 0u, ~tlo});
            if (b == 10) st_sc(ws, WS_FC + (u32)lane * 16u, (v4u){0u, ~tlo, 0u, ~tlo});
        }
        if (role == 0) {
            u32 so = piece(WS_HP, 2048, cg) + (ks * 8 + 2 * p + (u32)(lane >> 5)) * 16u;
            VMWAIT(WQL ? 4 : 8);
            PIN(xa); PIN(xs); PIN(cc);
            float mean, rstd;
            ln_stats(xa, mean, rstd);
            const u32 sp = pack2(silu(bflo(cc)), silu(bfhi(cc)));
            u32 s0l = sp & 0xffffu, s0h = sp << 16, s1l = sp >> 16, s1h = sp & 0xffff0000u;
            float xn0 = (bflo(xs) - mean) * rstd, xn1 = (bfhi(xs) - mean) * rstd;
            PIN(xn0); PIN(xn1); PIN(s0l); PIN(s0h); PIN(s1l); PIN(s1h); PIN(so);
            VMWAIT(WQL ? 0 : 4);
#pragma unroll
            for (int k = 0; k < 4; ++k) PIN(wm[k]);
            STAMP(22);
            float ta[4], tc[4];
#pragma unroll
            for (int k = 0; k < 2; ++k) {
                ta[2 * k] = red32(dot2(wm[1][k], s1l, dot2(wm[0][k], s0l, 0.f)), dot2(wm[1][k + 2], s1l, dot2(wm[0][k + 2], s0l, 0.f)));
                ta[2 * k + 1] = red32(dot2(wm[1][k], s1h, dot2(wm[0][k], s0h, 0.f)), dot2(wm[1][k + 2], s1h, dot2(wm[0][k + 2], s0h, 0.f)));
                tc[2 * k] = red32(dot2(wm[3][k], s1l, dot2(wm[2][k], s0l, 0.f)), dot2(wm[3][k + 2], s1l, dot2(wm[2][k + 2], s0l, 0.f)));
                tc[2 * k + 1] = red32(dot2(wm[3][k], s1h, dot2(wm[2][k], s0h, 0.f)), dot2(wm[3][k + 2], s1h, dot2(wm[2][k + 2], s0h, 0.f)));
            }
            float u0 = xn0 * (red16(tc[0], tc[2]) + 0.015625f) + red16(ta[0], ta[2]), u1 = xn1 * (red16(tc[1], tc[3]) + 0.015625f) + red16(ta[1], ta[3]);
            u0 += dpp<0x124>(u0); u1 += dpp<0x124>(u1); u0 += dpp<0x128>(u0); u1 += dpp<0x128>(u1);
            u32 e, od;
            both16(pack2(u0, u1), e, od);
            if ((lane & 28) == 0) st_sc(ws, so, (v4u){e, od, tlo, thi});
            STAMP(24);
            if (WQL) {
#pragma unroll
                for (int k = 0; k < 4; ++k) ld_nt_into(wr[k], P.wq + k * 16384, oq);
            }
        }
        u32 so = piece(WS_QP, 1024, head) + (((j & 2) + (u32)(lane >> 5)) * 16 + (j & 1) * 8 + (lane & 7)) * 16u, ho = piece(WS_HP, 2048, (j >> 1) * 8 + w) + (u32)lane * 16u;
        u32 la = F_QP + w * 256 + (g * 8 + (lane & 7)) * 8, lb = la + (lane & 8) * 128, ok = ((u32)y * 32 + g * 8) * 8192u + ((u32)xc * 4 + (w & 3)) * 256u + i15 * 16u;
        PIN(so); PIN(ho); PIN(la); PIN(lb); PIN(ok);
        u32 onel = 0x3f80u, oneh = 0x3f800000u;
        PIN(onel); PIN(oneh);
        v4u hg[2];
        UNDEF(hg[0]); UNDEF(hg[1]);
        __builtin_amdgcn_s_barrier();
        const u32 tm = now32();
        until(t0 + ST_LOOK);
        tb = time_base(ws + WS_ST, t0);
        const u32 ta = tb + max((u32)A_FIRST, tm - t0 + A_OWN);
        until(ta);
        ld_sc_into(hg[0], ws, ho); ld_sc_into(hg[1], ws + 1024, ho);
        __builtin_amdgcn_s_barrier();
        if (wave >= 4) {
#pragma unroll
            for (int k = 0; k < 8; ++k) ld_nt_into(wk[k], P.wuk + k * 4096, ok);
            VMWAIT(10);
        } else VMWAIT(2);
#pragma unroll
        for (int k = 0; k < 4; ++k) PIN(wr[k]);
        PIN(warm);
        STAMP(1);
        pair_rows(wr[0], wr[1]); pair_rows(wr[2], wr[3]);
#pragma unroll
        for (int k = 0; k < 4; ++k) PIN(wr[k]);
        if (wave >= 4) VMWAIT(8); else VMWAIT(0);
        PIN(hg[0]); PIN(hg[1]);
        if (__builtin_expect(ballot(neq3(hg[0].z, hg[1].z, tlo) | neq3(hg[0].w, hg[1].w, thi)) != 0ull, 0)) gather<2, 1024, 1>(ws, ho, tlo, thi, hg, budget);
        STAMP(3);
        {
            float hx = red16(red32(dot2(hg[1].x, onel, bflo(hg[0].x)), dot2(hg[1].y, onel, bflo(hg[0].y))), red32(dot2(hg[1].x, oneh, bfhi(hg[0].x)), dot2(hg[1].y, oneh, bfhi(hg[0].y))));
            hx += dpp<0x128>(hx);
            hx = asf((u32)__builtin_amdgcn_update_dpp((int)asu(hx), (int)asu(hx), 0x114, 0xf, 0x6, false));
            const u32 hp = pack2(hx, dpp<0xB1>(hx)), h01 = dppu<0x00>(hp), h23 = dppu<0xAA>(hp);
            float acc[8];
#pragma unroll
            for (int k = 0; k < 4; ++k) { acc[2 * k] = dot2(wr[2][k], h23, dot2(wr[0][k], h01, 0.f)); acc[2 * k + 1] = dot2(wr[3][k], h23, dot2(wr[1][k], h01, 0.f)); }
            float t[4];
#pragma unroll
            for (int k = 0; k < 4; ++k) t[k] = red32(acc[k], acc[k + 4]);
            float u0 = red16(t[0], t[2]), u1 = red16(t[1], t[3]);
            u0 += dpp<0x128>(u0); PIN(u0); u1 += dpp<0x128>(u1);
            *lp<f32x2>(la) = (f32x2){u0, u1};
        }
        barrier();
        if (wave == 0) {
            const LAS f32x2* qp = lp<f32x2>(lb);
            const f32x2 q2 = (qp[0] + qp[32]) + (qp[64] + qp[96]);
            float u0 = q2.x, u1 = q2.y;
            u0 += dpp<0x128>(u0); PIN(u0); u1 += dpp<0x128>(u1);
            u32 e, od;
            both16(pack2(u0, u1), e, od);
            if ((lane & 24) == 0) st_sc(ws, so, (v4u){e, od, tlo, thi});
            STAMP(2);
        }
        tq = ta + max((u32)A2_T, now32() - ta + A2_DELAY);
        if (y == ((xc * 5) & 7) + (y & 8) && tid < NQH) st_sc2(ws, WS_QH + (u32)lane * 128u + (u32)(xc * 2 + (y >> 3)) * 8u, (v2u){tq, tq ^ tlo});
        if (wave >= 4) {
            const u32 hk = (u32)xc * 4 + (w & 3);
            const u32 rho = 8 * ((hk & 7) >> 1) + 4 * (hk >> 4) + 2 * (hk & 1) + ((hk >> 3) & 1);
            const u32 og = piece(WS_QP, 1024, hk) + (u32)lane * 16u;
            u32 sq = WS_QABS + j * 8192u + (rho ^ ((tlo & 1) << 4)) * 256u + (u32)(y >> 2) * 64u + g * 16u, sc = WS_QCHK + (rho * 16 + (u32)y) * 16u;
            PIN(sq); PIN(sc);
            v4u qg[1];
            until(tq);
            gather<1, 0, 1>(ws, og, tlo, thi, qg, budget);
            __builtin_amdgcn_s_barrier();
            STAMP(23);
            u32 e, od, q0, q1, q2, q3;
            both16(pack2(red32(bflo(qg[0].x), bflo(qg[0].y)), red32(bfhi(qg[0].x), bfhi(qg[0].y))), e, od);
            both32(e, q0, q1); both32(od, q2, q3);
            float a[8];
#pragma unroll
            for (int k = 0; k < 8; ++k) { PIN(wk[k]); a[k] = dot2(wk[k][3], q3, dot2(wk[k][2], q2, dot2(wk[k][1], q1, dot2(wk[k][0], q0, 0.f)))); }
            float z[4];
#pragma unroll
            for (int k = 0; k < 4; ++k) {
                const float keep = (lane & 1) ? a[k + 4] : a[k], give = (lane & 1) ? a[k] : a[k + 4];
                float s = keep + dpp<0xB1>(give);
                s += dpp<0x122>(s); s += dpp<0x124>(s); s += dpp<0x128>(s);
                z[k] = s * (0.08838834764831845f * 1.4426950408889634f);
            }
            const u32 y0 = pack2(z[0], z[1]), y1 = pack2(z[2], z[3]);
            const u32 y2 = dppu<0xB1>(y0), y3 = dppu<0xB1>(y1);
            if (i15 == 0) st_sc(ws, sq, (v4u){y0, y1, y2, y3});
            u32 c0, c1;
            both16((y0 + y1) + (y2 + y3), c0, c1);
            both32(c0 + c1, c0, c1);
            if (lane == 0) st_sc(ws, sc, (v4u){c0 + c1, 0u, tlo, thi});
            STAMP(4);
        } else __builtin_amdgcn_s_barrier();
    }
    const u32 tlo = (u32)e64, thi = (u32)(e64 >> 32);
    const bool is_red = (y >> 2) == 1;
    const u32 rid = (u32)xc * 4 + j;
    bf16x8 bo[16], bu[4];
    u32 xo, aa, cw, cr, op, o, oc, wa, he, wl, rl, om, xr;
#define TAIL_ADDR() do { \
        xo = piece(WS_X1, 4096, (u32)(lane >> 3)) + (rid * 8 + (u32)(lane & 7)) * 16u; \
        aa = F_A + (i15 & 7) * 272 + g * 16; cw = F_CX + ((w - 2 * j) & 7) * 32 + i15 * 2; cr = F_CX + g * 16; \
        op = piece(WS_OUTP, 16384, 4 * w + g) + (u32)b * 128u + (i15 & 7) * 16u; \
        wl = F_LS + (i15 < 4 ? (w * 16 + g * 4 + i15) * 4 : 512u); rl = F_LS + (u32)lane * 8; PIN(wl); PIN(rl); \
        o = WS_PAY + head * PHD + (j * 128 + ((w * 16) ^ ((tlo & 1) << 6))) * 256u + (u32)lane * 16u; \
        oc = WS_CHK + ((w * 16 + (i15 & 3) * 4 + g) * 4 + j) * 512u + head * 16u; \
        wa = F_A + w * 272 + i15 * 16 + g * 4; \
        om = WS_ML + (rid >> 1) * 4096u + (g * 16 + (rid & 1) * 8 + (u32)(lane & 7)) * 16u; PIN(om); \
        he = WS_HE + (((u32)b + 17u * (u32)lane) & 127u) * HSTR + (u32)lane * 16u; PIN(he); \
        PIN(xo); PIN(aa); PIN(cw); PIN(cr); PIN(op); PIN(o); PIN(oc); PIN(wa); } while (0)
    {
        const u32 kq = (u32)b & 3, cgl = (u32)b >> 2;
        const u32 w4 = w & 3, mrow = kq * 128 + w4 * 32 + (u32)(lane >> 3);
        const u32 q4 = i15 >> 2, p4 = lane & 3;
        const u32 ra = 8 * (g & 1) + q4, rb = ra + 4;
        u32 xa[4], xb[4];
#pragma unroll
        for (int t2 = 0; t2 < 4; ++t2) {
            const u32 c0 = 8 * w + (p4 >> 1) + 2 * t2, x0 = F_WO + (8 * g + q4) * 1024 + 8 * (p4 & 1);
            xa[t2] = x0 + ((c0 ^ swz(ra)) << 4); xb[t2] = x0 + 4096 + ((c0 ^ swz(rb)) << 4);
            PIN(xa[t2]); PIN(xb[t2]);
        }
        u32 x0, x1, ma = F_ML + w * 256 + ((lane & 7) * 4 + g) * 8;
        {
            const u32 c0 = 2 * w + (p4 >> 1);
            x0 = F_UV + (8 * g + q4) * 256 + 8 * (p4 & 1) + ((c0 ^ swz(ra)) << 4); x1 = F_UV + (8 * g + q4 + 4) * 256 + 8 * (p4 & 1) + ((c0 ^ swz(rb)) << 4);
        }
        PIN(x0); PIN(x1); PIN(ma);
#define PIECE(k) until(tq + T_OWN + (k) * O_STEP)
#define WO_ROW(s, k) dma_pl(P.wo + ((j + (s)) & 3) * 16384, (head * 128 + 8 * w4 + (k)) * 1024u + (((u32)lane ^ swz(8 * (w4 & 1) + (k))) << 4), F_WO + ((s) * 32 + 8 * w4 + (k)) * 1024u)
#define UV_ROWS(i) do { const u32 e_ = w4 * 32 + (i) * 4 + g, c_ = ((e_ >> 2) & 1) * 64 + (e_ & 1) * 32 + ((e_ >> 1) & 1) * 16 + (e_ >> 3); \
            dma_nt(P.wuv, (j * 128 + c_) * 8192u + head * 256u + ((i15 ^ swz(e_ & 15)) << 4), F_UV + (w4 * 32 + (i) * 4) * 256u); } while (0)
        v4u mw[4];
        u32 cv[4];
#pragma unroll
        for (int k = 0; k < 4; ++k) { UNDEF(mw[k]); UNDEF(cv[k]); }
        u32 mo = mrow * 6144u + 2048u + cgl * 128u + (u32)(lane & 7) * 16u;
        PIN(mo);
        if (wave < 4) {
#pragma unroll
            for (int k = 0; k < 4; ++k) { PIECE(k); WO_ROW(0, 2 * k); WO_ROW(0, 2 * k + 1); }
            asm volatile("global_load_ushort %0, %4, %5\n\tglobal_load_ushort %1, %4, %5 offset:16\n\tglobal_load_ushort %2, %4, %5 offset:32\n\tglobal_load_ushort %3, %4, %5 offset:48"
                : "+v"(cv[0]), "+v"(cv[1]), "+v"(cv[2]), "+v"(cv[3]) : "v"(mrow * 2), "s"(P.c) : "memory");
            PIECE(4);
            ld_nt_into(mw[0], P.wmod, mo); ld_nt_into(mw[1], P.wmod + 8 * 3072, mo);
            VMWAIT(2);
        }
        TAIL_ADDR();
        until(tq + T_MID);
        STAMP(5);
        barrier();
        if (wave >= 4) {
#pragma unroll
            for (int k = 8; k < 32; ++k) WO_ROW(k >> 3, k & 7);
            STAMP(19);
        }
#pragma unroll
        for (int t2 = 0; t2 < 4; ++t2) { bo[4 * t2] = tr2(xa[t2], xb[t2]); xa[t2] += 32768; xb[t2] += 32768; PIN(xa[t2]); PIN(xb[t2]); }
        float u[2] = {0.f, 0.f};
        if (wave < 4) {
            const u32 t2 = now32() + U_GAP;
            until(t2);
            ld_nt_into(mw[2], P.wmod + 16 * 3072, mo); ld_nt_into(mw[3], P.wmod + 24 * 3072, mo);
#pragma unroll
            for (int k = 0; k < 4; ++k) { until(t2 + (k + 1) * U_STEP); UV_ROWS(2 * k); UV_ROWS(2 * k + 1); }
            VMWAIT(8);
#pragma unroll
            for (int k = 0; k < 4; ++k) { PIN(mw[k]); PIN(cv[k]); }
            const u32 sp = pack2(silu(bflo(cv[0])), silu(bflo(cv[1]))), sr = pack2(silu(bflo(cv[2])), silu(bflo(cv[3])));
            pair_rows(mw[0], mw[1]); pair_rows(mw[2], mw[3]);
            float t[4];
#pragma unroll
            for (int k = 0; k < 2; ++k) {
                t[2 * k] = red32(dot2(mw[2][k], sr, dot2(mw[0][k], sp, 0.f)), dot2(mw[2][k + 2], sr, dot2(mw[0][k + 2], sp, 0.f)));
                t[2 * k + 1] = red32(dot2(mw[3][k], sr, dot2(mw[1][k], sp, 0.f)), dot2(mw[3][k + 2], sr, dot2(mw[1][k + 2], sp, 0.f)));
            }
#pragma unroll
            for (int k = 0; k < 2; ++k) { float z = red16(t[k], t[k + 2]); z += dpp<0x128>(z); u[k] = z; }
        } else VMWAIT(0);
        PIN(u[0]); PIN(u[1]);
#pragma unroll
        for (int t2 = 0; t2 < 4; ++t2) PIN(bo[4 * t2]);
        STAMP(14);
        barrier();
        *lp<f32x2>(ma) = (f32x2){u[0], u[1]};
        if (tid < 12) *lp<u32>(F_ST + tid * 4) = 0u;
        barrier();
        if (wave == 7) {
            const LAS f32x2* q = lp<f32x2>(F_ML) + lane;
            const f32x2 s = q[0] + q[64];
            const float sx = red32(s.x, s.x), sy = red32(s.y, s.y);
            if (lane < 32) st_sc(ws, WS_ML + (cgl & 7) * 8192u + ((u32)lane >> 4) * 4096u + (cgl >> 3) * 1024u + (kq * 16 + ((u32)lane & 15)) * 16u, (v4u){asu(sx), asu(sy), tlo, thi});
        }
#pragma unroll
        for (int t2 = 0; t2 < 4; ++t2) { bo[4 * t2 + 1] = tr2(xa[t2], xb[t2]); bo[4 * t2 + 2] = tr2(xa[t2] + 32768, xb[t2] + 32768); bo[4 * t2 + 3] = tr2(xa[t2] + 65536, xb[t2] + 65536); }
        xr = ld_u32(P.x, (rid * 8 + (u32)(lane & 7)) * 4);
        VMWAIT(0);
        PIN(xr);
        if (wave >= 4 && T_MLP && (y < 12 || xc < 4)) {
            const u32 pg = (j * 128 + w4 * 32 + (u32)(lane >> 3)) * 3584u + ((u32)(y >> 2) * 8 + (u32)xc) * 128u + (u32)(lane & 7) * 16u;
#pragma unroll
            for (int k = 0; k < 4; ++k) { dma_pl(P.wg + k * 8 * 1792, pg, F_JUNK + w * 1024u); dma_pl(P.wu + k * 8 * 1792, pg, F_JUNK + w * 1024u); }
        }
#pragma unroll
        for (int k = 0; k < 16; ++k) PIN(bo[k]);
        STAMP(6);
        barrier();
#pragma unroll
        for (int s = 0; s < 4; ++s) bu[s] = tr2(x0 + s * 8192, x1 + s * 8192);
#pragma unroll
        for (int k = 0; k < 4; ++k) PIN(bu[k]);
    }
    u32 tk1, tk2;
    float ga0 = 0.f, ga1 = 0.f, gb0 = 0.f, gb1 = 0.f, xv0 = 0.f, xv1 = 0.f;
    STAMP(7);
    {
        v4u v[4], ck = {0u, 0u, 0u, 0u};
        u32 onel = 0x3f80u, oneh = 0x3f800000u;
        PIN(onel); PIN(oneh);
#pragma unroll
        for (int k = 0; k < 4; ++k) UNDEF(v[k]);
        v4u gm[4];
#pragma unroll
        for (int k = 0; k < 4; ++k) UNDEF(gm[k]);
        if (is_red && wave == 0) asm volatile("global_load_dwordx4 %0, %4, %5 sc1\n\tglobal_load_dwordx4 %1, %4, %5 offset:1024 sc1\n\tglobal_load_dwordx4 %2, %4, %5 offset:2048 sc1\n\tglobal_load_dwordx4 %3, %4, %5 offset:3072 sc1"
            : "+v"(gm[0]), "+v"(gm[1]), "+v"(gm[2]), "+v"(gm[3]) : "v"(om), "s"(ws) : "memory");
        look_times(ws, lane, tlo, budget, tk1, tk2);
#pragma unroll
        for (int k = 0; k < 4; ++k) PIN(gm[k]);
        if (is_red && wave == 0) {
            if (ballot((neq3(gm[0].z, gm[1].z, tlo) | neq3(gm[2].z, gm[3].z, tlo)) | (neq3(gm[0].w, gm[1].w, thi) | neq3(gm[2].w, gm[3].w, thi))) != 0ull) gather<4, 1024, 1>(ws, om, tlo, thi, gm, budget);
            xv0 = bflo(xr); xv1 = bfhi(xr);
            {
                const float s0 = red16(red32(asf(gm[1].x), asf(gm[2].x)), red32(asf(gm[1].y), asf(gm[2].y)));
                u32 e, od, sbp, cbp;
                both16(asu(s0), e, od);
                both32(pack2(asf(e), asf(od)), sbp, cbp);
                if (lane < 8) st_sc(ws, WS_SC + (rid * 8 + (u32)lane) * 16u, (v4u){cbp, sbp, tlo, thi});
            }
            ga0 = asf(gm[0].x); ga1 = asf(gm[0].y); gb0 = asf(gm[3].x); gb1 = asf(gm[3].y);
            ga0 = red32(ga0, ga0); ga0 = red16(ga0, ga0); ga1 = red32(ga1, ga1); ga1 = red16(ga1, ga1);
            gb0 = red32(gb0, gb0); gb0 = red16(gb0, gb0); gb1 = red32(gb1, gb1); gb1 = red16(gb1, gb1);
        }
        PIN(ga0); PIN(ga1); PIN(gb0); PIN(gb1); PIN(xv0); PIN(xv1);
        until(tk2);
        STAMP(8);
#pragma clang loop unroll(disable)
        for (;;) {
            asm volatile("global_load_dwordx4 %0, %5, %7 sc1\n\tglobal_load_dwordx4 %1, %5, %7 offset:1024 sc1\n\tglobal_load_dwordx4 %2, %5, %7 offset:2048 sc1\n\tglobal_load_dwordx4 %3, %5, %7 offset:3072 sc1\n\t"
                "s_mov_b64 exec, %8\n\ts_waitcnt vmcnt(3)\n\tglobal_load_dwordx4 %4, %6, %7 sc1\n\ts_mov_b64 exec, -1"
                : "+v"(v[0]), "+v"(v[1]), "+v"(v[2]), "+v"(v[3]), "+v"(ck) : "v"(o), "v"(oc), "s"(ws), "s"(0x000f000f000f000full) : "memory");
            float a[8];
#pragma unroll
            for (int q = 0; q < 4; ++q) { a[2 * q] = bflo(v[0][q]); a[2 * q + 1] = bfhi(v[0][q]); }
            u32 x = (v[0].x + v[0].y) + (v[0].z + v[0].w);
#pragma unroll
            for (int k = 1; k < 4; ++k) {
#pragma unroll
                for (int q = 0; q < 8; ++q) PIN(a[q]);
                PIN(x);
                asm volatile("s_waitcnt vmcnt(%1)" : "+v"(v[k]) : "i"(4 - k) : "memory");
                x = (x + v[k].x + v[k].y) + (v[k].z + v[k].w);
#pragma unroll
                for (int q = 0; q < 4; ++q) { a[2 * q] = dot2(v[k][q], onel, a[2 * q]); a[2 * q + 1] = dot2(v[k][q], oneh, a[2 * q + 1]); }
            }
            float t[4];
            x += dppu<0x128>(x);
#pragma unroll
            for (int k = 0; k < 4; ++k) t[k] = red32(a[k], a[k + 4]);
            x += dppu<0x124>(x);
            float u0 = red16(t[0], t[2]); PIN(u0);
            *lp<u32>(wa) = pack2(u0, red16(t[1], t[3]));
            PIN(x);
            asm volatile("s_waitcnt vmcnt(0)" : "+v"(ck) :: "memory");
            *lp<u32>(wl) = ck.y;
            x -= ck.x;
            x += dppu<0xB1>(x); x += dppu<0x4E>(x);
            const u64 bad = ballot((x | (ck.w ^ tlo)) != 0u) & 0x000f000f000f000full;
            if (__builtin_expect((bad | ballot(ck.z != 0u)) == 0ull, 1)) break;
            if (bad == 0ull || --budget <= 0) {
                const float m = i15 < 4 ? asf(ck.z) : -3.0e38f;
                float M = __builtin_fmaxf(m, dpp<0xB1>(m)); M = __builtin_fmaxf(M, dpp<0x4E>(M));
                M = asf(rfl(asu(max32(max16(M)))));
                const u32 f = pack2(ex2(m - M), 0.f);
                const u32 f0 = dppu<0x150>(f), f1 = dppu<0x151>(f), f2 = dppu<0x152>(f), f3 = dppu<0x153>(f);
#pragma unroll
                for (int q = 0; q < 4; ++q) {
                    a[2 * q] = dot2(v[3][q], f3, dot2(v[2][q], f2, dot2(v[1][q], f1, dot2(v[0][q], f0, 0.f))));
                    a[2 * q + 1] = dot2(v[3][q], f3 << 16, dot2(v[2][q], f2 << 16, dot2(v[1][q], f1 << 16, dot2(v[0][q], f0 << 16, 0.f))));
                }
#pragma unroll
                for (int k = 0; k < 4; ++k) t[k] = red32(a[k], a[k + 4]);
                *lp<u32>(wa) = pack2(red16(t[0], t[2]), red16(t[1], t[3]));
                *lp<float>(wl) = asf(ck.y) * bflo(f);
                *lp<float>(F_ST + w * 4) = M;
                *lp<u32>(F_ST + 32) = 1u;
                break;
            }
            __builtin_amdgcn_s_sleep(POLL_SLEEP);
        }
        STAMP(9);
    }
    barrier();
    u64 tc1;
    asm volatile("s_memrealtime %0" : "=s"(tc1));
    STAMP(10);
    float ls;
    {
        bf16x8 af[4];
#pragma unroll
        for (int s = 0; s < 4; ++s) af[s] = *lp<bf16x8>(aa + s * 64);
        const u32 rare = *lp<u32>(F_ST + 32);
        const f32x2 l2 = *lp<f32x2>(rl);
        SCHED();
        f32x4 d = {0.f, 0.f, 0.f, 0.f};
#pragma unroll
        for (int s = 0; s < 4; ++s) d = __builtin_amdgcn_mfma_f32_16x16x32_bf16(af[s], bu[s], d, 0, 0, 0);
        ls = l2.x + l2.y;
        if (__builtin_expect(rfl(rare) != 0u, 0)) {
            const f32x4 m0 = *lp<f32x4>(F_ST), m1 = *lp<f32x4>(F_ST + 16);
            const float M = max8(m0, m1);
#pragma unroll
            for (int k = 0; k < 4; ++k) d[k] *= ex2(((g & 1) ? m1[k] : m0[k]) - M);
            ls *= ex2(*lp<float>(F_ST + (u32)(lane >> 3) * 4) - M);
        }
        float c0 = d.x + d.y, c1 = d.z + d.w;
        PIN(c0); PIN(c1);
        float c = c0 + c1, c2 = c0 + c1;
        PIN(c); PIN(c2);
        c = red16(c, c2);
        *lp<u16>(cw) = (u16)pack2(c, c);
    }
    barrier();
    STAMP(13);
    {
        bf16x8 af[4];
#pragma unroll
        for (int s = 0; s < 4; ++s) af[s] = *lp<bf16x8>(cr + s * 64);
        ls += dpp<0x128>(ls); ls += dpp<0x124>(ls); ls += dpp<0x122>(ls); ls += dpp<0x121>(ls);
        ls = red16(ls, ls); ls = red32(ls, ls);
        ls = __builtin_amdgcn_rcpf(ls);
        v4u r = {0u, 0u, tlo, thi};
        PIN(ls); PIN(r);
        SCHED();
        f32x4 d[4];
#pragma unroll
        for (int t = 0; t < 4; ++t) d[t] = (f32x4){0.f, 0.f, 0.f, 0.f};
#pragma unroll
        for (int s = 0; s < 4; ++s) {
#pragma unroll
            for (int t = 0; t < 4; ++t) d[t] = __builtin_amdgcn_mfma_f32_16x16x32_bf16(af[s], bo[4 * t + s], d[t], 0, 0, 0);
            SCHED();
        }
        r.x = asu(((g & 2) ? ((g & 1) ? d[3].x : d[2].x) : ((g & 1) ? d[1].x : d[0].x)) * ls);
        r.y = dppu<0x128>(r.x);
        asm volatile("s_mov_b64 exec, %3\n\tglobal_store_dwordx4 %0, %1, %2 sc1\n\ts_mov_b64 exec, -1\n\ts_nop 0" :: "v"(op), "v"(r), "s"(ws), "s"(0x00ff00ff00ff00ffull) : "memory");
    }
    STAMP(11);
    asm volatile("" : "+s"(tc1));
    if (wave == 2 && lane < 8) st_sc(ws, he, (v4u){(u32)tc1, 0u, tlo, thi});
    if (b == 0 && tid == 64) st_sc2(ws, WS_EPOCH, (v2u){tlo, thi});
    if (is_red) {
        const f32x2 s = reduce16<8, true>(ws, piece(WS_OUTP, 16384, rid), tlo, thi, wave, lane, F_RD, tk2 + max((u32)D_T, (u32)tc1 - tk2 + D_DELAY), budget);
        const float x10 = xv0 + ga0 * s.x, x11 = xv1 + ga1 * s.y;
        if (wave == 0) st_sc(ws, xo, (v4u){asu(x10), asu(x11), tlo, thi});
        STAMP(12);
        u32 oa = (rid * 8 + (u32)lane) * 4u;
        PIN(oa);
        const f32x2 s2 = reduce16<6, false>(ws, piece(WS_YP, 16384, rid), tlo, thi, wave, lane, F_RV, now32() + F_DELAY, budget);
        if (tid < 8) asm volatile("global_store_dword %0, %1, %2" :: "v"(oa), "v"(pack2(x10 + gb0 * s2.x, x11 + gb1 * s2.y)), "s"(P.out) : "memory");
    }
    STAMP(15);
    STAMP_OUT();
}

extern "C" void kernel_launch(void* const* d_in, const int* in_sizes, int n_in, void* d_out, int out_size, void* d_ws, size_t ws_size, hipStream_t stream) {
    Params P;
    P.x = (const u16*)d_in[0]; P.ckv = (const u16*)d_in[1]; P.wq = (const u16*)d_in[2]; P.wuk = (const u16*)d_in[3];
    P.wuv = (const u16*)d_in[4]; P.wo = (const u16*)d_in[5]; P.wg = (const u16*)d_in[7]; P.wu = (const u16*)d_in[8];
    P.wd = (const u16*)d_in[9]; P.c = (const u16*)d_in[10]; P.wmod = (const u16*)d_in[11];
    P.out = (u16*)d_out; P.ws = (char*)d_ws + WS_BASE(ws_size);
    (void)hipFuncSetAttribute((const void*)fused_kernel, hipFuncAttributeMaxDynamicSharedMemorySize, LDS_BYTES);
    fused_kernel<<<256, 512, LDS_BYTES, stream>>>(P);
}
```

```cpp
#include <hip/hip_runtime.h>

typedef unsigned u32;
typedef unsigned long long u64;
typedef unsigned short u16;
typedef u32 v4u __attribute__((ext_vector_type(4)));
typedef u32 v2u __attribute__((ext_vector_type(2)));
typedef u32 v8u __attribute__((ext_vector_type(8)));
typedef u32 v16u __attribute__((ext_vector_type(16)));
typedef float f32x2 __attribute__((ext_vector_type(2)));
typedef float f32x4 __attribute__((ext_vector_type(4)));
typedef float f32x16 __attribute__((ext_vector_type(16)));
typedef short bf16x8 __attribute__((ext_vector_type(8)));
typedef short s16x4 __attribute__((ext_vector_type(4)));
typedef __bf16 bf16x2_t __attribute__((ext_vector_type(2)));
#define LAS __attribute__((address_space(3)))
#define DI __device__ __forceinline__

#define CFG(name, val) constexpr int name = val
CFG(BUDGET, 1 << 20);
CFG(POLL_SLEEP, 4);
CFG(HINT_SLEEP, 4);
#define PACING(X) \
    X(T_OPEN0, 50)        \
    X(T_OPEN1, 70)        \
    X(A_FIRST, 175)        \
    X(A_OWN, 60)        \
    X(ST_LOOK, 110)        \
    X(SKEW_MAX, 45) \
    X(A2_T, 165)        \
    X(A2_DELAY, 45)        \
    X(Q_FIRST, 350)        \
    X(Q_STEP, 40)        \
    X(Q_DELAY, 122)        \
    X(Q_PRE, 60)        \
    X(T_OWN, 40)        \
    X(O_STEP, 35) \
    X(T_MID, 220)        \
    X(U_GAP, 80)        \
    X(U_STEP, 30) \
    X(FC_A, 203)        \
    X(FC_B, 342)        \
    X(FC_C, 62)        \
    X(D_T, 203)        \
    X(D_DELAY, 78)        \
    X(E_T, 283)        \
    X(E_DELAY, 160)        \
    X(F_DELAY, 0)        \
    X(G_T, 70)        \
    X(G_2, 80)
#define X_(n, v) constexpr int n = v;
PACING(X_)
#define KD 10
DI u32 ktile(u32 s, u32 t) { return s * 262144u + t * 16384u; }
#define HW 5
#define QD 2
#define LAZY 64.f
#define CODE_STR 7168
#define WS_BASE(sz) (((sz) - (32u << 20)) & ~(size_t)((2u << 20) - 1))

constexpr u32 WS_EPOCH = 0;
DI u32 piece(u32 base, u32 size, u32 i) { return base + i * size; }
#define CH 262144u
constexpr u32 WS_ST = 1024;
constexpr u32 WS_QH = 2048, NQH = 6;
constexpr u32 WS_QCHK = 8192;
constexpr u32 WS_HP = CH;
constexpr u32 WS_QP = 2 * CH;
constexpr u32 WS_QABS = 3 * CH;
constexpr u32 WS_L = 25u << 18;
constexpr u32 WS_ML = WS_L;
constexpr u32 WS_X1 = WS_L + 65536;
constexpr u32 WS_SC = WS_L + 98304;
constexpr u32 HSTR = 1152, WS_HC = WS_L + 106496;
constexpr u32 WS_FC = WS_SC + 4096;
constexpr u32 WS_HE = WS_HC + 512;
constexpr u32 WS_OUTP = WS_L + 262144;
constexpr u32 WS_YP = WS_L + 786432;
constexpr u32 WS_CHK = WS_L + 1310720;
constexpr u32 WS_PAY = 2u << 20;
constexpr u32 PHD = 4 * 128 * 256 + 4096;
constexpr u32 WS_TL = 16u << 20;

constexpr u32 L_FL = 114688, L_P = L_FL + 1024, L_IMG = 131072;
constexpr u32 slot_no(u32 t) { return (u32)((0x9543621054362105ull >> (4 * t)) & 15u); }
constexpr u32 slot(int t) { return slot_no((u32)t) * 16384u; }
constexpr u32 vwin(int t) { return (slot_no((u32)t) < 3 ? 0u : slot_no((u32)t) < 7 ? 3u : 6u) * 16384u; }
static_assert(slot(15) == L_IMG + 16384, "");
constexpr u32 C_LAND = 0;
constexpr u32 C_KRD = 256, C_Z = 264, C_E = 268, C_P = 272, C_Q = 284, C_L = 288, C_BAD = 292, C_H = 296, C_TZ = 304;
constexpr u32 L_M = L_FL + 384, L_L = L_FL + 512;
constexpr u32 L_W = 0;
constexpr u32 L_W1 = 65536;
constexpr u32 L_WD = 98304;
constexpr u32 L_H2 = 32768;
constexpr u32 L_GU = 36864;
constexpr u32 F_QP = 0;
constexpr u32 F_WO = 0;
constexpr u32 F_UV = 131072;
constexpr u32 F_A = 0;
constexpr u32 F_ST = 4096;
constexpr u32 F_GO = F_ST + 40;
constexpr u32 F_CX = 4352;
constexpr u32 F_LS = 5120;
constexpr u32 F_ML = 6144;
constexpr u32 F_RD = 8192, F_RV = 9216;
constexpr u32 F_JUNK = 16384;
constexpr u32 LDS_BYTES = 163840;

struct Params {
    const u16 *wmod, *c, *x;
    char* ws;
    const u16 *wq, *ckv, *wuk, *wo, *wuv, *wg, *wu, *wd;
    u16* out;
};

DI float asf(u32 v) { return __builtin_bit_cast(float, v); }
DI u32 asu(float v) { return __builtin_bit_cast(u32, v); }
DI float bflo(u32 v) { return asf(v << 16); }
DI float bfhi(u32 v) { return asf(v & 0xffff0000u); }
DI u32 pack2(float a, float b) { f32x2 v = {a, b}; return __builtin_bit_cast(u32, __builtin_convertvector(v, bf16x2_t)); }
DI float dot2(u32 w, u32 x, float acc) { return __builtin_amdgcn_fdot2_f32_bf16(__builtin_bit_cast(bf16x2_t, w), __builtin_bit_cast(bf16x2_t, x), acc, false); }
DI void pair_rows(v4u& x, v4u& y) {
#pragma unroll
    for (int k = 0; k < 4; ++k) { const u32 a = x[k], b = y[k]; x[k] = __builtin_amdgcn_perm(b, a, 0x05040100u); y[k] = __builtin_amdgcn_perm(b, a, 0x07060302u); }
}
template <int CTRL> DI u32 dppu(u32 v) { return (u32)__builtin_amdgcn_update_dpp(0, (int)v, CTRL, 0xf, 0xf, true); }
template <int CTRL> DI float dpp(float v) { return asf(dppu<CTRL>(asu(v))); }
DI float red32(float a, float b) { auto r = __builtin_amdgcn_permlane32_swap(asu(a), asu(b), false, false); return asf((u32)r[0]) + asf((u32)r[1]); }
DI float red16(float a, float b) { auto r = __builtin_amdgcn_permlane16_swap(asu(a), asu(b), false, false); return asf((u32)r[0]) + asf((u32)r[1]); }
DI float max3(float a, float b, float c) { return __builtin_fmaxf(__builtin_fmaxf(a, b), c); }
DI float max4(f32x4 a) { return max3(max3(a.x, a.y, a.z), a.w, a.x); }
DI float max32(float a) { auto r = __builtin_amdgcn_permlane32_swap(asu(a), asu(a), false, false); return __builtin_fmaxf(asf((u32)r[0]), asf((u32)r[1])); }
DI float max16(float a) { auto r = __builtin_amdgcn_permlane16_swap(asu(a), asu(a), false, false); return __builtin_fmaxf(asf((u32)r[0]), asf((u32)r[1])); }
DI float max8(const f32x4& a, const f32x4& b) { return max3(max3(max3(a.x, a.y, a.z), a.w, b.x), max3(b.y, b.z, b.w), a.x); }
DI float sum4(f32x4 a) { return (a.x + a.y) + (a.z + a.w); }
DI u32 perm_lo(u32 a, u32 b) { return __builtin_amdgcn_perm(b, a, 0x05040100u); }
DI u32 perm_hi(u32 a, u32 b) { return __builtin_amdgcn_perm(b, a, 0x07060302u); }
DI u32 rfl(u32 v) { return (u32)__builtin_amdgcn_readfirstlane((int)v); }
DI void both16(u32 a, u32& e, u32& o) { auto r = __builtin_amdgcn_permlane16_swap(a, a, false, false); e = (u32)r[0]; o = (u32)r[1]; }
DI void both32(u32 a, u32& l, u32& h) { auto r = __builtin_amdgcn_permlane32_swap(a, a, false, false); l = (u32)r[0]; h = (u32)r[1]; }
DI float rowsum(float v) { v += dpp<0x128>(v); v += dpp<0x124>(v); v += dpp<0x122>(v); v += dpp<0x121>(v); return v; }
DI float ex2(float v) { return __builtin_amdgcn_exp2f(v); }
DI u32 neq3(u32 a, u32 b, u32 t) { return __builtin_amdgcn_bitop3_b32(a, b, t, 0x7e); }
DI u32 mix4(v4u v) { return (v.w << 9) + ((v.z << 6) + ((v.y << 3) + v.x)); }
DI u64 ballot(bool c) { return __builtin_amdgcn_ballot_w64(c); }

DI v4u ld_nt(const void* base, u32 off) { v4u r; asm volatile("global_load_dwordx4 %0, %1, %2 nt" : "=v"(r) : "v"(off), "s"(base) : "memory"); return r; }
DI u32 ld_u32(const void* base, u32 off) { u32 r; asm volatile("global_load_dword %0, %1, %2" : "=v"(r) : "v"(off), "s"(base) : "memory"); return r; }
DI u32 ld_u32_nt(const void* base, u32 off) { u32 r; asm volatile("global_load_dword %0, %1, %2 nt" : "=v"(r) : "v"(off), "s"(base) : "memory"); return r; }
DI u32 ld_u16(const void* base, u32 off) { u32 r; asm volatile("global_load_ushort %0, %1, %2" : "=v"(r) : "v"(off), "s"(base) : "memory"); return r; }
DI void st_sc(void* base, u32 off, v4u v) { asm volatile("global_store_dwordx4 %0, %1, %2 sc1\n\ts_nop 1" :: "v"(off), "v"(v), "s"(base) : "memory"); }
DI void st_sc_keep(void* base, u32 off, v4u v) { asm volatile("global_store_dwordx4 %0, %1, %2 sc1" :: "v"(off), "v"(v), "s"(base) : "memory"); }
DI void st_sc2(void* base, u32 off, v2u v) { asm volatile("global_store_dwordx2 %0, %1, %2 sc1\n\ts_nop 1" :: "v"(off), "v"(v), "s"(base) : "memory"); }
DI void dma_nt(const void* base, u32 off, u32 lds_dst) { asm volatile("s_mov_b32 m0, %1\n\ts_nop 0\n\tglobal_load_lds_dwordx4 %0, %2 nt" :: "v"(off), "s"(lds_dst), "s"(base) : "memory"); }
DI void dma_pl(const void* base, u32 off, u32 lds_dst) { asm volatile("s_mov_b32 m0, %1\n\ts_nop 0\n\tglobal_load_lds_dwordx4 %0, %2" :: "v"(off), "s"(lds_dst), "s"(base) : "memory"); }
DI void dma_sc(const void* base, u32 off, u32 lds_dst) { asm volatile("s_mov_b32 m0, %1\n\ts_nop 0\n\tglobal_load_lds_dwordx4 %0, %2 sc1" :: "v"(off), "s"(lds_dst), "s"(base) : "memory"); }
DI void ld_nt_into(v4u& r, const void* base, u32 off) { asm volatile("global_load_dwordx4 %0, %1, %2 nt" : "+v"(r) : "v"(off), "s"(base) : "memory"); }
DI void ld_sc_into(v4u& r, const void* base, u32 off) { asm volatile("global_load_dwordx4 %0, %1, %2 sc1" : "+v"(r) : "v"(off), "s"(base) : "memory"); }
#define VMWAIT(n) asm volatile("s_waitcnt vmcnt(%0)" :: "i"(n) : "memory")
#define LGKM0() asm volatile("s_waitcnt lgkmcnt(0)" ::: "memory")
#define PIN(x) asm volatile("" : "+v"(x))
#define UNDEF(x) asm volatile("" : "=v"(x))
#define SCHED() __builtin_amdgcn_sched_barrier(0)
DI void barrier() { LGKM0(); __builtin_amdgcn_s_barrier(); asm volatile("" ::: "memory"); }

template <typename T> DI LAS T* lp(u32 off) { return (LAS T*)(size_t)off; }
DI bf16x8 tr2(u32 a, u32 b) {
    const s16x4 lo = __builtin_amdgcn_ds_read_tr16_b64_v4i16(lp<s16x4>(a)), hi = __builtin_amdgcn_ds_read_tr16_b64_v4i16(lp<s16x4>(b));
    return __builtin_shufflevector(lo, hi, 0, 1, 2, 3, 4, 5, 6, 7);
}

#define STAMP(i) do {} while (0)
#define CYC0() do {} while (0)
#define STAMPC(i) do {} while (0)
#define STAMP_OUT() do {} while (0)

#define CDECL() do {} while (0)
#define CS(i) do {} while (0)
#define COUT(n) do {} while (0)
template <u32 S> DI void pass(const char* ws, u32 off, v4u (&v)[1]) {
    asm volatile("global_load_dwordx4 %0, %1, %2 sc1\n\ts_waitcnt vmcnt(0)" : "=v"(v[0]) : "v"(off), "s"(ws) : "memory");
}
template <u32 S> DI void pass(const char* ws, u32 off, v4u (&v)[2]) {
    asm volatile("global_load_dwordx4 %0, %2, %3 sc1\n\tglobal_load_dwordx4 %1, %2, %4 sc1\n\ts_waitcnt vmcnt(0)" : "=&v"(v[0]), "=&v"(v[1]) : "v"(off), "s"(ws), "s"(ws + S) : "memory");
}
template <u32 S> DI void pass(const char* ws, u32 off, v4u (&v)[4]) {
    asm volatile("global_load_dwordx4 %0, %4, %5 sc1\n\tglobal_load_dwordx4 %1, %4, %5 offset:%6 sc1\n\tglobal_load_dwordx4 %2, %4, %5 offset:%7 sc1\n\tglobal_load_dwordx4 %3, %4, %5 offset:%8 sc1\n\ts_waitcnt vmcnt(0)"
        : "=&v"(v[0]), "=&v"(v[1]), "=&v"(v[2]), "=&v"(v[3]) : "v"(off), "s"(ws), "i"(S), "i"(2 * S), "i"(3 * S) : "memory");
}
template <int N, u32 STRIDE, int SLP = POLL_SLEEP> DI void gather(const char* ws, u32 off, u32 tlo, u32 thi, v4u (&v)[N], int& budget) {
#pragma clang loop unroll(disable)
    do {
        pass<STRIDE>(ws, off, v);
        u32 bad = 0;
#pragma unroll
        for (int k = 0; k < N; ++k) bad |= (v[k].z ^ tlo) | (v[k].w ^ thi);
        if (ballot(bad != 0u) == 0ull) break;
        __builtin_amdgcn_s_sleep(SLP);
    } while (--budget > 0);
}
#define PSLP "0"
template <bool G8> DI void poll2(v4u& g0, v4u& g1, u32 o0, const char* b0, u32 o1, const char* b1, u32 tlo, u32 thi) {
    u32 n, t, u;
#define POLL2(A, B, C, D) asm volatile("s_mov_b32 %2, 0x200000\n.Lp%=:\n\ts_sleep " PSLP "\n\tglobal_load_dwordx4 v[232:235], %5, %6 sc1\n\tglobal_load_dwordx4 v[236:239], %7, %8 sc1\n\ts_sub_u32 %2, %2, 1\n\ts_waitcnt vmcnt(0)\n\t" \
        "v_bitop3_b32 %3, " A ", " B ", %9 bitop3:0x7e\n\tv_bitop3_b32 %4, " C ", " D ", %10 bitop3:0x7e\n\tv_or_b32 %3, %3, %4\n\tv_cmp_ne_u32 vcc, 0, %3\n\ts_cbranch_scc1 .Lq%=\n\ts_cbranch_vccnz .Lp%=\n.Lq%=:" \
        : "=&{v[232:235]}"(g0), "=&{v[236:239]}"(g1), "=&s"(n), "=&v"(t), "=&v"(u) : "v"(o0), "s"(b0), "v"(o1), "s"(b1), "s"(tlo), "s"(G8 ? tlo : thi) : "memory", "vcc", "scc")
    if (G8) POLL2("v233", "v235", "v237", "v239"); else POLL2("v234", "v238", "v235", "v239");
}
DI void flag_set(u32 fa, u32 off) {
    asm volatile("s_mov_b64 exec, 1\n\tds_write_b8_d16_hi %0, %0 offset:%1\n\ts_mov_b64 exec, -1" :: "v"(fa), "i"(off) : "memory");
}
DI u32 flag_get(u32 off) { return *(volatile LAS u32*)lp<u32>(L_FL + off); }
DI void wait4(u32 fb, u32 off) {
    u32 v, n;
    asm volatile("s_mov_b32 %1, 0x400000\n.Lw%=:\n\tds_read_b32 %0, %2 offset:%3\n\ts_sub_u32 %1, %1, 1\n\ts_waitcnt lgkmcnt(0)\n\tv_cmp_ne_u32 vcc, 0x1010101, %0\n\ts_cbranch_scc1 .Le%=\n\ts_cbranch_vccnz .Lw%=\n.Le%=:"
        : "=&v"(v), "=&s"(n) : "v"(fb), "i"(off) : "memory", "vcc", "scc");
}
DI void wait1(u32 fb, u32 off) {
    u32 v, n;
    asm volatile("s_mov_b32 %1, 0x400000\n.Lw%=:\n\tds_read_u8 %0, %2 offset:%3\n\ts_sub_u32 %1, %1, 1\n\ts_waitcnt lgkmcnt(0)\n\tv_cmp_ne_u32 vcc, 1, %0\n\ts_cbranch_scc1 .Le%=\n\ts_cbranch_vccnz .Lw%=\n.Le%=:"
        : "=&v"(v), "=&s"(n) : "v"(fb), "i"(off) : "memory", "vcc", "scc");
}
#define SPIN_HEAD "s_mov_b32 %[n], 0x400000\n.Lw%=:\n\t"
#define SPIN_TAIL(cmp) "s_sub_u32 %[n], %[n], 1\n\ts_waitcnt lgkmcnt(0)\n\t" cmp ", %[v]\n\ts_cbranch_scc1 .Le%=\n\ts_cbranch_vccnz .Lw%=\n.Le%=:"
DI void land_wait(u32 fl, u32 off) {
    u32 v, n;
    asm volatile(SPIN_HEAD "s_sleep 3\n\tds_read_b32 %[v], %[b] offset:%[o]\n\t" SPIN_TAIL("v_cmp_eq_u32 vcc, 0") : [v] "=&v"(v), [n] "=&s"(n) : [b] "v"(fl), [o] "i"(off) : "memory", "vcc", "scc");
}
DI bf16x8 wait1p(u32 fb, u32 off, u32 pa, u32 poff) {
    u32 v, n;
    bf16x8 p;
    asm volatile(SPIN_HEAD "ds_read_u8 %[v], %[b] offset:%[o]\n\tds_read_b128 %[p], %[a] offset:%[q]\n\t" SPIN_TAIL("v_cmp_ne_u32 vcc, 1")
        : [v] "=&v"(v), [p] "=&v"(p), [n] "=&s"(n) : [b] "v"(fb), [o] "i"(off), [a] "v"(pa), [q] "i"(poff) : "memory", "vcc", "scc");
    return p;
}
DI void wait4p(u32 fb, u32 off, u32 pa, u32 poff, bf16x8 (&p)[4]) {
    u32 v, n;
    asm volatile(SPIN_HEAD "ds_read_b32 %[v], %[b] offset:%[o]\n\tds_read_b128 %[p0], %[a] offset:%[q0]\n\tds_read_b128 %[p1], %[a] offset:%[q1]\n\tds_read_b128 %[p2], %[a] offset:%[q2]\n\tds_read_b128 %[p3], %[a] offset:%[q3]\n\t"
        SPIN_TAIL("v_cmp_ne_u32 vcc, 0x1010101")
        : [v] "=&v"(v), [p0] "=&v"(p[0]), [p1] "=&v"(p[1]), [p2] "=&v"(p[2]), [p3] "=&v"(p[3]), [n] "=&s"(n)
        : [b] "v"(fb), [o] "i"(off), [a] "v"(pa), [q0] "i"(poff), [q1] "i"(poff + 1024), [q2] "i"(poff + 2048), [q3] "i"(poff + 3072) : "memory", "vcc", "scc");
}
template <u32 KO> DI void land_wait_k(u32 fl, u32 off, u32 a0, u32 a1, u32 a2, u32 a3, bf16x8& f0, bf16x8& f1, bf16x8& f2, bf16x8& f3) {
    u32 v, n;
    asm volatile(SPIN_HEAD "ds_read_b32 %[v], %[b] offset:%[o]\n\tds_read_b128 %[p0], %[a0] offset:%[k]\n\tds_read_b128 %[p1], %[a1] offset:%[k]\n\tds_read_b128 %[p2], %[a2] offset:%[k]\n\tds_read_b128 %[p3], %[a3] offset:%[k]\n\t"
        SPIN_TAIL("v_cmp_eq_u32 vcc, 0")
        : [v] "=&v"(v), [p0] "=&v"(f0), [p1] "=&v"(f1), [p2] "=&v"(f2), [p3] "=&v"(f3), [n] "=&s"(n)
        : [b] "v"(fl), [o] "i"(off), [a0] "v"(a0), [a1] "v"(a1), [a2] "v"(a2), [a3] "v"(a3), [k] "i"(KO) : "memory", "vcc", "scc");
}
DI void dma_a(const char* src, const u32 (&od)[4], u32 dst) {
    asm volatile("s_mov_b32 m0, %3\n\ts_nop 0\n\tglobal_load_lds_dwordx4 %0, %2 nt\n\tglobal_load_lds_dwordx4 %1, %2 offset:1024 nt" :: "v"(od[0]), "v"(od[1]), "s"(src), "s"(dst) : "memory");
}
DI void dma_b(const char* src, const u32 (&od)[4], u32 dst, u64 pc, u32 flag) {
    asm volatile("s_mov_b32 m0, %4\n\ts_nop 0\n\tglobal_load_lds_dwordx4 %1, %3 offset:2048 nt\n\tglobal_load_lds_dwordx4 %2, %3 offset:3072 nt\n\t"
        "s_mov_b32 m0, %6\n\ts_mov_b64 exec, 1\n\tglobal_load_lds_dword %0, %5\n\ts_mov_b64 exec, -1"
        :: "v"(od[0]), "v"(od[2]), "v"(od[3]), "s"(src), "s"(dst), "s"(pc), "s"(flag) : "memory");
}
static_assert(C_LAND == 0, "");
static_assert(((L_FL >> 16) & 0xff) == 1, "flag_set writes bits 23:16 of the address");
template <int K, bool PRE = false> DI u32 hint_wait(const char* ws, u32 oh, u32 tlo, int& budget, v4u h0 = v4u()) {
    v4u h[1] = {h0};
    if (!PRE || __builtin_popcount((u32)ballot(h[0].z == tlo) & 0xffu) < K) {
#pragma clang loop unroll(disable)
        do {
            pass<0>(ws, oh, h);
            if (__builtin_popcount((u32)ballot(h[0].z == tlo) & 0xffu) >= K) break;
            __builtin_amdgcn_s_sleep(HINT_SLEEP);
        } while (--budget > 0);
    }
    const u32 now = (u32)__builtin_amdgcn_s_memrealtime();
    u32 age = h[0].z == tlo ? now - h[0].x : 0xffffu;
    age = age > 0xffffu ? 0u : age;
    age = min(age, dppu<0xB1>(age));
    age = min(age, dppu<0x4E>(age));
    age = min(age, dppu<0x124>(age));
    return now - rfl(age);
}
DI u32 now32() { return (u32)__builtin_amdgcn_s_memrealtime(); }
DI void until(u32 t) { while ((int)(now32() - t) < 0) __builtin_amdgcn_s_sleep(1); }
DI u32 time_base(const char* p, u32 t0) {
    v16u h;
    asm volatile("s_load_dwordx16 %0, %1, 0x0\n\ts_waitcnt lgkmcnt(0)" : "=s"(h) : "s"(p) : "memory");
    u32 m = 0, bad = 0;
#pragma unroll
    for (int k = 0; k < 8; ++k) { m = max(m, h[2 * k] - t0 + 150u); bad |= h[2 * k] ^ ~h[2 * k + 1]; }
    return (bad != 0u || m >= 300u) ? t0 + SKEW_MAX : t0 - 150u + m;
}
struct QTimer { u32 t, n; };
DI bool q_due(QTimer& q, const char* ws, u32 tlo) {
    const u32 now = now32();
    if ((int)(now - q.t) < 0) return false;
    if (q.n >= NQH) return true;
    v16u h0, h1;
    asm volatile("s_load_dwordx16 %0, %2, 0x0\n\ts_load_dwordx16 %1, %2, 0x40\n\ts_waitcnt lgkmcnt(0)" : "=&s"(h0), "=&s"(h1) : "s"(ws + WS_QH + q.n * 128u) : "memory");
    u32 m = 0xffffffffu, bad = 0;
#pragma unroll
    for (int k = 0; k < 8; ++k) { m = min(m, min(now + 1000u - h0[2 * k], now + 1000u - h1[2 * k])); bad |= (h0[2 * k] ^ h0[2 * k + 1] ^ tlo) | (h1[2 * k] ^ h1[2 * k + 1] ^ tlo); }
    if (bad == 0u) { q.n = NQH; q.t = now + 1000u - max(m, 900u) + Q_DELAY - Q_PRE; } else { ++q.n; q.t = now + Q_STEP; }
    return false;
}
DI void look_times(const char* ws, int lane, u32 tlo, int& budget, u32& t1, u32& t2) {
    v4u fc[1];
#pragma clang loop unroll(disable)
    do {
        pass<0>(ws, WS_FC + (u32)lane * 16u, fc);
        if (ballot(neq3(fc[0].y, fc[0].w, tlo)) == 0ull) break;
        until(now32() + 30);
    } while (--budget > 0);
    const u32 now = now32(), x = min(fc[0].x + 1000u - now, 2000u), z = min(fc[0].z + 1000u - now, 2000u);
    u32 m = max(x, z), a = x + z, e, od;
    m = max(m, dppu<0x128>(m)); m = max(m, dppu<0x124>(m)); m = max(m, dppu<0x122>(m)); m = max(m, dppu<0x121>(m));
    a += dppu<0x128>(a); a += dppu<0x124>(a); a += dppu<0x122>(a); a += dppu<0x121>(a);
    both16(m, e, od); both32(max(e, od), e, od);
    m = rfl(max(e, od));
    both16(a, e, od); both32(e + od, e, od);
    a = (rfl(e + od) >> 7) + FC_C;
    t1 = m < 2000u ? now - 1000u + a : now;
    t2 = m < 2000u ? now - 1000u + max(m, a) : now;
}
DI u32 swz(u32 row) { return ((row & 3u) << 2) | ((row >> 2) & 3u); }

#define EPOCH() do { asm volatile("s_load_dwordx2 %0, %1, 0x0\n\ts_waitcnt lgkmcnt(0)" : "=s"(e64) : "s"(ws) : "memory"); ++e64; } while (0)
DI void wave_sum2(float s1, float s2, float& t1, float& t2) {
    float z = red32(s1, s2);
    z = red16(z, z); z = rowsum(z);
    u32 a, b;
    both32(asu(z), a, b);
    t1 = asf(a); t2 = asf(b);
}
DI void ln_stats(const v4u& xa, float& mean, float& rstd) {
    const float pil = asf(rfl(xa[0]) << 16);
    float s1 = 0.f, s2 = 0.f;
#pragma unroll
    for (int k = 0; k < 4; ++k) { const float a = bflo(xa[k]) - pil, c = bfhi(xa[k]) - pil; s1 += a; s2 = __builtin_fmaf(a, a, s2); s1 += c; s2 = __builtin_fmaf(c, c, s2); }
    float t1, t2;
    wave_sum2(s1, s2, t1, t2);
    const float dm = t1 * (1.f / 512.f);
    mean = pil + dm;
    rstd = __builtin_amdgcn_rsqf(__builtin_fmaxf(t2 * (1.f / 512.f) - dm * dm, 0.f) + 1e-5f);
}
DI float sigm(float v) { return __builtin_amdgcn_rcpf(1.f + ex2(v * -1.4426950408889634f)); }
DI float silu(float v) { return v * sigm(v); }

template <int NW2, bool S8> DI f32x2 reduce16(const char* ws, u32 base, u32 tlo, u32 thi, int wave, int lane, u32 lds_red, u32 tgo, int& budget) {
    u32 la = lds_red + (u32)(wave * 16 + (S8 ? (lane & 7) + (lane >> 5) * 8 : (lane & 7) * 2 + (lane >> 5))) * 4, ra = lds_red + (u32)lane * 8, go = base + (u32)(wave * 64 + lane) * 16u;
    const bool two = NW2 == 8 || wave < NW2;
    const char* w2 = ws + (two ? 8192 : 0);
    float f2 = two ? 1.f : 0.f;
    PIN(la); PIN(ra); PIN(go);
    asm volatile("" : "+s"(w2), "+v"(f2));
    until(tgo);
    v4u g0, g1;
    poll2<false>(g0, g1, go, ws, go, w2, tlo, thi);
    float z = red32(__builtin_fmaf(asf(g1.x), f2, asf(g0.x)), __builtin_fmaf(asf(g1.y), f2, asf(g0.y)));
    z = red16(z, z); z += dpp<0x128>(z);
    *lp<float>(la) = z;
    barrier();
    f32x2 s = {0.f, 0.f};
    if (wave == 0) {
        s = *lp<f32x2>(ra);
        float t = red32(s.x, s.y);
        t = red16(t, t); t += dpp<0x128>(t);
        u32 a, c;
        both32(asu(t), a, c);
        s = (f32x2){asf(a), asf(c)};
    }
    return s;
}

#define HE_K 8
DI u32 mlp_id(int b) { const int xc = b & 7, yy = (b >> 3) - 16; return (u32)(((yy >> 2) * 8 + xc) * 4 + (yy & 3)); }
DI void mlp_part(const Params& P, int b, u32 w, int lane, u32 tlo, u32 thi, int& budget, u32& tl) {
    char* const ws = P.ws;
    const u32 gu_id = mlp_id(b), i15 = lane & 15, g = lane >> 4;
    u32 wo = (128 * w + ((u32)lane >> 1)) * 3584u + gu_id * 32u + ((u32)lane & 1) * 16u, wdo = (gu_id * 16 + g) * 1024u + w * 256u + i15 * 16u;
    PIN(wo); PIN(wdo);
    const u32 q = gu_id & 3;
    u32 ha = L_H2 + w * 256 + (((u32)lane - 16 * q) & 63) * 4, hr = L_H2 + w * 256 + g * 16 + min(i15 & 3, 2u) * 1088, ga = L_GU + w * 512 + i15 * 16 + g * 4096, gr = L_GU + (u32)lane * 16;
    u32 sa = L_GU + 2048 + (u32)(lane >> 5) * 16 + w * 4, heo = WS_HE + (u32)(b - 128) * HSTR + (u32)(lane & 7) * 16u, px = piece(WS_X1, 4096, (u32)(b & 7)) + (u32)(lane & 7) * 16u;
    PIN(sa); PIN(heo); PIN(px);
    u32 so = piece(WS_YP, 16384, 8 * w + (u32)(lane >> 3)) + (gu_id * 8 + (lane & 7)) * 16, sco = WS_SC + (64 * w + (u32)lane) * 16u;
    u32 ta = L_W + w * 8192u + (8 * g + (i15 >> 2)) * 32 + (lane & 3) * 8, xo = piece(WS_X1, 4096, (u32)(b & 7)) + w * 1024u + (u32)lane * 16u;
    PIN(ha); PIN(hr); PIN(ga); PIN(gr); PIN(so); PIN(sco); PIN(ta); PIN(xo);
    barrier();
    STAMP(10);
    u32 wdr[16], tk1, tk2;
    {
        look_times(ws, lane, tlo, budget, tk1, tk2);
        const u32 tg = tk2 + G_T;
        const u32 pg = (w * 128 + q * 32 + ((u32)lane >> 3)) * 3584u + (gu_id >> 2) * 128u + ((u32)lane & 7) * 16u;
        until(tg);
#pragma unroll
        for (int k = 0; k < 4; ++k) { dma_pl(P.wg + k * 8 * 1792, pg, L_W1 + w * 8192u + k * 1024u); dma_pl(P.wu + k * 8 * 1792, pg, L_W1 + w * 8192u + 4096u + k * 1024u); }
#pragma unroll
        for (int k = 0; k < 4; ++k) dma_nt(P.wd + k * 2048, wdo, L_WD + w * 4096u + k * 1024u);
        VMWAIT(4);
        until(tg + G_2);
    }
#pragma unroll
    for (int s = 1; s < 4; ++s) { const u32 r = ((q + s) & 3) * 32 * 1792; dma_pl(P.wg + r, wo, L_W + w * 8192u + s * 1024u); dma_pl(P.wu + r, wo, L_W + w * 8192u + 4096u + s * 1024u); }
    bf16x8 bg[4], bp[4];
    {
        const u32 t1 = L_W1 + w * 8192u + (8 * g + (i15 >> 2)) * 128 + q * 32 + (lane & 3) * 8;
        bg[0] = tr2(t1, t1 + 512); bp[0] = tr2(t1 + 4096, t1 + 4608);
    }
    v4u mg[1], hh;
    UNDEF(mg[0]); UNDEF(hh);
    ld_sc_into(mg[0], ws, sco);
    ld_sc_into(hh, ws, heo);
    STAMP(2);
    VMWAIT(8);
#pragma unroll
    for (int j = 0; j < 16; j += 2) {
        const u32 a = *lp<u32>(L_WD + w * 4096u + j * 256u + (u32)lane * 4), bb = *lp<u32>(L_WD + w * 4096u + j * 256u + 256u + (u32)lane * 4);
        wdr[j] = perm_lo(a, bb); wdr[j + 1] = perm_hi(a, bb);
    }
#pragma unroll
    for (int j = 0; j < 16; ++j) PIN(wdr[j]);
    VMWAIT(0);
    PIN(mg[0]); PIN(hh);
    STAMP(3);
#pragma unroll
    for (int s = 1; s < 4; ++s) { bg[s] = tr2(ta + s * 1024, ta + s * 1024 + 128); bp[s] = tr2(ta + 4096 + s * 1024, ta + 4096 + s * 1024 + 128); }
    if (__builtin_expect(ballot(((mg[0].z ^ tlo) | (mg[0].w ^ thi)) != 0u) != 0ull, 0)) gather<1, 0>(ws, sco, tlo, thi, mg, budget);
    float cb0 = 1.f + bflo(mg[0].x), cb1 = 1.f + bfhi(mg[0].x);
    *lp<u32>(ha + 1088) = pack2(cb0, cb1);
    *lp<u32>(ha + 2176) = mg[0].y;
    PIN(cb0); PIN(cb1);
    bf16x8 af[4];
    v4u yo = {0u, 0u, tlo, thi};
    PIN(yo);
#pragma unroll
    for (int s = 0; s < 4; ++s) { PIN(bg[s]); PIN(bp[s]); }
    STAMP(12);
    until(hint_wait<HE_K, true>(ws, heo, tlo, budget, hh) + E_DELAY);
    until(tk2 + E_T);
    STAMP(8);
    v4u xg, xp;
    poll2<false>(xg, xp, xo, ws, px, ws, tlo, thi);
    STAMP(13);
    float pil = asf(xp.x) + asf(xp.y);
    pil += dpp<0xB1>(pil); pil += dpp<0x4E>(pil); pil += dpp<0x141>(pil);
    float d0 = __builtin_fmaf(pil, -0.0625f, asf(xg.x)), d1 = __builtin_fmaf(pil, -0.0625f, asf(xg.y));
    PIN(d0); PIN(d1);
    {
        float e0 = d0 * cb0;
        PIN(e0);
        *lp<u32>(ha) = pack2(e0, d1 * cb1);
    }
    asm volatile("" ::: "memory");
#pragma unroll
    for (int s = 0; s < 4; ++s) af[s] = *lp<bf16x8>(hr + s * 64);
    SCHED();
    float z = red32(d0 + d1, __builtin_fmaf(d0, d0, d1 * d1));
    z = red16(z, z); z = rowsum(z);
    *lp<float>(sa) = z;
    SCHED();
    f32x4 dg = {0.f, 0.f, 0.f, 0.f}, du = {0.f, 0.f, 0.f, 0.f};
#pragma unroll
    for (int s = 0; s < 4; ++s) {
        dg = __builtin_amdgcn_mfma_f32_16x16x32_bf16(af[s], bg[s], dg, 0, 0, 0);
        du = __builtin_amdgcn_mfma_f32_16x16x32_bf16(af[s], bp[s], du, 0, 0, 0);
    }
    *lp<f32x4>(ga) = dg; *lp<f32x4>(ga + 256) = du;
    barrier();
    STAMP(11);
    const f32x4 q1 = *lp<f32x4>(L_GU + 2048), q2 = *lp<f32x4>(L_GU + 2064);
    const f32x4 p0 = *lp<f32x4>(gr), p1 = *lp<f32x4>(gr + 1024);
    const float dm = sum4(q1) * (1.f / 512.f);
    const float rstd = __builtin_amdgcn_rsqf(__builtin_fmaxf(sum4(q2) * (1.f / 512.f) - dm * dm, 0.f) + 1e-5f);
    float gx = p0.x + p1.x, gy = p0.y + p1.y;
    PIN(gx); PIN(gy);
    float s = rstd * (gx - dm * gy) + (p0.z + p1.z);
    s = red32(s, s);
    u32 gs, us;
    both16(asu(s), gs, us);
    const float gsum = asf(gs), usum = asf(us);
    const float act = gsum * usum * __builtin_amdgcn_rcpf(1.f + ex2(gsum * __builtin_fmaf(gsum * gsum, -0.044715f * 1.5957691216057308f * 1.4426950408889634f, -1.5957691216057308f * 1.4426950408889634f)));
    const u32 actp = pack2(act, dpp<0x101>(act));
    asm volatile("s_nop 1\n\t"
        "v_dot2c_f32_bf16_dpp %0, %2, %3 row_newbcast:0 row_mask:0xf bank_mask:0xf\n\tv_dot2c_f32_bf16_dpp %1, %2, %4 row_newbcast:0 row_mask:0xf bank_mask:0xf\n\t"
        "v_dot2c_f32_bf16_dpp %0, %2, %5 row_newbcast:2 row_mask:0xf bank_mask:0xf\n\tv_dot2c_f32_bf16_dpp %1, %2, %6 row_newbcast:2 row_mask:0xf bank_mask:0xf\n\t"
        "v_dot2c_f32_bf16_dpp %0, %2, %7 row_newbcast:4 row_mask:0xf bank_mask:0xf\n\tv_dot2c_f32_bf16_dpp %1, %2, %8 row_newbcast:4 row_mask:0xf bank_mask:0xf\n\t"
        "v_dot2c_f32_bf16_dpp %0, %2, %9 row_newbcast:6 row_mask:0xf bank_mask:0xf\n\tv_dot2c_f32_bf16_dpp %1, %2, %10 row_newbcast:6 row_mask:0xf bank_mask:0xf\n\t"
        "v_dot2c_f32_bf16_dpp %0, %2, %11 row_newbcast:8 row_mask:0xf bank_mask:0xf\n\tv_dot2c_f32_bf16_dpp %1, %2, %12 row_newbcast:8 row_mask:0xf bank_mask:0xf\n\t"
        "v_dot2c_f32_bf16_dpp %0, %2, %13 row_newbcast:10 row_mask:0xf bank_mask:0xf\n\tv_dot2c_f32_bf16_dpp %1, %2, %14 row_newbcast:10 row_mask:0xf bank_mask:0xf\n\t"
        "v_dot2c_f32_bf16_dpp %0, %2, %15 row_newbcast:12 row_mask:0xf bank_mask:0xf\n\tv_dot2c_f32_bf16_dpp %1, %2, %16 row_newbcast:12 row_mask:0xf bank_mask:0xf\n\t"
        "v_dot2c_f32_bf16_dpp %0, %2, %17 row_newbcast:14 row_mask:0xf bank_mask:0xf\n\tv_dot2c_f32_bf16_dpp %1, %2, %18 row_newbcast:14 row_mask:0xf bank_mask:0xf\n\ts_nop 2"
        : "+v"(yo.x), "+v"(yo.y) : "v"(actp), "v"(wdr[0]), "v"(wdr[1]), "v"(wdr[2]), "v"(wdr[3]), "v"(wdr[4]), "v"(wdr[5]), "v"(wdr[6]), "v"(wdr[7]),
          "v"(wdr[8]), "v"(wdr[9]), "v"(wdr[10]), "v"(wdr[11]), "v"(wdr[12]), "v"(wdr[13]), "v"(wdr[14]), "v"(wdr[15]));
    st_sc(ws, so, yo);
    STAMP(14);
}

#define ISSUE(h) do { if ((h) & 1) dma_b(ckv + ktile(sidx, (h) >> 1), od, slot((h) >> 1) + p * 4096u, pcf, L_FL + C_LAND + ((h) >> 1) * 16u + p * 4u); else dma_a(ckv + ktile(sidx, (h) >> 1), od, slot((h) >> 1) + p * 4096u); } while (0)
constexpr int ops_in(int h0, int h1) { int n = 0; for (int h = h0; h < h1; ++h) n += 2 + (h & 1); return n; }
DI void publish(char* ws, u32 g1, const f32x16 (&o)[4], v4u (&d)[8]) {
#pragma unroll
    for (int k = 0; k < 8; k += 2) {
        u32 c[8];
#pragma unroll
        for (int i = 0; i < 4; ++i) { c[i] = pack2(o[0][2 * k + i], o[1][2 * k + i]); c[4 + i] = pack2(o[2][2 * k + i], o[3][2 * k + i]); }
        SCHED();
#pragma unroll
        for (int i = 0; i < 2; ++i) {
            auto r0 = __builtin_amdgcn_permlane16_swap(c[2 * i], c[2 * i + 1], false, false);
            auto r1 = __builtin_amdgcn_permlane16_swap(c[4 + 2 * i], c[5 + 2 * i], false, false);
            d[k + i] = (v4u){(u32)r0[0], (u32)r0[1], (u32)r1[0], (u32)r1[1]};
            st_sc_keep(ws + WS_PAY + (k + i) * PHD, g1, d[k + i]);
        }
        SCHED();
    }
}
DI void stream_pv(const Params& P, int b, u32 p, int lane, const u32 (&od)[4], u64 pcf, u32 t0, u32& tl) {
    const u32 tz = t0; (void)tz;
    char* const ws = P.ws;
    const u32 sidx = (u32)b - 128, i15 = lane & 15, R = lane >> 4;
    const char* ckv = (const char*)P.ckv;
    u32 va[2][4];
    {
        const u32 hf = R >> 1, blk = R & 1, q4 = i15 >> 2, p4 = i15 & 3;
#pragma unroll
        for (int e = 0; e < 2; ++e)
#pragma unroll
            for (int cb = 0; cb < 4; ++cb) {
                const u32 row = 8 * hf + 4 * e + q4;
                va[e][cb] = p * 4096u + row * 256u + (((4 * cb + 2 * blk + (p4 >> 1)) ^ swz(row)) << 4) + 8 * (p4 & 1);
                PIN(va[e][cb]);
            }
    }
    u32 fb = L_FL, pa = L_P + (u32)lane * 16u;
    PIN(fb); PIN(pa);
    bf16x8 V[8][4];
    u32 vbase = 0;
#define VBASE(t) do { if (vwin(t) != vbase) { _Pragma("unroll") for (int i_ = 0; i_ < 8; ++i_) { va[i_ >> 2][i_ & 3] += vwin(t) - vbase; PIN(va[i_ >> 2][i_ & 3]); } vbase = vwin(t); } } while (0)
#define VREAD(t) do { VBASE(t); _Pragma("unroll") for (int cb_ = 0; cb_ < 4; ++cb_) V[(t) & 7][cb_] = tr2(va[0][cb_] + (slot(t) - vwin(t)), va[1][cb_] + (slot(t) - vwin(t))); } while (0)
    u64 e64;
    EPOCH();
    const u32 tlo = (u32)e64;
    u32 g1, gc, lr;
    {
        const u32 k = 4 * (i15 >> 3) + (i15 & 3);
        g1 = (p * 128u + (sidx ^ ((tlo & 1) << 6))) * 256u + R * (8 * PHD) + i15 * 16u;
        gc = WS_CHK + (sidx * 4 + p) * 512u + (8 * R + k) * 16u;
        lr = L_L + (8 * (k >> 1) + 4 * (R >> 1) + 2 * (k & 1) + (R & 1)) * 16u;
        PIN(g1); PIN(gc); PIN(lr);
    }
    bool held = false;
#pragma unroll
    for (int h = HW; h < 32; ++h) {
        const int l = h - HW;
        if (h >= 14 && h < 30 && !(h & 1)) wait1(fb, C_KRD + h / 2 - 7);
        if (h >= 12 && h < 30 && !held && rfl(flag_get(C_H)) != 0u) { wait4(fb, C_Q); held = true; }
        if (h == 30) wait4(fb, C_E);
        VMWAIT(ops_in(l + 1, h));
        if (l == 1) STAMP(18);
        if (l == 7) STAMP(19);
        if (l == 15) STAMP(1);
        if (h == 24) STAMP(2);
        ISSUE(h);
        if ((l & 1) && l < 16) VREAD(l >> 1);
    }
    static_assert(HW >= 3 && HW <= 6, "tile 12 must have landed here");
    f32x16 o[4];
#pragma unroll
    for (int cb = 0; cb < 4; ++cb)
#pragma unroll
        for (int i = 0; i < 16; ++i) o[cb][i] = 0.f;
    bf16x8 pb[4], pc[4];
#define PV(t, pp) do { SCHED(); _Pragma("unroll") for (int cb_ = 0; cb_ < 4; ++cb_) o[cb_] = __builtin_amdgcn_mfma_f32_32x32x16_bf16(pp[(t) & 3], V[(t) & 7][cb_], o[cb_], 0, 0, 0); SCHED(); } while (0)
#define PVR(t, pp, u) do { VBASE(u); _Pragma("unroll") for (int cb_ = 0; cb_ < 4; ++cb_) { SCHED(); \
        o[cb_] = __builtin_amdgcn_mfma_f32_32x32x16_bf16(pp[(t) & 3], V[(t) & 7][cb_], o[cb_], 0, 0, 0); SCHED(); \
        V[(u) & 7][cb_] = tr2(va[0][cb_] + (slot(u) - vwin(u)), va[1][cb_] + (slot(u) - vwin(u))); } SCHED(); } while (0)
    STAMP(3);
    if (p == 0) {
        const v4u z = *lp<v4u>(fb + C_TZ);
        const u32 t = now32();
        if (lane == 0) st_sc2(ws, WS_FC + sidx * 8u, (v2u){t + max((int)FC_A, FC_B - (int)min(min(t - z.x, t - z.y), min(t - z.z, t - z.w))), tlo});
    }
    wait4p(fb, C_P, pa, 0, pb);
    STAMP(7);
    CYC0();
    CDECL();
    CS(0);
#pragma unroll
    for (int t = 0; t < 5; ++t) { PVR(t, pb, t + 8); if (t < 4) pb[t] = *lp<bf16x8>(pa + (t + 4) * 1024); if (t == 0) CS(1); if (t == 1) CS(2); if (t == 3) CS(3); }
    CS(4);
    PV(5, pb); PV(6, pb);
    u32 pf = flag_get(C_P + 4);
#pragma unroll
    for (int i = 0; i < 4; ++i) pc[i] = *(volatile LAS bf16x8*)lp<bf16x8>(pa + (8 + i) * 1024);
    PV(7, pb);
    STAMP(4);
    CS(5);
    if (__builtin_expect(rfl(pf) != 0x01010101u, 0)) wait4p(fb, C_P + 4, pa, 8192, pc);
    CS(6);
    PV(8, pc);
    VMWAIT(5);
    PVR(9, pc, 13); PVR(10, pc, 14);
    pf = flag_get(C_P + 8);
#pragma unroll
    for (int i = 0; i < 3; ++i) pb[i] = *(volatile LAS bf16x8*)lp<bf16x8>(pa + (12 + i) * 1024);
    PV(11, pc);
    STAMP(5);
    CS(7);
    pf = rfl(pf);
    if (__builtin_expect(!(pf & 0x1u), 0)) pb[0] = wait1p(fb, C_P + 8, pa, 12 * 1024);
    PV(12, pb);
    if (__builtin_expect(!(pf & 0x100u), 0)) pb[1] = wait1p(fb, C_P + 9, pa, 13 * 1024);
    VBASE(15);
    PV(13, pb);
    if (__builtin_expect(!(pf & 0x10000u), 0)) pb[2] = wait1p(fb, C_P + 10, pa, 14 * 1024);
    VMWAIT(0);
    PVR(14, pb, 15);
    pb[3] = wait1p(fb, C_P + 11, pa, 15 * 1024);
    PV(15, pb);
    STAMP(6);
    STAMPC(29);
    CS(8);
    v4u d[8];
    publish(ws, g1, o, d);
    v2u fl = *(volatile LAS v2u*)lp<v2u>(fb + C_L);
    f32x4 lq = *(volatile LAS f32x4*)lp<f32x4>(lr);
    u32 mref = 0;
    STAMP(8);
    CS(9);
    u32 cs[8];
#pragma unroll
    for (int k = 0; k < 8; ++k) { cs[k] = mix4(d[k]); PIN(cs[k]); }
    if (__builtin_expect(rfl(fl.x) != 0x01010101u || rfl(fl.y) != 0u, 0)) {
        wait4(fb, C_L);
        if (rfl(flag_get(C_BAD)) != 0u) {
            barrier();
            barrier();
#pragma unroll
            for (int cb = 0; cb < 4; ++cb)
#pragma unroll
                for (int i = 0; i < 16; ++i) o[cb][i] = 0.f;
#pragma clang loop unroll(disable)
            for (u32 t = 0; t < 16; ++t) {
                if (t == 8) {
                    LGKM0();
#pragma clang loop unroll(disable)
                    for (u32 u = 0; u < 8; ++u) { dma_a(ckv + ktile(sidx, u), od, slot_no(u + 8) * 16384u + p * 4096u); dma_b(ckv + ktile(sidx, u), od, slot_no(u + 8) * 16384u + p * 4096u, pcf, L_FL + C_LAND); }
                    VMWAIT(0);
                }
                const u32 so = slot_no(t | 8) * 16384u - vbase;
#pragma unroll
                for (int cb = 0; cb < 4; ++cb) V[7][cb] = tr2(va[0][cb] + so, va[1][cb] + so);
                pc[3] = *lp<bf16x8>(pa + (t ^ 8) * 1024);
                PV(15, pc);
            }
            publish(ws, g1, o, d);
            asm volatile("s_nop 1");
#pragma unroll
            for (int k = 0; k < 8; ++k) { cs[k] = mix4(d[k]); PIN(cs[k]); }
            mref = *(volatile LAS u32*)lp<u32>(L_M + (lr - L_L) / 4);
        }
        lq = *(volatile LAS f32x4*)lp<f32x4>(lr);
    }
    {
        u32 t0, t1, t2, t3, u0, u1;
        asm volatile("s_nop 1\n\t"
            "v_add_u32_dpp %0, %6, %6 row_ror:8 row_mask:0xf bank_mask:0x3\n\tv_add_u32_dpp %1, %7, %7 row_ror:8 row_mask:0xf bank_mask:0x3\n\t"
            "v_add_u32_dpp %2, %8, %8 row_ror:8 row_mask:0xf bank_mask:0x3\n\tv_add_u32_dpp %3, %9, %9 row_ror:8 row_mask:0xf bank_mask:0x3\n\t"
            "v_add_u32_dpp %0, %10, %10 row_ror:8 row_mask:0xf bank_mask:0xc\n\tv_add_u32_dpp %1, %11, %11 row_ror:8 row_mask:0xf bank_mask:0xc\n\t"
            "v_add_u32_dpp %2, %12, %12 row_ror:8 row_mask:0xf bank_mask:0xc\n\tv_add_u32_dpp %3, %13, %13 row_ror:8 row_mask:0xf bank_mask:0xc\n\t"
            "v_add_u32_dpp %4, %0, %0 row_half_mirror row_mask:0xf bank_mask:0x5\n\tv_add_u32_dpp %5, %1, %1 row_half_mirror row_mask:0xf bank_mask:0x5\n\t"
            "v_add_u32_dpp %4, %2, %2 row_half_mirror row_mask:0xf bank_mask:0xa\n\tv_add_u32_dpp %5, %3, %3 row_half_mirror row_mask:0xf bank_mask:0xa\n\t"
            "s_nop 0\n\t"
            "v_add_u32_dpp %4, %4, %4 quad_perm:[1,0,3,2] row_mask:0xf bank_mask:0xf\n\ts_nop 0\n\tv_add_u32_dpp %5, %5, %5 quad_perm:[1,0,3,2] row_mask:0xf bank_mask:0xf\n\t"
            "v_add_u32_dpp %4, %4, %4 quad_perm:[2,3,0,1] row_mask:0xf bank_mask:0xf\n\ts_nop 0\n\tv_add_u32_dpp %5, %5, %5 quad_perm:[2,3,0,1] row_mask:0xf bank_mask:0xf\n\ts_nop 0"
            : "=&v"(t0), "=&v"(t1), "=&v"(t2), "=&v"(t3), "=&v"(u0), "=&v"(u1)
            : "v"(cs[0]), "v"(cs[1]), "v"(cs[2]), "v"(cs[3]), "v"(cs[4]), "v"(cs[5]), "v"(cs[6]), "v"(cs[7]));
        const u32 x = (gc & 16u) ? u1 : u0;
        const v4u cg = {(gc & 32u) ? dppu<0x104>(x) : x, asu(sum4(lq)), mref, tlo};
        asm volatile("s_mov_b64 exec, %3\n\tglobal_store_dwordx4 %0, %1, %2 sc1\n\ts_mov_b64 exec, -1\n\ts_nop 0" :: "v"(gc), "v"(cg), "s"(ws), "s"(0x0f0f0f0f0f0f0f0full) : "memory");
    }
    CS(10);
    STAMP(9);
    COUT(11);
    barrier();
}

DI void p_write(const f32x4& s0, const f32x4& s1, u32 pa, f32x4& a, f32x4& c) {
#pragma unroll
    for (int k = 0; k < 4; ++k) a[k] = ex2(s0[k]);
#pragma unroll
    for (int k = 0; k < 4; ++k) c[k] = ex2(s1[k]);
    *lp<v2u>(pa) = (v2u){pack2(a.x, a.y), pack2(a.z, a.w)};
    *lp<v2u>(pa + 256) = (v2u){pack2(c.x, c.y), pack2(c.z, c.w)};
}
template <u32 KO> DI void qk_second(const bf16x8 (&q0)[16], const bf16x8 (&q1)[16], const u32 (&a)[16], u32 fw, u32 off, f32x4& s0, f32x4& s1, u64& cw) {
    bf16x8 fa[16];
    land_wait_k<KO>(fw, off, a[0], a[1], a[2], a[3], fa[0], fa[1], fa[2], fa[3]);
#pragma unroll
    for (int i = 0; i < 4; ++i) { s0[i] = 0.f; s1[i] = 0.f; }
#pragma unroll
    for (int s = 0; s < 16; ++s) {
        SCHED();
        s0 = __builtin_amdgcn_mfma_f32_16x16x32_bf16(fa[s], q0[s], s0, 0, 0, 0);
        if (s < KD - 4) { SCHED(); fa[2 * s + 4] = *lp<bf16x8>(a[2 * s + 4] + KO); SCHED(); }
        s1 = __builtin_amdgcn_mfma_f32_16x16x32_bf16(fa[s], q1[s], s1, 0, 0, 0);
        SCHED();
        if (s < KD - 4) fa[2 * s + 5] = *lp<bf16x8>(a[2 * s + 5] + KO);
        else if (s + KD < 16) fa[s + KD] = *lp<bf16x8>(a[s + KD] + KO);
    }
}
DI void stream_qk(const Params& P, int b, u32 w, int lane, u32 t0, int& budget, u32& tl) {
    char* const ws = P.ws;
    const int xc = b & 7, yy = (b >> 3) - 16;
    const u32 i15 = lane & 15, g = lane >> 4;
    u64 e64;
    EPOCH();
    const u32 tlo = (u32)e64, thi = (u32)(e64 >> 32);
    const u32 ak = g * 4096u + i15 * 256u + (swz(i15) << 4);
    const u32 c16 = i15 ^ g ^ ((w & 1) ? 12u : 0u);
    u32 qo = WS_QABS + (w ^ ((tlo & 1) << 1)) * 2048u + g * 256u + c16 * 16u, co = WS_QCHK + w * 2048u + (u32)lane * 16u;
    u32 va = L_IMG + ((i15 & 3) ^ 2) * 8192u + (8 * w + g) * 256u + (((i15 >> 2) ^ ((w & 1) ? 3u : 0u)) << 6) + ((i15 & 3) << 4);
    u32 fa = L_FL + w, fb = L_FL, fw = L_FL + 16 * w + (i15 & 3) * 4, pa = L_P + w * 1024u + (g >> 1) * 512u + i15 * 16u + (g & 1) * 8u;
    u32 bq[4];
#pragma unroll
    for (int k = 0; k < 4; ++k) { bq[k] = L_IMG + i15 * 256u + (((g * 4 + k) ^ (i15 & 3) ^ ((i15 & 8) ? 12u : 0u)) << 4); PIN(bq[k]); }
    PIN(qo); PIN(co); PIN(va); PIN(fa); PIN(fb); PIN(fw); PIN(pa);
    u32 pa4 = pa + 4096;
    PIN(pa4);
    v4u chk0, chk1;
    UNDEF(chk0); UNDEF(chk1);
#define REQ_Q() do { \
        _Pragma("unroll") for (int k = 0; k < 8; ++k) dma_sc(ws + (k & 1) * 1024 + (k >> 1) * 8192, qo, L_IMG + w * 2048u + (k & 1) * 1024u + ((k >> 1) ^ 2) * 8192u); \
        ld_sc_into(chk0, ws, co); ld_sc_into(chk1, ws + 1024, co); } while (0)
    u32 warm;
    UNDEF(warm);
    if (w == 3) asm volatile("global_load_dword %0, %1, %2\n\tglobal_load_dword %0, %1, %3\n\tglobal_load_dword %0, %1, %4\n\tglobal_load_dword %0, %5, %6" : "+v"(warm)
        : "v"(((u32)lane & 1) * (1792u * 1024u - 4u)), "s"(P.wg), "s"(P.wu), "s"(P.wd), "v"(WS_PAY + (((u32)lane & 3) * 10 + 1) * PHD + 4 * 128 * 256), "s"(ws) : "memory");
    __builtin_amdgcn_s_setprio(3);
    bf16x8 k0[16], k1[16];
    land_wait(fw, C_LAND);
    VMWAIT(0);
    PIN(warm);
    {
        u32 a = ak + slot_no(w) * 16384u; PIN(a);
#pragma unroll
        for (int s = 0; s < 16; ++s) k0[s] = *lp<bf16x8>(a ^ (u32)(s << 4));
        flag_set(fa, C_KRD);
    }
    QTimer qt = {time_base(ws + WS_ST, t0) + Q_FIRST, 0u};
    u32 ka[16];
    {
        u32 a = ak + slot_no(w + 8) * 16384u; PIN(a);
#pragma unroll
        for (int s = 0; s < 16; ++s) { ka[s] = a ^ (u32)(s << 4); PIN(ka[s]); }
    }
    bool asked = false;
#pragma clang loop unroll(disable)
    while (ballot(*(volatile LAS u32*)lp<u32>(fw + C_LAND + 64) == 0u) != 0ull) {
        if (q_due(qt, ws, tlo)) {
            flag_set(fa, C_H);
            until(qt.t + Q_PRE);
            REQ_Q();
            flag_set(fa, C_Q);
            land_wait(fw, C_LAND + 64);
            asked = true;
            break;
        }
        __builtin_amdgcn_s_sleep(3);
    }
    asm volatile("" ::: "memory");
    {
        u32 a = ak + slot_no(w + 4) * 16384u; PIN(a);
#pragma unroll
        for (int s = 0; s < 16; ++s) k1[s] = *lp<bf16x8>(a ^ (u32)(s << 4));
        flag_set(fa, C_KRD + 4);
    }
#pragma unroll
    for (int s = 0; s < 16; ++s) { PIN(k0[s]); PIN(k1[s]); }
    STAMP(1);
    if (!asked) {
#pragma clang loop unroll(disable)
        while (!q_due(qt, ws, tlo)) __builtin_amdgcn_s_sleep(1);
        flag_set(fa, C_H);
        until(qt.t + Q_PRE);
    }
    STAMP(22);
    u32 tz_;
#pragma clang loop unroll(disable)
    do {
        if (!asked) {
            REQ_Q();
            flag_set(fa, C_Q);
        }
        asked = false;
        STAMP(20);
        VMWAIT(0);
        tz_ = now32();
        STAMP(4);
        PIN(chk0); PIN(chk1);
        const v4u x0 = *(volatile LAS v4u*)lp<v4u>(va), x1 = *(volatile LAS v4u*)lp<v4u>(va ^ 16u), x2 = *(volatile LAS v4u*)lp<v4u>(va ^ 32u), x3 = *(volatile LAS v4u*)lp<v4u>(va ^ 48u);
        const v4u y0 = *(volatile LAS v4u*)lp<v4u>(va + 1024), y1 = *(volatile LAS v4u*)lp<v4u>((va ^ 16u) + 1024), y2 = *(volatile LAS v4u*)lp<v4u>((va ^ 32u) + 1024), y3 = *(volatile LAS v4u*)lp<v4u>((va ^ 48u) + 1024);
        const v4u xs = (x0 + x1) + (x2 + x3), ys = (y0 + y1) + (y2 + y3);
        const u32 bad = ((mix4(xs) ^ chk0.x) | (chk0.z ^ tlo) | (chk0.w ^ thi)) | ((mix4(ys) ^ chk1.x) | (chk1.z ^ tlo) | (chk1.w ^ thi));
        if (ballot(bad != 0u) == 0ull) break;
        __builtin_amdgcn_s_sleep(4);
    } while (--budget > 0);
    flag_set(fa, C_Z);
    *lp<u32>(L_FL + C_TZ + w * 4) = tz_;
    STAMP(5);
    wait4(fb, C_Z);
    STAMP(6);
    const u32 tz = t0; (void)tz;
    CYC0();
    u64 cs_[12];
    CS(0);
    f32x4 s[8];
#pragma unroll
    for (int i = 0; i < 4; ++i) s[i] = (f32x4){0.f, 0.f, 0.f, 0.f};
    bf16x8 q0[16], q1[16];
#define QREAD(s_) do { q0[s_] = *lp<bf16x8>(bq[(s_) & 3] + (((s_) >> 2) ^ 2) * 8192); q1[s_] = *lp<bf16x8>(bq[(s_) & 3] + (((s_) >> 2) ^ 2) * 8192 + 4096); } while (0)
#pragma unroll
    for (int i = 0; i < QD; ++i) QREAD(i);
#pragma unroll
    for (int i = 0; i < 16; ++i) {
        if (i + QD < 16) QREAD(i + QD);
        if (i + QD == 7) flag_set(fa, C_E);
        SCHED();
        s[0] = __builtin_amdgcn_mfma_f32_16x16x32_bf16(k0[i], q0[i], s[0], 0, 0, 0);
        s[1] = __builtin_amdgcn_mfma_f32_16x16x32_bf16(k0[i], q1[i], s[1], 0, 0, 0);
        s[2] = __builtin_amdgcn_mfma_f32_16x16x32_bf16(k1[i], q0[i], s[2], 0, 0, 0);
        s[3] = __builtin_amdgcn_mfma_f32_16x16x32_bf16(k1[i], q1[i], s[3], 0, 0, 0);
        SCHED();
    }
    STAMP(21);
    STAMPC(29);
    CS(1);
    float l0, l1;
    f32x4 e0, e1, e2, e3;
    p_write(s[0], s[1], pa, e0, e1);
    p_write(s[2], s[3], pa4, e2, e3);
    flag_set(fa, C_P);
    SCHED();
    STAMP(24);
    CS(2);
    l0 = sum4(e0 + e2); l1 = sum4(e1 + e3);
    pa4 = pa + 8192;
    PIN(l0); PIN(l1); PIN(pa4);
    qk_second<0>(q0, q1, ka, fw, C_LAND + 128, s[4], s[5], cs_[3]);
    CS(4);
    p_write(s[4], s[5], pa4, e0, e1);
    flag_set(fa, C_P + 4);
    SCHED();
    STAMP(26);
    CS(5);
    l0 += sum4(e0); l1 += sum4(e1);
    float m0 = __builtin_fmaxf(max8(s[0], s[2]), max4(s[4])), m1 = __builtin_fmaxf(max8(s[1], s[3]), max4(s[5]));
    u32 ln = (u32)lane; PIN(ln);
    u32 la = L_L + (ln & 31) * 16 + w * 4, pl = pa + 12288;
    PIN(l0); PIN(l1); PIN(m0); PIN(m1); PIN(la); PIN(pl);
    if (w == 3) wait4(fb, C_P);
    qk_second<49152>(q0, q1, ka, fw, C_LAND + 192, s[6], s[7], cs_[6]);
    CS(7);
    STAMP(27);
    p_write(s[6], s[7], pl, e0, e1);
    flag_set(fa, C_P + 8);
    SCHED();
    STAMP(28);
    CS(8);
    {
        float z = red16(l0 + sum4(e0), l1 + sum4(e1));
        *lp<float>(la) = red32(z, z);
    }
    m0 = __builtin_fmaxf(m0, max4(s[6])); m1 = __builtin_fmaxf(m1, max4(s[7]));
    if (ballot(!(__builtin_fmaxf(__builtin_fabsf(m0), __builtin_fabsf(m1)) <= LAZY)) != 0ull) flag_set(fa, C_BAD);
    flag_set(fa, C_L);
    CS(9);
    COUT(10);
    __builtin_amdgcn_s_setprio(0);
    wait4(fb, C_L);
    if (__builtin_expect(rfl(flag_get(C_BAD)) != 0u, 0)) {
        m0 = max32(max16(m0)); m1 = max32(max16(m1));
        *lp<float>(la + (L_IMG + 4096 - L_L)) = (ln & 16) ? m1 : m0;
        barrier();
        const float m = max4(*lp<f32x4>((la & ~15u) + (L_IMG + 4096 - L_L)));
        *lp<float>(L_M + (ln & 31) * 4) = m;
        u32 e, od;
        both16(asu(m), e, od);
        l0 = 0.f; l1 = 0.f;
#pragma unroll
        for (int i = 0; i < 4; ++i) { p_write(s[2 * i] - asf(e), s[2 * i + 1] - asf(od), pa + 4096 * i, e0, e1); l0 += sum4(e0); l1 += sum4(e1); }
        const float z = red16(l0, l1);
        *lp<float>(la) = red32(z, z);
        barrier();
    }
    if (yy < 12 || xc < 4) mlp_part(P, b, w, lane, tlo, thi, budget, tl);
}

constexpr int QMAP = 0;
#define CODE_Y 0x13
__global__ void __launch_bounds__(512, 2) fused_kernel(Params) {
    v16u ka;
    v8u kb;
    v2u kc;
    asm volatile("s_load_dwordx16 %0, %3, 0x0\n\ts_load_dwordx8 %1, %3, 0x40\n\ts_load_dwordx2 %2, %3, 0x60" : "=&s"(ka), "=&s"(kb), "=&s"(kc) : "s"(__builtin_amdgcn_kernarg_segment_ptr()));
    SCHED();
    const int tid = threadIdx.x, lane = tid & 63;
    const int wave = __builtin_amdgcn_readfirstlane(tid >> 6);
    const int b = blockIdx.x, xc = b & 7, y = b >> 3;
    int budget = BUDGET;
    u32 tl = 0;
    u64 pc0;
    asm volatile("s_getpc_b64 %0\n\ts_nop 0" : "=s"(pc0));
    *lp<u32>(L_FL + (u32)(wave >> 2) * 1024 + (u32)(wave & 3) * 4 + (u32)lane * 16) = 0u;
    if (wave >= 4 && (y & CODE_Y) == 0x10) {
        asm volatile("s_mov_b32 m0, %1\n\ts_nop 0\n\tglobal_load_lds_dword %0, %2\n\tglobal_load_lds_dword %0, %3\n\tglobal_load_lds_dword %0, %4"
            :: "v"((u32)lane * 64u + (u32)(wave - 4) * CODE_STR), "s"(L_IMG + (u32)(wave - 4) * 2048u), "s"(pc0), "s"(pc0 + 4096), "s"(pc0 + 8192) : "memory");
    }
    const u32 head = (u32)(xc * 4 + (y >> 2)), j = (u32)y & 3, w = (u32)wave;
    const u32 i15 = lane & 15, g = lane >> 4;
    const u32 ks = b & 15, cg = (u32)(b >> 4) * 2 + (w & 1);
    const u32 hq = QMAP ? (u32)(xc >> 1) * 8 + (u32)(y >> 1) : head, mh = QMAP ? (u32)xc & 1 : j >> 1, dh = j & 1;
    u32 od[4], oi[4], oq = (mh * 256 + w * 32 + (lane & 8) * 2 + g) * 8192u + hq * 256u + dh * 128u + (lane & 7) * 16u;
#pragma unroll
    for (int k = 0; k < 4; ++k) { od[k] = (3 * k + g) * 1024u + w * 256u + ((i15 ^ (g << 2) ^ (u32)k) << 4); PIN(od[k]); }
    oi[0] = (ks * 32 + 2 * (u32)(lane >> 2)) * 2; oi[1] = (u32)lane * 16; oi[2] = cg * 64u + (lane & 3) * 16u; oi[3] = oi[0] * 3072u + oi[2]; oi[2] += g * 4u;
#pragma unroll
    for (int k = 0; k < 4; ++k) PIN(oi[k]);
    PIN(oq);
    v4u wm[4], xa;
    u32 cc, xs;
#pragma unroll
    for (int k = 0; k < 4; ++k) UNDEF(wm[k]);
    UNDEF(xa); UNDEF(cc); UNDEF(xs);
    int role = (0xffaa5550u >> ((y & 16) + 2 * wave)) & 3;
    asm volatile("s_waitcnt lgkmcnt(0)" : "+s"(role), "+s"(ka), "+s"(kb), "+s"(kc) :: "memory");
#define KP(T, v, i) ((T)((u64)v[i + 1] << 32 | v[i]))
    Params P;
    P.wmod = KP(const u16*, ka, 0); P.c = KP(const u16*, ka, 2); P.x = KP(const u16*, ka, 4); P.ws = KP(char*, ka, 6);
    P.wq = KP(const u16*, ka, 8); P.ckv = KP(const u16*, ka, 10); P.wuk = KP(const u16*, ka, 12); P.wo = KP(const u16*, ka, 14);
    P.wuv = KP(const u16*, kb, 0); P.wg = KP(const u16*, kb, 2); P.wu = KP(const u16*, kb, 4); P.wd = KP(const u16*, kb, 6); P.out = KP(u16*, kc, 0);
    char* const ws = P.ws;
    if (role == 0) {
        asm volatile("global_load_dword %0, %3, %4\n\tglobal_load_dwordx4 %1, %5, %6\n\tglobal_load_dword %2, %7, %6" : "+v"(cc), "+v"(xa), "+v"(xs)
            : "v"(oi[0]), "s"(P.c), "v"(oi[1]), "s"(P.x), "v"(oi[2]) : "memory");
        asm volatile("global_load_dwordx4 %0, %4, %5 nt\n\tglobal_load_dwordx4 %1, %4, %6 nt\n\tglobal_load_dwordx4 %2, %4, %5 offset:1024 nt\n\tglobal_load_dwordx4 %3, %4, %6 offset:1024 nt"
            : "+v"(wm[0]), "+v"(wm[1]), "+v"(wm[2]), "+v"(wm[3]) : "v"(oi[3]), "s"(P.wmod), "s"(P.wmod + 3072) : "memory");
    }
    const char* const ckv = (const char*)P.ckv;
    const u32 sidx = (u32)b - 128;
    const u64 pcf = pc0 - w * 256u;
    __builtin_amdgcn_s_barrier();
    u64 t0_;
    asm volatile("s_memrealtime %0" : "=s"(t0_));
    v4u wr[4];
#pragma unroll
    for (int k = 0; k < 4; ++k) UNDEF(wr[k]);
    if (role == 1) {
#pragma unroll
        for (int k = 0; k < 4; ++k) ld_nt_into(wr[k], P.wq + k * 16384, oq);
    }
    asm volatile("s_waitcnt lgkmcnt(0)" : "+s"(t0_));
    const u32 t0 = (u32)t0_;
    STAMP(20);
    asm volatile("" : "+s"(role));
    if (role >= 2) {
        if (role == 2) {
            const u32 p = w;
#pragma unroll
            for (int h = 0; h < HW; ++h) {
                if (h == 0) until(t0 + T_OPEN0);
                if (h == 2) until(t0 + T_OPEN1);
                ISSUE(h);
            }
            stream_pv(P, b, w, lane, od, pcf, t0, tl);
        } else stream_qk(P, b, w - 4, lane, t0, budget, tl);
        STAMP(15);
        STAMP_OUT();
        return;
    }
    u64 e64;
    u32 tb, tq;
    {
        const u32 p = lane & 3;
        v4u wk[8];
#pragma unroll
        for (int k = 0; k < 8; ++k) UNDEF(wk[k]);
        if (b < 8 && tid == 128) st_sc2(ws, WS_ST + (u32)b * 8u, (v2u){t0, ~t0});
        u32 warm;
        UNDEF(warm);
        if (wave == 7) asm volatile("global_load_dword %0, %1, %2\n\tglobal_load_dword %0, %1, %3\n\tglobal_load_dword %0, %1, %4\n\tglobal_load_dword %0, %5, %6" : "+v"(warm)
            : "v"(((u32)lane & 1) * ((4u << 20) - 4u)), "s"(P.wuk), "s"(P.wo), "s"(P.wuv), "v"(WS_PAY + (((u32)lane & 3) * 10 + 1) * PHD + 4 * 128 * 256), "s"(ws) : "memory");
        EPOCH();
        const u32 tlo = (u32)e64, thi = (u32)(e64 >> 32);
        if (wave == 3) {
            const u32 rh = 8 * ((hq & 7) >> 1) + 4 * (hq >> 4) + 2 * (hq & 1) + ((hq >> 3) & 1);
            if (mh + dh == 0 && lane < 16) st_sc(ws, WS_QCHK + rh * 256u + (u32)lane * 16u, (v4u){0u, 0u, ~tlo, 0u});
            if (b == 9 && lane < 8 * NQH) st_sc(ws, WS_QH + (u32)lane * 16u, (v4u){0u, ~tlo, 0u, ~tlo});
            if (b == 10) st_sc(ws, WS_FC + (u32)lane * 16u, (v4u){0u, ~tlo, 0u, ~tlo});
        }
        if (role == 0) {
            u32 so = piece(WS_HP, 2048, cg) + (ks * 8 + 2 * p + (u32)(lane >> 5)) * 16u;
            VMWAIT(4);
            PIN(xa); PIN(xs); PIN(cc);
            float mean, rstd;
            ln_stats(xa, mean, rstd);
            const u32 sp = pack2(silu(bflo(cc)), silu(bfhi(cc)));
            u32 s0l = sp & 0xffffu, s0h = sp << 16, s1l = sp >> 16, s1h = sp & 0xffff0000u;
            float xn0 = (bflo(xs) - mean) * rstd, xn1 = (bfhi(xs) - mean) * rstd;
            PIN(xn0); PIN(xn1); PIN(s0l); PIN(s0h); PIN(s1l); PIN(s1h); PIN(so);
            VMWAIT(0);
#pragma unroll
            for (int k = 0; k < 4; ++k) PIN(wm[k]);
            STAMP(22);
            float ta[4], tc[4];
#pragma unroll
            for (int k = 0; k < 2; ++k) {
                ta[2 * k] = red32(dot2(wm[1][k], s1l, dot2(wm[0][k], s0l, 0.f)), dot2(wm[1][k + 2], s1l, dot2(wm[0][k + 2], s0l, 0.f)));
                ta[2 * k + 1] = red32(dot2(wm[1][k], s1h, dot2(wm[0][k], s0h, 0.f)), dot2(wm[1][k + 2], s1h, dot2(wm[0][k + 2], s0h, 0.f)));
                tc[2 * k] = red32(dot2(wm[3][k], s1l, dot2(wm[2][k], s0l, 0.f)), dot2(wm[3][k + 2], s1l, dot2(wm[2][k + 2], s0l, 0.f)));
                tc[2 * k + 1] = red32(dot2(wm[3][k], s1h, dot2(wm[2][k], s0h, 0.f)), dot2(wm[3][k + 2], s1h, dot2(wm[2][k + 2], s0h, 0.f)));
            }
            float u0 = xn0 * (red16(tc[0], tc[2]) + 0.015625f) + red16(ta[0], ta[2]), u1 = xn1 * (red16(tc[1], tc[3]) + 0.015625f) + red16(ta[1], ta[3]);
            u0 += dpp<0x124>(u0); u1 += dpp<0x124>(u1); u0 += dpp<0x128>(u0); u1 += dpp<0x128>(u1);
            u32 e, od;
            both16(pack2(u0, u1), e, od);
            if ((lane & 28) == 0) st_sc(ws, so, (v4u){e, od, tlo, thi});
            STAMP(24);
#pragma unroll
            for (int k = 0; k < 4; ++k) ld_nt_into(wr[k], P.wq + k * 16384, oq);
        }
        u32 so = piece(WS_QP, 1024, hq) + ((mh * 2 + (u32)(lane >> 5)) * 16 + dh * 8 + (lane & 7)) * 16u, ho = piece(WS_HP, 2048, mh * 8 + w) + (u32)lane * 16u;
        u32 la = F_QP + w * 256 + (g * 8 + (lane & 7)) * 8, lb = la + (lane & 8) * 128, ok = ((u32)y * 32 + g * 8) * 8192u + ((u32)xc * 4 + (w & 3)) * 256u + i15 * 16u;
        PIN(so); PIN(ho); PIN(la); PIN(lb); PIN(ok);
        u32 onel = 0x3f80u, oneh = 0x3f800000u;
        PIN(onel); PIN(oneh);
        v4u hg[2];
        UNDEF(hg[0]); UNDEF(hg[1]);
        __builtin_amdgcn_s_barrier();
        const u32 tm = now32();
        until(t0 + ST_LOOK);
        tb = time_base(ws + WS_ST, t0);
        const u32 ta = tb + max((u32)A_FIRST, tm - t0 + A_OWN);
        until(ta);
        ld_sc_into(hg[0], ws, ho); ld_sc_into(hg[1], ws + 1024, ho);
        __builtin_amdgcn_s_barrier();
        if (wave >= 4) {
#pragma unroll
            for (int k = 0; k < 8; ++k) ld_nt_into(wk[k], P.wuk + k * 4096, ok);
            VMWAIT(10);
        } else VMWAIT(2);
#pragma unroll
        for (int k = 0; k < 4; ++k) PIN(wr[k]);
        PIN(warm);
        STAMP(1);
        pair_rows(wr[0], wr[1]); pair_rows(wr[2], wr[3]);
#pragma unroll
        for (int k = 0; k < 4; ++k) PIN(wr[k]);
        if (wave >= 4) VMWAIT(8); else VMWAIT(0);
        PIN(hg[0]); PIN(hg[1]);
        if (__builtin_expect(ballot(neq3(hg[0].z, hg[1].z, tlo) | neq3(hg[0].w, hg[1].w, thi)) != 0ull, 0)) gather<2, 1024, 1>(ws, ho, tlo, thi, hg, budget);
        STAMP(3);
        {
            float hx = red16(red32(dot2(hg[1].x, onel, bflo(hg[0].x)), dot2(hg[1].y, onel, bflo(hg[0].y))), red32(dot2(hg[1].x, oneh, bfhi(hg[0].x)), dot2(hg[1].y, oneh, bfhi(hg[0].y))));
            hx += dpp<0x128>(hx);
            hx = asf((u32)__builtin_amdgcn_update_dpp((int)asu(hx), (int)asu(hx), 0x114, 0xf, 0x6, false));
            const u32 hp = pack2(hx, dpp<0xB1>(hx)), h01 = dppu<0x00>(hp), h23 = dppu<0xAA>(hp);
            float acc[8];
#pragma unroll
            for (int k = 0; k < 4; ++k) { acc[2 * k] = dot2(wr[2][k], h23, dot2(wr[0][k], h01, 0.f)); acc[2 * k + 1] = dot2(wr[3][k], h23, dot2(wr[1][k], h01, 0.f)); }
            float t[4];
#pragma unroll
            for (int k = 0; k < 4; ++k) t[k] = red32(acc[k], acc[k + 4]);
            float u0 = red16(t[0], t[2]), u1 = red16(t[1], t[3]);
            u0 += dpp<0x128>(u0); PIN(u0); u1 += dpp<0x128>(u1);
            *lp<f32x2>(la) = (f32x2){u0, u1};
        }
        barrier();
        if (wave == 0) {
            const LAS f32x2* qp = lp<f32x2>(lb);
            const f32x2 q2 = (qp[0] + qp[32]) + (qp[64] + qp[96]);
            float u0 = q2.x, u1 = q2.y;
            u0 += dpp<0x128>(u0); PIN(u0); u1 += dpp<0x128>(u1);
            u32 e, od;
            both16(pack2(u0, u1), e, od);
            if ((lane & 24) == 0) st_sc(ws, so, (v4u){e, od, tlo, thi});
            STAMP(2);
        }
        tq = ta + max((u32)A2_T, now32() - ta + A2_DELAY);
        if (y == ((xc * 5) & 7) + (y & 8) && tid < NQH) st_sc2(ws, WS_QH + (u32)lane * 128u + (u32)(xc * 2 + (y >> 3)) * 8u, (v2u){tq, tq ^ tlo});
        if (wave >= 4) {
            const u32 hk = (u32)xc * 4 + (w & 3);
            const u32 rho = 8 * ((hk & 7) >> 1) + 4 * (hk >> 4) + 2 * (hk & 1) + ((hk >> 3) & 1);
            const u32 og = piece(WS_QP, 1024, hk) + (u32)lane * 16u;
            u32 sq = WS_QABS + j * 8192u + (rho ^ ((tlo & 1) << 4)) * 256u + (u32)(y >> 2) * 64u + g * 16u, sc = WS_QCHK + (rho * 16 + (u32)y) * 16u;
            PIN(sq); PIN(sc);
            v4u qg[1];
            until(tq);
            gather<1, 0, 1>(ws, og, tlo, thi, qg, budget);
            __builtin_amdgcn_s_barrier();
            STAMP(23);
            u32 e, od, q0, q1, q2, q3;
            both16(pack2(red32(bflo(qg[0].x), bflo(qg[0].y)), red32(bfhi(qg[0].x), bfhi(qg[0].y))), e, od);
            both32(e, q0, q1); both32(od, q2, q3);
            float a[8];
#pragma unroll
            for (int k = 0; k < 8; ++k) { PIN(wk[k]); a[k] = dot2(wk[k][3], q3, dot2(wk[k][2], q2, dot2(wk[k][1], q1, dot2(wk[k][0], q0, 0.f)))); }
            float z[4];
#pragma unroll
            for (int k = 0; k < 4; ++k) {
                const float keep = (lane & 1) ? a[k + 4] : a[k], give = (lane & 1) ? a[k] : a[k + 4];
                float s = keep + dpp<0xB1>(give);
                s += dpp<0x122>(s); s += dpp<0x124>(s); s += dpp<0x128>(s);
                z[k] = s * (0.08838834764831845f * 1.4426950408889634f);
            }
            const u32 y0 = pack2(z[0], z[1]), y1 = pack2(z[2], z[3]);
            const u32 y2 = dppu<0xB1>(y0), y3 = dppu<0xB1>(y1);
            if (i15 == 0) st_sc(ws, sq, (v4u){y0, y1, y2, y3});
            u32 c0, c1;
            both16(mix4((v4u){y0, y1, y2, y3}), c0, c1);
            both32(c0 + c1, c0, c1);
            if (lane == 0) st_sc(ws, sc, (v4u){c0 + c1, 0u, tlo, thi});
            STAMP(4);
        } else __builtin_amdgcn_s_barrier();
    }
    const u32 tlo = (u32)e64, thi = (u32)(e64 >> 32);
    const bool is_red = (y >> 2) == 1;
    const u32 rid = (u32)xc * 4 + j;
    bf16x8 bo[16], bu[4];
    u32 xo, aa, cw, cr, op, o, oc, wa, he, wl, rl, om, xr;
#define TAIL_ADDR() do { \
        xo = piece(WS_X1, 4096, (u32)(lane >> 3)) + (rid * 8 + (u32)(lane & 7)) * 16u; \
        aa = F_A + (i15 & 7) * 272 + g * 16; cw = F_CX + ((w - 2 * j) & 7) * 32 + i15 * 2; cr = F_CX + g * 16; \
        op = piece(WS_OUTP, 16384, 4 * w + g) + (u32)b * 128u + (i15 & 7) * 16u; \
        wl = F_LS + (i15 < 4 ? (w * 16 + g * 4 + i15) * 4 : 512u); rl = F_LS + (u32)lane * 8; PIN(wl); PIN(rl); \
        o = WS_PAY + head * PHD + (j * 128 + ((w * 16) ^ ((tlo & 1) << 6))) * 256u + (u32)lane * 16u; \
        oc = WS_CHK + ((w * 16 + (i15 & 3) * 4 + g) * 4 + j) * 512u + head * 16u; \
        wa = F_A + w * 272 + i15 * 16 + g * 4; \
        om = WS_ML + (rid >> 1) * 4096u + (g * 16 + (rid & 1) * 8 + (u32)(lane & 7)) * 16u; PIN(om); \
        he = WS_HE + (((u32)b + 17u * (u32)lane) & 127u) * HSTR + (u32)lane * 16u; PIN(he); \
        PIN(xo); PIN(aa); PIN(cw); PIN(cr); PIN(op); PIN(o); PIN(oc); PIN(wa); } while (0)
    {
        const u32 kq = (u32)b & 3, cgl = (u32)b >> 2;
        const u32 w4 = w & 3, mrow = kq * 128 + w4 * 32 + (u32)(lane >> 3);
        const u32 q4 = i15 >> 2, p4 = lane & 3;
        const u32 ra = 8 * (g & 1) + q4, rb = ra + 4;
        u32 xa[4], xb[4];
#pragma unroll
        for (int t2 = 0; t2 < 4; ++t2) {
            const u32 c0 = 8 * w + (p4 >> 1) + 2 * t2, x0 = F_WO + (8 * g + q4) * 1024 + 8 * (p4 & 1);
            xa[t2] = x0 + ((c0 ^ swz(ra)) << 4); xb[t2] = x0 + 4096 + ((c0 ^ swz(rb)) << 4);
            PIN(xa[t2]); PIN(xb[t2]);
        }
        u32 x0, x1, ma = F_ML + w * 256 + ((lane & 7) * 4 + g) * 8;
        {
            const u32 c0 = 2 * w + (p4 >> 1);
            x0 = F_UV + (8 * g + q4) * 256 + 8 * (p4 & 1) + ((c0 ^ swz(ra)) << 4); x1 = F_UV + (8 * g + q4 + 4) * 256 + 8 * (p4 & 1) + ((c0 ^ swz(rb)) << 4);
        }
        PIN(x0); PIN(x1); PIN(ma);
#define PIECE(k) until(tq + T_OWN + (k) * O_STEP)
#define WO_ROW(s, k) dma_pl(P.wo + ((j + (s)) & 3) * 16384, (head * 128 + 8 * w4 + (k)) * 1024u + (((u32)lane ^ swz(8 * (w4 & 1) + (k))) << 4), F_WO + ((s) * 32 + 8 * w4 + (k)) * 1024u)
#define UV_ROWS(i) do { const u32 e_ = w4 * 32 + (i) * 4 + g, c_ = ((e_ >> 2) & 1) * 64 + (e_ & 1) * 32 + ((e_ >> 1) & 1) * 16 + (e_ >> 3); \
            dma_nt(P.wuv, (j * 128 + c_) * 8192u + head * 256u + ((i15 ^ swz(e_ & 15)) << 4), F_UV + (w4 * 32 + (i) * 4) * 256u); } while (0)
        v4u mw[4];
        u32 cv[4];
#pragma unroll
        for (int k = 0; k < 4; ++k) { UNDEF(mw[k]); UNDEF(cv[k]); }
        u32 mo = mrow * 6144u + 2048u + cgl * 128u + (u32)(lane & 7) * 16u;
        PIN(mo);
        if (wave < 4) {
#pragma unroll
            for (int k = 0; k < 4; ++k) { PIECE(k); WO_ROW(0, 2 * k); WO_ROW(0, 2 * k + 1); }
            asm volatile("global_load_ushort %0, %4, %5\n\tglobal_load_ushort %1, %4, %5 offset:16\n\tglobal_load_ushort %2, %4, %5 offset:32\n\tglobal_load_ushort %3, %4, %5 offset:48"
                : "+v"(cv[0]), "+v"(cv[1]), "+v"(cv[2]), "+v"(cv[3]) : "v"(mrow * 2), "s"(P.c) : "memory");
            PIECE(4);
            ld_nt_into(mw[0], P.wmod, mo); ld_nt_into(mw[1], P.wmod + 8 * 3072, mo);
            VMWAIT(2);
        }
        TAIL_ADDR();
        until(tq + T_MID);
        STAMP(5);
        barrier();
        if (wave >= 4) {
#pragma unroll
            for (int k = 8; k < 32; ++k) WO_ROW(k >> 3, k & 7);
            STAMP(19);
        }
#pragma unroll
        for (int t2 = 0; t2 < 4; ++t2) { bo[4 * t2] = tr2(xa[t2], xb[t2]); xa[t2] += 32768; xb[t2] += 32768; PIN(xa[t2]); PIN(xb[t2]); }
        float u[2] = {0.f, 0.f};
        if (wave < 4) {
            const u32 t2 = now32() + U_GAP;
            until(t2);
            ld_nt_into(mw[2], P.wmod + 16 * 3072, mo); ld_nt_into(mw[3], P.wmod + 24 * 3072, mo);
#pragma unroll
            for (int k = 0; k < 4; ++k) { until(t2 + (k + 1) * U_STEP); UV_ROWS(2 * k); UV_ROWS(2 * k + 1); }
            VMWAIT(8);
#pragma unroll
            for (int k = 0; k < 4; ++k) { PIN(mw[k]); PIN(cv[k]); }
            const u32 sp = pack2(silu(bflo(cv[0])), silu(bflo(cv[1]))), sr = pack2(silu(bflo(cv[2])), silu(bflo(cv[3])));
            pair_rows(mw[0], mw[1]); pair_rows(mw[2], mw[3]);
            float t[4];
#pragma unroll
            for (int k = 0; k < 2; ++k) {
                t[2 * k] = red32(dot2(mw[2][k], sr, dot2(mw[0][k], sp, 0.f)), dot2(mw[2][k + 2], sr, dot2(mw[0][k + 2], sp, 0.f)));
                t[2 * k + 1] = red32(dot2(mw[3][k], sr, dot2(mw[1][k], sp, 0.f)), dot2(mw[3][k + 2], sr, dot2(mw[1][k + 2], sp, 0.f)));
            }
#pragma unroll
            for (int k = 0; k < 2; ++k) { float z = red16(t[k], t[k + 2]); z += dpp<0x128>(z); u[k] = z; }
        } else VMWAIT(0);
        PIN(u[0]); PIN(u[1]);
#pragma unroll
        for (int t2 = 0; t2 < 4; ++t2) PIN(bo[4 * t2]);
        STAMP(14);
        barrier();
        *lp<f32x2>(ma) = (f32x2){u[0], u[1]};
        if (tid < 12) *lp<u32>(F_ST + tid * 4) = 0u;
        barrier();
        if (wave == 7) {
            const LAS f32x2* q = lp<f32x2>(F_ML) + lane;
            const f32x2 s = q[0] + q[64];
            const float sx = red32(s.x, s.x), sy = red32(s.y, s.y);
            if (lane < 32) st_sc(ws, WS_ML + (cgl & 7) * 8192u + ((u32)lane >> 4) * 4096u + (cgl >> 3) * 1024u + (kq * 16 + ((u32)lane & 15)) * 16u, (v4u){asu(sx), asu(sy), tlo, thi});
        }
#pragma unroll
        for (int t2 = 0; t2 < 4; ++t2) { bo[4 * t2 + 1] = tr2(xa[t2], xb[t2]); bo[4 * t2 + 2] = tr2(xa[t2] + 32768, xb[t2] + 32768); bo[4 * t2 + 3] = tr2(xa[t2] + 65536, xb[t2] + 65536); }
        xr = ld_u32(P.x, (rid * 8 + (u32)(lane & 7)) * 4);
        VMWAIT(0);
        PIN(xr);
#pragma unroll
        for (int k = 0; k < 16; ++k) PIN(bo[k]);
        STAMP(6);
        barrier();
#pragma unroll
        for (int s = 0; s < 4; ++s) bu[s] = tr2(x0 + s * 8192, x1 + s * 8192);
#pragma unroll
        for (int k = 0; k < 4; ++k) PIN(bu[k]);
    }
    u32 tk1, tk2;
    float ga0 = 0.f, ga1 = 0.f, gb0 = 0.f, gb1 = 0.f, xv0 = 0.f, xv1 = 0.f;
    STAMP(7);
    {
        v4u v[4], ck = {0u, 0u, 0u, 0u};
        u32 onel = 0x3f80u, oneh = 0x3f800000u;
        PIN(onel); PIN(oneh);
#pragma unroll
        for (int k = 0; k < 4; ++k) UNDEF(v[k]);
        v4u gm[4];
#pragma unroll
        for (int k = 0; k < 4; ++k) UNDEF(gm[k]);
        if (is_red && wave == 0) asm volatile("global_load_dwordx4 %0, %4, %5 sc1\n\tglobal_load_dwordx4 %1, %4, %5 offset:1024 sc1\n\tglobal_load_dwordx4 %2, %4, %5 offset:2048 sc1\n\tglobal_load_dwordx4 %3, %4, %5 offset:3072 sc1"
            : "+v"(gm[0]), "+v"(gm[1]), "+v"(gm[2]), "+v"(gm[3]) : "v"(om), "s"(ws) : "memory");
        look_times(ws, lane, tlo, budget, tk1, tk2);
#pragma unroll
        for (int k = 0; k < 4; ++k) PIN(gm[k]);
        if (is_red && wave == 0) {
            if (ballot((neq3(gm[0].z, gm[1].z, tlo) | neq3(gm[2].z, gm[3].z, tlo)) | (neq3(gm[0].w, gm[1].w, thi) | neq3(gm[2].w, gm[3].w, thi))) != 0ull) gather<4, 1024, 1>(ws, om, tlo, thi, gm, budget);
            xv0 = bflo(xr); xv1 = bfhi(xr);
            {
                const float s0 = red16(red32(asf(gm[1].x), asf(gm[2].x)), red32(asf(gm[1].y), asf(gm[2].y)));
                u32 e, od, sbp, cbp;
                both16(asu(s0), e, od);
                both32(pack2(asf(e), asf(od)), sbp, cbp);
                if (lane < 8) st_sc(ws, WS_SC + (rid * 8 + (u32)lane) * 16u, (v4u){cbp, sbp, tlo, thi});
            }
            ga0 = asf(gm[0].x); ga1 = asf(gm[0].y); gb0 = asf(gm[3].x); gb1 = asf(gm[3].y);
            ga0 = red32(ga0, ga0); ga0 = red16(ga0, ga0); ga1 = red32(ga1, ga1); ga1 = red16(ga1, ga1);
            gb0 = red32(gb0, gb0); gb0 = red16(gb0, gb0); gb1 = red32(gb1, gb1); gb1 = red16(gb1, gb1);
        }
        PIN(ga0); PIN(ga1); PIN(gb0); PIN(gb1); PIN(xv0); PIN(xv1);
        until(tk2);
        STAMP(8);
#pragma clang loop unroll(disable)
        for (;;) {
            asm volatile("global_load_dwordx4 %0, %5, %7 sc1\n\tglobal_load_dwordx4 %1, %5, %7 offset:1024 sc1\n\tglobal_load_dwordx4 %2, %5, %7 offset:2048 sc1\n\tglobal_load_dwordx4 %3, %5, %7 offset:3072 sc1\n\t"
                "s_mov_b64 exec, %8\n\ts_waitcnt vmcnt(3)\n\tglobal_load_dwordx4 %4, %6, %7 sc1\n\ts_mov_b64 exec, -1"
                : "+v"(v[0]), "+v"(v[1]), "+v"(v[2]), "+v"(v[3]), "+v"(ck) : "v"(o), "v"(oc), "s"(ws), "s"(0x000f000f000f000full) : "memory");
            float a[8];
#pragma unroll
            for (int q = 0; q < 4; ++q) { a[2 * q] = bflo(v[0][q]); a[2 * q + 1] = bfhi(v[0][q]); }
            u32 x = mix4(v[0]);
#pragma unroll
            for (int k = 1; k < 4; ++k) {
#pragma unroll
                for (int q = 0; q < 8; ++q) PIN(a[q]);
                PIN(x);
                asm volatile("s_waitcnt vmcnt(%1)" : "+v"(v[k]) : "i"(4 - k) : "memory");
                x += mix4(v[k]);
#pragma unroll
                for (int q = 0; q < 4; ++q) { a[2 * q] = dot2(v[k][q], onel, a[2 * q]); a[2 * q + 1] = dot2(v[k][q], oneh, a[2 * q + 1]); }
            }
            float t[4];
            x += dppu<0x128>(x);
#pragma unroll
            for (int k = 0; k < 4; ++k) t[k] = red32(a[k], a[k + 4]);
            x += dppu<0x124>(x);
            float u0 = red16(t[0], t[2]); PIN(u0);
            *lp<u32>(wa) = pack2(u0, red16(t[1], t[3]));
            PIN(x);
            asm volatile("s_waitcnt vmcnt(0)" : "+v"(ck) :: "memory");
            *lp<u32>(wl) = ck.y;
            x -= ck.x;
            x += dppu<0xB1>(x); x += dppu<0x4E>(x);
            const u64 bad = ballot((x | (ck.w ^ tlo)) != 0u) & 0x000f000f000f000full;
            if (__builtin_expect((bad | ballot(ck.z != 0u)) == 0ull, 1)) break;
            if (bad == 0ull || --budget <= 0) {
                const float m = i15 < 4 ? asf(ck.z) : -3.0e38f;
                float M = __builtin_fmaxf(m, dpp<0xB1>(m)); M = __builtin_fmaxf(M, dpp<0x4E>(M));
                M = asf(rfl(asu(max32(max16(M)))));
                const u32 f = pack2(ex2(m - M), 0.f);
                const u32 f0 = dppu<0x150>(f), f1 = dppu<0x151>(f), f2 = dppu<0x152>(f), f3 = dppu<0x153>(f);
#pragma unroll
                for (int q = 0; q < 4; ++q) {
                    a[2 * q] = dot2(v[3][q], f3, dot2(v[2][q], f2, dot2(v[1][q], f1, dot2(v[0][q], f0, 0.f))));
                    a[2 * q + 1] = dot2(v[3][q], f3 << 16, dot2(v[2][q], f2 << 16, dot2(v[1][q], f1 << 16, dot2(v[0][q], f0 << 16, 0.f))));
                }
#pragma unroll
                for (int k = 0; k < 4; ++k) t[k] = red32(a[k], a[k + 4]);
                *lp<u32>(wa) = pack2(red16(t[0], t[2]), red16(t[1], t[3]));
                *lp<float>(wl) = asf(ck.y) * bflo(f);
                *lp<float>(F_ST + w * 4) = M;
                *lp<u32>(F_ST + 32) = 1u;
                break;
            }
            __builtin_amdgcn_s_sleep(POLL_SLEEP);
        }
        STAMP(9);
    }
    barrier();
    u64 tc1;
    asm volatile("s_memrealtime %0" : "=s"(tc1));
    STAMP(10);
    float ls;
    {
        bf16x8 af[4];
#pragma unroll
        for (int s = 0; s < 4; ++s) af[s] = *lp<bf16x8>(aa + s * 64);
        const u32 rare = *lp<u32>(F_ST + 32);
        const f32x2 l2 = *lp<f32x2>(rl);
        SCHED();
        f32x4 d = {0.f, 0.f, 0.f, 0.f};
#pragma unroll
        for (int s = 0; s < 4; ++s) d = __builtin_amdgcn_mfma_f32_16x16x32_bf16(af[s], bu[s], d, 0, 0, 0);
        ls = l2.x + l2.y;
        if (__builtin_expect(rfl(rare) != 0u, 0)) {
            const f32x4 m0 = *lp<f32x4>(F_ST), m1 = *lp<f32x4>(F_ST + 16);
            const float M = max8(m0, m1);
#pragma unroll
            for (int k = 0; k < 4; ++k) d[k] *= ex2(((g & 1) ? m1[k] : m0[k]) - M);
            ls *= ex2(*lp<float>(F_ST + (u32)(lane >> 3) * 4) - M);
        }
        float c0 = d.x + d.y, c1 = d.z + d.w;
        PIN(c0); PIN(c1);
        float c = c0 + c1, c2 = c0 + c1;
        PIN(c); PIN(c2);
        c = red16(c, c2);
        *lp<u16>(cw) = (u16)pack2(c, c);
    }
    barrier();
    STAMP(13);
    {
        bf16x8 af[4];
#pragma unroll
        for (int s = 0; s < 4; ++s) af[s] = *lp<bf16x8>(cr + s * 64);
        ls += dpp<0x128>(ls); ls += dpp<0x124>(ls); ls += dpp<0x122>(ls); ls += dpp<0x121>(ls);
        ls = red16(ls, ls); ls = red32(ls, ls);
        ls = __builtin_amdgcn_rcpf(ls);
        v4u r = {0u, 0u, tlo, thi};
        PIN(ls); PIN(r);
        SCHED();
        f32x4 d[4];
#pragma unroll
        for (int t = 0; t < 4; ++t) d[t] = (f32x4){0.f, 0.f, 0.f, 0.f};
#pragma unroll
        for (int s = 0; s < 4; ++s) {
#pragma unroll
            for (int t = 0; t < 4; ++t) d[t] = __builtin_amdgcn_mfma_f32_16x16x32_bf16(af[s], bo[4 * t + s], d[t], 0, 0, 0);
            SCHED();
        }
        r.x = asu(((g & 2) ? ((g & 1) ? d[3].x : d[2].x) : ((g & 1) ? d[1].x : d[0].x)) * ls);
        r.y = dppu<0x128>(r.x);
        asm volatile("s_mov_b64 exec, %3\n\tglobal_store_dwordx4 %0, %1, %2 sc1\n\ts_mov_b64 exec, -1\n\ts_nop 0" :: "v"(op), "v"(r), "s"(ws), "s"(0x00ff00ff00ff00ffull) : "memory");
    }
    STAMP(11);
    asm volatile("" : "+s"(tc1));
    if (wave == 2 && lane < 8) st_sc(ws, he, (v4u){(u32)tc1, 0u, tlo, thi});
    if (b == 0 && tid == 64) st_sc2(ws, WS_EPOCH, (v2u){tlo, thi});
    if (is_red) {
        const f32x2 s = reduce16<8, true>(ws, piece(WS_OUTP, 16384, rid), tlo, thi, wave, lane, F_RD, tk2 + max((u32)D_T, (u32)tc1 - tk2 + D_DELAY), budget);
        const float x10 = xv0 + ga0 * s.x, x11 = xv1 + ga1 * s.y;
        if (wave == 0) st_sc(ws, xo, (v4u){asu(x10), asu(x11), tlo, thi});
        STAMP(12);
        u32 oa = (rid * 8 + (u32)lane) * 4u;
        PIN(oa);
        const f32x2 s2 = reduce16<6, false>(ws, piece(WS_YP, 16384, rid), tlo, thi, wave, lane, F_RV, now32() + F_DELAY, budget);
        if (tid < 8) asm volatile("global_store_dword %0, %1, %2" :: "v"(oa), "v"(pack2(x10 + gb0 * s2.x, x11 + gb1 * s2.y)), "s"(P.out) : "memory");
    }
    STAMP(15);
    STAMP_OUT();
}

extern "C" void kernel_launch(void* const* d_in, const int* in_sizes, int n_in, void* d_out, int out_size, void* d_ws, size_t ws_size, hipStream_t stream) {
    Params P;
    P.x = (const u16*)d_in[0]; P.ckv = (const u16*)d_in[1]; P.wq = (const u16*)d_in[2]; P.wuk = (const u16*)d_in[3];
    P.wuv = (const u16*)d_in[4]; P.wo = (const u16*)d_in[5]; P.wg = (const u16*)d_in[7]; P.wu = (const u16*)d_in[8];
    P.wd = (const u16*)d_in[9]; P.c = (const u16*)d_in[10]; P.wmod = (const u16*)d_in[11];
    P.out = (u16*)d_out; P.ws = (char*)d_ws + WS_BASE(ws_size);
    (void)hipFuncSetAttribute((const void*)fused_kernel, hipFuncAttributeMaxDynamicSharedMemorySize, LDS_BYTES);
    fused_kernel<<<256, 512, LDS_BYTES, stream>>>(P);
}
```

```cpp
#include <hip/hip_runtime.h>

typedef unsigned u32;
typedef unsigned long long u64;
typedef unsigned short u16;
typedef u32 v4u __attribute__((ext_vector_type(4)));
typedef u32 v2u __attribute__((ext_vector_type(2)));
typedef u32 v8u __attribute__((ext_vector_type(8)));
typedef u32 v16u __attribute__((ext_vector_type(16)));
typedef float f32x2 __attribute__((ext_vector_type(2)));
typedef float f32x4 __attribute__((ext_vector_type(4)));
typedef float f32x16 __attribute__((ext_vector_type(16)));
typedef short bf16x8 __attribute__((ext_vector_type(8)));
typedef short s16x4 __attribute__((ext_vector_type(4)));
typedef __bf16 bf16x2_t __attribute__((ext_vector_type(2)));
#define LAS __attribute__((address_space(3)))
#define DI __device__ __forceinline__

#define CFG(name, val) constexpr int name = val
CFG(BUDGET, 1 << 20);
CFG(POLL_SLEEP, 4);
CFG(HINT_SLEEP, 4);
#define PACING(X) \
    X(T_OPEN0, 50)        \
    X(T_OPEN1, 70)        \
    X(A_FIRST, 185)        \
    X(A_OWN, 60)        \
    X(ST_LOOK, 110)        \
    X(SKEW_MAX, 45) \
    X(A2_T, 165)        \
    X(A2_DELAY, 45)        \
    X(Q_FIRST, 360)        \
    X(Q_STEP, 40)        \
    X(Q_DELAY, 140)        \
    X(Q_PRE, 75)        \
    X(T_OWN, 40)        \
    X(O_STEP, 35) \
    X(T_MID, 220)        \
    X(U_GAP, 65)        \
    X(U_STEP, 30) \
    X(FC_A, 203)        \
    X(FC_B, 342)        \
    X(FC_C, 62)        \
    X(D_T, 203)        \
    X(D_DELAY, 78)        \
    X(E_T, 283)        \
    X(E_DELAY, 160)        \
    X(F_DELAY, 0)        \
    X(G_T, 70)        \
    X(H_MIN, 18) \
    X(G_2, 80)
#define X_(n, v) constexpr int n = v;
PACING(X_)
#define KD 10
DI u32 ktile(u32 s, u32 t) { return s * 262144u + t * 16384u; }
constexpr int HW = 5;
#define QD 2
#define LAZY 64.f
#define CODE_STR 7168
#define WS_BASE(sz) (((sz) - (32u << 20)) & ~(size_t)((2u << 20) - 1))

constexpr u32 WS_EPOCH = 0;
DI u32 piece(u32 base, u32 size, u32 i) { return base + i * size; }
#define CH 262144u
constexpr u32 WS_ST = 1024;
constexpr u32 WS_QH = 2048, NQH = 6;
constexpr u32 WS_QCHK = 8192;
constexpr u32 WS_HP = CH;
constexpr u32 WS_QP = 2 * CH;
constexpr u32 WS_QABS = 3 * CH;
constexpr u32 WS_L = 25u << 18;
constexpr u32 WS_ML = WS_L;
constexpr u32 WS_X1 = WS_L + 65536;
constexpr u32 WS_SC = WS_L + 98304;
constexpr u32 HSTR = 1152, WS_HC = WS_L + 106496;
constexpr u32 WS_FC = WS_SC + 4096;
constexpr u32 WS_HE = WS_HC + 512;
constexpr u32 WS_OUTP = WS_L + 262144;
constexpr u32 WS_YP = WS_L + 786432;
constexpr u32 WS_CHK = WS_L + 1310720;
constexpr u32 WS_PAY = 2u << 20;
constexpr u32 PHD = 4 * 128 * 256 + 4096;
constexpr u32 WS_TL = 16u << 20;

constexpr u32 L_FL = 114688, L_P = L_FL + 1024, L_IMG = 131072;
constexpr u32 slot_no(u32 t) { return (u32)((0x9543621054362105ull >> (4 * t)) & 15u); }
constexpr u32 slot(int t) { return slot_no((u32)t) * 16384u; }
constexpr u32 vwin(int t) { return (slot_no((u32)t) < 3 ? 0u : slot_no((u32)t) < 7 ? 3u : 6u) * 16384u; }
static_assert(slot(15) == L_IMG + 16384, "");
constexpr u32 C_LAND = 0;
constexpr u32 C_GATE = 256;
constexpr u32 C_Z = 336, C_P = 340, C_L = 352, C_BAD = 356, C_TZ = 368;
constexpr u32 L_M = L_FL + 384, L_L = L_FL + 512;
constexpr u32 L_W = 0;
constexpr u32 L_W1 = 65536;
constexpr u32 L_WD = 98304;
constexpr u32 L_H2 = 32768;
constexpr u32 L_GU = 36864;
constexpr u32 F_QP = 0;
constexpr u32 F_WO = 0;
constexpr u32 F_UV = 131072;
constexpr u32 F_A = 0;
constexpr u32 F_ST = 4096;
constexpr u32 F_GO = F_ST + 40;
constexpr u32 F_CX = 4352;
constexpr u32 F_LS = 5120;
constexpr u32 F_ML = 6144;
constexpr u32 F_RD = 8192, F_RV = 9216;
constexpr u32 F_JUNK = 16384;
constexpr u32 LDS_BYTES = 163840;

struct Params {
    const u16 *wmod, *c, *x;
    char* ws;
    const u16 *wq, *ckv, *wuk, *wo, *wuv, *wg, *wu, *wd;
    u16* out;
};

DI float asf(u32 v) { return __builtin_bit_cast(float, v); }
DI u32 asu(float v) { return __builtin_bit_cast(u32, v); }
DI float bflo(u32 v) { return asf(v << 16); }
DI float bfhi(u32 v) { return asf(v & 0xffff0000u); }
DI u32 pack2(float a, float b) { f32x2 v = {a, b}; return __builtin_bit_cast(u32, __builtin_convertvector(v, bf16x2_t)); }
DI float dot2(u32 w, u32 x, float acc) { return __builtin_amdgcn_fdot2_f32_bf16(__builtin_bit_cast(bf16x2_t, w), __builtin_bit_cast(bf16x2_t, x), acc, false); }
DI void pair_rows(v4u& x, v4u& y) {
#pragma unroll
    for (int k = 0; k < 4; ++k) { const u32 a = x[k], b = y[k]; x[k] = __builtin_amdgcn_perm(b, a, 0x05040100u); y[k] = __builtin_amdgcn_perm(b, a, 0x07060302u); }
}
template <int CTRL> DI u32 dppu(u32 v) { return (u32)__builtin_amdgcn_update_dpp(0, (int)v, CTRL, 0xf, 0xf, true); }
template <int CTRL> DI float dpp(float v) { return asf(dppu<CTRL>(asu(v))); }
DI float red32(float a, float b) { auto r = __builtin_amdgcn_permlane32_swap(asu(a), asu(b), false, false); return asf((u32)r[0]) + asf((u32)r[1]); }
DI float red16(float a, float b) { auto r = __builtin_amdgcn_permlane16_swap(asu(a), asu(b), false, false); return asf((u32)r[0]) + asf((u32)r[1]); }
DI float max3(float a, float b, float c) { return __builtin_fmaxf(__builtin_fmaxf(a, b), c); }
DI float max4(f32x4 a) { return max3(max3(a.x, a.y, a.z), a.w, a.x); }
DI float max32(float a) { auto r = __builtin_amdgcn_permlane32_swap(asu(a), asu(a), false, false); return __builtin_fmaxf(asf((u32)r[0]), asf((u32)r[1])); }
DI float max16(float a) { auto r = __builtin_amdgcn_permlane16_swap(asu(a), asu(a), false, false); return __builtin_fmaxf(asf((u32)r[0]), asf((u32)r[1])); }
DI float max8(const f32x4& a, const f32x4& b) { return max3(max3(max3(a.x, a.y, a.z), a.w, b.x), max3(b.y, b.z, b.w), a.x); }
DI float sum4(f32x4 a) { return (a.x + a.y) + (a.z + a.w); }
DI u32 perm_lo(u32 a, u32 b) { return __builtin_amdgcn_perm(b, a, 0x05040100u); }
DI u32 perm_hi(u32 a, u32 b) { return __builtin_amdgcn_perm(b, a, 0x07060302u); }
DI u32 rfl(u32 v) { return (u32)__builtin_amdgcn_readfirstlane((int)v); }
DI void both16(u32 a, u32& e, u32& o) { auto r = __builtin_amdgcn_permlane16_swap(a, a, false, false); e = (u32)r[0]; o = (u32)r[1]; }
DI void both32(u32 a, u32& l, u32& h) { auto r = __builtin_amdgcn_permlane32_swap(a, a, false, false); l = (u32)r[0]; h = (u32)r[1]; }
DI float rowsum(float v) { v += dpp<0x128>(v); v += dpp<0x124>(v); v += dpp<0x122>(v); v += dpp<0x121>(v); return v; }
DI float ex2(float v) { return __builtin_amdgcn_exp2f(v); }
DI u32 neq3(u32 a, u32 b, u32 t) { return __builtin_amdgcn_bitop3_b32(a, b, t, 0x7e); }
DI u32 mix4(v4u v) { return (v.w << 9) + ((v.z << 6) + ((v.y << 3) + v.x)); }
DI u64 ballot(bool c) { return __builtin_amdgcn_ballot_w64(c); }

DI v4u ld_nt(const void* base, u32 off) { v4u r; asm volatile("global_load_dwordx4 %0, %1, %2 nt" : "=v"(r) : "v"(off), "s"(base) : "memory"); return r; }
DI u32 ld_u32(const void* base, u32 off) { u32 r; asm volatile("global_load_dword %0, %1, %2" : "=v"(r) : "v"(off), "s"(base) : "memory"); return r; }
DI u32 ld_u32_nt(const void* base, u32 off) { u32 r; asm volatile("global_load_dword %0, %1, %2 nt" : "=v"(r) : "v"(off), "s"(base) : "memory"); return r; }
DI u32 ld_u16(const void* base, u32 off) { u32 r; asm volatile("global_load_ushort %0, %1, %2" : "=v"(r) : "v"(off), "s"(base) : "memory"); return r; }
DI void st_sc(void* base, u32 off, v4u v) { asm volatile("global_store_dwordx4 %0, %1, %2 sc1\n\ts_nop 1" :: "v"(off), "v"(v), "s"(base) : "memory"); }
DI void st_sc_keep(void* base, u32 off, v4u v) { asm volatile("global_store_dwordx4 %0, %1, %2 sc1" :: "v"(off), "v"(v), "s"(base) : "memory"); }
DI void st_sc2(void* base, u32 off, v2u v) { asm volatile("global_store_dwordx2 %0, %1, %2 sc1\n\ts_nop 1" :: "v"(off), "v"(v), "s"(base) : "memory"); }
DI void dma_nt(const void* base, u32 off, u32 lds_dst) { asm volatile("s_mov_b32 m0, %1\n\ts_nop 0\n\tglobal_load_lds_dwordx4 %0, %2 nt" :: "v"(off), "s"(lds_dst), "s"(base) : "memory"); }
DI void dma_pl(const void* base, u32 off, u32 lds_dst) { asm volatile("s_mov_b32 m0, %1\n\ts_nop 0\n\tglobal_load_lds_dwordx4 %0, %2" :: "v"(off), "s"(lds_dst), "s"(base) : "memory"); }
DI void dma_sc(const void* base, u32 off, u32 lds_dst) { asm volatile("s_mov_b32 m0, %1\n\ts_nop 0\n\tglobal_load_lds_dwordx4 %0, %2 sc1" :: "v"(off), "s"(lds_dst), "s"(base) : "memory"); }
DI void ld_nt_into(v4u& r, const void* base, u32 off) { asm volatile("global_load_dwordx4 %0, %1, %2 nt" : "+v"(r) : "v"(off), "s"(base) : "memory"); }
DI void ld_sc_into(v4u& r, const void* base, u32 off) { asm volatile("global_load_dwordx4 %0, %1, %2 sc1" : "+v"(r) : "v"(off), "s"(base) : "memory"); }
#define VMWAIT(n) asm volatile("s_waitcnt vmcnt(%0)" :: "i"(n) : "memory")
#define LGKM0() asm volatile("s_waitcnt lgkmcnt(0)" ::: "memory")
#define PIN(x) asm volatile("" : "+v"(x))
#define UNDEF(x) asm volatile("" : "=v"(x))
#define SCHED() __builtin_amdgcn_sched_barrier(0)
DI void barrier() { LGKM0(); __builtin_amdgcn_s_barrier(); asm volatile("" ::: "memory"); }

template <typename T> DI LAS T* lp(u32 off) { return (LAS T*)(size_t)off; }
DI bf16x8 tr2(u32 a, u32 b) {
    const s16x4 lo = __builtin_amdgcn_ds_read_tr16_b64_v4i16(lp<s16x4>(a)), hi = __builtin_amdgcn_ds_read_tr16_b64_v4i16(lp<s16x4>(b));
    return __builtin_shufflevector(lo, hi, 0, 1, 2, 3, 4, 5, 6, 7);
}

#define STAMP(i) do {} while (0)
#define CYC0() do {} while (0)
#define STAMPC(i) do {} while (0)
#define STAMP_OUT() do {} while (0)

#define CDECL() do {} while (0)
#define CS(i) do {} while (0)
#define COUT(n) do {} while (0)
template <u32 S> DI void pass(const char* ws, u32 off, v4u (&v)[1]) {
    asm volatile("global_load_dwordx4 %0, %1, %2 sc1\n\ts_waitcnt vmcnt(0)" : "=v"(v[0]) : "v"(off), "s"(ws) : "memory");
}
template <u32 S> DI void pass(const char* ws, u32 off, v4u (&v)[2]) {
    asm volatile("global_load_dwordx4 %0, %2, %3 sc1\n\tglobal_load_dwordx4 %1, %2, %4 sc1\n\ts_waitcnt vmcnt(0)" : "=&v"(v[0]), "=&v"(v[1]) : "v"(off), "s"(ws), "s"(ws + S) : "memory");
}
template <u32 S> DI void pass(const char* ws, u32 off, v4u (&v)[4]) {
    asm volatile("global_load_dwordx4 %0, %4, %5 sc1\n\tglobal_load_dwordx4 %1, %4, %5 offset:%6 sc1\n\tglobal_load_dwordx4 %2, %4, %5 offset:%7 sc1\n\tglobal_load_dwordx4 %3, %4, %5 offset:%8 sc1\n\ts_waitcnt vmcnt(0)"
        : "=&v"(v[0]), "=&v"(v[1]), "=&v"(v[2]), "=&v"(v[3]) : "v"(off), "s"(ws), "i"(S), "i"(2 * S), "i"(3 * S) : "memory");
}
template <int N, u32 STRIDE, int SLP = POLL_SLEEP> DI void gather(const char* ws, u32 off, u32 tlo, u32 thi, v4u (&v)[N], int& budget) {
#pragma clang loop unroll(disable)
    do {
        pass<STRIDE>(ws, off, v);
        u32 bad = 0;
#pragma unroll
        for (int k = 0; k < N; ++k) bad |= (v[k].z ^ tlo) | (v[k].w ^ thi);
        if (ballot(bad != 0u) == 0ull) break;
        __builtin_amdgcn_s_sleep(SLP);
    } while (--budget > 0);
}
#define PSLP "0"
template <bool G8> DI void poll2(v4u& g0, v4u& g1, u32 o0, const char* b0, u32 o1, const char* b1, u32 tlo, u32 thi) {
    u32 n, t, u;
#define POLL2(A, B, C, D) asm volatile("s_mov_b32 %2, 0x200000\n.Lp%=:\n\ts_sleep " PSLP "\n\tglobal_load_dwordx4 v[232:235], %5, %6 sc1\n\tglobal_load_dwordx4 v[236:239], %7, %8 sc1\n\ts_sub_u32 %2, %2, 1\n\ts_waitcnt vmcnt(0)\n\t" \
        "v_bitop3_b32 %3, " A ", " B ", %9 bitop3:0x7e\n\tv_bitop3_b32 %4, " C ", " D ", %10 bitop3:0x7e\n\tv_or_b32 %3, %3, %4\n\tv_cmp_ne_u32 vcc, 0, %3\n\ts_cbranch_scc1 .Lq%=\n\ts_cbranch_vccnz .Lp%=\n.Lq%=:" \
        : "=&{v[232:235]}"(g0), "=&{v[236:239]}"(g1), "=&s"(n), "=&v"(t), "=&v"(u) : "v"(o0), "s"(b0), "v"(o1), "s"(b1), "s"(tlo), "s"(G8 ? tlo : thi) : "memory", "vcc", "scc")
    if (G8) POLL2("v233", "v235", "v237", "v239"); else POLL2("v234", "v238", "v235", "v239");
}
DI void flag_set(u32 fa, u32 off) {
    asm volatile("s_mov_b64 exec, 1\n\tds_write_b8_d16_hi %0, %0 offset:%1\n\ts_mov_b64 exec, -1" :: "v"(fa), "i"(off) : "memory");
}
DI void gate_add(u32 ga, u32 v, u64 lanes) {
    asm volatile("s_mov_b64 exec, %2\n\tds_add_u32 %0, %1\n\ts_mov_b64 exec, -1" :: "v"(ga), "v"(v), "s"(lanes) : "memory");
}
DI void gate_or(u32 ga, u32 v, u64 lanes) {
    asm volatile("s_mov_b64 exec, %2\n\tds_or_b32 %0, %1\n\ts_mov_b64 exec, -1" :: "v"(ga), "v"(v), "s"(lanes) : "memory");
}
DI u32 flag_get(u32 off) { return *(volatile LAS u32*)lp<u32>(L_FL + off); }
DI void wait4(u32 fb, u32 off) {
    u32 v, n;
    asm volatile("s_mov_b32 %1, 0x400000\n.Lw%=:\n\tds_read_b32 %0, %2 offset:%3\n\ts_sub_u32 %1, %1, 1\n\ts_waitcnt lgkmcnt(0)\n\tv_cmp_ne_u32 vcc, 0x1010101, %0\n\ts_cbranch_scc1 .Le%=\n\ts_cbranch_vccnz .Lw%=\n.Le%=:"
        : "=&v"(v), "=&s"(n) : "v"(fb), "i"(off) : "memory", "vcc", "scc");
}
#define SPIN_HEAD "s_mov_b32 %[n], 0x400000\n.Lw%=:\n\t"
#define SPIN_TAIL(cmp) "s_sub_u32 %[n], %[n], 1\n\ts_waitcnt lgkmcnt(0)\n\t" cmp ", %[v]\n\ts_cbranch_scc1 .Le%=\n\ts_cbranch_vccnz .Lw%=\n.Le%=:"
DI void land_wait(u32 fl, u32 off) {
    u32 v, n;
    asm volatile(SPIN_HEAD "s_sleep 3\n\tds_read_b32 %[v], %[b] offset:%[o]\n\t" SPIN_TAIL("v_cmp_eq_u32 vcc, 0") : [v] "=&v"(v), [n] "=&s"(n) : [b] "v"(fl), [o] "i"(off) : "memory", "vcc", "scc");
}
DI void gate_wait(u32 v, u32 fg, u32 off) {
    u32 n;
    asm volatile("v_cmp_ne_u32 vcc, 0, %[v]\n\ts_cbranch_vccz .Le%=\n\t" SPIN_HEAD "ds_read_u8 %[v], %[b] offset:%[o]\n\t" SPIN_TAIL("v_cmp_ne_u32 vcc, 0") : [v] "+v"(v), [n] "=&s"(n) : [b] "v"(fg), [o] "i"(off) : "memory", "vcc", "scc");
}
DI bf16x8 wait1p(u32 fb, u32 off, u32 pa, u32 poff) {
    u32 v, n;
    bf16x8 p;
    asm volatile(SPIN_HEAD "ds_read_u8 %[v], %[b] offset:%[o]\n\tds_read_b128 %[p], %[a] offset:%[q]\n\t" SPIN_TAIL("v_cmp_ne_u32 vcc, 1")
        : [v] "=&v"(v), [p] "=&v"(p), [n] "=&s"(n) : [b] "v"(fb), [o] "i"(off), [a] "v"(pa), [q] "i"(poff) : "memory", "vcc", "scc");
    return p;
}
DI void wait4p(u32 fb, u32 off, u32 pa, u32 poff, bf16x8 (&p)[4]) {
    u32 v, n;
    asm volatile(SPIN_HEAD "ds_read_b32 %[v], %[b] offset:%[o]\n\tds_read_b128 %[p0], %[a] offset:%[q0]\n\tds_read_b128 %[p1], %[a] offset:%[q1]\n\tds_read_b128 %[p2], %[a] offset:%[q2]\n\tds_read_b128 %[p3], %[a] offset:%[q3]\n\t"
        SPIN_TAIL("v_cmp_ne_u32 vcc, 0x1010101")
        : [v] "=&v"(v), [p0] "=&v"(p[0]), [p1] "=&v"(p[1]), [p2] "=&v"(p[2]), [p3] "=&v"(p[3]), [n] "=&s"(n)
        : [b] "v"(fb), [o] "i"(off), [a] "v"(pa), [q0] "i"(poff), [q1] "i"(poff + 1024), [q2] "i"(poff + 2048), [q3] "i"(poff + 3072) : "memory", "vcc", "scc");
}
template <u32 KO> DI void land_wait_k(u32 fl, u32 off, u32 a0, u32 a1, u32 a2, u32 a3, bf16x8& f0, bf16x8& f1, bf16x8& f2, bf16x8& f3) {
    u32 v, n;
    asm volatile(SPIN_HEAD "ds_read_b32 %[v], %[b] offset:%[o]\n\tds_read_b128 %[p0], %[a0] offset:%[k]\n\tds_read_b128 %[p1], %[a1] offset:%[k]\n\tds_read_b128 %[p2], %[a2] offset:%[k]\n\tds_read_b128 %[p3], %[a3] offset:%[k]\n\t"
        SPIN_TAIL("v_cmp_eq_u32 vcc, 0")
        : [v] "=&v"(v), [p0] "=&v"(f0), [p1] "=&v"(f1), [p2] "=&v"(f2), [p3] "=&v"(f3), [n] "=&s"(n)
        : [b] "v"(fl), [o] "i"(off), [a0] "v"(a0), [a1] "v"(a1), [a2] "v"(a2), [a3] "v"(a3), [k] "i"(KO) : "memory", "vcc", "scc");
}
DI void dma_a(const char* src, const u32 (&od)[4], u32 dst) {
    asm volatile("s_mov_b32 m0, %3\n\ts_nop 0\n\tglobal_load_lds_dwordx4 %0, %2 nt\n\tglobal_load_lds_dwordx4 %1, %2 offset:1024 nt" :: "v"(od[0]), "v"(od[1]), "s"(src), "s"(dst) : "memory");
}
DI void dma_b(const char* src, const u32 (&od)[4], u32 dst, u64 pc, u32 flag) {
    asm volatile("s_mov_b32 m0, %4\n\ts_nop 0\n\tglobal_load_lds_dwordx4 %1, %3 offset:2048 nt\n\tglobal_load_lds_dwordx4 %2, %3 offset:3072 nt\n\t"
        "s_mov_b32 m0, %6\n\ts_mov_b64 exec, 1\n\tglobal_load_lds_dword %0, %5\n\ts_mov_b64 exec, -1"
        :: "v"(od[0]), "v"(od[2]), "v"(od[3]), "s"(src), "s"(dst), "s"(pc), "s"(flag) : "memory");
}
static_assert(C_LAND == 0, "");
static_assert(((L_FL >> 16) & 0xff) == 1, "flag_set writes bits 23:16 of the address");
template <int K, bool PRE = false> DI u32 hint_wait(const char* ws, u32 oh, u32 tlo, int& budget, v4u h0 = v4u()) {
    v4u h[1] = {h0};
    if (!PRE || __builtin_popcount((u32)ballot(h[0].z == tlo) & 0xffu) < K) {
#pragma clang loop unroll(disable)
        do {
            pass<0>(ws, oh, h);
            if (__builtin_popcount((u32)ballot(h[0].z == tlo) & 0xffu) >= K) break;
            __builtin_amdgcn_s_sleep(HINT_SLEEP);
        } while (--budget > 0);
    }
    const u32 now = (u32)__builtin_amdgcn_s_memrealtime();
    u32 age = h[0].z == tlo ? now - h[0].x : 0xffffu;
    age = age > 0xffffu ? 0u : age;
    age = min(age, dppu<0xB1>(age));
    age = min(age, dppu<0x4E>(age));
    age = min(age, dppu<0x124>(age));
    return now - rfl(age);
}
DI u32 now32() { return (u32)__builtin_amdgcn_s_memrealtime(); }
DI void until(u32 t) { while ((int)(now32() - t) < 0) __builtin_amdgcn_s_sleep(1); }
DI u32 time_base(const char* p, u32 t0) {
    v16u h;
    asm volatile("s_load_dwordx16 %0, %1, 0x0\n\ts_waitcnt lgkmcnt(0)" : "=s"(h) : "s"(p) : "memory");
    u32 m = 0, bad = 0;
#pragma unroll
    for (int k = 0; k < 8; ++k) { m = max(m, h[2 * k] - t0 + 150u); bad |= h[2 * k] ^ ~h[2 * k + 1]; }
    return (bad != 0u || m >= 300u) ? t0 + SKEW_MAX : t0 - 150u + m;
}
struct QTimer { u32 t, n; };
DI bool q_due(QTimer& q, const char* ws, u32 tlo) {
    const u32 now = now32();
    if ((int)(now - q.t) < 0) return false;
    if (q.n >= NQH) return true;
    v16u h0, h1;
    asm volatile("s_load_dwordx16 %0, %2, 0x0\n\ts_load_dwordx16 %1, %2, 0x40\n\ts_waitcnt lgkmcnt(0)" : "=&s"(h0), "=&s"(h1) : "s"(ws + WS_QH + q.n * 128u) : "memory");
    u32 m = 0xffffffffu, bad = 0;
#pragma unroll
    for (int k = 0; k < 8; ++k) { m = min(m, min(now + 1000u - h0[2 * k], now + 1000u - h1[2 * k])); bad |= (h0[2 * k] ^ h0[2 * k + 1] ^ tlo) | (h1[2 * k] ^ h1[2 * k + 1] ^ tlo); }
    if (bad == 0u) { q.n = NQH; q.t = now + 1000u - max(m, 900u) + Q_DELAY - Q_PRE; } else { ++q.n; q.t = now + Q_STEP; }
    return false;
}
DI void look_times(const char* ws, int lane, u32 tlo, int& budget, u32& t1, u32& t2) {
    v4u fc[1];
#pragma clang loop unroll(disable)
    do {
        pass<0>(ws, WS_FC + (u32)lane * 16u, fc);
        if (ballot(neq3(fc[0].y, fc[0].w, tlo)) == 0ull) break;
        until(now32() + 30);
    } while (--budget > 0);
    const u32 now = now32(), x = min(fc[0].x + 1000u - now, 2000u), z = min(fc[0].z + 1000u - now, 2000u);
    u32 m = max(x, z), a = x + z, e, od;
    m = max(m, dppu<0x128>(m)); m = max(m, dppu<0x124>(m)); m = max(m, dppu<0x122>(m)); m = max(m, dppu<0x121>(m));
    a += dppu<0x128>(a); a += dppu<0x124>(a); a += dppu<0x122>(a); a += dppu<0x121>(a);
    both16(m, e, od); both32(max(e, od), e, od);
    m = rfl(max(e, od));
    both16(a, e, od); both32(e + od, e, od);
    a = (rfl(e + od) >> 7) + FC_C;
    t1 = m < 2000u ? now - 1000u + a : now;
    t2 = m < 2000u ? now - 1000u + max(m, a) : now;
}
DI u32 swz(u32 row) { return ((row & 3u) << 2) | ((row >> 2) & 3u); }

#define EPOCH() do { asm volatile("s_load_dwordx2 %0, %1, 0x0\n\ts_waitcnt lgkmcnt(0)" : "=s"(e64) : "s"(ws) : "memory"); ++e64; } while (0)
DI void wave_sum2(float s1, float s2, float& t1, float& t2) {
    float z = red32(s1, s2);
    z = red16(z, z); z = rowsum(z);
    u32 a, b;
    both32(asu(z), a, b);
    t1 = asf(a); t2 = asf(b);
}
DI void ln_stats(const v4u& xa, float& mean, float& rstd) {
    const float pil = asf(rfl(xa[0]) << 16);
    float s1 = 0.f, s2 = 0.f;
#pragma unroll
    for (int k = 0; k < 4; ++k) { const float a = bflo(xa[k]) - pil, c = bfhi(xa[k]) - pil; s1 += a; s2 = __builtin_fmaf(a, a, s2); s1 += c; s2 = __builtin_fmaf(c, c, s2); }
    float t1, t2;
    wave_sum2(s1, s2, t1, t2);
    const float dm = t1 * (1.f / 512.f);
    mean = pil + dm;
    rstd = __builtin_amdgcn_rsqf(__builtin_fmaxf(t2 * (1.f / 512.f) - dm * dm, 0.f) + 1e-5f);
}
DI float sigm(float v) { return __builtin_amdgcn_rcpf(1.f + ex2(v * -1.4426950408889634f)); }
DI float silu(float v) { return v * sigm(v); }

template <int NW2, bool S8> DI f32x2 reduce16(const char* ws, u32 base, u32 tlo, u32 thi, int wave, int lane, u32 lds_red, u32 tgo, int& budget) {
    u32 la = lds_red + (u32)(wave * 16 + (S8 ? (lane & 7) + (lane >> 5) * 8 : (lane & 7) * 2 + (lane >> 5))) * 4, ra = lds_red + (u32)lane * 8, go = base + (u32)(wave * 64 + lane) * 16u;
    const bool two = NW2 == 8 || wave < NW2;
    const char* w2 = ws + (two ? 8192 : 0);
    float f2 = two ? 1.f : 0.f;
    PIN(la); PIN(ra); PIN(go);
    asm volatile("" : "+s"(w2), "+v"(f2));
    until(tgo);
    v4u g0, g1;
    poll2<false>(g0, g1, go, ws, go, w2, tlo, thi);
    float z = red32(__builtin_fmaf(asf(g1.x), f2, asf(g0.x)), __builtin_fmaf(asf(g1.y), f2, asf(g0.y)));
    z = red16(z, z); z += dpp<0x128>(z);
    *lp<float>(la) = z;
    barrier();
    f32x2 s = {0.f, 0.f};
    if (wave == 0) {
        s = *lp<f32x2>(ra);
        float t = red32(s.x, s.y);
        t = red16(t, t); t += dpp<0x128>(t);
        u32 a, c;
        both32(asu(t), a, c);
        s = (f32x2){asf(a), asf(c)};
    }
    return s;
}

#define HE_K 8
DI u32 mlp_id(int b) { const int xc = b & 7, yy = (b >> 3) - 16; return (u32)(((yy >> 2) * 8 + xc) * 4 + (yy & 3)); }
DI void mlp_part(const Params& P, int b, u32 w, int lane, u32 tlo, u32 thi, int& budget, u32& tl) {
    char* const ws = P.ws;
    const u32 gu_id = mlp_id(b), i15 = lane & 15, g = lane >> 4;
    u32 wo = (128 * w + ((u32)lane >> 1)) * 3584u + gu_id * 32u + ((u32)lane & 1) * 16u, wdo = (gu_id * 16 + g) * 1024u + w * 256u + i15 * 16u;
    PIN(wo); PIN(wdo);
    const u32 q = gu_id & 3;
    u32 ha = L_H2 + w * 256 + (((u32)lane - 16 * q) & 63) * 4, hr = L_H2 + w * 256 + g * 16 + min(i15 & 3, 2u) * 1088, ga = L_GU + w * 512 + i15 * 16 + g * 4096, gr = L_GU + (u32)lane * 16;
    u32 sa = L_GU + 2048 + (u32)(lane >> 5) * 16 + w * 4, heo = WS_HE + (u32)(b - 128) * HSTR + (u32)(lane & 7) * 16u, px = piece(WS_X1, 4096, (u32)(b & 7)) + (u32)(lane & 7) * 16u;
    PIN(sa); PIN(heo); PIN(px);
    u32 so = piece(WS_YP, 16384, 8 * w + (u32)(lane >> 3)) + (gu_id * 8 + (lane & 7)) * 16, sco = WS_SC + (64 * w + (u32)lane) * 16u;
    u32 ta = L_W + w * 8192u + (8 * g + (i15 >> 2)) * 32 + (lane & 3) * 8, xo = piece(WS_X1, 4096, (u32)(b & 7)) + w * 1024u + (u32)lane * 16u;
    PIN(ha); PIN(hr); PIN(ga); PIN(gr); PIN(so); PIN(sco); PIN(ta); PIN(xo);
    barrier();
    STAMP(10);
    u32 wdr[16], tk1, tk2;
    {
        look_times(ws, lane, tlo, budget, tk1, tk2);
        const u32 tg = tk2 + G_T;
        const u32 pg = (w * 128 + q * 32 + ((u32)lane >> 3)) * 3584u + (gu_id >> 2) * 128u + ((u32)lane & 7) * 16u;
        until(tg);
#pragma unroll
        for (int k = 0; k < 4; ++k) { dma_pl(P.wg + k * 8 * 1792, pg, L_W1 + w * 8192u + k * 1024u); dma_pl(P.wu + k * 8 * 1792, pg, L_W1 + w * 8192u + 4096u + k * 1024u); }
#pragma unroll
        for (int k = 0; k < 4; ++k) dma_nt(P.wd + k * 2048, wdo, L_WD + w * 4096u + k * 1024u);
        VMWAIT(4);
        until(tg + G_2);
    }
#pragma unroll
    for (int s = 1; s < 4; ++s) { const u32 r = ((q + s) & 3) * 32 * 1792; dma_pl(P.wg + r, wo, L_W + w * 8192u + s * 1024u); dma_pl(P.wu + r, wo, L_W + w * 8192u + 4096u + s * 1024u); }
    bf16x8 bg[4], bp[4];
    {
        const u32 t1 = L_W1 + w * 8192u + (8 * g + (i15 >> 2)) * 128 + q * 32 + (lane & 3) * 8;
        bg[0] = tr2(t1, t1 + 512); bp[0] = tr2(t1 + 4096, t1 + 4608);
    }
    v4u mg[1], hh;
    UNDEF(mg[0]); UNDEF(hh);
    ld_sc_into(mg[0], ws, sco);
    ld_sc_into(hh, ws, heo);
    STAMP(2);
    VMWAIT(8);
#pragma unroll
    for (int j = 0; j < 16; j += 2) {
        const u32 a = *lp<u32>(L_WD + w * 4096u + j * 256u + (u32)lane * 4), bb = *lp<u32>(L_WD + w * 4096u + j * 256u + 256u + (u32)lane * 4);
        wdr[j] = perm_lo(a, bb); wdr[j + 1] = perm_hi(a, bb);
    }
#pragma unroll
    for (int j = 0; j < 16; ++j) PIN(wdr[j]);
    VMWAIT(0);
    PIN(mg[0]); PIN(hh);
    STAMP(3);
#pragma unroll
    for (int s = 1; s < 4; ++s) { bg[s] = tr2(ta + s * 1024, ta + s * 1024 + 128); bp[s] = tr2(ta + 4096 + s * 1024, ta + 4096 + s * 1024 + 128); }
    if (__builtin_expect(ballot(((mg[0].z ^ tlo) | (mg[0].w ^ thi)) != 0u) != 0ull, 0)) gather<1, 0>(ws, sco, tlo, thi, mg, budget);
    float cb0 = 1.f + bflo(mg[0].x), cb1 = 1.f + bfhi(mg[0].x);
    *lp<u32>(ha + 1088) = pack2(cb0, cb1);
    *lp<u32>(ha + 2176) = mg[0].y;
    PIN(cb0); PIN(cb1);
    bf16x8 af[4];
    v4u yo = {0u, 0u, tlo, thi};
    PIN(yo);
#pragma unroll
    for (int s = 0; s < 4; ++s) { PIN(bg[s]); PIN(bp[s]); }
    STAMP(12);
    until(hint_wait<HE_K, true>(ws, heo, tlo, budget, hh) + E_DELAY);
    until(tk2 + E_T);
    STAMP(8);
    v4u xg, xp;
    poll2<false>(xg, xp, xo, ws, px, ws, tlo, thi);
    STAMP(13);
    float pil = asf(xp.x) + asf(xp.y);
    pil += dpp<0xB1>(pil); pil += dpp<0x4E>(pil); pil += dpp<0x141>(pil);
    float d0 = __builtin_fmaf(pil, -0.0625f, asf(xg.x)), d1 = __builtin_fmaf(pil, -0.0625f, asf(xg.y));
    PIN(d0); PIN(d1);
    {
        float e0 = d0 * cb0;
        PIN(e0);
        *lp<u32>(ha) = pack2(e0, d1 * cb1);
    }
    asm volatile("" ::: "memory");
#pragma unroll
    for (int s = 0; s < 4; ++s) af[s] = *lp<bf16x8>(hr + s * 64);
    SCHED();
    float z = red32(d0 + d1, __builtin_fmaf(d0, d0, d1 * d1));
    z = red16(z, z); z = rowsum(z);
    *lp<float>(sa) = z;
    SCHED();
    f32x4 dg = {0.f, 0.f, 0.f, 0.f}, du = {0.f, 0.f, 0.f, 0.f};
#pragma unroll
    for (int s = 0; s < 4; ++s) {
        dg = __builtin_amdgcn_mfma_f32_16x16x32_bf16(af[s], bg[s], dg, 0, 0, 0);
        du = __builtin_amdgcn_mfma_f32_16x16x32_bf16(af[s], bp[s], du, 0, 0, 0);
    }
    *lp<f32x4>(ga) = dg; *lp<f32x4>(ga + 256) = du;
    barrier();
    STAMP(11);
    const f32x4 q1 = *lp<f32x4>(L_GU + 2048), q2 = *lp<f32x4>(L_GU + 2064);
    const f32x4 p0 = *lp<f32x4>(gr), p1 = *lp<f32x4>(gr + 1024);
    const float dm = sum4(q1) * (1.f / 512.f);
    const float rstd = __builtin_amdgcn_rsqf(__builtin_fmaxf(sum4(q2) * (1.f / 512.f) - dm * dm, 0.f) + 1e-5f);
    float gx = p0.x + p1.x, gy = p0.y + p1.y;
    PIN(gx); PIN(gy);
    float s = rstd * (gx - dm * gy) + (p0.z + p1.z);
    s = red32(s, s);
    u32 gs, us;
    both16(asu(s), gs, us);
    const float gsum = asf(gs), usum = asf(us);
    const float act = gsum * usum * __builtin_amdgcn_rcpf(1.f + ex2(gsum * __builtin_fmaf(gsum * gsum, -0.044715f * 1.5957691216057308f * 1.4426950408889634f, -1.5957691216057308f * 1.4426950408889634f)));
    const u32 actp = pack2(act, dpp<0x101>(act));
    asm volatile("s_nop 1\n\t"
        "v_dot2c_f32_bf16_dpp %0, %2, %3 row_newbcast:0 row_mask:0xf bank_mask:0xf\n\tv_dot2c_f32_bf16_dpp %1, %2, %4 row_newbcast:0 row_mask:0xf bank_mask:0xf\n\t"
        "v_dot2c_f32_bf16_dpp %0, %2, %5 row_newbcast:2 row_mask:0xf bank_mask:0xf\n\tv_dot2c_f32_bf16_dpp %1, %2, %6 row_newbcast:2 row_mask:0xf bank_mask:0xf\n\t"
        "v_dot2c_f32_bf16_dpp %0, %2, %7 row_newbcast:4 row_mask:0xf bank_mask:0xf\n\tv_dot2c_f32_bf16_dpp %1, %2, %8 row_newbcast:4 row_mask:0xf bank_mask:0xf\n\t"
        "v_dot2c_f32_bf16_dpp %0, %2, %9 row_newbcast:6 row_mask:0xf bank_mask:0xf\n\tv_dot2c_f32_bf16_dpp %1, %2, %10 row_newbcast:6 row_mask:0xf bank_mask:0xf\n\t"
        "v_dot2c_f32_bf16_dpp %0, %2, %11 row_newbcast:8 row_mask:0xf bank_mask:0xf\n\tv_dot2c_f32_bf16_dpp %1, %2, %12 row_newbcast:8 row_mask:0xf bank_mask:0xf\n\t"
        "v_dot2c_f32_bf16_dpp %0, %2, %13 row_newbcast:10 row_mask:0xf bank_mask:0xf\n\tv_dot2c_f32_bf16_dpp %1, %2, %14 row_newbcast:10 row_mask:0xf bank_mask:0xf\n\t"
        "v_dot2c_f32_bf16_dpp %0, %2, %15 row_newbcast:12 row_mask:0xf bank_mask:0xf\n\tv_dot2c_f32_bf16_dpp %1, %2, %16 row_newbcast:12 row_mask:0xf bank_mask:0xf\n\t"
        "v_dot2c_f32_bf16_dpp %0, %2, %17 row_newbcast:14 row_mask:0xf bank_mask:0xf\n\tv_dot2c_f32_bf16_dpp %1, %2, %18 row_newbcast:14 row_mask:0xf bank_mask:0xf\n\ts_nop 2"
        : "+v"(yo.x), "+v"(yo.y) : "v"(actp), "v"(wdr[0]), "v"(wdr[1]), "v"(wdr[2]), "v"(wdr[3]), "v"(wdr[4]), "v"(wdr[5]), "v"(wdr[6]), "v"(wdr[7]),
          "v"(wdr[8]), "v"(wdr[9]), "v"(wdr[10]), "v"(wdr[11]), "v"(wdr[12]), "v"(wdr[13]), "v"(wdr[14]), "v"(wdr[15]));
    st_sc(ws, so, yo);
    STAMP(14);
}

#define ISSUE(h) do { if ((h) & 1) dma_b(ckv + ktile(sidx, (h) >> 1), od, slot((h) >> 1) + p * 4096u, pcf, L_FL + C_LAND + ((h) >> 1) * 16u + p * 4u); else dma_a(ckv + ktile(sidx, (h) >> 1), od, slot((h) >> 1) + p * 4096u); } while (0)
constexpr int ops_in(int h0, int h1) { int n = 0; for (int h = h0; h < h1; ++h) n += 2 + (h & 1); return n; }
DI void publish(char* ws, u32 g1, const f32x16 (&o)[4], v4u (&d)[8]) {
#pragma unroll
    for (int k = 0; k < 8; k += 2) {
        u32 c[8];
#pragma unroll
        for (int i = 0; i < 4; ++i) { c[i] = pack2(o[0][2 * k + i], o[1][2 * k + i]); c[4 + i] = pack2(o[2][2 * k + i], o[3][2 * k + i]); }
        SCHED();
#pragma unroll
        for (int i = 0; i < 2; ++i) {
            auto r0 = __builtin_amdgcn_permlane16_swap(c[2 * i], c[2 * i + 1], false, false);
            auto r1 = __builtin_amdgcn_permlane16_swap(c[4 + 2 * i], c[5 + 2 * i], false, false);
            d[k + i] = (v4u){(u32)r0[0], (u32)r0[1], (u32)r1[0], (u32)r1[1]};
            st_sc_keep(ws + WS_PAY + (k + i) * PHD, g1, d[k + i]);
        }
        SCHED();
    }
}
DI void stream_pv(const Params& P, int b, u32 p, int lane, const u32 (&od)[4], u64 pcf, u32 t0, u32& tl) {
    const u32 tz = t0; (void)tz;
    char* const ws = P.ws;
    const u32 sidx = (u32)b - 128, i15 = lane & 15, R = lane >> 4;
    const char* ckv = (const char*)P.ckv;
    u32 va[2][4];
    {
        const u32 hf = R >> 1, blk = R & 1, q4 = i15 >> 2, p4 = i15 & 3;
#pragma unroll
        for (int e = 0; e < 2; ++e)
#pragma unroll
            for (int cb = 0; cb < 4; ++cb) {
                const u32 row = 8 * hf + 4 * e + q4;
                va[e][cb] = p * 4096u + row * 256u + (((4 * cb + 2 * blk + (p4 >> 1)) ^ swz(row)) << 4) + 8 * (p4 & 1);
                PIN(va[e][cb]);
            }
    }
    u32 fb = L_FL, pa = L_P + (u32)lane * 16u;
    PIN(fb); PIN(pa);
    bf16x8 V[8][4];
    u32 vbase = 0;
#define VBASE(t) do { if (vwin(t) != vbase) { _Pragma("unroll") for (int i_ = 0; i_ < 8; ++i_) { va[i_ >> 2][i_ & 3] += vwin(t) - vbase; PIN(va[i_ >> 2][i_ & 3]); } vbase = vwin(t); } } while (0)
#define VREAD(t) do { VBASE(t); _Pragma("unroll") for (int cb_ = 0; cb_ < 4; ++cb_) V[(t) & 7][cb_] = tr2(va[0][cb_] + (slot(t) - vwin(t)), va[1][cb_] + (slot(t) - vwin(t))); } while (0)
    u64 e64;
    EPOCH();
    const u32 tlo = (u32)e64;
    u32 g1, gc, lr;
    {
        const u32 k = 4 * (i15 >> 3) + (i15 & 3);
        g1 = (p * 128u + (sidx ^ ((tlo & 1) << 6))) * 256u + R * (8 * PHD) + i15 * 16u;
        gc = WS_CHK + (sidx * 4 + p) * 512u + (8 * R + k) * 16u;
        lr = L_L + (8 * (k >> 1) + 4 * (R >> 1) + 2 * (k & 1) + (R & 1)) * 16u;
        PIN(g1); PIN(gc); PIN(lr);
    }
    u32 fg = L_FL + (lane & 1) * 4, gv = 0;
    PIN(fg);
#pragma unroll
    for (int h = HW; h < 32; ++h) {
        const int l = h - HW;
        if (h >= 12 && h < 31) gate_wait(gv, fg, C_GATE + (h / 2 - 6) * 8);
        VMWAIT(ops_in(l + 1, h));
        if (l == 1) STAMP(18);
        if (l == 7) STAMP(19);
        if (l == 15) STAMP(1);
        if (h == 24) STAMP(2);
        ISSUE(h);
        if (h >= 11 && h < 30) gv = *(volatile LAS unsigned char*)lp<unsigned char>(fg + C_GATE + ((h + 1) / 2 - 6) * 8);
        if ((l & 1) && l < 16) VREAD(l >> 1);
    }
    static_assert(HW >= 3 && HW <= 6, "tile 12 must have landed here");
    f32x16 o[4];
#pragma unroll
    for (int cb = 0; cb < 4; ++cb)
#pragma unroll
        for (int i = 0; i < 16; ++i) o[cb][i] = 0.f;
    bf16x8 pb[4], pc[4];
#define PV(t, pp) do { SCHED(); _Pragma("unroll") for (int cb_ = 0; cb_ < 4; ++cb_) o[cb_] = __builtin_amdgcn_mfma_f32_32x32x16_bf16(pp[(t) & 3], V[(t) & 7][cb_], o[cb_], 0, 0, 0); SCHED(); } while (0)
#define PVR(t, pp, u) do { VBASE(u); _Pragma("unroll") for (int cb_ = 0; cb_ < 4; ++cb_) { SCHED(); \
        o[cb_] = __builtin_amdgcn_mfma_f32_32x32x16_bf16(pp[(t) & 3], V[(t) & 7][cb_], o[cb_], 0, 0, 0); SCHED(); \
        V[(u) & 7][cb_] = tr2(va[0][cb_] + (slot(u) - vwin(u)), va[1][cb_] + (slot(u) - vwin(u))); } SCHED(); } while (0)
    STAMP(3);
    if (p == 0) {
        const v4u z = *lp<v4u>(fb + C_TZ);
        const u32 t = now32();
        if (lane == 0) st_sc2(ws, WS_FC + sidx * 8u, (v2u){t + max((int)FC_A, FC_B - (int)min(min(t - z.x, t - z.y), min(t - z.z, t - z.w))), tlo});
    }
    wait4p(fb, C_P, pa, 0, pb);
    STAMP(7);
    CYC0();
    CDECL();
    CS(0);
#pragma unroll
    for (int t = 0; t < 5; ++t) { PVR(t, pb, t + 8); if (t < 4) pb[t] = *lp<bf16x8>(pa + (t + 4) * 1024); if (t == 0) CS(1); if (t == 1) CS(2); if (t == 3) CS(3); }
    CS(4);
    PV(5, pb); PV(6, pb);
    u32 pf = flag_get(C_P + 4);
#pragma unroll
    for (int i = 0; i < 4; ++i) pc[i] = *(volatile LAS bf16x8*)lp<bf16x8>(pa + (8 + i) * 1024);
    PV(7, pb);
    STAMP(4);
    CS(5);
    if (__builtin_expect(rfl(pf) != 0x01010101u, 0)) wait4p(fb, C_P + 4, pa, 8192, pc);
    CS(6);
    PV(8, pc);
    VMWAIT(5);
    PVR(9, pc, 13); PVR(10, pc, 14);
    pf = flag_get(C_P + 8);
#pragma unroll
    for (int i = 0; i < 3; ++i) pb[i] = *(volatile LAS bf16x8*)lp<bf16x8>(pa + (12 + i) * 1024);
    PV(11, pc);
    STAMP(5);
    CS(7);
    pf = rfl(pf);
    if (__builtin_expect(!(pf & 0x1u), 0)) pb[0] = wait1p(fb, C_P + 8, pa, 12 * 1024);
    PV(12, pb);
    if (__builtin_expect(!(pf & 0x100u), 0)) pb[1] = wait1p(fb, C_P + 9, pa, 13 * 1024);
    VBASE(15);
    PV(13, pb);
    if (__builtin_expect(!(pf & 0x10000u), 0)) pb[2] = wait1p(fb, C_P + 10, pa, 14 * 1024);
    VMWAIT(0);
    PVR(14, pb, 15);
    pb[3] = wait1p(fb, C_P + 11, pa, 15 * 1024);
    PV(15, pb);
    STAMP(6);
    STAMPC(29);
    CS(8);
    v4u d[8];
    publish(ws, g1, o, d);
    v2u fl = *(volatile LAS v2u*)lp<v2u>(fb + C_L);
    f32x4 lq = *(volatile LAS f32x4*)lp<f32x4>(lr);
    u32 mref = 0;
    STAMP(8);
    CS(9);
    u32 cs[8];
#pragma unroll
    for (int k = 0; k < 8; ++k) { cs[k] = mix4(d[k]); PIN(cs[k]); }
    if (__builtin_expect(rfl(fl.x) != 0x01010101u || rfl(fl.y) != 0u, 0)) {
        wait4(fb, C_L);
        if (rfl(flag_get(C_BAD)) != 0u) {
            barrier();
            barrier();
#pragma unroll
            for (int cb = 0; cb < 4; ++cb)
#pragma unroll
                for (int i = 0; i < 16; ++i) o[cb][i] = 0.f;
#pragma clang loop unroll(disable)
            for (u32 t = 0; t < 16; ++t) {
                if (t == 8) {
                    LGKM0();
#pragma clang loop unroll(disable)
                    for (u32 u = 0; u < 8; ++u) { dma_a(ckv + ktile(sidx, u), od, slot_no(u + 8) * 16384u + p * 4096u); dma_b(ckv + ktile(sidx, u), od, slot_no(u + 8) * 16384u + p * 4096u, pcf, L_FL + C_LAND); }
                    VMWAIT(0);
                }
                const u32 so = slot_no(t | 8) * 16384u - vbase;
#pragma unroll
                for (int cb = 0; cb < 4; ++cb) V[7][cb] = tr2(va[0][cb] + so, va[1][cb] + so);
                pc[3] = *lp<bf16x8>(pa + (t ^ 8) * 1024);
                PV(15, pc);
            }
            publish(ws, g1, o, d);
            asm volatile("s_nop 1");
#pragma unroll
            for (int k = 0; k < 8; ++k) { cs[k] = mix4(d[k]); PIN(cs[k]); }
            mref = *(volatile LAS u32*)lp<u32>(L_M + (lr - L_L) / 4);
        }
        lq = *(volatile LAS f32x4*)lp<f32x4>(lr);
    }
    {
        u32 t0, t1, t2, t3, u0, u1;
        asm volatile("s_nop 1\n\t"
            "v_add_u32_dpp %0, %6, %6 row_ror:8 row_mask:0xf bank_mask:0x3\n\tv_add_u32_dpp %1, %7, %7 row_ror:8 row_mask:0xf bank_mask:0x3\n\t"
            "v_add_u32_dpp %2, %8, %8 row_ror:8 row_mask:0xf bank_mask:0x3\n\tv_add_u32_dpp %3, %9, %9 row_ror:8 row_mask:0xf bank_mask:0x3\n\t"
            "v_add_u32_dpp %0, %10, %10 row_ror:8 row_mask:0xf bank_mask:0xc\n\tv_add_u32_dpp %1, %11, %11 row_ror:8 row_mask:0xf bank_mask:0xc\n\t"
            "v_add_u32_dpp %2, %12, %12 row_ror:8 row_mask:0xf bank_mask:0xc\n\tv_add_u32_dpp %3, %13, %13 row_ror:8 row_mask:0xf bank_mask:0xc\n\t"
            "v_add_u32_dpp %4, %0, %0 row_half_mirror row_mask:0xf bank_mask:0x5\n\tv_add_u32_dpp %5, %1, %1 row_half_mirror row_mask:0xf bank_mask:0x5\n\t"
            "v_add_u32_dpp %4, %2, %2 row_half_mirror row_mask:0xf bank_mask:0xa\n\tv_add_u32_dpp %5, %3, %3 row_half_mirror row_mask:0xf bank_mask:0xa\n\t"
            "s_nop 0\n\t"
            "v_add_u32_dpp %4, %4, %4 quad_perm:[1,0,3,2] row_mask:0xf bank_mask:0xf\n\ts_nop 0\n\tv_add_u32_dpp %5, %5, %5 quad_perm:[1,0,3,2] row_mask:0xf bank_mask:0xf\n\t"
            "v_add_u32_dpp %4, %4, %4 quad_perm:[2,3,0,1] row_mask:0xf bank_mask:0xf\n\ts_nop 0\n\tv_add_u32_dpp %5, %5, %5 quad_perm:[2,3,0,1] row_mask:0xf bank_mask:0xf\n\ts_nop 0"
            : "=&v"(t0), "=&v"(t1), "=&v"(t2), "=&v"(t3), "=&v"(u0), "=&v"(u1)
            : "v"(cs[0]), "v"(cs[1]), "v"(cs[2]), "v"(cs[3]), "v"(cs[4]), "v"(cs[5]), "v"(cs[6]), "v"(cs[7]));
        const u32 x = (gc & 16u) ? u1 : u0;
        const v4u cg = {(gc & 32u) ? dppu<0x104>(x) : x, asu(sum4(lq)), mref, tlo};
        asm volatile("s_mov_b64 exec, %3\n\tglobal_store_dwordx4 %0, %1, %2 sc1\n\ts_mov_b64 exec, -1\n\ts_nop 0" :: "v"(gc), "v"(cg), "s"(ws), "s"(0x0f0f0f0f0f0f0f0full) : "memory");
    }
    CS(10);
    STAMP(9);
    COUT(11);
    barrier();
}

DI void p_write(const f32x4& s0, const f32x4& s1, u32 pa, f32x4& a, f32x4& c) {
#pragma unroll
    for (int k = 0; k < 4; ++k) a[k] = ex2(s0[k]);
#pragma unroll
    for (int k = 0; k < 4; ++k) c[k] = ex2(s1[k]);
    *lp<v2u>(pa) = (v2u){pack2(a.x, a.y), pack2(a.z, a.w)};
    *lp<v2u>(pa + 256) = (v2u){pack2(c.x, c.y), pack2(c.z, c.w)};
}
template <u32 KO> DI void qk_second(const bf16x8 (&q0)[16], const bf16x8 (&q1)[16], const u32 (&a)[16], u32 fw, u32 off, f32x4& s0, f32x4& s1, u64& cw) {
    bf16x8 fa[16];
    land_wait_k<KO>(fw, off, a[0], a[1], a[2], a[3], fa[0], fa[1], fa[2], fa[3]);
#pragma unroll
    for (int i = 0; i < 4; ++i) { s0[i] = 0.f; s1[i] = 0.f; }
#pragma unroll
    for (int s = 0; s < 16; ++s) {
        SCHED();
        s0 = __builtin_amdgcn_mfma_f32_16x16x32_bf16(fa[s], q0[s], s0, 0, 0, 0);
        if (s < KD - 4) { SCHED(); fa[2 * s + 4] = *lp<bf16x8>(a[2 * s + 4] + KO); SCHED(); }
        s1 = __builtin_amdgcn_mfma_f32_16x16x32_bf16(fa[s], q1[s], s1, 0, 0, 0);
        SCHED();
        if (s < KD - 4) fa[2 * s + 5] = *lp<bf16x8>(a[2 * s + 5] + KO);
        else if (s + KD < 16) fa[s + KD] = *lp<bf16x8>(a[s + KD] + KO);
    }
}
DI void stream_qk(const Params& P, int b, u32 w, int lane, u32 t0, int& budget, u32& tl) {
    char* const ws = P.ws;
    const int xc = b & 7, yy = (b >> 3) - 16;
    const u32 i15 = lane & 15, g = lane >> 4;
    u64 e64;
    EPOCH();
    const u32 tlo = (u32)e64, thi = (u32)(e64 >> 32);
    const u32 ak = g * 4096u + i15 * 256u + (swz(i15) << 4);
    const u32 c16 = i15 ^ g ^ ((w & 1) ? 12u : 0u);
    u32 qo = WS_QABS + (w ^ ((tlo & 1) << 1)) * 2048u + g * 256u + c16 * 16u, co = WS_QCHK + w * 1024u + (u32)lane * 16u;
    u32 va = L_IMG + ((lane & 1) ^ 1) * 16384u + (8 * w + (u32)(lane >> 3)) * 256u + (((((u32)lane >> 1) & 3) ^ ((w & 1) ? 3u : 0u)) << 6) + ((((u32)lane >> 2) & 2) + (lane & 1)) * 16u;
    u32 fa = L_FL + w, fb = L_FL, fw = L_FL + 16 * w + (i15 & 3) * 4, pa = L_P + w * 1024u + (g >> 1) * 512u + i15 * 16u + (g & 1) * 8u;
    u32 bq[4];
#pragma unroll
    for (int k = 0; k < 4; ++k) { bq[k] = L_IMG + i15 * 256u + (((g * 4 + k) ^ (i15 & 3) ^ ((i15 & 8) ? 12u : 0u)) << 4); PIN(bq[k]); }
    PIN(qo); PIN(co); PIN(va); PIN(fa); PIN(fb); PIN(fw); PIN(pa);
    u32 pa4 = pa + 4096;
    PIN(pa4);
    u32 gk = L_FL + C_GATE + (u32)lane * 8, m1_ = ~0u;
    PIN(gk); PIN(m1_);
#define HOLD() do { const u64 late_ = (1ull << (H_MIN / 2 - 6)) - 1; gate_or(gk + 4, 0x80u, 0x1ffull ^ late_); until(qt.t + Q_PRE); gate_or(gk + 4, 0x80u, 0x1ffull); } while (0)
    v4u chk;
    UNDEF(chk);
#define REQ_Q() do { \
        _Pragma("unroll") for (int k_ = 0; k_ < 8; ++k_) { const u32 k = (u32)(k_ ^ xc), x = (k & 1) * 1024u + (k >> 1) * 8192u; dma_sc(ws + x, qo, L_IMG + w * 2048u + (x ^ 16384u)); } \
        ld_sc_into(chk, ws, co); } while (0)
    u32 warm;
    UNDEF(warm);
    if (w == 3) asm volatile("global_load_dword %0, %1, %2\n\tglobal_load_dword %0, %1, %3\n\tglobal_load_dword %0, %1, %4\n\tglobal_load_dword %0, %5, %6" : "+v"(warm)
        : "v"(((u32)lane & 1) * (1792u * 1024u - 4u)), "s"(P.wg), "s"(P.wu), "s"(P.wd), "v"(WS_PAY + (((u32)lane & 3) * 10 + 1) * PHD + 4 * 128 * 256), "s"(ws) : "memory");
    __builtin_amdgcn_s_setprio(3);
    bf16x8 k0[16], k1[16];
    land_wait(fw, C_LAND);
    VMWAIT(0);
    PIN(warm);
    {
        u32 a = ak + slot_no(w) * 16384u; PIN(a);
#pragma unroll
        for (int s = 0; s < 16; ++s) k0[s] = *lp<bf16x8>(a ^ (u32)(s << 4));
        gate_add(gk, m1_, 2ull << w);
    }
    QTimer qt = {time_base(ws + WS_ST, t0) + Q_FIRST, 0u};
    u32 ka[16];
    {
        u32 a = ak + slot_no(w + 8) * 16384u; PIN(a);
#pragma unroll
        for (int s = 0; s < 16; ++s) { ka[s] = a ^ (u32)(s << 4); PIN(ka[s]); }
    }
    bool asked = false;
#pragma clang loop unroll(disable)
    while (ballot(*(volatile LAS u32*)lp<u32>(fw + C_LAND + 64) == 0u) != 0ull) {
        if (q_due(qt, ws, tlo)) {
            HOLD();
            REQ_Q();
            gate_add(gk + 4, 0x20u, 0x1ffull);
            land_wait(fw, C_LAND + 64);
            asked = true;
            break;
        }
        __builtin_amdgcn_s_sleep(3);
    }
    asm volatile("" ::: "memory");
    {
        u32 a = ak + slot_no(w + 4) * 16384u; PIN(a);
#pragma unroll
        for (int s = 0; s < 16; ++s) k1[s] = *lp<bf16x8>(a ^ (u32)(s << 4));
        gate_add(gk, m1_, 32ull << w);
    }
#pragma unroll
    for (int s = 0; s < 16; ++s) { PIN(k0[s]); PIN(k1[s]); }
    STAMP(1);
    if (!asked) {
#pragma clang loop unroll(disable)
        while (!q_due(qt, ws, tlo)) __builtin_amdgcn_s_sleep(1);
        HOLD();
        REQ_Q();
        gate_add(gk + 4, 0x20u, 0x1ffull);
    }
    asked = true;
    STAMP(22);
    u32 tz_;
#pragma clang loop unroll(disable)
    do {
        if (!asked) REQ_Q();
        asked = false;
        STAMP(20);
        VMWAIT(0);
        tz_ = now32();
        STAMP(4);
        PIN(chk);
        const v4u x0 = *(volatile LAS v4u*)lp<v4u>(va), x1 = *(volatile LAS v4u*)lp<v4u>(va ^ 16u), x2 = *(volatile LAS v4u*)lp<v4u>(va ^ 32u), x3 = *(volatile LAS v4u*)lp<v4u>(va ^ 48u);
        const v4u y0 = *(volatile LAS v4u*)lp<v4u>(va + 8192), y1 = *(volatile LAS v4u*)lp<v4u>((va ^ 16u) + 8192), y2 = *(volatile LAS v4u*)lp<v4u>((va ^ 32u) + 8192), y3 = *(volatile LAS v4u*)lp<v4u>((va ^ 48u) + 8192);
        const v4u xs = (x0 + x1) + (x2 + x3), ys = (y0 + y1) + (y2 + y3);
        const u32 bad = (mix4(xs) ^ chk.x) | (mix4(ys) ^ chk.z) | neq3(chk.y, chk.w, tlo);
        if (ballot(bad != 0u) == 0ull) break;
        __builtin_amdgcn_s_sleep(4);
    } while (--budget > 0);
    flag_set(fa, C_Z);
    *lp<u32>(L_FL + C_TZ + w * 4) = tz_;
    STAMP(5);
    wait4(fb, C_Z);
    STAMP(6);
    const u32 tz = t0; (void)tz;
    CYC0();
    u64 cs_[12];
    CS(0);
    f32x4 s[8];
#pragma unroll
    for (int i = 0; i < 4; ++i) s[i] = (f32x4){0.f, 0.f, 0.f, 0.f};
    bf16x8 q0[16], q1[16];
#define QREAD(s_) do { q0[s_] = *lp<bf16x8>(bq[(s_) & 3] + (((s_) >> 2) ^ 2) * 8192); q1[s_] = *lp<bf16x8>(bq[(s_) & 3] + (((s_) >> 2) ^ 2) * 8192 + 4096); } while (0)
#pragma unroll
    for (int i = 0; i < QD; ++i) QREAD(i);
#pragma unroll
    for (int i = 0; i < 16; ++i) {
        if (i + QD < 16) QREAD(i + QD);
        if (i + QD == 7) gate_add(gk, m1_, 0x200ull);
        SCHED();
        s[0] = __builtin_amdgcn_mfma_f32_16x16x32_bf16(k0[i], q0[i], s[0], 0, 0, 0);
        s[1] = __builtin_amdgcn_mfma_f32_16x16x32_bf16(k0[i], q1[i], s[1], 0, 0, 0);
        s[2] = __builtin_amdgcn_mfma_f32_16x16x32_bf16(k1[i], q0[i], s[2], 0, 0, 0);
        s[3] = __builtin_amdgcn_mfma_f32_16x16x32_bf16(k1[i], q1[i], s[3], 0, 0, 0);
        SCHED();
    }
    STAMP(21);
    STAMPC(29);
    CS(1);
    float l0, l1;
    f32x4 e0, e1, e2, e3;
    p_write(s[0], s[1], pa, e0, e1);
    p_write(s[2], s[3], pa4, e2, e3);
    flag_set(fa, C_P);
    SCHED();
    STAMP(24);
    CS(2);
    l0 = sum4(e0 + e2); l1 = sum4(e1 + e3);
    pa4 = pa + 8192;
    PIN(l0); PIN(l1); PIN(pa4);
    qk_second<0>(q0, q1, ka, fw, C_LAND + 128, s[4], s[5], cs_[3]);
    CS(4);
    p_write(s[4], s[5], pa4, e0, e1);
    flag_set(fa, C_P + 4);
    SCHED();
    STAMP(26);
    CS(5);
    l0 += sum4(e0); l1 += sum4(e1);
    float m0 = __builtin_fmaxf(max8(s[0], s[2]), max4(s[4])), m1 = __builtin_fmaxf(max8(s[1], s[3]), max4(s[5]));
    u32 ln = (u32)lane; PIN(ln);
    u32 la = L_L + (ln & 31) * 16 + w * 4, pl = pa + 12288;
    PIN(l0); PIN(l1); PIN(m0); PIN(m1); PIN(la); PIN(pl);
    if (w == 3) wait4(fb, C_P);
    qk_second<49152>(q0, q1, ka, fw, C_LAND + 192, s[6], s[7], cs_[6]);
    CS(7);
    STAMP(27);
    p_write(s[6], s[7], pl, e0, e1);
    flag_set(fa, C_P + 8);
    SCHED();
    STAMP(28);
    CS(8);
    {
        float z = red16(l0 + sum4(e0), l1 + sum4(e1));
        *lp<float>(la) = red32(z, z);
    }
    m0 = __builtin_fmaxf(m0, max4(s[6])); m1 = __builtin_fmaxf(m1, max4(s[7]));
    if (ballot(!(__builtin_fmaxf(__builtin_fabsf(m0), __builtin_fabsf(m1)) <= LAZY)) != 0ull) flag_set(fa, C_BAD);
    flag_set(fa, C_L);
    CS(9);
    COUT(10);
    __builtin_amdgcn_s_setprio(0);
    wait4(fb, C_L);
    if (__builtin_expect(rfl(flag_get(C_BAD)) != 0u, 0)) {
        m0 = max32(max16(m0)); m1 = max32(max16(m1));
        *lp<float>(la + (L_IMG + 4096 - L_L)) = (ln & 16) ? m1 : m0;
        barrier();
        const float m = max4(*lp<f32x4>((la & ~15u) + (L_IMG + 4096 - L_L)));
        *lp<float>(L_M + (ln & 31) * 4) = m;
        u32 e, od;
        both16(asu(m), e, od);
        l0 = 0.f; l1 = 0.f;
#pragma unroll
        for (int i = 0; i < 4; ++i) { p_write(s[2 * i] - asf(e), s[2 * i + 1] - asf(od), pa + 4096 * i, e0, e1); l0 += sum4(e0); l1 += sum4(e1); }
        const float z = red16(l0, l1);
        *lp<float>(la) = red32(z, z);
        barrier();
    }
    if (yy < 12 || xc < 4) mlp_part(P, b, w, lane, tlo, thi, budget, tl);
}

constexpr int QMAP = 0;
#define CODE_Y 0x13
__global__ void __launch_bounds__(512, 2) fused_kernel(Params) {
    v16u ka;
    v8u kb;
    v2u kc;
    asm volatile("s_load_dwordx16 %0, %3, 0x0\n\ts_load_dwordx8 %1, %3, 0x40\n\ts_load_dwordx2 %2, %3, 0x60" : "=&s"(ka), "=&s"(kb), "=&s"(kc) : "s"(__builtin_amdgcn_kernarg_segment_ptr()));
    SCHED();
    const int tid = threadIdx.x, lane = tid & 63;
    const int wave = __builtin_amdgcn_readfirstlane(tid >> 6);
    const int b = blockIdx.x, xc = b & 7, y = b >> 3;
    int budget = BUDGET;
    u32 tl = 0;
    u64 pc0;
    asm volatile("s_getpc_b64 %0\n\ts_nop 0" : "=s"(pc0));
    *lp<u32>(L_FL + (u32)(wave >> 2) * 1024 + (u32)(wave & 3) * 4 + (u32)lane * 16) = (wave & 1) ? 0u : (u32)(0x4111111110ull >> (min((u32)(lane * 2 + ((wave >> 1) & 1) - C_GATE / 8), 15u) * 4)) & 15u;
    if (wave >= 4 && (y & CODE_Y) == 0x10) {
        asm volatile("s_mov_b32 m0, %1\n\ts_nop 0\n\tglobal_load_lds_dword %0, %2\n\tglobal_load_lds_dword %0, %3\n\tglobal_load_lds_dword %0, %4"
            :: "v"((u32)lane * 64u + (u32)(wave - 4) * CODE_STR), "s"(L_IMG + (u32)(wave - 4) * 2048u), "s"(pc0), "s"(pc0 + 4096), "s"(pc0 + 8192) : "memory");
    }
    const u32 head = (u32)(xc * 4 + (y >> 2)), j = (u32)y & 3, w = (u32)wave;
    const u32 i15 = lane & 15, g = lane >> 4;
    const u32 ks = b & 15, cg = (u32)(b >> 4) * 2 + (w & 1);
    const u32 hq = QMAP ? (u32)(xc >> 1) * 8 + (u32)(y >> 1) : head, mh = QMAP ? (u32)xc & 1 : j >> 1, dh = j & 1;
    u32 od[4], oi[4], oq = (mh * 256 + w * 32 + (lane & 8) * 2 + g) * 8192u + hq * 256u + dh * 128u + (lane & 7) * 16u;
#pragma unroll
    for (int k = 0; k < 4; ++k) { od[k] = (3 * k + g) * 1024u + w * 256u + ((i15 ^ (g << 2) ^ (u32)k) << 4); PIN(od[k]); }
    oi[0] = (ks * 32 + 2 * (u32)(lane >> 2)) * 2; oi[1] = (u32)lane * 16; oi[2] = cg * 64u + (lane & 3) * 16u; oi[3] = oi[0] * 3072u + oi[2]; oi[2] += g * 4u;
#pragma unroll
    for (int k = 0; k < 4; ++k) PIN(oi[k]);
    PIN(oq);
    v4u wm[4], xa;
    u32 cc, xs;
#pragma unroll
    for (int k = 0; k < 4; ++k) UNDEF(wm[k]);
    UNDEF(xa); UNDEF(cc); UNDEF(xs);
    int role = (0xffaa5550u >> ((y & 16) + 2 * wave)) & 3;
    asm volatile("s_waitcnt lgkmcnt(0)" : "+s"(role), "+s"(ka), "+s"(kb), "+s"(kc) :: "memory");
#define KP(T, v, i) ((T)((u64)v[i + 1] << 32 | v[i]))
    Params P;
    P.wmod = KP(const u16*, ka, 0); P.c = KP(const u16*, ka, 2); P.x = KP(const u16*, ka, 4); P.ws = KP(char*, ka, 6);
    P.wq = KP(const u16*, ka, 8); P.ckv = KP(const u16*, ka, 10); P.wuk = KP(const u16*, ka, 12); P.wo = KP(const u16*, ka, 14);
    P.wuv = KP(const u16*, kb, 0); P.wg = KP(const u16*, kb, 2); P.wu = KP(const u16*, kb, 4); P.wd = KP(const u16*, kb, 6); P.out = KP(u16*, kc, 0);
    char* const ws = P.ws;
    if (role == 0) {
        asm volatile("global_load_dword %0, %3, %4\n\tglobal_load_dwordx4 %1, %5, %6\n\tglobal_load_dword %2, %7, %6" : "+v"(cc), "+v"(xa), "+v"(xs)
            : "v"(oi[0]), "s"(P.c), "v"(oi[1]), "s"(P.x), "v"(oi[2]) : "memory");
        asm volatile("global_load_dwordx4 %0, %4, %5 nt\n\tglobal_load_dwordx4 %1, %4, %6 nt\n\tglobal_load_dwordx4 %2, %4, %5 offset:1024 nt\n\tglobal_load_dwordx4 %3, %4, %6 offset:1024 nt"
            : "+v"(wm[0]), "+v"(wm[1]), "+v"(wm[2]), "+v"(wm[3]) : "v"(oi[3]), "s"(P.wmod), "s"(P.wmod + 3072) : "memory");
    }
    const char* const ckv = (const char*)P.ckv;
    const u32 sidx = (u32)b - 128;
    const u64 pcf = pc0 - w * 256u;
    __builtin_amdgcn_s_barrier();
    u64 t0_;
    asm volatile("s_memrealtime %0" : "=s"(t0_));
    v4u wr[4];
#pragma unroll
    for (int k = 0; k < 4; ++k) UNDEF(wr[k]);
    if (role == 1) {
#pragma unroll
        for (int k = 0; k < 4; ++k) ld_nt_into(wr[k], P.wq + k * 16384, oq);
    }
    asm volatile("s_waitcnt lgkmcnt(0)" : "+s"(t0_));
    const u32 t0 = (u32)t0_;
    STAMP(20);
    asm volatile("" : "+s"(role));
    if (role >= 2) {
        if (role == 2) {
            const u32 p = w;
#pragma unroll
            for (int h = 0; h < HW; ++h) {
                if (h == 0) until(t0 + T_OPEN0);
                if (h == 2) until(t0 + T_OPEN1);
                ISSUE(h);
            }
            stream_pv(P, b, w, lane, od, pcf, t0, tl);
        } else stream_qk(P, b, w - 4, lane, t0, budget, tl);
        STAMP(15);
        STAMP_OUT();
        return;
    }
    u64 e64;
    u32 tb, tq;
    {
        const u32 p = lane & 3;
        v4u wk[8];
#pragma unroll
        for (int k = 0; k < 8; ++k) UNDEF(wk[k]);
        if (b < 8 && tid == 128) st_sc2(ws, WS_ST + (u32)b * 8u, (v2u){t0, ~t0});
        u32 warm;
        UNDEF(warm);
        if (wave == 7) asm volatile("global_load_dword %0, %1, %2\n\tglobal_load_dword %0, %1, %3\n\tglobal_load_dword %0, %1, %4\n\tglobal_load_dword %0, %5, %6" : "+v"(warm)
            : "v"(((u32)lane & 1) * ((4u << 20) - 4u)), "s"(P.wuk), "s"(P.wo), "s"(P.wuv), "v"(WS_PAY + (((u32)lane & 3) * 10 + 1) * PHD + 4 * 128 * 256), "s"(ws) : "memory");
        EPOCH();
        const u32 tlo = (u32)e64, thi = (u32)(e64 >> 32);
        if (wave == 3) {
            const u32 rh = 8 * ((hq & 7) >> 1) + 4 * (hq >> 4) + 2 * (hq & 1) + ((hq >> 3) & 1);
            if (mh + dh == 0 && lane < 8) st_sc(ws, WS_QCHK + rh * 128u + (u32)lane * 16u, (v4u){0u, ~tlo, 0u, ~tlo});
            if (b == 9 && lane < 8 * NQH) st_sc(ws, WS_QH + (u32)lane * 16u, (v4u){0u, ~tlo, 0u, ~tlo});
            if (b == 10) st_sc(ws, WS_FC + (u32)lane * 16u, (v4u){0u, ~tlo, 0u, ~tlo});
        }
        if (role == 0) {
            u32 so = piece(WS_HP, 2048, cg) + (ks * 8 + 2 * p + (u32)(lane >> 5)) * 16u;
            VMWAIT(4);
            PIN(xa); PIN(xs); PIN(cc);
            float mean, rstd;
            ln_stats(xa, mean, rstd);
            const u32 sp = pack2(silu(bflo(cc)), silu(bfhi(cc)));
            u32 s0l = sp & 0xffffu, s0h = sp << 16, s1l = sp >> 16, s1h = sp & 0xffff0000u;
            float xn0 = (bflo(xs) - mean) * rstd, xn1 = (bfhi(xs) - mean) * rstd;
            PIN(xn0); PIN(xn1); PIN(s0l); PIN(s0h); PIN(s1l); PIN(s1h); PIN(so);
            VMWAIT(0);
#pragma unroll
            for (int k = 0; k < 4; ++k) PIN(wm[k]);
            STAMP(22);
            float ta[4], tc[4];
#pragma unroll
            for (int k = 0; k < 2; ++k) {
                ta[2 * k] = red32(dot2(wm[1][k], s1l, dot2(wm[0][k], s0l, 0.f)), dot2(wm[1][k + 2], s1l, dot2(wm[0][k + 2], s0l, 0.f)));
                ta[2 * k + 1] = red32(dot2(wm[1][k], s1h, dot2(wm[0][k], s0h, 0.f)), dot2(wm[1][k + 2], s1h, dot2(wm[0][k + 2], s0h, 0.f)));
                tc[2 * k] = red32(dot2(wm[3][k], s1l, dot2(wm[2][k], s0l, 0.f)), dot2(wm[3][k + 2], s1l, dot2(wm[2][k + 2], s0l, 0.f)));
                tc[2 * k + 1] = red32(dot2(wm[3][k], s1h, dot2(wm[2][k], s0h, 0.f)), dot2(wm[3][k + 2], s1h, dot2(wm[2][k + 2], s0h, 0.f)));
            }
            float u0 = xn0 * (red16(tc[0], tc[2]) + 0.015625f) + red16(ta[0], ta[2]), u1 = xn1 * (red16(tc[1], tc[3]) + 0.015625f) + red16(ta[1], ta[3]);
            u0 += dpp<0x124>(u0); u1 += dpp<0x124>(u1); u0 += dpp<0x128>(u0); u1 += dpp<0x128>(u1);
            u32 e, od;
            both16(pack2(u0, u1), e, od);
            if ((lane & 28) == 0) st_sc(ws, so, (v4u){e, od, tlo, thi});
            STAMP(24);
#pragma unroll
            for (int k = 0; k < 4; ++k) ld_nt_into(wr[k], P.wq + k * 16384, oq);
        }
        u32 so = piece(WS_QP, 1024, hq) + ((mh * 2 + (u32)(lane >> 5)) * 16 + dh * 8 + (lane & 7)) * 16u, ho = piece(WS_HP, 2048, mh * 8 + w) + (u32)lane * 16u;
        u32 la = F_QP + w * 256 + (g * 8 + (lane & 7)) * 8, lb = la + (lane & 8) * 128, ok = ((u32)y * 32 + g * 8) * 8192u + ((u32)xc * 4 + (w & 3)) * 256u + i15 * 16u;
        PIN(so); PIN(ho); PIN(la); PIN(lb); PIN(ok);
        u32 onel = 0x3f80u, oneh = 0x3f800000u;
        PIN(onel); PIN(oneh);
        v4u hg[2];
        UNDEF(hg[0]); UNDEF(hg[1]);
        __builtin_amdgcn_s_barrier();
        const u32 tm = now32();
        until(t0 + ST_LOOK);
        tb = time_base(ws + WS_ST, t0);
        const u32 ta = tb + max((u32)A_FIRST, tm - t0 + A_OWN);
        until(ta);
        ld_sc_into(hg[0], ws, ho); ld_sc_into(hg[1], ws + 1024, ho);
        __builtin_amdgcn_s_barrier();
        if (wave >= 4) {
#pragma unroll
            for (int k = 0; k < 8; ++k) ld_nt_into(wk[k], P.wuk + k * 4096, ok);
            VMWAIT(10);
        } else VMWAIT(2);
#pragma unroll
        for (int k = 0; k < 4; ++k) PIN(wr[k]);
        PIN(warm);
        STAMP(1);
        pair_rows(wr[0], wr[1]); pair_rows(wr[2], wr[3]);
#pragma unroll
        for (int k = 0; k < 4; ++k) PIN(wr[k]);
        if (wave >= 4) VMWAIT(8); else VMWAIT(0);
        PIN(hg[0]); PIN(hg[1]);
        if (__builtin_expect(ballot(neq3(hg[0].z, hg[1].z, tlo) | neq3(hg[0].w, hg[1].w, thi)) != 0ull, 0)) gather<2, 1024, 1>(ws, ho, tlo, thi, hg, budget);
        STAMP(3);
        {
            float hx = red16(red32(dot2(hg[1].x, onel, bflo(hg[0].x)), dot2(hg[1].y, onel, bflo(hg[0].y))), red32(dot2(hg[1].x, oneh, bfhi(hg[0].x)), dot2(hg[1].y, oneh, bfhi(hg[0].y))));
            hx += dpp<0x128>(hx);
            hx = asf((u32)__builtin_amdgcn_update_dpp((int)asu(hx), (int)asu(hx), 0x114, 0xf, 0x6, false));
            const u32 hp = pack2(hx, dpp<0xB1>(hx)), h01 = dppu<0x00>(hp), h23 = dppu<0xAA>(hp);
            float acc[8];
#pragma unroll
            for (int k = 0; k < 4; ++k) { acc[2 * k] = dot2(wr[2][k], h23, dot2(wr[0][k], h01, 0.f)); acc[2 * k + 1] = dot2(wr[3][k], h23, dot2(wr[1][k], h01, 0.f)); }
            float t[4];
#pragma unroll
            for (int k = 0; k < 4; ++k) t[k] = red32(acc[k], acc[k + 4]);
            float u0 = red16(t[0], t[2]), u1 = red16(t[1], t[3]);
            u0 += dpp<0x128>(u0); PIN(u0); u1 += dpp<0x128>(u1);
            *lp<f32x2>(la) = (f32x2){u0, u1};
        }
        barrier();
        if (wave == 0) {
            const LAS f32x2* qp = lp<f32x2>(lb);
            const f32x2 q2 = (qp[0] + qp[32]) + (qp[64] + qp[96]);
            float u0 = q2.x, u1 = q2.y;
            u0 += dpp<0x128>(u0); PIN(u0); u1 += dpp<0x128>(u1);
            u32 e, od;
            both16(pack2(u0, u1), e, od);
            if ((lane & 24) == 0) st_sc(ws, so, (v4u){e, od, tlo, thi});
            STAMP(2);
        }
        tq = ta + max((u32)A2_T, now32() - ta + A2_DELAY);
        if (y == ((xc * 5) & 7) + (y & 8) && tid < NQH) st_sc2(ws, WS_QH + (u32)lane * 128u + (u32)(xc * 2 + (y >> 3)) * 8u, (v2u){tq, tq ^ tlo});
        if (wave >= 4) {
            const u32 hk = (u32)xc * 4 + (w & 3);
            const u32 rho = 8 * ((hk & 7) >> 1) + 4 * (hk >> 4) + 2 * (hk & 1) + ((hk >> 3) & 1);
            const u32 og = piece(WS_QP, 1024, hk) + (u32)lane * 16u;
            u32 sq = WS_QABS + j * 8192u + (rho ^ ((tlo & 1) << 4)) * 256u + (u32)(y >> 2) * 64u + g * 16u, sc = WS_QCHK + (rho * 16 + (u32)y) * 8u;
            PIN(sq); PIN(sc);
            v4u qg[1];
            until(tq);
            gather<1, 0, 1>(ws, og, tlo, thi, qg, budget);
            __builtin_amdgcn_s_barrier();
            STAMP(23);
            u32 e, od, q0, q1, q2, q3;
            both16(pack2(red32(bflo(qg[0].x), bflo(qg[0].y)), red32(bfhi(qg[0].x), bfhi(qg[0].y))), e, od);
            both32(e, q0, q1); both32(od, q2, q3);
            float a[8];
#pragma unroll
            for (int k = 0; k < 8; ++k) { PIN(wk[k]); a[k] = dot2(wk[k][3], q3, dot2(wk[k][2], q2, dot2(wk[k][1], q1, dot2(wk[k][0], q0, 0.f)))); }
            float z[4];
#pragma unroll
            for (int k = 0; k < 4; ++k) {
                const float keep = (lane & 1) ? a[k + 4] : a[k], give = (lane & 1) ? a[k] : a[k + 4];
                float s = keep + dpp<0xB1>(give);
                s += dpp<0x122>(s); s += dpp<0x124>(s); s += dpp<0x128>(s);
                z[k] = s * (0.08838834764831845f * 1.4426950408889634f);
            }
            const u32 y0 = pack2(z[0], z[1]), y1 = pack2(z[2], z[3]);
            const u32 y2 = dppu<0xB1>(y0), y3 = dppu<0xB1>(y1);
            if (i15 == 0) st_sc(ws, sq, (v4u){y0, y1, y2, y3});
            u32 c0, c1;
            both16(mix4((v4u){y0, y1, y2, y3}), c0, c1);
            both32(c0 + c1, c0, c1);
            if (lane == 0) st_sc2(ws, sc, (v2u){c0 + c1, tlo});
            STAMP(4);
        } else __builtin_amdgcn_s_barrier();
    }
    const u32 tlo = (u32)e64, thi = (u32)(e64 >> 32);
    const bool is_red = (y >> 2) == 1;
    const u32 rid = (u32)xc * 4 + j;
    bf16x8 bo[16], bu[4];
    u32 xo, aa, cw, cr, op, o, oc, wa, he, wl, rl, om, xr;
#define TAIL_ADDR() do { \
        xo = piece(WS_X1, 4096, (u32)(lane >> 3)) + (rid * 8 + (u32)(lane & 7)) * 16u; \
        aa = F_A + (i15 & 7) * 272 + g * 16; cw = F_CX + ((w - 2 * j) & 7) * 32 + i15 * 2; cr = F_CX + g * 16; \
        op = piece(WS_OUTP, 16384, 4 * w + g) + (u32)b * 128u + (i15 & 7) * 16u; \
        wl = F_LS + (i15 < 4 ? (w * 16 + g * 4 + i15) * 4 : 512u); rl = F_LS + (u32)lane * 8; PIN(wl); PIN(rl); \
        o = WS_PAY + head * PHD + (j * 128 + ((w * 16) ^ ((tlo & 1) << 6))) * 256u + (u32)lane * 16u; \
        oc = WS_CHK + ((w * 16 + (i15 & 3) * 4 + g) * 4 + j) * 512u + head * 16u; \
        wa = F_A + w * 272 + i15 * 16 + g * 4; \
        om = WS_ML + (rid >> 1) * 4096u + (g * 16 + (rid & 1) * 8 + (u32)(lane & 7)) * 16u; PIN(om); \
        he = WS_HE + (((u32)b + 17u * (u32)lane) & 127u) * HSTR + (u32)lane * 16u; PIN(he); \
        PIN(xo); PIN(aa); PIN(cw); PIN(cr); PIN(op); PIN(o); PIN(oc); PIN(wa); } while (0)
    {
        const u32 kq = (u32)b & 3, cgl = (u32)b >> 2;
        const u32 w4 = w & 3, mrow = kq * 128 + w4 * 32 + (u32)(lane >> 3);
        const u32 q4 = i15 >> 2, p4 = lane & 3;
        const u32 ra = 8 * (g & 1) + q4, rb = ra + 4;
        u32 xa[4], xb[4];
#pragma unroll
        for (int t2 = 0; t2 < 4; ++t2) {
            const u32 c0 = 8 * w + (p4 >> 1) + 2 * t2, x0 = F_WO + (8 * g + q4) * 1024 + 8 * (p4 & 1);
            xa[t2] = x0 + ((c0 ^ swz(ra)) << 4); xb[t2] = x0 + 4096 + ((c0 ^ swz(rb)) << 4);
            PIN(xa[t2]); PIN(xb[t2]);
        }
        u32 x0, x1, ma = F_ML + w * 256 + ((lane & 7) * 4 + g) * 8;
        {
            const u32 c0 = 2 * w + (p4 >> 1);
            x0 = F_UV + (8 * g + q4) * 256 + 8 * (p4 & 1) + ((c0 ^ swz(ra)) << 4); x1 = F_UV + (8 * g + q4 + 4) * 256 + 8 * (p4 & 1) + ((c0 ^ swz(rb)) << 4);
        }
        PIN(x0); PIN(x1); PIN(ma);
#define PIECE(k) until(tq + T_OWN + (k) * O_STEP)
#define WO_ROW(s, k) dma_pl(P.wo + ((j + (s)) & 3) * 16384, (head * 128 + 8 * w4 + (k)) * 1024u + (((u32)lane ^ swz(8 * (w4 & 1) + (k))) << 4), F_WO + ((s) * 32 + 8 * w4 + (k)) * 1024u)
#define UV_ROWS(i) do { const u32 e_ = w4 * 32 + (i) * 4 + g, c_ = ((e_ >> 2) & 1) * 64 + (e_ & 1) * 32 + ((e_ >> 1) & 1) * 16 + (e_ >> 3); \
            dma_nt(P.wuv, (j * 128 + c_) * 8192u + head * 256u + ((i15 ^ swz(e_ & 15)) << 4), F_UV + (w4 * 32 + (i) * 4) * 256u); } while (0)
        v4u mw[4];
        u32 cv[4];
#pragma unroll
        for (int k = 0; k < 4; ++k) { UNDEF(mw[k]); UNDEF(cv[k]); }
        u32 mo = mrow * 6144u + 2048u + cgl * 128u + (u32)(lane & 7) * 16u;
        PIN(mo);
        if (wave < 4) {
#pragma unroll
            for (int k = 0; k < 4; ++k) { PIECE(k); WO_ROW(0, 2 * k); WO_ROW(0, 2 * k + 1); }
            asm volatile("global_load_ushort %0, %4, %5\n\tglobal_load_ushort %1, %4, %5 offset:16\n\tglobal_load_ushort %2, %4, %5 offset:32\n\tglobal_load_ushort %3, %4, %5 offset:48"
                : "+v"(cv[0]), "+v"(cv[1]), "+v"(cv[2]), "+v"(cv[3]) : "v"(mrow * 2), "s"(P.c) : "memory");
            PIECE(4);
            ld_nt_into(mw[0], P.wmod, mo); ld_nt_into(mw[1], P.wmod + 8 * 3072, mo);
            VMWAIT(2);
        }
        TAIL_ADDR();
        until(tq + T_MID);
        STAMP(5);
        barrier();
        if (wave >= 4) {
#pragma unroll
            for (int k = 8; k < 32; ++k) WO_ROW(k >> 3, k & 7);
            STAMP(19);
        }
#pragma unroll
        for (int t2 = 0; t2 < 4; ++t2) { bo[4 * t2] = tr2(xa[t2], xb[t2]); xa[t2] += 32768; xb[t2] += 32768; PIN(xa[t2]); PIN(xb[t2]); }
        float u[2] = {0.f, 0.f};
        if (wave < 4) {
            const u32 t2 = now32() + U_GAP;
            until(t2);
            ld_nt_into(mw[2], P.wmod + 16 * 3072, mo); ld_nt_into(mw[3], P.wmod + 24 * 3072, mo);
#pragma unroll
            for (int k = 0; k < 4; ++k) { until(t2 + (k + 1) * U_STEP); UV_ROWS(2 * k); UV_ROWS(2 * k + 1); }
            VMWAIT(8);
#pragma unroll
            for (int k = 0; k < 4; ++k) { PIN(mw[k]); PIN(cv[k]); }
            const u32 sp = pack2(silu(bflo(cv[0])), silu(bflo(cv[1]))), sr = pack2(silu(bflo(cv[2])), silu(bflo(cv[3])));
            pair_rows(mw[0], mw[1]); pair_rows(mw[2], mw[3]);
            float t[4];
#pragma unroll
            for (int k = 0; k < 2; ++k) {
                t[2 * k] = red32(dot2(mw[2][k], sr, dot2(mw[0][k], sp, 0.f)), dot2(mw[2][k + 2], sr, dot2(mw[0][k + 2], sp, 0.f)));
                t[2 * k + 1] = red32(dot2(mw[3][k], sr, dot2(mw[1][k], sp, 0.f)), dot2(mw[3][k + 2], sr, dot2(mw[1][k + 2], sp, 0.f)));
            }
#pragma unroll
            for (int k = 0; k < 2; ++k) { float z = red16(t[k], t[k + 2]); z += dpp<0x128>(z); u[k] = z; }
        } else VMWAIT(0);
        PIN(u[0]); PIN(u[1]);
#pragma unroll
        for (int t2 = 0; t2 < 4; ++t2) PIN(bo[4 * t2]);
        STAMP(14);
        barrier();
        *lp<f32x2>(ma) = (f32x2){u[0], u[1]};
        if (tid < 12) *lp<u32>(F_ST + tid * 4) = 0u;
        barrier();
        if (wave == 7) {
            const LAS f32x2* q = lp<f32x2>(F_ML) + lane;
            const f32x2 s = q[0] + q[64];
            const float sx = red32(s.x, s.x), sy = red32(s.y, s.y);
            if (lane < 32) st_sc(ws, WS_ML + (cgl & 7) * 8192u + ((u32)lane >> 4) * 4096u + (cgl >> 3) * 1024u + (kq * 16 + ((u32)lane & 15)) * 16u, (v4u){asu(sx), asu(sy), tlo, thi});
        }
#pragma unroll
        for (int t2 = 0; t2 < 4; ++t2) { bo[4 * t2 + 1] = tr2(xa[t2], xb[t2]); bo[4 * t2 + 2] = tr2(xa[t2] + 32768, xb[t2] + 32768); bo[4 * t2 + 3] = tr2(xa[t2] + 65536, xb[t2] + 65536); }
        xr = ld_u32(P.x, (rid * 8 + (u32)(lane & 7)) * 4);
        VMWAIT(0);
        PIN(xr);
#pragma unroll
        for (int k = 0; k < 16; ++k) PIN(bo[k]);
        STAMP(6);
        barrier();
#pragma unroll
        for (int s = 0; s < 4; ++s) bu[s] = tr2(x0 + s * 8192, x1 + s * 8192);
#pragma unroll
        for (int k = 0; k < 4; ++k) PIN(bu[k]);
    }
    u32 tk1, tk2;
    float ga0 = 0.f, ga1 = 0.f, gb0 = 0.f, gb1 = 0.f, xv0 = 0.f, xv1 = 0.f;
    STAMP(7);
    {
        v4u v[4], ck = {0u, 0u, 0u, 0u};
        u32 onel = 0x3f80u, oneh = 0x3f800000u;
        PIN(onel); PIN(oneh);
#pragma unroll
        for (int k = 0; k < 4; ++k) UNDEF(v[k]);
        v4u gm[4];
#pragma unroll
        for (int k = 0; k < 4; ++k) UNDEF(gm[k]);
        if (is_red && wave == 0) asm volatile("global_load_dwordx4 %0, %4, %5 sc1\n\tglobal_load_dwordx4 %1, %4, %5 offset:1024 sc1\n\tglobal_load_dwordx4 %2, %4, %5 offset:2048 sc1\n\tglobal_load_dwordx4 %3, %4, %5 offset:3072 sc1"
            : "+v"(gm[0]), "+v"(gm[1]), "+v"(gm[2]), "+v"(gm[3]) : "v"(om), "s"(ws) : "memory");
        look_times(ws, lane, tlo, budget, tk1, tk2);
#pragma unroll
        for (int k = 0; k < 4; ++k) PIN(gm[k]);
        if (is_red && wave == 0) {
            if (ballot((neq3(gm[0].z, gm[1].z, tlo) | neq3(gm[2].z, gm[3].z, tlo)) | (neq3(gm[0].w, gm[1].w, thi) | neq3(gm[2].w, gm[3].w, thi))) != 0ull) gather<4, 1024, 1>(ws, om, tlo, thi, gm, budget);
            xv0 = bflo(xr); xv1 = bfhi(xr);
            {
                const float s0 = red16(red32(asf(gm[1].x), asf(gm[2].x)), red32(asf(gm[1].y), asf(gm[2].y)));
                u32 e, od, sbp, cbp;
                both16(asu(s0), e, od);
                both32(pack2(asf(e), asf(od)), sbp, cbp);
                if (lane < 8) st_sc(ws, WS_SC + (rid * 8 + (u32)lane) * 16u, (v4u){cbp, sbp, tlo, thi});
            }
            ga0 = asf(gm[0].x); ga1 = asf(gm[0].y); gb0 = asf(gm[3].x); gb1 = asf(gm[3].y);
            ga0 = red32(ga0, ga0); ga0 = red16(ga0, ga0); ga1 = red32(ga1, ga1); ga1 = red16(ga1, ga1);
            gb0 = red32(gb0, gb0); gb0 = red16(gb0, gb0); gb1 = red32(gb1, gb1); gb1 = red16(gb1, gb1);
        }
        PIN(ga0); PIN(ga1); PIN(gb0); PIN(gb1); PIN(xv0); PIN(xv1);
        until(tk2);
        STAMP(8);
#pragma clang loop unroll(disable)
        for (;;) {
            asm volatile("global_load_dwordx4 %0, %5, %7 sc1\n\tglobal_load_dwordx4 %1, %5, %7 offset:1024 sc1\n\tglobal_load_dwordx4 %2, %5, %7 offset:2048 sc1\n\tglobal_load_dwordx4 %3, %5, %7 offset:3072 sc1\n\t"
                "s_mov_b64 exec, %8\n\ts_waitcnt vmcnt(3)\n\tglobal_load_dwordx4 %4, %6, %7 sc1\n\ts_mov_b64 exec, -1"
                : "+v"(v[0]), "+v"(v[1]), "+v"(v[2]), "+v"(v[3]), "+v"(ck) : "v"(o), "v"(oc), "s"(ws), "s"(0x000f000f000f000full) : "memory");
            float a[8];
#pragma unroll
            for (int q = 0; q < 4; ++q) { a[2 * q] = bflo(v[0][q]); a[2 * q + 1] = bfhi(v[0][q]); }
            u32 x = mix4(v[0]);
#pragma unroll
            for (int k = 1; k < 4; ++k) {
#pragma unroll
                for (int q = 0; q < 8; ++q) PIN(a[q]);
                PIN(x);
                asm volatile("s_waitcnt vmcnt(%1)" : "+v"(v[k]) : "i"(4 - k) : "memory");
                x += mix4(v[k]);
#pragma unroll
                for (int q = 0; q < 4; ++q) { a[2 * q] = dot2(v[k][q], onel, a[2 * q]); a[2 * q + 1] = dot2(v[k][q], oneh, a[2 * q + 1]); }
            }
            float t[4];
            x += dppu<0x128>(x);
#pragma unroll
            for (int k = 0; k < 4; ++k) t[k] = red32(a[k], a[k + 4]);
            x += dppu<0x124>(x);
            float u0 = red16(t[0], t[2]); PIN(u0);
            *lp<u32>(wa) = pack2(u0, red16(t[1], t[3]));
            PIN(x);
            asm volatile("s_waitcnt vmcnt(0)" : "+v"(ck) :: "memory");
            *lp<u32>(wl) = ck.y;
            x -= ck.x;
            x += dppu<0xB1>(x); x += dppu<0x4E>(x);
            const u64 bad = ballot((x | (ck.w ^ tlo)) != 0u) & 0x000f000f000f000full;
            if (__builtin_expect((bad | ballot(ck.z != 0u)) == 0ull, 1)) break;
            if (bad == 0ull || --budget <= 0) {
                const float m = i15 < 4 ? asf(ck.z) : -3.0e38f;
                float M = __builtin_fmaxf(m, dpp<0xB1>(m)); M = __builtin_fmaxf(M, dpp<0x4E>(M));
                M = asf(rfl(asu(max32(max16(M)))));
                const u32 f = pack2(ex2(m - M), 0.f);
                const u32 f0 = dppu<0x150>(f), f1 = dppu<0x151>(f), f2 = dppu<0x152>(f), f3 = dppu<0x153>(f);
#pragma unroll
                for (int q = 0; q < 4; ++q) {
                    a[2 * q] = dot2(v[3][q], f3, dot2(v[2][q], f2, dot2(v[1][q], f1, dot2(v[0][q], f0, 0.f))));
                    a[2 * q + 1] = dot2(v[3][q], f3 << 16, dot2(v[2][q], f2 << 16, dot2(v[1][q], f1 << 16, dot2(v[0][q], f0 << 16, 0.f))));
                }
#pragma unroll
                for (int k = 0; k < 4; ++k) t[k] = red32(a[k], a[k + 4]);
                *lp<u32>(wa) = pack2(red16(t[0], t[2]), red16(t[1], t[3]));
                *lp<float>(wl) = asf(ck.y) * bflo(f);
                *lp<float>(F_ST + w * 4) = M;
                *lp<u32>(F_ST + 32) = 1u;
                break;
            }
            __builtin_amdgcn_s_sleep(POLL_SLEEP);
        }
        STAMP(9);
    }
    barrier();
    u64 tc1;
    asm volatile("s_memrealtime %0" : "=s"(tc1));
    STAMP(10);
    float ls;
    {
        bf16x8 af[4];
#pragma unroll
        for (int s = 0; s < 4; ++s) af[s] = *lp<bf16x8>(aa + s * 64);
        const u32 rare = *lp<u32>(F_ST + 32);
        const f32x2 l2 = *lp<f32x2>(rl);
        SCHED();
        f32x4 d = {0.f, 0.f, 0.f, 0.f};
#pragma unroll
        for (int s = 0; s < 4; ++s) d = __builtin_amdgcn_mfma_f32_16x16x32_bf16(af[s], bu[s], d, 0, 0, 0);
        ls = l2.x + l2.y;
        if (__builtin_expect(rfl(rare) != 0u, 0)) {
            const f32x4 m0 = *lp<f32x4>(F_ST), m1 = *lp<f32x4>(F_ST + 16);
            const float M = max8(m0, m1);
#pragma unroll
            for (int k = 0; k < 4; ++k) d[k] *= ex2(((g & 1) ? m1[k] : m0[k]) - M);
            ls *= ex2(*lp<float>(F_ST + (u32)(lane >> 3) * 4) - M);
        }
        float c0 = d.x + d.y, c1 = d.z + d.w;
        PIN(c0); PIN(c1);
        float c = c0 + c1, c2 = c0 + c1;
        PIN(c); PIN(c2);
        c = red16(c, c2);
        *lp<u16>(cw) = (u16)pack2(c, c);
    }
    barrier();
    STAMP(13);
    {
        bf16x8 af[4];
#pragma unroll
        for (int s = 0; s < 4; ++s) af[s] = *lp<bf16x8>(cr + s * 64);
        ls += dpp<0x128>(ls); ls += dpp<0x124>(ls); ls += dpp<0x122>(ls); ls += dpp<0x121>(ls);
        ls = red16(ls, ls); ls = red32(ls, ls);
        ls = __builtin_amdgcn_rcpf(ls);
        v4u r = {0u, 0u, tlo, thi};
        PIN(ls); PIN(r);
        SCHED();
        f32x4 d[4];
#pragma unroll
        for (int t = 0; t < 4; ++t) d[t] = (f32x4){0.f, 0.f, 0.f, 0.f};
#pragma unroll
        for (int s = 0; s < 4; ++s) {
#pragma unroll
            for (int t = 0; t < 4; ++t) d[t] = __builtin_amdgcn_mfma_f32_16x16x32_bf16(af[s], bo[4 * t + s], d[t], 0, 0, 0);
            SCHED();
        }
        r.x = asu(((g & 2) ? ((g & 1) ? d[3].x : d[2].x) : ((g & 1) ? d[1].x : d[0].x)) * ls);
        r.y = dppu<0x128>(r.x);
        asm volatile("s_mov_b64 exec, %3\n\tglobal_store_dwordx4 %0, %1, %2 sc1\n\ts_mov_b64 exec, -1\n\ts_nop 0" :: "v"(op), "v"(r), "s"(ws), "s"(0x00ff00ff00ff00ffull) : "memory");
    }
    STAMP(11);
    asm volatile("" : "+s"(tc1));
    if (wave == 2 && lane < 8) st_sc(ws, he, (v4u){(u32)tc1, 0u, tlo, thi});
    if (b == 0 && tid == 64) st_sc2(ws, WS_EPOCH, (v2u){tlo, thi});
    if (is_red) {
        const f32x2 s = reduce16<8, true>(ws, piece(WS_OUTP, 16384, rid), tlo, thi, wave, lane, F_RD, tk2 + max((u32)D_T, (u32)tc1 - tk2 + D_DELAY), budget);
        const float x10 = xv0 + ga0 * s.x, x11 = xv1 + ga1 * s.y;
        if (wave == 0) st_sc(ws, xo, (v4u){asu(x10), asu(x11), tlo, thi});
        STAMP(12);
        u32 oa = (rid * 8 + (u32)lane) * 4u;
        PIN(oa);
        const f32x2 s2 = reduce16<6, false>(ws, piece(WS_YP, 16384, rid), tlo, thi, wave, lane, F_RV, now32() + F_DELAY, budget);
        if (tid < 8) asm volatile("global_store_dword %0, %1, %2" :: "v"(oa), "v"(pack2(x10 + gb0 * s2.x, x11 + gb1 * s2.y)), "s"(P.out) : "memory");
    }
    STAMP(15);
    STAMP_OUT();
}

extern "C" void kernel_launch(void* const* d_in, const int* in_sizes, int n_in, void* d_out, int out_size, void* d_ws, size_t ws_size, hipStream_t stream) {
    Params P;
    P.x = (const u16*)d_in[0]; P.ckv = (const u16*)d_in[1]; P.wq = (const u16*)d_in[2]; P.wuk = (const u16*)d_in[3];
    P.wuv = (const u16*)d_in[4]; P.wo = (const u16*)d_in[5]; P.wg = (const u16*)d_in[7]; P.wu = (const u16*)d_in[8];
    P.wd = (const u16*)d_in[9]; P.c = (const u16*)d_in[10]; P.wmod = (const u16*)d_in[11];
    P.out = (u16*)d_out; P.ws = (char*)d_ws + WS_BASE(ws_size);
    (void)hipFuncSetAttribute((const void*)fused_kernel, hipFuncAttributeMaxDynamicSharedMemorySize, LDS_BYTES);
    fused_kernel<<<256, 512, LDS_BYTES, stream>>>(P);
}
```

```cpp
#include <hip/hip_runtime.h>

typedef unsigned u32;
typedef unsigned long long u64;
typedef unsigned short u16;
typedef u32 v4u __attribute__((ext_vector_type(4)));
typedef u32 v2u __attribute__((ext_vector_type(2)));
typedef u32 v8u __attribute__((ext_vector_type(8)));
typedef u32 v16u __attribute__((ext_vector_type(16)));
typedef float f32x2 __attribute__((ext_vector_type(2)));
typedef float f32x4 __attribute__((ext_vector_type(4)));
typedef float f32x16 __attribute__((ext_vector_type(16)));
typedef short bf16x8 __attribute__((ext_vector_type(8)));
typedef short s16x4 __attribute__((ext_vector_type(4)));
typedef __bf16 bf16x2_t __attribute__((ext_vector_type(2)));
#define LAS __attribute__((address_space(3)))
#define DI __device__ __forceinline__

#define CFG(name, val) constexpr int name = val
CFG(BUDGET, 1 << 20);
CFG(POLL_SLEEP, 4);
CFG(HINT_SLEEP, 4);
#define PACING(X) \
    X(T_OPEN0, 50)        \
    X(T_OPEN1, 70)        \
    X(A_FIRST, 185)        \
    X(A_OWN, 60)        \
    X(ST_LOOK, 110)        \
    X(SKEW_MAX, 45) \
    X(A2_T, 165)        \
    X(A2_DELAY, 45)        \
    X(Q_FIRST, 360)        \
    X(Q_STEP, 40)        \
    X(Q_DELAY, 140)        \
    X(Q_PRE, 75)        \
    X(T_OWN, 40)        \
    X(O_STEP, 35) \
    X(T_MID, 220)        \
    X(U_GAP, 65)        \
    X(U_STEP, 30) \
    X(FC_A, 203)        \
    X(FC_B, 342)        \
    X(FC_C, 62)        \
    X(D_T, 203)        \
    X(D_DELAY, 78)        \
    X(E_T, 283)        \
    X(E_DELAY, 160)        \
    X(F_LIM, 285) X(F_T, 392)        \
    X(F_DELAY, 120)        \
    X(G_T, 70)        \
    X(H_MIN, 18) \
    X(G_2, 80)
#define X_(n, v) constexpr int n = v;
PACING(X_)
#define KD 10
DI u32 ktile(u32 s, u32 t) { return s * 262144u + t * 16384u; }
constexpr int HW = 5;
#define QD 2
#define LAZY 64.f
#define CODE_STR 7168
#define WS_BASE(sz) (((sz) - (32u << 20)) & ~(size_t)((2u << 20) - 1))

constexpr u32 WS_EPOCH = 0;
constexpr u32 NSLOT = 16;
DI char* late_ws(char* ws, u32 tlo) { return ws - (size_t)(tlo & (NSLOT - 1)) * (8u << 20); }
DI u32 late_flip(u32 tlo) { return ((tlo / NSLOT) & 1) << 6; }
DI u32 piece(u32 base, u32 size, u32 i) { return base + i * size; }
#define CH 262144u
constexpr u32 WS_ST = 1024;
constexpr u32 WS_QH = 2048, NQH = 6;
constexpr u32 WS_QCHK = 8192;
constexpr u32 WS_HP = CH;
constexpr u32 WS_QP = 2 * CH;
constexpr u32 WS_QABS = 3 * CH;
constexpr u32 WS_L = 25u << 18;
constexpr u32 WS_ML = WS_L;
constexpr u32 WS_X1 = WS_L + 65536;
constexpr u32 WS_SC = WS_L + 98304;
constexpr u32 HSTR = 1152, WS_HC = WS_L + 106496;
constexpr u32 WS_FC = WS_SC + 4096;
constexpr u32 WS_HE = WS_HC + 512;
constexpr u32 WS_OUTP = WS_L + 262144;
constexpr u32 WS_YP = WS_L + 786432;
constexpr u32 WS_CHK = WS_L + 1310720;
constexpr u32 WS_PAY = 2u << 20;
constexpr u32 PHD = 4 * 128 * 256 + 4096;
constexpr u32 WS_TL = 16u << 20;

constexpr u32 L_FL = 114688, L_P = L_FL + 1024, L_IMG = 131072;
constexpr u32 slot_no(u32 t) { return (u32)((0x9543621054362105ull >> (4 * t)) & 15u); }
constexpr u32 slot(int t) { return slot_no((u32)t) * 16384u; }
constexpr u32 vwin(int t) { return (slot_no((u32)t) < 3 ? 0u : slot_no((u32)t) < 7 ? 3u : 6u) * 16384u; }
static_assert(slot(15) == L_IMG + 16384, "");
constexpr u32 C_LAND = 0;
constexpr u32 C_GATE = 256;
constexpr u32 C_Z = 336, C_P = 340, C_L = 352, C_BAD = 356, C_TZ = 368;
constexpr u32 L_M = L_FL + 384, L_L = L_FL + 512;
constexpr u32 L_W = 0;
constexpr u32 L_W1 = 65536;
constexpr u32 L_WD = 98304;
constexpr u32 L_H2 = 32768;
constexpr u32 L_GU = 36864;
constexpr u32 F_QP = 0;
constexpr u32 F_WO = 0;
constexpr u32 F_UV = 131072;
constexpr u32 F_A = 0;
constexpr u32 F_ST = 4096;
constexpr u32 F_GO = F_ST + 40;
constexpr u32 F_CX = 4352;
constexpr u32 F_LS = 5120;
constexpr u32 F_ML = 6144;
constexpr u32 F_RD = 8192, F_RV = 9216;
constexpr u32 F_JUNK = 16384;
constexpr u32 LDS_BYTES = 163840;

struct Params {
    const u16 *wmod, *c, *x;
    char* ws;
    const u16 *wq, *ckv, *wuk, *wo, *wuv, *wg, *wu, *wd;
    u16* out;
};

DI float asf(u32 v) { return __builtin_bit_cast(float, v); }
DI u32 asu(float v) { return __builtin_bit_cast(u32, v); }
DI float bflo(u32 v) { return asf(v << 16); }
DI float bfhi(u32 v) { return asf(v & 0xffff0000u); }
DI u32 pack2(float a, float b) { f32x2 v = {a, b}; return __builtin_bit_cast(u32, __builtin_convertvector(v, bf16x2_t)); }
DI float dot2(u32 w, u32 x, float acc) { return __builtin_amdgcn_fdot2_f32_bf16(__builtin_bit_cast(bf16x2_t, w), __builtin_bit_cast(bf16x2_t, x), acc, false); }
DI void pair_rows(v4u& x, v4u& y) {
#pragma unroll
    for (int k = 0; k < 4; ++k) { const u32 a = x[k], b = y[k]; x[k] = __builtin_amdgcn_perm(b, a, 0x05040100u); y[k] = __builtin_amdgcn_perm(b, a, 0x07060302u); }
}
template <int CTRL> DI u32 dppu(u32 v) { return (u32)__builtin_amdgcn_update_dpp(0, (int)v, CTRL, 0xf, 0xf, true); }
template <int CTRL> DI float dpp(float v) { return asf(dppu<CTRL>(asu(v))); }
DI float red32(float a, float b) { auto r = __builtin_amdgcn_permlane32_swap(asu(a), asu(b), false, false); return asf((u32)r[0]) + asf((u32)r[1]); }
DI float red16(float a, float b) { auto r = __builtin_amdgcn_permlane16_swap(asu(a), asu(b), false, false); return asf((u32)r[0]) + asf((u32)r[1]); }
DI float max3(float a, float b, float c) { return __builtin_fmaxf(__builtin_fmaxf(a, b), c); }
DI float max4(f32x4 a) { return max3(max3(a.x, a.y, a.z), a.w, a.x); }
DI float max32(float a) { auto r = __builtin_amdgcn_permlane32_swap(asu(a), asu(a), false, false); return __builtin_fmaxf(asf((u32)r[0]), asf((u32)r[1])); }
DI float max16(float a) { auto r = __builtin_amdgcn_permlane16_swap(asu(a), asu(a), false, false); return __builtin_fmaxf(asf((u32)r[0]), asf((u32)r[1])); }
DI float max8(const f32x4& a, const f32x4& b) { return max3(max3(max3(a.x, a.y, a.z), a.w, b.x), max3(b.y, b.z, b.w), a.x); }
DI float sum4(f32x4 a) { return (a.x + a.y) + (a.z + a.w); }
DI u32 perm_lo(u32 a, u32 b) { return __builtin_amdgcn_perm(b, a, 0x05040100u); }
DI u32 perm_hi(u32 a, u32 b) { return __builtin_amdgcn_perm(b, a, 0x07060302u); }
DI u32 rfl(u32 v) { return (u32)__builtin_amdgcn_readfirstlane((int)v); }
DI void both16(u32 a, u32& e, u32& o) { auto r = __builtin_amdgcn_permlane16_swap(a, a, false, false); e = (u32)r[0]; o = (u32)r[1]; }
DI void both32(u32 a, u32& l, u32& h) { auto r = __builtin_amdgcn_permlane32_swap(a, a, false, false); l = (u32)r[0]; h = (u32)r[1]; }
DI float rowsum(float v) { v += dpp<0x128>(v); v += dpp<0x124>(v); v += dpp<0x122>(v); v += dpp<0x121>(v); return v; }
DI float ex2(float v) { return __builtin_amdgcn_exp2f(v); }
DI u32 neq3(u32 a, u32 b, u32 t) { return __builtin_amdgcn_bitop3_b32(a, b, t, 0x7e); }
DI u32 mix4(v4u v) { return (v.w << 9) + ((v.z << 6) + ((v.y << 3) + v.x)); }
DI u64 ballot(bool c) { return __builtin_amdgcn_ballot_w64(c); }

DI v4u ld_nt(const void* base, u32 off) { v4u r; asm volatile("global_load_dwordx4 %0, %1, %2 nt" : "=v"(r) : "v"(off), "s"(base) : "memory"); return r; }
DI u32 ld_u32(const void* base, u32 off) { u32 r; asm volatile("global_load_dword %0, %1, %2" : "=v"(r) : "v"(off), "s"(base) : "memory"); return r; }
DI u32 ld_u32_nt(const void* base, u32 off) { u32 r; asm volatile("global_load_dword %0, %1, %2 nt" : "=v"(r) : "v"(off), "s"(base) : "memory"); return r; }
DI u32 ld_u16(const void* base, u32 off) { u32 r; asm volatile("global_load_ushort %0, %1, %2" : "=v"(r) : "v"(off), "s"(base) : "memory"); return r; }
DI void st_sc(void* base, u32 off, v4u v) { asm volatile("global_store_dwordx4 %0, %1, %2 sc1\n\ts_nop 1" :: "v"(off), "v"(v), "s"(base) : "memory"); }
DI void st_sc_keep(void* base, u32 off, v4u v) { asm volatile("global_store_dwordx4 %0, %1, %2 sc1" :: "v"(off), "v"(v), "s"(base) : "memory"); }
DI void st_sc2(void* base, u32 off, v2u v) { asm volatile("global_store_dwordx2 %0, %1, %2 sc1\n\ts_nop 1" :: "v"(off), "v"(v), "s"(base) : "memory"); }
DI void dma_nt(const void* base, u32 off, u32 lds_dst) { asm volatile("s_mov_b32 m0, %1\n\ts_nop 0\n\tglobal_load_lds_dwordx4 %0, %2 nt" :: "v"(off), "s"(lds_dst), "s"(base) : "memory"); }
DI void dma_pl(const void* base, u32 off, u32 lds_dst) { asm volatile("s_mov_b32 m0, %1\n\ts_nop 0\n\tglobal_load_lds_dwordx4 %0, %2" :: "v"(off), "s"(lds_dst), "s"(base) : "memory"); }
DI void dma_sc(const void* base, u32 off, u32 lds_dst) { asm volatile("s_mov_b32 m0, %1\n\ts_nop 0\n\tglobal_load_lds_dwordx4 %0, %2 sc1" :: "v"(off), "s"(lds_dst), "s"(base) : "memory"); }
DI void ld_nt_into(v4u& r, const void* base, u32 off) { asm volatile("global_load_dwordx4 %0, %1, %2 nt" : "+v"(r) : "v"(off), "s"(base) : "memory"); }
DI void ld_sc_into(v4u& r, const void* base, u32 off) { asm volatile("global_load_dwordx4 %0, %1, %2 sc1" : "+v"(r) : "v"(off), "s"(base) : "memory"); }
#define VMWAIT(n) asm volatile("s_waitcnt vmcnt(%0)" :: "i"(n) : "memory")
#define LGKM0() asm volatile("s_waitcnt lgkmcnt(0)" ::: "memory")
#define PIN(x) asm volatile("" : "+v"(x))
#define UNDEF(x) asm volatile("" : "=v"(x))
#define SCHED() __builtin_amdgcn_sched_barrier(0)
DI void barrier() { LGKM0(); __builtin_amdgcn_s_barrier(); asm volatile("" ::: "memory"); }

template <typename T> DI LAS T* lp(u32 off) { return (LAS T*)(size_t)off; }
DI bf16x8 tr2(u32 a, u32 b) {
    const s16x4 lo = __builtin_amdgcn_ds_read_tr16_b64_v4i16(lp<s16x4>(a)), hi = __builtin_amdgcn_ds_read_tr16_b64_v4i16(lp<s16x4>(b));
    return __builtin_shufflevector(lo, hi, 0, 1, 2, 3, 4, 5, 6, 7);
}

#define STAMP(i) do {} while (0)
#define CYC0() do {} while (0)
#define STAMPC(i) do {} while (0)
#define STAMP_OUT() do {} while (0)

#define CDECL() do {} while (0)
#define CS(i) do {} while (0)
#define COUT(n) do {} while (0)
template <u32 S> DI void pass(const char* ws, u32 off, v4u (&v)[1]) {
    asm volatile("global_load_dwordx4 %0, %1, %2 sc1\n\ts_waitcnt vmcnt(0)" : "=v"(v[0]) : "v"(off), "s"(ws) : "memory");
}
template <u32 S> DI void pass(const char* ws, u32 off, v4u (&v)[2]) {
    asm volatile("global_load_dwordx4 %0, %2, %3 sc1\n\tglobal_load_dwordx4 %1, %2, %4 sc1\n\ts_waitcnt vmcnt(0)" : "=&v"(v[0]), "=&v"(v[1]) : "v"(off), "s"(ws), "s"(ws + S) : "memory");
}
template <u32 S> DI void pass(const char* ws, u32 off, v4u (&v)[4]) {
    asm volatile("global_load_dwordx4 %0, %4, %5 sc1\n\tglobal_load_dwordx4 %1, %4, %5 offset:%6 sc1\n\tglobal_load_dwordx4 %2, %4, %5 offset:%7 sc1\n\tglobal_load_dwordx4 %3, %4, %5 offset:%8 sc1\n\ts_waitcnt vmcnt(0)"
        : "=&v"(v[0]), "=&v"(v[1]), "=&v"(v[2]), "=&v"(v[3]) : "v"(off), "s"(ws), "i"(S), "i"(2 * S), "i"(3 * S) : "memory");
}
template <int N, u32 STRIDE, int SLP = POLL_SLEEP> DI void gather(const char* ws, u32 off, u32 tlo, u32 thi, v4u (&v)[N], int& budget) {
#pragma clang loop unroll(disable)
    do {
        pass<STRIDE>(ws, off, v);
        u32 bad = 0;
#pragma unroll
        for (int k = 0; k < N; ++k) bad |= (v[k].z ^ tlo) | (v[k].w ^ thi);
        if (ballot(bad != 0u) == 0ull) break;
        __builtin_amdgcn_s_sleep(SLP);
    } while (--budget > 0);
}
#define PSLP "0"
template <bool G8> DI void poll2(v4u& g0, v4u& g1, u32 o0, const char* b0, u32 o1, const char* b1, u32 tlo, u32 thi) {
    u32 n, t, u;
#define POLL2(A, B, C, D) asm volatile("s_mov_b32 %2, 0x200000\n.Lp%=:\n\ts_sleep " PSLP "\n\tglobal_load_dwordx4 v[232:235], %5, %6 sc1\n\tglobal_load_dwordx4 v[236:239], %7, %8 sc1\n\ts_sub_u32 %2, %2, 1\n\ts_waitcnt vmcnt(0)\n\t" \
        "v_bitop3_b32 %3, " A ", " B ", %9 bitop3:0x7e\n\tv_bitop3_b32 %4, " C ", " D ", %10 bitop3:0x7e\n\tv_or_b32 %3, %3, %4\n\tv_cmp_ne_u32 vcc, 0, %3\n\ts_cbranch_scc1 .Lq%=\n\ts_cbranch_vccnz .Lp%=\n.Lq%=:" \
        : "=&{v[232:235]}"(g0), "=&{v[236:239]}"(g1), "=&s"(n), "=&v"(t), "=&v"(u) : "v"(o0), "s"(b0), "v"(o1), "s"(b1), "s"(tlo), "s"(G8 ? tlo : thi) : "memory", "vcc", "scc")
    if (G8) POLL2("v233", "v235", "v237", "v239"); else POLL2("v234", "v238", "v235", "v239");
}
DI void flag_set(u32 fa, u32 off) {
    asm volatile("s_mov_b64 exec, 1\n\tds_write_b8_d16_hi %0, %0 offset:%1\n\ts_mov_b64 exec, -1" :: "v"(fa), "i"(off) : "memory");
}
DI void gate_add(u32 ga, u32 v, u64 lanes) {
    asm volatile("s_mov_b64 exec, %2\n\tds_add_u32 %0, %1\n\ts_mov_b64 exec, -1" :: "v"(ga), "v"(v), "s"(lanes) : "memory");
}
DI void gate_or(u32 ga, u32 v, u64 lanes) {
    asm volatile("s_mov_b64 exec, %2\n\tds_or_b32 %0, %1\n\ts_mov_b64 exec, -1" :: "v"(ga), "v"(v), "s"(lanes) : "memory");
}
DI u32 flag_get(u32 off) { return *(volatile LAS u32*)lp<u32>(L_FL + off); }
DI void wait4(u32 fb, u32 off) {
    u32 v, n;
    asm volatile("s_mov_b32 %1, 0x400000\n.Lw%=:\n\tds_read_b32 %0, %2 offset:%3\n\ts_sub_u32 %1, %1, 1\n\ts_waitcnt lgkmcnt(0)\n\tv_cmp_ne_u32 vcc, 0x1010101, %0\n\ts_cbranch_scc1 .Le%=\n\ts_cbranch_vccnz .Lw%=\n.Le%=:"
        : "=&v"(v), "=&s"(n) : "v"(fb), "i"(off) : "memory", "vcc", "scc");
}
#define SPIN_HEAD "s_mov_b32 %[n], 0x400000\n.Lw%=:\n\t"
#define SPIN_TAIL(cmp) "s_sub_u32 %[n], %[n], 1\n\ts_waitcnt lgkmcnt(0)\n\t" cmp ", %[v]\n\ts_cbranch_scc1 .Le%=\n\ts_cbranch_vccnz .Lw%=\n.Le%=:"
DI void land_wait(u32 fl, u32 off) {
    u32 v, n;
    asm volatile(SPIN_HEAD "s_sleep 3\n\tds_read_b32 %[v], %[b] offset:%[o]\n\t" SPIN_TAIL("v_cmp_eq_u32 vcc, 0") : [v] "=&v"(v), [n] "=&s"(n) : [b] "v"(fl), [o] "i"(off) : "memory", "vcc", "scc");
}
DI void gate_wait(u32 v, u32 fg, u32 off) {
    u32 n;
    asm volatile("v_cmp_ne_u32 vcc, 0, %[v]\n\ts_cbranch_vccz .Le%=\n\t" SPIN_HEAD "ds_read_u8 %[v], %[b] offset:%[o]\n\t" SPIN_TAIL("v_cmp_ne_u32 vcc, 0") : [v] "+v"(v), [n] "=&s"(n) : [b] "v"(fg), [o] "i"(off) : "memory", "vcc", "scc");
}
DI bf16x8 wait1p(u32 fb, u32 off, u32 pa, u32 poff) {
    u32 v, n;
    bf16x8 p;
    asm volatile(SPIN_HEAD "ds_read_u8 %[v], %[b] offset:%[o]\n\tds_read_b128 %[p], %[a] offset:%[q]\n\t" SPIN_TAIL("v_cmp_ne_u32 vcc, 1")
        : [v] "=&v"(v), [p] "=&v"(p), [n] "=&s"(n) : [b] "v"(fb), [o] "i"(off), [a] "v"(pa), [q] "i"(poff) : "memory", "vcc", "scc");
    return p;
}
DI void wait4p(u32 fb, u32 off, u32 pa, u32 poff, bf16x8 (&p)[4]) {
    u32 v, n;
    asm volatile(SPIN_HEAD "ds_read_b32 %[v], %[b] offset:%[o]\n\tds_read_b128 %[p0], %[a] offset:%[q0]\n\tds_read_b128 %[p1], %[a] offset:%[q1]\n\tds_read_b128 %[p2], %[a] offset:%[q2]\n\tds_read_b128 %[p3], %[a] offset:%[q3]\n\t"
        SPIN_TAIL("v_cmp_ne_u32 vcc, 0x1010101")
        : [v] "=&v"(v), [p0] "=&v"(p[0]), [p1] "=&v"(p[1]), [p2] "=&v"(p[2]), [p3] "=&v"(p[3]), [n] "=&s"(n)
        : [b] "v"(fb), [o] "i"(off), [a] "v"(pa), [q0] "i"(poff), [q1] "i"(poff + 1024), [q2] "i"(poff + 2048), [q3] "i"(poff + 3072) : "memory", "vcc", "scc");
}
template <u32 KO> DI void land_wait_k(u32 fl, u32 off, u32 a0, u32 a1, u32 a2, u32 a3, bf16x8& f0, bf16x8& f1, bf16x8& f2, bf16x8& f3) {
    u32 v, n;
    asm volatile(SPIN_HEAD "ds_read_b32 %[v], %[b] offset:%[o]\n\tds_read_b128 %[p0], %[a0] offset:%[k]\n\tds_read_b128 %[p1], %[a1] offset:%[k]\n\tds_read_b128 %[p2], %[a2] offset:%[k]\n\tds_read_b128 %[p3], %[a3] offset:%[k]\n\t"
        SPIN_TAIL("v_cmp_eq_u32 vcc, 0")
        : [v] "=&v"(v), [p0] "=&v"(f0), [p1] "=&v"(f1), [p2] "=&v"(f2), [p3] "=&v"(f3), [n] "=&s"(n)
        : [b] "v"(fl), [o] "i"(off), [a0] "v"(a0), [a1] "v"(a1), [a2] "v"(a2), [a3] "v"(a3), [k] "i"(KO) : "memory", "vcc", "scc");
}
DI void dma_a(const char* src, const u32 (&od)[4], u32 dst) {
    asm volatile("s_mov_b32 m0, %3\n\ts_nop 0\n\tglobal_load_lds_dwordx4 %0, %2 nt\n\tglobal_load_lds_dwordx4 %1, %2 offset:1024 nt" :: "v"(od[0]), "v"(od[1]), "s"(src), "s"(dst) : "memory");
}
DI void dma_b(const char* src, const u32 (&od)[4], u32 dst, u64 pc, u32 flag) {
    asm volatile("s_mov_b32 m0, %4\n\ts_nop 0\n\tglobal_load_lds_dwordx4 %1, %3 offset:2048 nt\n\tglobal_load_lds_dwordx4 %2, %3 offset:3072 nt\n\t"
        "s_mov_b32 m0, %6\n\ts_mov_b64 exec, 1\n\tglobal_load_lds_dword %0, %5\n\ts_mov_b64 exec, -1"
        :: "v"(od[0]), "v"(od[2]), "v"(od[3]), "s"(src), "s"(dst), "s"(pc), "s"(flag) : "memory");
}
static_assert(C_LAND == 0, "");
static_assert(((L_FL >> 16) & 0xff) == 1, "flag_set writes bits 23:16 of the address");
template <int K, bool PRE = false> DI u32 hint_wait(const char* ws, u32 oh, u32 tlo, int& budget, v4u h0 = v4u()) {
    v4u h[1] = {h0};
    if (!PRE || __builtin_popcount((u32)ballot(h[0].z == tlo) & 0xffu) < K) {
#pragma clang loop unroll(disable)
        do {
            pass<0>(ws, oh, h);
            if (__builtin_popcount((u32)ballot(h[0].z == tlo) & 0xffu) >= K) break;
            __builtin_amdgcn_s_sleep(HINT_SLEEP);
        } while (--budget > 0);
    }
    const u32 now = (u32)__builtin_amdgcn_s_memrealtime();
    u32 age = h[0].z == tlo ? now - h[0].x : 0xffffu;
    age = age > 0xffffu ? 0u : age;
    age = min(age, dppu<0xB1>(age));
    age = min(age, dppu<0x4E>(age));
    age = min(age, dppu<0x124>(age));
    return now - rfl(age);
}
DI u32 now32() { return (u32)__builtin_amdgcn_s_memrealtime(); }
DI void until(u32 t) { while ((int)(now32() - t) < 0) __builtin_amdgcn_s_sleep(1); }
DI u32 time_base(const char* p, u32 t0) {
    v16u h;
    asm volatile("s_load_dwordx16 %0, %1, 0x0\n\ts_waitcnt lgkmcnt(0)" : "=s"(h) : "s"(p) : "memory");
    u32 m = 0, bad = 0;
#pragma unroll
    for (int k = 0; k < 8; ++k) { m = max(m, h[2 * k] - t0 + 150u); bad |= h[2 * k] ^ ~h[2 * k + 1]; }
    return (bad != 0u || m >= 300u) ? t0 + SKEW_MAX : t0 - 150u + m;
}
struct QTimer { u32 t, n; };
DI bool q_due(QTimer& q, const char* ws, u32 tlo) {
    const u32 now = now32();
    if ((int)(now - q.t) < 0) return false;
    if (q.n >= NQH) return true;
    v16u h0, h1;
    asm volatile("s_load_dwordx16 %0, %2, 0x0\n\ts_load_dwordx16 %1, %2, 0x40\n\ts_waitcnt lgkmcnt(0)" : "=&s"(h0), "=&s"(h1) : "s"(ws + WS_QH + q.n * 128u) : "memory");
    u32 m = 0xffffffffu, bad = 0;
#pragma unroll
    for (int k = 0; k < 8; ++k) { m = min(m, min(now + 1000u - h0[2 * k], now + 1000u - h1[2 * k])); bad |= (h0[2 * k] ^ h0[2 * k + 1] ^ tlo) | (h1[2 * k] ^ h1[2 * k + 1] ^ tlo); }
    if (bad == 0u) { q.n = NQH; q.t = now + 1000u - max(m, 900u) + Q_DELAY - Q_PRE; } else { ++q.n; q.t = now + Q_STEP; }
    return false;
}
DI void look_times(const char* ws, int lane, u32 tlo, int& budget, u32& t1, u32& t2) {
    v4u fc[1];
#pragma clang loop unroll(disable)
    do {
        pass<0>(ws, WS_FC + (u32)lane * 16u, fc);
        if (ballot(neq3(fc[0].y, fc[0].w, tlo)) == 0ull) break;
        until(now32() + 30);
    } while (--budget > 0);
    const u32 now = now32(), x = min(fc[0].x + 1000u - now, 2000u), z = min(fc[0].z + 1000u - now, 2000u);
    u32 m = max(x, z), a = x + z, e, od;
    m = max(m, dppu<0x128>(m)); m = max(m, dppu<0x124>(m)); m = max(m, dppu<0x122>(m)); m = max(m, dppu<0x121>(m));
    a += dppu<0x128>(a); a += dppu<0x124>(a); a += dppu<0x122>(a); a += dppu<0x121>(a);
    both16(m, e, od); both32(max(e, od), e, od);
    m = rfl(max(e, od));
    both16(a, e, od); both32(e + od, e, od);
    a = (rfl(e + od) >> 7) + FC_C;
    t1 = m < 2000u ? now - 1000u + a : now;
    t2 = m < 2000u ? now - 1000u + max(m, a) : now;
}
DI u32 swz(u32 row) { return ((row & 3u) << 2) | ((row >> 2) & 3u); }

#define EPOCH() do { asm volatile("s_load_dwordx2 %0, %1, 0x0\n\ts_waitcnt lgkmcnt(0)" : "=s"(e64) : "s"(ws) : "memory"); ++e64; } while (0)
DI void wave_sum2(float s1, float s2, float& t1, float& t2) {
    float z = red32(s1, s2);
    z = red16(z, z); z = rowsum(z);
    u32 a, b;
    both32(asu(z), a, b);
    t1 = asf(a); t2 = asf(b);
}
DI void ln_stats(const v4u& xa, float& mean, float& rstd) {
    const float pil = asf(rfl(xa[0]) << 16);
    float s1 = 0.f, s2 = 0.f;
#pragma unroll
    for (int k = 0; k < 4; ++k) { const float a = bflo(xa[k]) - pil, c = bfhi(xa[k]) - pil; s1 += a; s2 = __builtin_fmaf(a, a, s2); s1 += c; s2 = __builtin_fmaf(c, c, s2); }
    float t1, t2;
    wave_sum2(s1, s2, t1, t2);
    const float dm = t1 * (1.f / 512.f);
    mean = pil + dm;
    rstd = __builtin_amdgcn_rsqf(__builtin_fmaxf(t2 * (1.f / 512.f) - dm * dm, 0.f) + 1e-5f);
}
DI float sigm(float v) { return __builtin_amdgcn_rcpf(1.f + ex2(v * -1.4426950408889634f)); }
DI float silu(float v) { return v * sigm(v); }

template <int NW2, bool S8> DI f32x2 reduce16(const char* ws, u32 base, u32 tlo, u32 thi, int wave, int lane, u32 lds_red, u32 tgo, int& budget) {
    u32 la = lds_red + (u32)(wave * 16 + (S8 ? (lane & 7) + (lane >> 5) * 8 : (lane & 7) * 2 + (lane >> 5))) * 4, ra = lds_red + (u32)lane * 8, go = base + (u32)(wave * 64 + lane) * 16u;
    const bool two = NW2 == 8 || wave < NW2;
    const char* w2 = ws + (two ? 8192 : 0);
    float f2 = two ? 1.f : 0.f;
    PIN(la); PIN(ra); PIN(go);
    asm volatile("" : "+s"(w2), "+v"(f2));
    until(tgo);
    v4u g0, g1;
    poll2<false>(g0, g1, go, ws, go, w2, tlo, thi);
    float z = red32(__builtin_fmaf(asf(g1.x), f2, asf(g0.x)), __builtin_fmaf(asf(g1.y), f2, asf(g0.y)));
    z = red16(z, z); z += dpp<0x128>(z);
    *lp<float>(la) = z;
    barrier();
    f32x2 s = {0.f, 0.f};
    if (wave == 0) {
        s = *lp<f32x2>(ra);
        float t = red32(s.x, s.y);
        t = red16(t, t); t += dpp<0x128>(t);
        u32 a, c;
        both32(asu(t), a, c);
        s = (f32x2){asf(a), asf(c)};
    }
    return s;
}

#define HE_K 8
DI u32 mlp_id(int b) { const int xc = b & 7, yy = (b >> 3) - 16; return (u32)(((yy >> 2) * 8 + xc) * 4 + (yy & 3)); }
DI void mlp_part(const Params& P, int b, u32 w, int lane, u32 tlo, u32 thi, int& budget, u32& tl) {
    char* const ws = late_ws(P.ws, tlo);
    const u32 gu_id = mlp_id(b), i15 = lane & 15, g = lane >> 4;
    u32 wo = (128 * w + ((u32)lane >> 1)) * 3584u + gu_id * 32u + ((u32)lane & 1) * 16u, wdo = (gu_id * 16 + g) * 1024u + w * 256u + i15 * 16u;
    PIN(wo); PIN(wdo);
    const u32 q = gu_id & 3;
    u32 ha = L_H2 + w * 256 + (((u32)lane - 16 * q) & 63) * 4, hr = L_H2 + w * 256 + g * 16 + min(i15 & 3, 2u) * 1088, ga = L_GU + w * 512 + i15 * 16 + g * 4096, gr = L_GU + (u32)lane * 16;
    u32 sa = L_GU + 2048 + (u32)(lane >> 5) * 16 + w * 4, heo = WS_HE + (u32)(b - 128) * HSTR + (u32)(lane & 7) * 16u, px = piece(WS_X1, 4096, (u32)(b & 7)) + (u32)(lane & 7) * 16u;
    PIN(sa); PIN(heo); PIN(px);
    u32 so = piece(WS_YP, 16384, 8 * w + (u32)(lane >> 3)) + (gu_id * 8 + (lane & 7)) * 16, sco = WS_SC + (64 * w + (u32)lane) * 16u;
    u32 ta = L_W + w * 8192u + (8 * g + (i15 >> 2)) * 32 + (lane & 3) * 8, xo = piece(WS_X1, 4096, (u32)(b & 7)) + w * 1024u + (u32)lane * 16u;
    PIN(ha); PIN(hr); PIN(ga); PIN(gr); PIN(so); PIN(sco); PIN(ta); PIN(xo);
    barrier();
    STAMP(10);
    u32 wdr[16], tk1, tk2;
    {
        look_times(ws, lane, tlo, budget, tk1, tk2);
        const u32 tg = tk2 + G_T;
        const u32 pg = (w * 128 + q * 32 + ((u32)lane >> 3)) * 3584u + (gu_id >> 2) * 128u + ((u32)lane & 7) * 16u;
        until(tg);
#pragma unroll
        for (int k = 0; k < 4; ++k) { dma_pl(P.wg + k * 8 * 1792, pg, L_W1 + w * 8192u + k * 1024u); dma_pl(P.wu + k * 8 * 1792, pg, L_W1 + w * 8192u + 4096u + k * 1024u); }
#pragma unroll
        for (int k = 0; k < 4; ++k) dma_nt(P.wd + k * 2048, wdo, L_WD + w * 4096u + k * 1024u);
        VMWAIT(4);
        until(tg + G_2);
    }
#pragma unroll
    for (int s = 1; s < 4; ++s) { const u32 r = ((q + s) & 3) * 32 * 1792; dma_pl(P.wg + r, wo, L_W + w * 8192u + s * 1024u); dma_pl(P.wu + r, wo, L_W + w * 8192u + 4096u + s * 1024u); }
    bf16x8 bg[4], bp[4];
    {
        const u32 t1 = L_W1 + w * 8192u + (8 * g + (i15 >> 2)) * 128 + q * 32 + (lane & 3) * 8;
        bg[0] = tr2(t1, t1 + 512); bp[0] = tr2(t1 + 4096, t1 + 4608);
    }
    v4u mg[1], hh;
    UNDEF(mg[0]); UNDEF(hh);
    ld_sc_into(mg[0], ws, sco);
    ld_sc_into(hh, ws, heo);
    STAMP(2);
    VMWAIT(8);
#pragma unroll
    for (int j = 0; j < 16; j += 2) {
        const u32 a = *lp<u32>(L_WD + w * 4096u + j * 256u + (u32)lane * 4), bb = *lp<u32>(L_WD + w * 4096u + j * 256u + 256u + (u32)lane * 4);
        wdr[j] = perm_lo(a, bb); wdr[j + 1] = perm_hi(a, bb);
    }
#pragma unroll
    for (int j = 0; j < 16; ++j) PIN(wdr[j]);
    VMWAIT(0);
    PIN(mg[0]); PIN(hh);
    STAMP(3);
#pragma unroll
    for (int s = 1; s < 4; ++s) { bg[s] = tr2(ta + s * 1024, ta + s * 1024 + 128); bp[s] = tr2(ta + 4096 + s * 1024, ta + 4096 + s * 1024 + 128); }
    if (__builtin_expect(ballot(((mg[0].z ^ tlo) | (mg[0].w ^ thi)) != 0u) != 0ull, 0)) gather<1, 0>(ws, sco, tlo, thi, mg, budget);
    float cb0 = 1.f + bflo(mg[0].x), cb1 = 1.f + bfhi(mg[0].x);
    *lp<u32>(ha + 1088) = pack2(cb0, cb1);
    *lp<u32>(ha + 2176) = mg[0].y;
    PIN(cb0); PIN(cb1);
    bf16x8 af[4];
    v4u yo = {0u, 0u, tlo, thi};
    PIN(yo);
#pragma unroll
    for (int s = 0; s < 4; ++s) { PIN(bg[s]); PIN(bp[s]); }
    STAMP(12);
    until(hint_wait<HE_K, true>(ws, heo, tlo, budget, hh) + E_DELAY);
    until(tk2 + E_T);
    STAMP(8);
    v4u xg, xp;
    poll2<false>(xg, xp, xo, ws, px, ws, tlo, thi);
    STAMP(13);
    float pil = asf(xp.x) + asf(xp.y);
    pil += dpp<0xB1>(pil); pil += dpp<0x4E>(pil); pil += dpp<0x141>(pil);
    float d0 = __builtin_fmaf(pil, -0.0625f, asf(xg.x)), d1 = __builtin_fmaf(pil, -0.0625f, asf(xg.y));
    PIN(d0); PIN(d1);
    {
        float e0 = d0 * cb0;
        PIN(e0);
        *lp<u32>(ha) = pack2(e0, d1 * cb1);
    }
    asm volatile("" ::: "memory");
#pragma unroll
    for (int s = 0; s < 4; ++s) af[s] = *lp<bf16x8>(hr + s * 64);
    SCHED();
    float z = red32(d0 + d1, __builtin_fmaf(d0, d0, d1 * d1));
    z = red16(z, z); z = rowsum(z);
    *lp<float>(sa) = z;
    SCHED();
    f32x4 dg = {0.f, 0.f, 0.f, 0.f}, du = {0.f, 0.f, 0.f, 0.f};
#pragma unroll
    for (int s = 0; s < 4; ++s) {
        dg = __builtin_amdgcn_mfma_f32_16x16x32_bf16(af[s], bg[s], dg, 0, 0, 0);
        du = __builtin_amdgcn_mfma_f32_16x16x32_bf16(af[s], bp[s], du, 0, 0, 0);
    }
    *lp<f32x4>(ga) = dg; *lp<f32x4>(ga + 256) = du;
    barrier();
    STAMP(11);
    const f32x4 q1 = *lp<f32x4>(L_GU + 2048), q2 = *lp<f32x4>(L_GU + 2064);
    const f32x4 p0 = *lp<f32x4>(gr), p1 = *lp<f32x4>(gr + 1024);
    const float dm = sum4(q1) * (1.f / 512.f);
    const float rstd = __builtin_amdgcn_rsqf(__builtin_fmaxf(sum4(q2) * (1.f / 512.f) - dm * dm, 0.f) + 1e-5f);
    float gx = p0.x + p1.x, gy = p0.y + p1.y;
    PIN(gx); PIN(gy);
    float s = rstd * (gx - dm * gy) + (p0.z + p1.z);
    s = red32(s, s);
    u32 gs, us;
    both16(asu(s), gs, us);
    const float gsum = asf(gs), usum = asf(us);
    const float act = gsum * usum * __builtin_amdgcn_rcpf(1.f + ex2(gsum * __builtin_fmaf(gsum * gsum, -0.044715f * 1.5957691216057308f * 1.4426950408889634f, -1.5957691216057308f * 1.4426950408889634f)));
    const u32 actp = pack2(act, dpp<0x101>(act));
    asm volatile("s_nop 1\n\t"
        "v_dot2c_f32_bf16_dpp %0, %2, %3 row_newbcast:0 row_mask:0xf bank_mask:0xf\n\tv_dot2c_f32_bf16_dpp %1, %2, %4 row_newbcast:0 row_mask:0xf bank_mask:0xf\n\t"
        "v_dot2c_f32_bf16_dpp %0, %2, %5 row_newbcast:2 row_mask:0xf bank_mask:0xf\n\tv_dot2c_f32_bf16_dpp %1, %2, %6 row_newbcast:2 row_mask:0xf bank_mask:0xf\n\t"
        "v_dot2c_f32_bf16_dpp %0, %2, %7 row_newbcast:4 row_mask:0xf bank_mask:0xf\n\tv_dot2c_f32_bf16_dpp %1, %2, %8 row_newbcast:4 row_mask:0xf bank_mask:0xf\n\t"
        "v_dot2c_f32_bf16_dpp %0, %2, %9 row_newbcast:6 row_mask:0xf bank_mask:0xf\n\tv_dot2c_f32_bf16_dpp %1, %2, %10 row_newbcast:6 row_mask:0xf bank_mask:0xf\n\t"
        "v_dot2c_f32_bf16_dpp %0, %2, %11 row_newbcast:8 row_mask:0xf bank_mask:0xf\n\tv_dot2c_f32_bf16_dpp %1, %2, %12 row_newbcast:8 row_mask:0xf bank_mask:0xf\n\t"
        "v_dot2c_f32_bf16_dpp %0, %2, %13 row_newbcast:10 row_mask:0xf bank_mask:0xf\n\tv_dot2c_f32_bf16_dpp %1, %2, %14 row_newbcast:10 row_mask:0xf bank_mask:0xf\n\t"
        "v_dot2c_f32_bf16_dpp %0, %2, %15 row_newbcast:12 row_mask:0xf bank_mask:0xf\n\tv_dot2c_f32_bf16_dpp %1, %2, %16 row_newbcast:12 row_mask:0xf bank_mask:0xf\n\t"
        "v_dot2c_f32_bf16_dpp %0, %2, %17 row_newbcast:14 row_mask:0xf bank_mask:0xf\n\tv_dot2c_f32_bf16_dpp %1, %2, %18 row_newbcast:14 row_mask:0xf bank_mask:0xf\n\ts_nop 2"
        : "+v"(yo.x), "+v"(yo.y) : "v"(actp), "v"(wdr[0]), "v"(wdr[1]), "v"(wdr[2]), "v"(wdr[3]), "v"(wdr[4]), "v"(wdr[5]), "v"(wdr[6]), "v"(wdr[7]),
          "v"(wdr[8]), "v"(wdr[9]), "v"(wdr[10]), "v"(wdr[11]), "v"(wdr[12]), "v"(wdr[13]), "v"(wdr[14]), "v"(wdr[15]));
    st_sc(ws, so, yo);
    STAMP(14);
}

#define ISSUE(h) do { if ((h) & 1) dma_b(ckv + ktile(sidx, (h) >> 1), od, slot((h) >> 1) + p * 4096u, pcf, L_FL + C_LAND + ((h) >> 1) * 16u + p * 4u); else dma_a(ckv + ktile(sidx, (h) >> 1), od, slot((h) >> 1) + p * 4096u); } while (0)
constexpr int ops_in(int h0, int h1) { int n = 0; for (int h = h0; h < h1; ++h) n += 2 + (h & 1); return n; }
DI void publish(char* ws, u32 g1, const f32x16 (&o)[4], v4u (&d)[8]) {
#pragma unroll
    for (int k = 0; k < 8; k += 2) {
        u32 c[8];
#pragma unroll
        for (int i = 0; i < 4; ++i) { c[i] = pack2(o[0][2 * k + i], o[1][2 * k + i]); c[4 + i] = pack2(o[2][2 * k + i], o[3][2 * k + i]); }
        SCHED();
#pragma unroll
        for (int i = 0; i < 2; ++i) {
            auto r0 = __builtin_amdgcn_permlane16_swap(c[2 * i], c[2 * i + 1], false, false);
            auto r1 = __builtin_amdgcn_permlane16_swap(c[4 + 2 * i], c[5 + 2 * i], false, false);
            d[k + i] = (v4u){(u32)r0[0], (u32)r0[1], (u32)r1[0], (u32)r1[1]};
            st_sc_keep(ws + WS_PAY + (k + i) * PHD, g1, d[k + i]);
        }
        SCHED();
    }
}
DI void stream_pv(const Params& P, int b, u32 p, int lane, const u32 (&od)[4], u64 pcf, u32 t0, u32& tl) {
    const u32 tz = t0; (void)tz;
    char* ws = P.ws;
    const u32 sidx = (u32)b - 128, i15 = lane & 15, R = lane >> 4;
    const char* ckv = (const char*)P.ckv;
    u32 va[2][4];
    {
        const u32 hf = R >> 1, blk = R & 1, q4 = i15 >> 2, p4 = i15 & 3;
#pragma unroll
        for (int e = 0; e < 2; ++e)
#pragma unroll
            for (int cb = 0; cb < 4; ++cb) {
                const u32 row = 8 * hf + 4 * e + q4;
                va[e][cb] = p * 4096u + row * 256u + (((4 * cb + 2 * blk + (p4 >> 1)) ^ swz(row)) << 4) + 8 * (p4 & 1);
                PIN(va[e][cb]);
            }
    }
    u32 fb = L_FL, pa = L_P + (u32)lane * 16u;
    PIN(fb); PIN(pa);
    bf16x8 V[8][4];
    u32 vbase = 0;
#define VBASE(t) do { if (vwin(t) != vbase) { _Pragma("unroll") for (int i_ = 0; i_ < 8; ++i_) { va[i_ >> 2][i_ & 3] += vwin(t) - vbase; PIN(va[i_ >> 2][i_ & 3]); } vbase = vwin(t); } } while (0)
#define VREAD(t) do { VBASE(t); _Pragma("unroll") for (int cb_ = 0; cb_ < 4; ++cb_) V[(t) & 7][cb_] = tr2(va[0][cb_] + (slot(t) - vwin(t)), va[1][cb_] + (slot(t) - vwin(t))); } while (0)
    u64 e64;
    EPOCH();
    const u32 tlo = (u32)e64;
    ws = late_ws(ws, tlo);
    u32 g1, gc, lr;
    {
        const u32 k = 4 * (i15 >> 3) + (i15 & 3);
        g1 = (p * 128u + (sidx ^ late_flip(tlo))) * 256u + R * (8 * PHD) + i15 * 16u;
        gc = WS_CHK + (sidx * 4 + p) * 512u + (8 * R + k) * 16u;
        lr = L_L + (8 * (k >> 1) + 4 * (R >> 1) + 2 * (k & 1) + (R & 1)) * 16u;
        PIN(g1); PIN(gc); PIN(lr);
    }
    u32 fg = L_FL + (lane & 1) * 4, gv = 0;
    PIN(fg);
#pragma unroll
    for (int h = HW; h < 32; ++h) {
        const int l = h - HW;
        if (h >= 12 && h < 31) gate_wait(gv, fg, C_GATE + (h / 2 - 6) * 8);
        VMWAIT(ops_in(l + 1, h));
        if (l == 1) STAMP(18);
        if (l == 7) STAMP(19);
        if (l == 15) STAMP(1);
        if (h == 24) STAMP(2);
        ISSUE(h);
        if (h >= 11 && h < 30) gv = *(volatile LAS unsigned char*)lp<unsigned char>(fg + C_GATE + ((h + 1) / 2 - 6) * 8);
        if ((l & 1) && l < 16) VREAD(l >> 1);
    }
    static_assert(HW >= 3 && HW <= 6, "tile 12 must have landed here");
    f32x16 o[4];
#pragma unroll
    for (int cb = 0; cb < 4; ++cb)
#pragma unroll
        for (int i = 0; i < 16; ++i) o[cb][i] = 0.f;
    bf16x8 pb[4], pc[4];
#define PV(t, pp) do { SCHED(); _Pragma("unroll") for (int cb_ = 0; cb_ < 4; ++cb_) o[cb_] = __builtin_amdgcn_mfma_f32_32x32x16_bf16(pp[(t) & 3], V[(t) & 7][cb_], o[cb_], 0, 0, 0); SCHED(); } while (0)
#define PVR(t, pp, u) do { VBASE(u); _Pragma("unroll") for (int cb_ = 0; cb_ < 4; ++cb_) { SCHED(); \
        o[cb_] = __builtin_amdgcn_mfma_f32_32x32x16_bf16(pp[(t) & 3], V[(t) & 7][cb_], o[cb_], 0, 0, 0); SCHED(); \
        V[(u) & 7][cb_] = tr2(va[0][cb_] + (slot(u) - vwin(u)), va[1][cb_] + (slot(u) - vwin(u))); } SCHED(); } while (0)
    STAMP(3);
    if (p == 0) {
        const v4u z = *lp<v4u>(fb + C_TZ);
        const u32 t = now32();
        if (lane == 0) st_sc2(ws, WS_FC + sidx * 8u, (v2u){t + max((int)FC_A, FC_B - (int)min(min(t - z.x, t - z.y), min(t - z.z, t - z.w))), tlo});
    }
    wait4p(fb, C_P, pa, 0, pb);
    STAMP(7);
    CYC0();
    CDECL();
    CS(0);
#pragma unroll
    for (int t = 0; t < 5; ++t) { PVR(t, pb, t + 8); if (t < 4) pb[t] = *lp<bf16x8>(pa + (t + 4) * 1024); if (t == 0) CS(1); if (t == 1) CS(2); if (t == 3) CS(3); }
    CS(4);
    PV(5, pb); PV(6, pb);
    u32 pf = flag_get(C_P + 4);
#pragma unroll
    for (int i = 0; i < 4; ++i) pc[i] = *(volatile LAS bf16x8*)lp<bf16x8>(pa + (8 + i) * 1024);
    PV(7, pb);
    STAMP(4);
    CS(5);
    if (__builtin_expect(rfl(pf) != 0x01010101u, 0)) wait4p(fb, C_P + 4, pa, 8192, pc);
    CS(6);
    PV(8, pc);
    VMWAIT(5);
    PVR(9, pc, 13); PVR(10, pc, 14);
    pf = flag_get(C_P + 8);
#pragma unroll
    for (int i = 0; i < 3; ++i) pb[i] = *(volatile LAS bf16x8*)lp<bf16x8>(pa + (12 + i) * 1024);
    PV(11, pc);
    STAMP(5);
    CS(7);
    pf = rfl(pf);
    if (__builtin_expect(!(pf & 0x1u), 0)) pb[0] = wait1p(fb, C_P + 8, pa, 12 * 1024);
    PV(12, pb);
    if (__builtin_expect(!(pf & 0x100u), 0)) pb[1] = wait1p(fb, C_P + 9, pa, 13 * 1024);
    VBASE(15);
    PV(13, pb);
    if (__builtin_expect(!(pf & 0x10000u), 0)) pb[2] = wait1p(fb, C_P + 10, pa, 14 * 1024);
    VMWAIT(0);
    PVR(14, pb, 15);
    pb[3] = wait1p(fb, C_P + 11, pa, 15 * 1024);
    PV(15, pb);
    STAMP(6);
    STAMPC(29);
    CS(8);
    v4u d[8];
    publish(ws, g1, o, d);
    v2u fl = *(volatile LAS v2u*)lp<v2u>(fb + C_L);
    f32x4 lq = *(volatile LAS f32x4*)lp<f32x4>(lr);
    u32 mref = 0;
    STAMP(8);
    CS(9);
    u32 cs[8];
#pragma unroll
    for (int k = 0; k < 8; ++k) { cs[k] = mix4(d[k]); PIN(cs[k]); }
    if (__builtin_expect(rfl(fl.x) != 0x01010101u || rfl(fl.y) != 0u, 0)) {
        wait4(fb, C_L);
        if (rfl(flag_get(C_BAD)) != 0u) {
            barrier();
            barrier();
#pragma unroll
            for (int cb = 0; cb < 4; ++cb)
#pragma unroll
                for (int i = 0; i < 16; ++i) o[cb][i] = 0.f;
#pragma clang loop unroll(disable)
            for (u32 t = 0; t < 16; ++t) {
                if (t == 8) {
                    LGKM0();
#pragma clang loop unroll(disable)
                    for (u32 u = 0; u < 8; ++u) { dma_a(ckv + ktile(sidx, u), od, slot_no(u + 8) * 16384u + p * 4096u); dma_b(ckv + ktile(sidx, u), od, slot_no(u + 8) * 16384u + p * 4096u, pcf, L_FL + C_LAND); }
                    VMWAIT(0);
                }
                const u32 so = slot_no(t | 8) * 16384u - vbase;
#pragma unroll
                for (int cb = 0; cb < 4; ++cb) V[7][cb] = tr2(va[0][cb] + so, va[1][cb] + so);
                pc[3] = *lp<bf16x8>(pa + (t ^ 8) * 1024);
                PV(15, pc);
            }
            publish(ws, g1, o, d);
            asm volatile("s_nop 1");
#pragma unroll
            for (int k = 0; k < 8; ++k) { cs[k] = mix4(d[k]); PIN(cs[k]); }
            mref = *(volatile LAS u32*)lp<u32>(L_M + (lr - L_L) / 4);
        }
        lq = *(volatile LAS f32x4*)lp<f32x4>(lr);
    }
    {
        u32 t0, t1, t2, t3, u0, u1;
        asm volatile("s_nop 1\n\t"
            "v_add_u32_dpp %0, %6, %6 row_ror:8 row_mask:0xf bank_mask:0x3\n\tv_add_u32_dpp %1, %7, %7 row_ror:8 row_mask:0xf bank_mask:0x3\n\t"
            "v_add_u32_dpp %2, %8, %8 row_ror:8 row_mask:0xf bank_mask:0x3\n\tv_add_u32_dpp %3, %9, %9 row_ror:8 row_mask:0xf bank_mask:0x3\n\t"
            "v_add_u32_dpp %0, %10, %10 row_ror:8 row_mask:0xf bank_mask:0xc\n\tv_add_u32_dpp %1, %11, %11 row_ror:8 row_mask:0xf bank_mask:0xc\n\t"
            "v_add_u32_dpp %2, %12, %12 row_ror:8 row_mask:0xf bank_mask:0xc\n\tv_add_u32_dpp %3, %13, %13 row_ror:8 row_mask:0xf bank_mask:0xc\n\t"
            "v_add_u32_dpp %4, %0, %0 row_half_mirror row_mask:0xf bank_mask:0x5\n\tv_add_u32_dpp %5, %1, %1 row_half_mirror row_mask:0xf bank_mask:0x5\n\t"
            "v_add_u32_dpp %4, %2, %2 row_half_mirror row_mask:0xf bank_mask:0xa\n\tv_add_u32_dpp %5, %3, %3 row_half_mirror row_mask:0xf bank_mask:0xa\n\t"
            "s_nop 0\n\t"
            "v_add_u32_dpp %4, %4, %4 quad_perm:[1,0,3,2] row_mask:0xf bank_mask:0xf\n\ts_nop 0\n\tv_add_u32_dpp %5, %5, %5 quad_perm:[1,0,3,2] row_mask:0xf bank_mask:0xf\n\t"
            "v_add_u32_dpp %4, %4, %4 quad_perm:[2,3,0,1] row_mask:0xf bank_mask:0xf\n\ts_nop 0\n\tv_add_u32_dpp %5, %5, %5 quad_perm:[2,3,0,1] row_mask:0xf bank_mask:0xf\n\ts_nop 0"
            : "=&v"(t0), "=&v"(t1), "=&v"(t2), "=&v"(t3), "=&v"(u0), "=&v"(u1)
            : "v"(cs[0]), "v"(cs[1]), "v"(cs[2]), "v"(cs[3]), "v"(cs[4]), "v"(cs[5]), "v"(cs[6]), "v"(cs[7]));
        const u32 x = (gc & 16u) ? u1 : u0;
        const v4u cg = {(gc & 32u) ? dppu<0x104>(x) : x, asu(sum4(lq)), mref, tlo};
        asm volatile("s_mov_b64 exec, %3\n\tglobal_store_dwordx4 %0, %1, %2 sc1\n\ts_mov_b64 exec, -1\n\ts_nop 0" :: "v"(gc), "v"(cg), "s"(ws), "s"(0x0f0f0f0f0f0f0f0full) : "memory");
    }
    CS(10);
    STAMP(9);
    COUT(11);
    barrier();
}

DI void p_write(const f32x4& s0, const f32x4& s1, u32 pa, f32x4& a, f32x4& c) {
#pragma unroll
    for (int k = 0; k < 4; ++k) a[k] = ex2(s0[k]);
#pragma unroll
    for (int k = 0; k < 4; ++k) c[k] = ex2(s1[k]);
    *lp<v2u>(pa) = (v2u){pack2(a.x, a.y), pack2(a.z, a.w)};
    *lp<v2u>(pa + 256) = (v2u){pack2(c.x, c.y), pack2(c.z, c.w)};
}
template <u32 KO> DI void qk_second(const bf16x8 (&q0)[16], const bf16x8 (&q1)[16], const u32 (&a)[16], u32 fw, u32 off, f32x4& s0, f32x4& s1, u64& cw) {
    bf16x8 fa[16];
    land_wait_k<KO>(fw, off, a[0], a[1], a[2], a[3], fa[0], fa[1], fa[2], fa[3]);
#pragma unroll
    for (int i = 0; i < 4; ++i) { s0[i] = 0.f; s1[i] = 0.f; }
#pragma unroll
    for (int s = 0; s < 16; ++s) {
        SCHED();
        s0 = __builtin_amdgcn_mfma_f32_16x16x32_bf16(fa[s], q0[s], s0, 0, 0, 0);
        if (s < KD - 4) { SCHED(); fa[2 * s + 4] = *lp<bf16x8>(a[2 * s + 4] + KO); SCHED(); }
        s1 = __builtin_amdgcn_mfma_f32_16x16x32_bf16(fa[s], q1[s], s1, 0, 0, 0);
        SCHED();
        if (s < KD - 4) fa[2 * s + 5] = *lp<bf16x8>(a[2 * s + 5] + KO);
        else if (s + KD < 16) fa[s + KD] = *lp<bf16x8>(a[s + KD] + KO);
    }
}
DI void stream_qk(const Params& P, int b, u32 w, int lane, u32 t0, int& budget, u32& tl) {
    char* const ws = P.ws;
    const int xc = b & 7, yy = (b >> 3) - 16;
    const u32 i15 = lane & 15, g = lane >> 4;
    u64 e64;
    EPOCH();
    const u32 tlo = (u32)e64, thi = (u32)(e64 >> 32);
    const u32 ak = g * 4096u + i15 * 256u + (swz(i15) << 4);
    const u32 c16 = i15 ^ g ^ ((w & 1) ? 12u : 0u);
    u32 qo = WS_QABS + (w ^ ((tlo & 1) << 1)) * 2048u + g * 256u + c16 * 16u, co = WS_QCHK + w * 1024u + (u32)lane * 16u;
    u32 va = L_IMG + ((lane & 1) ^ 1) * 16384u + (8 * w + (u32)(lane >> 3)) * 256u + (((((u32)lane >> 1) & 3) ^ ((w & 1) ? 3u : 0u)) << 6) + ((((u32)lane >> 2) & 2) + (lane & 1)) * 16u;
    u32 fa = L_FL + w, fb = L_FL, fw = L_FL + 16 * w + (i15 & 3) * 4, pa = L_P + w * 1024u + (g >> 1) * 512u + i15 * 16u + (g & 1) * 8u;
    u32 bq[4];
#pragma unroll
    for (int k = 0; k < 4; ++k) { bq[k] = L_IMG + i15 * 256u + (((g * 4 + k) ^ (i15 & 3) ^ ((i15 & 8) ? 12u : 0u)) << 4); PIN(bq[k]); }
    PIN(qo); PIN(co); PIN(va); PIN(fa); PIN(fb); PIN(fw); PIN(pa);
    u32 pa4 = pa + 4096;
    PIN(pa4);
    u32 gk = L_FL + C_GATE + (u32)lane * 8, m1_ = ~0u;
    PIN(gk); PIN(m1_);
#define HOLD() do { const u64 late_ = (1ull << (H_MIN / 2 - 6)) - 1; gate_or(gk + 4, 0x80u, 0x1ffull ^ late_); until(qt.t + Q_PRE); gate_or(gk + 4, 0x80u, 0x1ffull); } while (0)
    v4u chk;
    UNDEF(chk);
#define REQ_Q() do { \
        _Pragma("unroll") for (int k_ = 0; k_ < 8; ++k_) { const u32 k = (u32)(k_ ^ xc), x = (k & 1) * 1024u + (k >> 1) * 8192u; dma_sc(ws + x, qo, L_IMG + w * 2048u + (x ^ 16384u)); } \
        ld_sc_into(chk, ws, co); } while (0)
    u32 warm;
    UNDEF(warm);
    if (w == 3) asm volatile("global_load_dword %0, %1, %2\n\tglobal_load_dword %0, %1, %3\n\tglobal_load_dword %0, %1, %4\n\tglobal_load_dword %0, %5, %6" : "+v"(warm)
        : "v"(((u32)lane & 1) * (1792u * 1024u - 4u)), "s"(P.wg), "s"(P.wu), "s"(P.wd), "v"(WS_PAY + (((u32)lane & 3) * 10 + 1) * PHD + 4 * 128 * 256), "s"(late_ws(ws, tlo)) : "memory");
    __builtin_amdgcn_s_setprio(3);
    bf16x8 k0[16], k1[16];
    land_wait(fw, C_LAND);
    VMWAIT(0);
    PIN(warm);
    {
        u32 a = ak + slot_no(w) * 16384u; PIN(a);
#pragma unroll
        for (int s = 0; s < 16; ++s) k0[s] = *lp<bf16x8>(a ^ (u32)(s << 4));
        gate_add(gk, m1_, 2ull << w);
    }
    QTimer qt = {time_base(ws + WS_ST, t0) + Q_FIRST, 0u};
    u32 ka[16];
    {
        u32 a = ak + slot_no(w + 8) * 16384u; PIN(a);
#pragma unroll
        for (int s = 0; s < 16; ++s) { ka[s] = a ^ (u32)(s << 4); PIN(ka[s]); }
    }
    bool asked = false;
#pragma clang loop unroll(disable)
    while (ballot(*(volatile LAS u32*)lp<u32>(fw + C_LAND + 64) == 0u) != 0ull) {
        if (q_due(qt, ws, tlo)) {
            HOLD();
            REQ_Q();
            gate_add(gk + 4, 0x20u, 0x1ffull);
            land_wait(fw, C_LAND + 64);
            asked = true;
            break;
        }
        __builtin_amdgcn_s_sleep(3);
    }
    asm volatile("" ::: "memory");
    {
        u32 a = ak + slot_no(w + 4) * 16384u; PIN(a);
#pragma unroll
        for (int s = 0; s < 16; ++s) k1[s] = *lp<bf16x8>(a ^ (u32)(s << 4));
        gate_add(gk, m1_, 32ull << w);
    }
#pragma unroll
    for (int s = 0; s < 16; ++s) { PIN(k0[s]); PIN(k1[s]); }
    STAMP(1);
    if (!asked) {
#pragma clang loop unroll(disable)
        while (!q_due(qt, ws, tlo)) __builtin_amdgcn_s_sleep(1);
        HOLD();
        REQ_Q();
        gate_add(gk + 4, 0x20u, 0x1ffull);
    }
    asked = true;
    STAMP(22);
    u32 tz_;
#pragma clang loop unroll(disable)
    do {
        if (!asked) REQ_Q();
        asked = false;
        STAMP(20);
        VMWAIT(0);
        tz_ = now32();
        STAMP(4);
        PIN(chk);
        const v4u x0 = *(volatile LAS v4u*)lp<v4u>(va), x1 = *(volatile LAS v4u*)lp<v4u>(va ^ 16u), x2 = *(volatile LAS v4u*)lp<v4u>(va ^ 32u), x3 = *(volatile LAS v4u*)lp<v4u>(va ^ 48u);
        const v4u y0 = *(volatile LAS v4u*)lp<v4u>(va + 8192), y1 = *(volatile LAS v4u*)lp<v4u>((va ^ 16u) + 8192), y2 = *(volatile LAS v4u*)lp<v4u>((va ^ 32u) + 8192), y3 = *(volatile LAS v4u*)lp<v4u>((va ^ 48u) + 8192);
        const v4u xs = (x0 + x1) + (x2 + x3), ys = (y0 + y1) + (y2 + y3);
        const u32 bad = (mix4(xs) ^ chk.x) | (mix4(ys) ^ chk.z) | neq3(chk.y, chk.w, tlo);
        if (ballot(bad != 0u) == 0ull) break;
        __builtin_amdgcn_s_sleep(4);
    } while (--budget > 0);
    flag_set(fa, C_Z);
    *lp<u32>(L_FL + C_TZ + w * 4) = tz_;
    STAMP(5);
    wait4(fb, C_Z);
    STAMP(6);
    const u32 tz = t0; (void)tz;
    CYC0();
    u64 cs_[12];
    CS(0);
    f32x4 s[8];
#pragma unroll
    for (int i = 0; i < 4; ++i) s[i] = (f32x4){0.f, 0.f, 0.f, 0.f};
    bf16x8 q0[16], q1[16];
#define QREAD(s_) do { q0[s_] = *lp<bf16x8>(bq[(s_) & 3] + (((s_) >> 2) ^ 2) * 8192); q1[s_] = *lp<bf16x8>(bq[(s_) & 3] + (((s_) >> 2) ^ 2) * 8192 + 4096); } while (0)
#pragma unroll
    for (int i = 0; i < QD; ++i) QREAD(i);
#pragma unroll
    for (int i = 0; i < 16; ++i) {
        if (i + QD < 16) QREAD(i + QD);
        if (i + QD == 7) gate_add(gk, m1_, 0x200ull);
        SCHED();
        s[0] = __builtin_amdgcn_mfma_f32_16x16x32_bf16(k0[i], q0[i], s[0], 0, 0, 0);
        s[1] = __builtin_amdgcn_mfma_f32_16x16x32_bf16(k0[i], q1[i], s[1], 0, 0, 0);
        s[2] = __builtin_amdgcn_mfma_f32_16x16x32_bf16(k1[i], q0[i], s[2], 0, 0, 0);
        s[3] = __builtin_amdgcn_mfma_f32_16x16x32_bf16(k1[i], q1[i], s[3], 0, 0, 0);
        SCHED();
    }
    STAMP(21);
    STAMPC(29);
    CS(1);
    float l0, l1;
    f32x4 e0, e1, e2, e3;
    p_write(s[0], s[1], pa, e0, e1);
    p_write(s[2], s[3], pa4, e2, e3);
    flag_set(fa, C_P);
    SCHED();
    STAMP(24);
    CS(2);
    l0 = sum4(e0 + e2); l1 = sum4(e1 + e3);
    pa4 = pa + 8192;
    PIN(l0); PIN(l1); PIN(pa4);
    qk_second<0>(q0, q1, ka, fw, C_LAND + 128, s[4], s[5], cs_[3]);
    CS(4);
    p_write(s[4], s[5], pa4, e0, e1);
    flag_set(fa, C_P + 4);
    SCHED();
    STAMP(26);
    CS(5);
    l0 += sum4(e0); l1 += sum4(e1);
    float m0 = __builtin_fmaxf(max8(s[0], s[2]), max4(s[4])), m1 = __builtin_fmaxf(max8(s[1], s[3]), max4(s[5]));
    u32 ln = (u32)lane; PIN(ln);
    u32 la = L_L + (ln & 31) * 16 + w * 4, pl = pa + 12288;
    PIN(l0); PIN(l1); PIN(m0); PIN(m1); PIN(la); PIN(pl);
    if (w == 3) wait4(fb, C_P);
    qk_second<49152>(q0, q1, ka, fw, C_LAND + 192, s[6], s[7], cs_[6]);
    CS(7);
    STAMP(27);
    p_write(s[6], s[7], pl, e0, e1);
    flag_set(fa, C_P + 8);
    SCHED();
    STAMP(28);
    CS(8);
    {
        float z = red16(l0 + sum4(e0), l1 + sum4(e1));
        *lp<float>(la) = red32(z, z);
    }
    m0 = __builtin_fmaxf(m0, max4(s[6])); m1 = __builtin_fmaxf(m1, max4(s[7]));
    if (ballot(!(__builtin_fmaxf(__builtin_fabsf(m0), __builtin_fabsf(m1)) <= LAZY)) != 0ull) flag_set(fa, C_BAD);
    flag_set(fa, C_L);
    CS(9);
    COUT(10);
    __builtin_amdgcn_s_setprio(0);
    wait4(fb, C_L);
    if (__builtin_expect(rfl(flag_get(C_BAD)) != 0u, 0)) {
        m0 = max32(max16(m0)); m1 = max32(max16(m1));
        *lp<float>(la + (L_IMG + 4096 - L_L)) = (ln & 16) ? m1 : m0;
        barrier();
        const float m = max4(*lp<f32x4>((la & ~15u) + (L_IMG + 4096 - L_L)));
        *lp<float>(L_M + (ln & 31) * 4) = m;
        u32 e, od;
        both16(asu(m), e, od);
        l0 = 0.f; l1 = 0.f;
#pragma unroll
        for (int i = 0; i < 4; ++i) { p_write(s[2 * i] - asf(e), s[2 * i + 1] - asf(od), pa + 4096 * i, e0, e1); l0 += sum4(e0); l1 += sum4(e1); }
        const float z = red16(l0, l1);
        *lp<float>(la) = red32(z, z);
        barrier();
    }
    if (yy < 12 || xc < 4) mlp_part(P, b, w, lane, tlo, thi, budget, tl);
}

constexpr int QMAP = 0;
#define CODE_Y 0x13
__global__ void __launch_bounds__(512, 2) fused_kernel(Params) {
    v16u ka;
    v8u kb;
    v2u kc;
    asm volatile("s_load_dwordx16 %0, %3, 0x0\n\ts_load_dwordx8 %1, %3, 0x40\n\ts_load_dwordx2 %2, %3, 0x60" : "=&s"(ka), "=&s"(kb), "=&s"(kc) : "s"(__builtin_amdgcn_kernarg_segment_ptr()));
    SCHED();
    const int tid = threadIdx.x, lane = tid & 63;
    const int wave = __builtin_amdgcn_readfirstlane(tid >> 6);
    const int b = blockIdx.x, xc = b & 7, y = b >> 3;
    int budget = BUDGET;
    u32 tl = 0;
    u64 pc0;
    asm volatile("s_getpc_b64 %0\n\ts_nop 0" : "=s"(pc0));
    *lp<u32>(L_FL + (u32)(wave >> 2) * 1024 + (u32)(wave & 3) * 4 + (u32)lane * 16) = (wave & 1) ? 0u : (u32)(0x4111111110ull >> (min((u32)(lane * 2 + ((wave >> 1) & 1) - C_GATE / 8), 15u) * 4)) & 15u;
    if (wave >= 4 && (y & CODE_Y) == 0x10) {
        asm volatile("s_mov_b32 m0, %1\n\ts_nop 0\n\tglobal_load_lds_dword %0, %2\n\tglobal_load_lds_dword %0, %3\n\tglobal_load_lds_dword %0, %4"
            :: "v"((u32)lane * 64u + (u32)(wave - 4) * CODE_STR), "s"(L_IMG + (u32)(wave - 4) * 2048u), "s"(pc0), "s"(pc0 + 4096), "s"(pc0 + 8192) : "memory");
    }
    const u32 head = (u32)(xc * 4 + (y >> 2)), j = (u32)y & 3, w = (u32)wave;
    const u32 i15 = lane & 15, g = lane >> 4;
    const u32 ks = b & 15, cg = (u32)(b >> 4) * 2 + (w & 1);
    const u32 hq = QMAP ? (u32)(xc >> 1) * 8 + (u32)(y >> 1) : head, mh = QMAP ? (u32)xc & 1 : j >> 1, dh = j & 1;
    u32 od[4], oi[4], oq = (mh * 256 + w * 32 + (lane & 8) * 2 + g) * 8192u + hq * 256u + dh * 128u + (lane & 7) * 16u;
#pragma unroll
    for (int k = 0; k < 4; ++k) { od[k] = (3 * k + g) * 1024u + w * 256u + ((i15 ^ (g << 2) ^ (u32)k) << 4); PIN(od[k]); }
    oi[0] = (ks * 32 + 2 * (u32)(lane >> 2)) * 2; oi[1] = (u32)lane * 16; oi[2] = cg * 64u + (lane & 3) * 16u; oi[3] = oi[0] * 3072u + oi[2]; oi[2] += g * 4u;
#pragma unroll
    for (int k = 0; k < 4; ++k) PIN(oi[k]);
    PIN(oq);
    v4u wm[4], xa;
    u32 cc, xs;
#pragma unroll
    for (int k = 0; k < 4; ++k) UNDEF(wm[k]);
    UNDEF(xa); UNDEF(cc); UNDEF(xs);
    int role = (0xffaa5550u >> ((y & 16) + 2 * wave)) & 3;
    asm volatile("s_waitcnt lgkmcnt(0)" : "+s"(role), "+s"(ka), "+s"(kb), "+s"(kc) :: "memory");
#define KP(T, v, i) ((T)((u64)v[i + 1] << 32 | v[i]))
    Params P;
    P.wmod = KP(const u16*, ka, 0); P.c = KP(const u16*, ka, 2); P.x = KP(const u16*, ka, 4); P.ws = KP(char*, ka, 6);
    P.wq = KP(const u16*, ka, 8); P.ckv = KP(const u16*, ka, 10); P.wuk = KP(const u16*, ka, 12); P.wo = KP(const u16*, ka, 14);
    P.wuv = KP(const u16*, kb, 0); P.wg = KP(const u16*, kb, 2); P.wu = KP(const u16*, kb, 4); P.wd = KP(const u16*, kb, 6); P.out = KP(u16*, kc, 0);
    char* ws = P.ws;
    if (role == 0) {
        asm volatile("global_load_dword %0, %3, %4\n\tglobal_load_dwordx4 %1, %5, %6\n\tglobal_load_dword %2, %7, %6" : "+v"(cc), "+v"(xa), "+v"(xs)
            : "v"(oi[0]), "s"(P.c), "v"(oi[1]), "s"(P.x), "v"(oi[2]) : "memory");
        asm volatile("global_load_dwordx4 %0, %4, %5 nt\n\tglobal_load_dwordx4 %1, %4, %6 nt\n\tglobal_load_dwordx4 %2, %4, %5 offset:1024 nt\n\tglobal_load_dwordx4 %3, %4, %6 offset:1024 nt"
            : "+v"(wm[0]), "+v"(wm[1]), "+v"(wm[2]), "+v"(wm[3]) : "v"(oi[3]), "s"(P.wmod), "s"(P.wmod + 3072) : "memory");
    }
    const char* const ckv = (const char*)P.ckv;
    const u32 sidx = (u32)b - 128;
    const u64 pcf = pc0 - w * 256u;
    __builtin_amdgcn_s_barrier();
    u64 t0_;
    asm volatile("s_memrealtime %0" : "=s"(t0_));
    v4u wr[4];
#pragma unroll
    for (int k = 0; k < 4; ++k) UNDEF(wr[k]);
    if (role == 1) {
#pragma unroll
        for (int k = 0; k < 4; ++k) ld_nt_into(wr[k], P.wq + k * 16384, oq);
    }
    asm volatile("s_waitcnt lgkmcnt(0)" : "+s"(t0_));
    const u32 t0 = (u32)t0_;
    STAMP(20);
    asm volatile("" : "+s"(role));
    if (role >= 2) {
        if (role == 2) {
            const u32 p = w;
#pragma unroll
            for (int h = 0; h < HW; ++h) {
                if (h == 0) until(t0 + T_OPEN0);
                if (h == 2) until(t0 + T_OPEN1);
                ISSUE(h);
            }
            stream_pv(P, b, w, lane, od, pcf, t0, tl);
        } else stream_qk(P, b, w - 4, lane, t0, budget, tl);
        STAMP(15);
        STAMP_OUT();
        return;
    }
    u64 e64;
    u32 tb, tq;
    {
        const u32 p = lane & 3;
        v4u wk[8];
#pragma unroll
        for (int k = 0; k < 8; ++k) UNDEF(wk[k]);
        if (b < 8 && tid == 128) st_sc2(ws, WS_ST + (u32)b * 8u, (v2u){t0, ~t0});
        EPOCH();
        const u32 tlo = (u32)e64, thi = (u32)(e64 >> 32);
        u32 warm;
        UNDEF(warm);
        if (wave == 7) asm volatile("global_load_dword %0, %1, %2\n\tglobal_load_dword %0, %1, %3\n\tglobal_load_dword %0, %1, %4\n\tglobal_load_dword %0, %5, %6" : "+v"(warm)
            : "v"(((u32)lane & 1) * ((4u << 20) - 4u)), "s"(P.wuk), "s"(P.wo), "s"(P.wuv), "v"(WS_PAY + (((u32)lane & 3) * 10 + 1) * PHD + 4 * 128 * 256), "s"(late_ws(ws, tlo)) : "memory");
        if (wave == 3) {
            const u32 rh = 8 * ((hq & 7) >> 1) + 4 * (hq >> 4) + 2 * (hq & 1) + ((hq >> 3) & 1);
            if (mh + dh == 0 && lane < 8) st_sc(ws, WS_QCHK + rh * 128u + (u32)lane * 16u, (v4u){0u, ~tlo, 0u, ~tlo});
            if (b == 9 && lane < 8 * NQH) st_sc(ws, WS_QH + (u32)lane * 16u, (v4u){0u, ~tlo, 0u, ~tlo});
            if (b == 10) st_sc(late_ws(ws, tlo), WS_FC + (u32)lane * 16u, (v4u){0u, ~tlo, 0u, ~tlo});
        }
        if (role == 0) {
            u32 so = piece(WS_HP, 2048, cg) + (ks * 8 + 2 * p + (u32)(lane >> 5)) * 16u;
            VMWAIT(4);
            PIN(xa); PIN(xs); PIN(cc);
            float mean, rstd;
            ln_stats(xa, mean, rstd);
            const u32 sp = pack2(silu(bflo(cc)), silu(bfhi(cc)));
            u32 s0l = sp & 0xffffu, s0h = sp << 16, s1l = sp >> 16, s1h = sp & 0xffff0000u;
            float xn0 = (bflo(xs) - mean) * rstd, xn1 = (bfhi(xs) - mean) * rstd;
            PIN(xn0); PIN(xn1); PIN(s0l); PIN(s0h); PIN(s1l); PIN(s1h); PIN(so);
            VMWAIT(0);
#pragma unroll
            for (int k = 0; k < 4; ++k) PIN(wm[k]);
            STAMP(22);
            float ta[4], tc[4];
#pragma unroll
            for (int k = 0; k < 2; ++k) {
                ta[2 * k] = red32(dot2(wm[1][k], s1l, dot2(wm[0][k], s0l, 0.f)), dot2(wm[1][k + 2], s1l, dot2(wm[0][k + 2], s0l, 0.f)));
                ta[2 * k + 1] = red32(dot2(wm[1][k], s1h, dot2(wm[0][k], s0h, 0.f)), dot2(wm[1][k + 2], s1h, dot2(wm[0][k + 2], s0h, 0.f)));
                tc[2 * k] = red32(dot2(wm[3][k], s1l, dot2(wm[2][k], s0l, 0.f)), dot2(wm[3][k + 2], s1l, dot2(wm[2][k + 2], s0l, 0.f)));
                tc[2 * k + 1] = red32(dot2(wm[3][k], s1h, dot2(wm[2][k], s0h, 0.f)), dot2(wm[3][k + 2], s1h, dot2(wm[2][k + 2], s0h, 0.f)));
            }
            float u0 = xn0 * (red16(tc[0], tc[2]) + 0.015625f) + red16(ta[0], ta[2]), u1 = xn1 * (red16(tc[1], tc[3]) + 0.015625f) + red16(ta[1], ta[3]);
            u0 += dpp<0x124>(u0); u1 += dpp<0x124>(u1); u0 += dpp<0x128>(u0); u1 += dpp<0x128>(u1);
            u32 e, od;
            both16(pack2(u0, u1), e, od);
            if ((lane & 28) == 0) st_sc(ws, so, (v4u){e, od, tlo, thi});
            STAMP(24);
#pragma unroll
            for (int k = 0; k < 4; ++k) ld_nt_into(wr[k], P.wq + k * 16384, oq);
        }
        u32 so = piece(WS_QP, 1024, hq) + ((mh * 2 + (u32)(lane >> 5)) * 16 + dh * 8 + (lane & 7)) * 16u, ho = piece(WS_HP, 2048, mh * 8 + w) + (u32)lane * 16u;
        u32 la = F_QP + w * 256 + (g * 8 + (lane & 7)) * 8, lb = la + (lane & 8) * 128, ok = ((u32)y * 32 + g * 8) * 8192u + ((u32)xc * 4 + (w & 3)) * 256u + i15 * 16u;
        PIN(so); PIN(ho); PIN(la); PIN(lb); PIN(ok);
        u32 onel = 0x3f80u, oneh = 0x3f800000u;
        PIN(onel); PIN(oneh);
        v4u hg[2];
        UNDEF(hg[0]); UNDEF(hg[1]);
        __builtin_amdgcn_s_barrier();
        const u32 tm = now32();
        until(t0 + ST_LOOK);
        tb = time_base(ws + WS_ST, t0);
        const u32 ta = tb + max((u32)A_FIRST, tm - t0 + A_OWN);
        until(ta);
        ld_sc_into(hg[0], ws, ho); ld_sc_into(hg[1], ws + 1024, ho);
        __builtin_amdgcn_s_barrier();
        if (wave >= 4) {
#pragma unroll
            for (int k = 0; k < 8; ++k) ld_nt_into(wk[k], P.wuk + k * 4096, ok);
            VMWAIT(10);
        } else VMWAIT(2);
#pragma unroll
        for (int k = 0; k < 4; ++k) PIN(wr[k]);
        PIN(warm);
        STAMP(1);
        pair_rows(wr[0], wr[1]); pair_rows(wr[2], wr[3]);
#pragma unroll
        for (int k = 0; k < 4; ++k) PIN(wr[k]);
        if (wave >= 4) VMWAIT(8); else VMWAIT(0);
        PIN(hg[0]); PIN(hg[1]);
        if (__builtin_expect(ballot(neq3(hg[0].z, hg[1].z, tlo) | neq3(hg[0].w, hg[1].w, thi)) != 0ull, 0)) gather<2, 1024, 1>(ws, ho, tlo, thi, hg, budget);
        STAMP(3);
        {
            float hx = red16(red32(dot2(hg[1].x, onel, bflo(hg[0].x)), dot2(hg[1].y, onel, bflo(hg[0].y))), red32(dot2(hg[1].x, oneh, bfhi(hg[0].x)), dot2(hg[1].y, oneh, bfhi(hg[0].y))));
            hx += dpp<0x128>(hx);
            hx = asf((u32)__builtin_amdgcn_update_dpp((int)asu(hx), (int)asu(hx), 0x114, 0xf, 0x6, false));
            const u32 hp = pack2(hx, dpp<0xB1>(hx)), h01 = dppu<0x00>(hp), h23 = dppu<0xAA>(hp);
            float acc[8];
#pragma unroll
            for (int k = 0; k < 4; ++k) { acc[2 * k] = dot2(wr[2][k], h23, dot2(wr[0][k], h01, 0.f)); acc[2 * k + 1] = dot2(wr[3][k], h23, dot2(wr[1][k], h01, 0.f)); }
            float t[4];
#pragma unroll
            for (int k = 0; k < 4; ++k) t[k] = red32(acc[k], acc[k + 4]);
            float u0 = red16(t[0], t[2]), u1 = red16(t[1], t[3]);
            u0 += dpp<0x128>(u0); PIN(u0); u1 += dpp<0x128>(u1);
            *lp<f32x2>(la) = (f32x2){u0, u1};
        }
        barrier();
        if (wave == 0) {
            const LAS f32x2* qp = lp<f32x2>(lb);
            const f32x2 q2 = (qp[0] + qp[32]) + (qp[64] + qp[96]);
            float u0 = q2.x, u1 = q2.y;
            u0 += dpp<0x128>(u0); PIN(u0); u1 += dpp<0x128>(u1);
            u32 e, od;
            both16(pack2(u0, u1), e, od);
            if ((lane & 24) == 0) st_sc(ws, so, (v4u){e, od, tlo, thi});
            STAMP(2);
        }
        tq = ta + max((u32)A2_T, now32() - ta + A2_DELAY);
        if (y == ((xc * 5) & 7) + (y & 8) && tid < NQH) st_sc2(ws, WS_QH + (u32)lane * 128u + (u32)(xc * 2 + (y >> 3)) * 8u, (v2u){tq, tq ^ tlo});
        if (wave >= 4) {
            const u32 hk = (u32)xc * 4 + (w & 3);
            const u32 rho = 8 * ((hk & 7) >> 1) + 4 * (hk >> 4) + 2 * (hk & 1) + ((hk >> 3) & 1);
            const u32 og = piece(WS_QP, 1024, hk) + (u32)lane * 16u;
            u32 sq = WS_QABS + j * 8192u + (rho ^ ((tlo & 1) << 4)) * 256u + (u32)(y >> 2) * 64u + g * 16u, sc = WS_QCHK + (rho * 16 + (u32)y) * 8u;
            PIN(sq); PIN(sc);
            v4u qg[1];
            until(tq);
            gather<1, 0, 1>(ws, og, tlo, thi, qg, budget);
            __builtin_amdgcn_s_barrier();
            STAMP(23);
            u32 e, od, q0, q1, q2, q3;
            both16(pack2(red32(bflo(qg[0].x), bflo(qg[0].y)), red32(bfhi(qg[0].x), bfhi(qg[0].y))), e, od);
            both32(e, q0, q1); both32(od, q2, q3);
            float a[8];
#pragma unroll
            for (int k = 0; k < 8; ++k) { PIN(wk[k]); a[k] = dot2(wk[k][3], q3, dot2(wk[k][2], q2, dot2(wk[k][1], q1, dot2(wk[k][0], q0, 0.f)))); }
            float z[4];
#pragma unroll
            for (int k = 0; k < 4; ++k) {
                const float keep = (lane & 1) ? a[k + 4] : a[k], give = (lane & 1) ? a[k] : a[k + 4];
                float s = keep + dpp<0xB1>(give);
                s += dpp<0x122>(s); s += dpp<0x124>(s); s += dpp<0x128>(s);
                z[k] = s * (0.08838834764831845f * 1.4426950408889634f);
            }
            const u32 y0 = pack2(z[0], z[1]), y1 = pack2(z[2], z[3]);
            const u32 y2 = dppu<0xB1>(y0), y3 = dppu<0xB1>(y1);
            if (i15 == 0) st_sc(ws, sq, (v4u){y0, y1, y2, y3});
            u32 c0, c1;
            both16(mix4((v4u){y0, y1, y2, y3}), c0, c1);
            both32(c0 + c1, c0, c1);
            if (lane == 0) st_sc2(ws, sc, (v2u){c0 + c1, tlo});
            STAMP(4);
        } else __builtin_amdgcn_s_barrier();
    }
    const u32 tlo = (u32)e64, thi = (u32)(e64 >> 32);
    ws = late_ws(ws, tlo);
    const bool is_red = (y >> 2) == 1;
    const u32 rid = (u32)xc * 4 + j;
    bf16x8 bo[16], bu[4];
    u32 xo, aa, cw, cr, op, o, oc, wa, he, wl, rl, om, xr;
#define TAIL_ADDR() do { \
        xo = piece(WS_X1, 4096, (u32)(lane >> 3)) + (rid * 8 + (u32)(lane & 7)) * 16u; \
        aa = F_A + (i15 & 7) * 272 + g * 16; cw = F_CX + ((w - 2 * j) & 7) * 32 + i15 * 2; cr = F_CX + g * 16; \
        op = piece(WS_OUTP, 16384, 4 * w + g) + (u32)b * 128u + (i15 & 7) * 16u; \
        wl = F_LS + (i15 < 4 ? (w * 16 + g * 4 + i15) * 4 : 512u); rl = F_LS + (u32)lane * 8; PIN(wl); PIN(rl); \
        o = WS_PAY + head * PHD + (j * 128 + ((w * 16) ^ late_flip(tlo))) * 256u + (u32)lane * 16u; \
        oc = WS_CHK + ((w * 16 + (i15 & 3) * 4 + g) * 4 + j) * 512u + head * 16u; \
        wa = F_A + w * 272 + i15 * 16 + g * 4; \
        om = WS_ML + (rid >> 1) * 4096u + (g * 16 + (rid & 1) * 8 + (u32)(lane & 7)) * 16u; PIN(om); \
        he = WS_HE + (((u32)b + 17u * (u32)lane) & 127u) * HSTR + (u32)lane * 16u; PIN(he); \
        PIN(xo); PIN(aa); PIN(cw); PIN(cr); PIN(op); PIN(o); PIN(oc); PIN(wa); } while (0)
    {
        const u32 kq = (u32)b & 3, cgl = (u32)b >> 2;
        const u32 w4 = w & 3, mrow = kq * 128 + w4 * 32 + (u32)(lane >> 3);
        const u32 q4 = i15 >> 2, p4 = lane & 3;
        const u32 ra = 8 * (g & 1) + q4, rb = ra + 4;
        u32 xa[4], xb[4];
#pragma unroll
        for (int t2 = 0; t2 < 4; ++t2) {
            const u32 c0 = 8 * w + (p4 >> 1) + 2 * t2, x0 = F_WO + (8 * g + q4) * 1024 + 8 * (p4 & 1);
            xa[t2] = x0 + ((c0 ^ swz(ra)) << 4); xb[t2] = x0 + 4096 + ((c0 ^ swz(rb)) << 4);
            PIN(xa[t2]); PIN(xb[t2]);
        }
        u32 x0, x1, ma = F_ML + w * 256 + ((lane & 7) * 4 + g) * 8;
        {
            const u32 c0 = 2 * w + (p4 >> 1);
            x0 = F_UV + (8 * g + q4) * 256 + 8 * (p4 & 1) + ((c0 ^ swz(ra)) << 4); x1 = F_UV + (8 * g + q4 + 4) * 256 + 8 * (p4 & 1) + ((c0 ^ swz(rb)) << 4);
        }
        PIN(x0); PIN(x1); PIN(ma);
#define PIECE(k) until(tq + T_OWN + (k) * O_STEP)
#define WO_ROW(s, k) dma_pl(P.wo + ((j + (s)) & 3) * 16384, (head * 128 + 8 * w4 + (k)) * 1024u + (((u32)lane ^ swz(8 * (w4 & 1) + (k))) << 4), F_WO + ((s) * 32 + 8 * w4 + (k)) * 1024u)
#define UV_ROWS(i) do { const u32 e_ = w4 * 32 + (i) * 4 + g, c_ = ((e_ >> 2) & 1) * 64 + (e_ & 1) * 32 + ((e_ >> 1) & 1) * 16 + (e_ >> 3); \
            dma_nt(P.wuv, (j * 128 + c_) * 8192u + head * 256u + ((i15 ^ swz(e_ & 15)) << 4), F_UV + (w4 * 32 + (i) * 4) * 256u); } while (0)
        v4u mw[4];
        u32 cv[4];
#pragma unroll
        for (int k = 0; k < 4; ++k) { UNDEF(mw[k]); UNDEF(cv[k]); }
        u32 mo = mrow * 6144u + 2048u + cgl * 128u + (u32)(lane & 7) * 16u;
        PIN(mo);
        if (wave < 4) {
#pragma unroll
            for (int k = 0; k < 4; ++k) { PIECE(k); WO_ROW(0, 2 * k); WO_ROW(0, 2 * k + 1); }
            asm volatile("global_load_ushort %0, %4, %5\n\tglobal_load_ushort %1, %4, %5 offset:16\n\tglobal_load_ushort %2, %4, %5 offset:32\n\tglobal_load_ushort %3, %4, %5 offset:48"
                : "+v"(cv[0]), "+v"(cv[1]), "+v"(cv[2]), "+v"(cv[3]) : "v"(mrow * 2), "s"(P.c) : "memory");
            PIECE(4);
            ld_nt_into(mw[0], P.wmod, mo); ld_nt_into(mw[1], P.wmod + 8 * 3072, mo);
            VMWAIT(2);
        }
        TAIL_ADDR();
        until(tq + T_MID);
        STAMP(5);
        barrier();
        if (wave >= 4) {
#pragma unroll
            for (int k = 8; k < 32; ++k) WO_ROW(k >> 3, k & 7);
            STAMP(19);
        }
#pragma unroll
        for (int t2 = 0; t2 < 4; ++t2) { bo[4 * t2] = tr2(xa[t2], xb[t2]); xa[t2] += 32768; xb[t2] += 32768; PIN(xa[t2]); PIN(xb[t2]); }
        float u[2] = {0.f, 0.f};
        if (wave < 4) {
            const u32 t2 = now32() + U_GAP;
            until(t2);
            ld_nt_into(mw[2], P.wmod + 16 * 3072, mo); ld_nt_into(mw[3], P.wmod + 24 * 3072, mo);
#pragma unroll
            for (int k = 0; k < 4; ++k) { until(t2 + (k + 1) * U_STEP); UV_ROWS(2 * k); UV_ROWS(2 * k + 1); }
            VMWAIT(8);
#pragma unroll
            for (int k = 0; k < 4; ++k) { PIN(mw[k]); PIN(cv[k]); }
            const u32 sp = pack2(silu(bflo(cv[0])), silu(bflo(cv[1]))), sr = pack2(silu(bflo(cv[2])), silu(bflo(cv[3])));
            pair_rows(mw[0], mw[1]); pair_rows(mw[2], mw[3]);
            float t[4];
#pragma unroll
            for (int k = 0; k < 2; ++k) {
                t[2 * k] = red32(dot2(mw[2][k], sr, dot2(mw[0][k], sp, 0.f)), dot2(mw[2][k + 2], sr, dot2(mw[0][k + 2], sp, 0.f)));
                t[2 * k + 1] = red32(dot2(mw[3][k], sr, dot2(mw[1][k], sp, 0.f)), dot2(mw[3][k + 2], sr, dot2(mw[1][k + 2], sp, 0.f)));
            }
#pragma unroll
            for (int k = 0; k < 2; ++k) { float z = red16(t[k], t[k + 2]); z += dpp<0x128>(z); u[k] = z; }
        } else VMWAIT(0);
        PIN(u[0]); PIN(u[1]);
#pragma unroll
        for (int t2 = 0; t2 < 4; ++t2) PIN(bo[4 * t2]);
        STAMP(14);
        barrier();
        *lp<f32x2>(ma) = (f32x2){u[0], u[1]};
        if (tid < 12) *lp<u32>(F_ST + tid * 4) = 0u;
        barrier();
        if (wave == 7) {
            const LAS f32x2* q = lp<f32x2>(F_ML) + lane;
            const f32x2 s = q[0] + q[64];
            const float sx = red32(s.x, s.x), sy = red32(s.y, s.y);
            if (lane < 32) st_sc(ws, WS_ML + (cgl & 7) * 8192u + ((u32)lane >> 4) * 4096u + (cgl >> 3) * 1024u + (kq * 16 + ((u32)lane & 15)) * 16u, (v4u){asu(sx), asu(sy), tlo, thi});
        }
#pragma unroll
        for (int t2 = 0; t2 < 4; ++t2) { bo[4 * t2 + 1] = tr2(xa[t2], xb[t2]); bo[4 * t2 + 2] = tr2(xa[t2] + 32768, xb[t2] + 32768); bo[4 * t2 + 3] = tr2(xa[t2] + 65536, xb[t2] + 65536); }
        xr = ld_u32(P.x, (rid * 8 + (u32)(lane & 7)) * 4);
        VMWAIT(0);
        PIN(xr);
#pragma unroll
        for (int k = 0; k < 16; ++k) PIN(bo[k]);
        STAMP(6);
        barrier();
#pragma unroll
        for (int s = 0; s < 4; ++s) bu[s] = tr2(x0 + s * 8192, x1 + s * 8192);
#pragma unroll
        for (int k = 0; k < 4; ++k) PIN(bu[k]);
    }
    u32 tk1, tk2;
    float ga0 = 0.f, ga1 = 0.f, gb0 = 0.f, gb1 = 0.f, xv0 = 0.f, xv1 = 0.f;
    STAMP(7);
    {
        v4u v[4], ck = {0u, 0u, 0u, 0u};
        u32 onel = 0x3f80u, oneh = 0x3f800000u;
        PIN(onel); PIN(oneh);
#pragma unroll
        for (int k = 0; k < 4; ++k) UNDEF(v[k]);
        v4u gm[4];
#pragma unroll
        for (int k = 0; k < 4; ++k) UNDEF(gm[k]);
        if (is_red && wave == 0) asm volatile("global_load_dwordx4 %0, %4, %5 sc1\n\tglobal_load_dwordx4 %1, %4, %5 offset:1024 sc1\n\tglobal_load_dwordx4 %2, %4, %5 offset:2048 sc1\n\tglobal_load_dwordx4 %3, %4, %5 offset:3072 sc1"
            : "+v"(gm[0]), "+v"(gm[1]), "+v"(gm[2]), "+v"(gm[3]) : "v"(om), "s"(ws) : "memory");
        look_times(ws, lane, tlo, budget, tk1, tk2);
#pragma unroll
        for (int k = 0; k < 4; ++k) PIN(gm[k]);
        if (is_red && wave == 0) {
            if (ballot((neq3(gm[0].z, gm[1].z, tlo) | neq3(gm[2].z, gm[3].z, tlo)) | (neq3(gm[0].w, gm[1].w, thi) | neq3(gm[2].w, gm[3].w, thi))) != 0ull) gather<4, 1024, 1>(ws, om, tlo, thi, gm, budget);
            xv0 = bflo(xr); xv1 = bfhi(xr);
            {
                const float s0 = red16(red32(asf(gm[1].x), asf(gm[2].x)), red32(asf(gm[1].y), asf(gm[2].y)));
                u32 e, od, sbp, cbp;
                both16(asu(s0), e, od);
                both32(pack2(asf(e), asf(od)), sbp, cbp);
                if (lane < 8) st_sc(ws, WS_SC + (rid * 8 + (u32)lane) * 16u, (v4u){cbp, sbp, tlo, thi});
            }
            ga0 = asf(gm[0].x); ga1 = asf(gm[0].y); gb0 = asf(gm[3].x); gb1 = asf(gm[3].y);
            ga0 = red32(ga0, ga0); ga0 = red16(ga0, ga0); ga1 = red32(ga1, ga1); ga1 = red16(ga1, ga1);
            gb0 = red32(gb0, gb0); gb0 = red16(gb0, gb0); gb1 = red32(gb1, gb1); gb1 = red16(gb1, gb1);
        }
        PIN(ga0); PIN(ga1); PIN(gb0); PIN(gb1); PIN(xv0); PIN(xv1);
        until(tk2);
        STAMP(8);
#pragma clang loop unroll(disable)
        for (;;) {
            asm volatile("global_load_dwordx4 %0, %5, %7 sc1\n\tglobal_load_dwordx4 %1, %5, %7 offset:1024 sc1\n\tglobal_load_dwordx4 %2, %5, %7 offset:2048 sc1\n\tglobal_load_dwordx4 %3, %5, %7 offset:3072 sc1\n\t"
                "s_mov_b64 exec, %8\n\ts_waitcnt vmcnt(3)\n\tglobal_load_dwordx4 %4, %6, %7 sc1\n\ts_mov_b64 exec, -1"
                : "+v"(v[0]), "+v"(v[1]), "+v"(v[2]), "+v"(v[3]), "+v"(ck) : "v"(o), "v"(oc), "s"(ws), "s"(0x000f000f000f000full) : "memory");
            float a[8];
#pragma unroll
            for (int q = 0; q < 4; ++q) { a[2 * q] = bflo(v[0][q]); a[2 * q + 1] = bfhi(v[0][q]); }
            u32 x = mix4(v[0]);
#pragma unroll
            for (int k = 1; k < 4; ++k) {
#pragma unroll
                for (int q = 0; q < 8; ++q) PIN(a[q]);
                PIN(x);
                asm volatile("s_waitcnt vmcnt(%1)" : "+v"(v[k]) : "i"(4 - k) : "memory");
                x += mix4(v[k]);
#pragma unroll
                for (int q = 0; q < 4; ++q) { a[2 * q] = dot2(v[k][q], onel, a[2 * q]); a[2 * q + 1] = dot2(v[k][q], oneh, a[2 * q + 1]); }
            }
            float t[4];
            x += dppu<0x128>(x);
#pragma unroll
            for (int k = 0; k < 4; ++k) t[k] = red32(a[k], a[k + 4]);
            x += dppu<0x124>(x);
            float u0 = red16(t[0], t[2]); PIN(u0);
            *lp<u32>(wa) = pack2(u0, red16(t[1], t[3]));
            PIN(x);
            asm volatile("s_waitcnt vmcnt(0)" : "+v"(ck) :: "memory");
            *lp<u32>(wl) = ck.y;
            x -= ck.x;
            x += dppu<0xB1>(x); x += dppu<0x4E>(x);
            const u64 bad = ballot((x | (ck.w ^ tlo)) != 0u) & 0x000f000f000f000full;
            if (__builtin_expect((bad | ballot(ck.z != 0u)) == 0ull, 1)) break;
            if (bad == 0ull || --budget <= 0) {
                const float m = i15 < 4 ? asf(ck.z) : -3.0e38f;
                float M = __builtin_fmaxf(m, dpp<0xB1>(m)); M = __builtin_fmaxf(M, dpp<0x4E>(M));
                M = asf(rfl(asu(max32(max16(M)))));
                const u32 f = pack2(ex2(m - M), 0.f);
                const u32 f0 = dppu<0x150>(f), f1 = dppu<0x151>(f), f2 = dppu<0x152>(f), f3 = dppu<0x153>(f);
#pragma unroll
                for (int q = 0; q < 4; ++q) {
                    a[2 * q] = dot2(v[3][q], f3, dot2(v[2][q], f2, dot2(v[1][q], f1, dot2(v[0][q], f0, 0.f))));
                    a[2 * q + 1] = dot2(v[3][q], f3 << 16, dot2(v[2][q], f2 << 16, dot2(v[1][q], f1 << 16, dot2(v[0][q], f0 << 16, 0.f))));
                }
#pragma unroll
                for (int k = 0; k < 4; ++k) t[k] = red32(a[k], a[k + 4]);
                *lp<u32>(wa) = pack2(red16(t[0], t[2]), red16(t[1], t[3]));
                *lp<float>(wl) = asf(ck.y) * bflo(f);
                *lp<float>(F_ST + w * 4) = M;
                *lp<u32>(F_ST + 32) = 1u;
                break;
            }
            __builtin_amdgcn_s_sleep(POLL_SLEEP);
        }
        STAMP(9);
    }
    barrier();
    u64 tc1;
    asm volatile("s_memrealtime %0" : "=s"(tc1));
    STAMP(10);
    float ls;
    {
        bf16x8 af[4];
#pragma unroll
        for (int s = 0; s < 4; ++s) af[s] = *lp<bf16x8>(aa + s * 64);
        const u32 rare = *lp<u32>(F_ST + 32);
        const f32x2 l2 = *lp<f32x2>(rl);
        SCHED();
        f32x4 d = {0.f, 0.f, 0.f, 0.f};
#pragma unroll
        for (int s = 0; s < 4; ++s) d = __builtin_amdgcn_mfma_f32_16x16x32_bf16(af[s], bu[s], d, 0, 0, 0);
        ls = l2.x + l2.y;
        if (__builtin_expect(rfl(rare) != 0u, 0)) {
            const f32x4 m0 = *lp<f32x4>(F_ST), m1 = *lp<f32x4>(F_ST + 16);
            const float M = max8(m0, m1);
#pragma unroll
            for (int k = 0; k < 4; ++k) d[k] *= ex2(((g & 1) ? m1[k] : m0[k]) - M);
            ls *= ex2(*lp<float>(F_ST + (u32)(lane >> 3) * 4) - M);
        }
        float c0 = d.x + d.y, c1 = d.z + d.w;
        PIN(c0); PIN(c1);
        float c = c0 + c1, c2 = c0 + c1;
        PIN(c); PIN(c2);
        c = red16(c, c2);
        *lp<u16>(cw) = (u16)pack2(c, c);
    }
    barrier();
    STAMP(13);
    {
        bf16x8 af[4];
#pragma unroll
        for (int s = 0; s < 4; ++s) af[s] = *lp<bf16x8>(cr + s * 64);
        ls += dpp<0x128>(ls); ls += dpp<0x124>(ls); ls += dpp<0x122>(ls); ls += dpp<0x121>(ls);
        ls = red16(ls, ls); ls = red32(ls, ls);
        ls = __builtin_amdgcn_rcpf(ls);
        v4u r = {0u, 0u, tlo, thi};
        PIN(ls); PIN(r);
        SCHED();
        f32x4 d[4];
#pragma unroll
        for (int t = 0; t < 4; ++t) d[t] = (f32x4){0.f, 0.f, 0.f, 0.f};
#pragma unroll
        for (int s = 0; s < 4; ++s) {
#pragma unroll
            for (int t = 0; t < 4; ++t) d[t] = __builtin_amdgcn_mfma_f32_16x16x32_bf16(af[s], bo[4 * t + s], d[t], 0, 0, 0);
            SCHED();
        }
        r.x = asu(((g & 2) ? ((g & 1) ? d[3].x : d[2].x) : ((g & 1) ? d[1].x : d[0].x)) * ls);
        r.y = dppu<0x128>(r.x);
        asm volatile("s_mov_b64 exec, %3\n\tglobal_store_dwordx4 %0, %1, %2 sc1\n\ts_mov_b64 exec, -1\n\ts_nop 0" :: "v"(op), "v"(r), "s"(ws), "s"(0x00ff00ff00ff00ffull) : "memory");
    }
    STAMP(11);
    asm volatile("" : "+s"(tc1));
    if (wave == 2 && lane < 8) st_sc(ws, he, (v4u){(u32)tc1, 0u, tlo, thi});
    if (b == 0 && tid == 64) st_sc2(P.ws, WS_EPOCH, (v2u){tlo, thi});
    if (is_red) {
        const f32x2 s = reduce16<8, true>(ws, piece(WS_OUTP, 16384, rid), tlo, thi, wave, lane, F_RD, tk2 + max((u32)D_T, (u32)tc1 - tk2 + D_DELAY), budget);
        const float x10 = xv0 + ga0 * s.x, x11 = xv1 + ga1 * s.y;
        if (wave == 0) st_sc(ws, xo, (v4u){asu(x10), asu(x11), tlo, thi});
        STAMP(12);
        u32 oa = (rid * 8 + (u32)lane) * 4u;
        PIN(oa);
        const u32 tx = now32() - tk2;
        const f32x2 s2 = reduce16<6, false>(ws, piece(WS_YP, 16384, rid), tlo, thi, wave, lane, F_RV, tk2 + (tx > (u32)F_LIM ? tx : max((u32)F_T, tx + F_DELAY)), budget);
        if (tid < 8) asm volatile("global_store_dword %0, %1, %2" :: "v"(oa), "v"(pack2(x10 + gb0 * s2.x, x11 + gb1 * s2.y)), "s"(P.out) : "memory");
    }
    STAMP(15);
    STAMP_OUT();
}

extern "C" void kernel_launch(void* const* d_in, const int* in_sizes, int n_in, void* d_out, int out_size, void* d_ws, size_t ws_size, hipStream_t stream) {
    Params P;
    P.x = (const u16*)d_in[0]; P.ckv = (const u16*)d_in[1]; P.wq = (const u16*)d_in[2]; P.wuk = (const u16*)d_in[3];
    P.wuv = (const u16*)d_in[4]; P.wo = (const u16*)d_in[5]; P.wg = (const u16*)d_in[7]; P.wu = (const u16*)d_in[8];
    P.wd = (const u16*)d_in[9]; P.c = (const u16*)d_in[10]; P.wmod = (const u16*)d_in[11];
    P.out = (u16*)d_out; P.ws = (char*)d_ws + WS_BASE(ws_size);
    (void)hipFuncSetAttribute((const void*)fused_kernel, hipFuncAttributeMaxDynamicSharedMemorySize, LDS_BYTES);
    fused_kernel<<<256, 512, LDS_BYTES, stream>>>(P);
}
```
